```python
import torch

def _softmax(x, axis=-1):
    return torch.softmax(x, dim=axis)
def _gelu(x):
    return torch.nn.functional.gelu(x, approximate="tanh")
def _silu(x):
    return torch.nn.functional.silu(x)
def _sigmoid(x):
    return torch.sigmoid(x)
def _layernorm(x, axis=-1, eps=1e-5):
    m = x.mean(dim=axis, keepdim=True)
    v = x.var(dim=axis, keepdim=True, unbiased=False)
    return (x - m) / torch.sqrt(v + eps)
def _scatter_add(tgt, idx, src, axis=0):
    return tgt.clone().index_add_(axis, idx, src)
def _segment_sum(src, seg, n):
    out = torch.zeros((n,) + src.shape[1:], dtype=src.dtype)
    return out.index_add_(0, seg, src)
def _pad(x, pw):
    flat = tuple(v for pair in reversed(pw) for v in pair)
    return torch.nn.functional.pad(x, flat)

kernel_name = 'standalone__kernel_systems_refs_featgraph__graph'

def setup_inputs():

    _w = (lambda a: (lambda t: t.to(torch.bfloat16) if t.is_floating_point() else t)(torch.as_tensor(a)))
    N, deg, D = 25000, 16, 128
    E = N * deg
    scale = D ** -0.5
    src = (lambda lo, hi, *s: torch.randint(lo, hi, s))(0, N, E).to(torch.int64)
    dst = (lambda lo, hi, *s: torch.randint(lo, hi, s))(0, N, E).to(torch.int64)
    return {
        "x": _w((lambda *s: torch.randn(*s, dtype=torch.bfloat16) * 0.8)(N, D)),
        "src": _w(src),
        "dst": _w(dst),
        "w_q": _w((lambda *s: torch.randn(*s, dtype=torch.bfloat16) * 0.8)(D, D) * scale),
        "w_v": _w((lambda *s: torch.randn(*s, dtype=torch.bfloat16) * 0.05)(D, D) * scale),
    }

def reference(x, src, dst, w_q, w_v):
    N, D = x.shape
    xs = x[src]
    xd = x[dst]
    q = xd @ w_q
    logits = torch.einsum('ed,ed->e', xs, q) * (float(D) ** -0.5)
    alpha = _sigmoid(logits)
    msg = alpha[:, None] * (xs @ w_v)
    return _segment_sum(msg, dst, N)
```

```cpp
#include <hip/hip_runtime.h>
#include <hip/hip_bf16.h>
#include <stdint.h>

static __device__ __forceinline__ float bf2f(unsigned short v) { return __uint_as_float(((unsigned)v) << 16); }
static __device__ __forceinline__ unsigned short f2bf(float f) { __hip_bfloat16 b = __float2bfloat16(f); return *reinterpret_cast<unsigned short*>(&b); }

__global__ void k_q(const unsigned short* x, const unsigned short* wq, unsigned short* q, float* agg, int N) {
    int n = blockIdx.x, o = threadIdx.x;
    __shared__ float xs[128];
    xs[o] = bf2f(x[n * 128 + o]);
    __syncthreads();
    float acc = 0.f;
    for (int d = 0; d < 128; ++d) acc += xs[d] * bf2f(wq[d * 128 + o]);
    q[n * 128 + o] = f2bf(acc);
    agg[n * 128 + o] = 0.f;
}
__global__ void k_edge(const unsigned short* x, const unsigned short* q, const int* src, const int* dst, float* agg, int E) {
    int e = blockIdx.x * 4 + (threadIdx.x >> 6);
    int l = threadIdx.x & 63;
    if (e >= E) return;
    int s = src[e], d = dst[e];
    float x0 = bf2f(x[s * 128 + 2 * l]), x1 = bf2f(x[s * 128 + 2 * l + 1]);
    float p = x0 * bf2f(q[d * 128 + 2 * l]) + x1 * bf2f(q[d * 128 + 2 * l + 1]);
    for (int m = 32; m; m >>= 1) p += __shfl_xor(p, m);
    float a = 1.f / (1.f + __expf(-p * 0.08838834764831845f));
    atomicAdd(&agg[d * 128 + 2 * l], a * x0);
    atomicAdd(&agg[d * 128 + 2 * l + 1], a * x1);
}
__global__ void k_v(const float* agg, const unsigned short* wv, unsigned short* out, int N) {
    int n = blockIdx.x, o = threadIdx.x;
    __shared__ float xs[128];
    xs[o] = bf2f(f2bf(agg[n * 128 + o]));
    __syncthreads();
    float acc = 0.f;
    for (int d = 0; d < 128; ++d) acc += xs[d] * bf2f(wv[d * 128 + o]);
    out[n * 128 + o] = f2bf(acc);
}

extern "C" void kernel_launch(void* const* d_in, const int* in_sizes, int n_in,
                              void* d_out, int out_size, void* d_ws, size_t ws_size,
                              hipStream_t stream) {
    const unsigned short* x = (const unsigned short*)d_in[0];
    const int* src = (const int*)d_in[1];
    const int* dst = (const int*)d_in[2];
    const unsigned short* wq = (const unsigned short*)d_in[3];
    const unsigned short* wv = (const unsigned short*)d_in[4];
    int N = in_sizes[0] / 128, E = in_sizes[1];
    float* agg = (float*)d_ws;
    unsigned short* q = (unsigned short*)((char*)d_ws + (size_t)N * 128 * 4);
    k_q<<<N, 128, 0, stream>>>(x, wq, q, agg, N);
    k_edge<<<(E + 3) / 4, 256, 0, stream>>>(x, q, src, dst, agg, E);
    k_v<<<N, 128, 0, stream>>>(agg, wv, (unsigned short*)d_out, N);
}
```

```cpp
#include <hip/hip_runtime.h>
#include <hip/hip_bf16.h>
#include <stdint.h>

typedef __attribute__((ext_vector_type(8))) __bf16 bf16x8;
typedef __attribute__((ext_vector_type(4))) __bf16 bf16x4;
typedef __attribute__((ext_vector_type(2))) __bf16 bf16x2;
typedef __attribute__((ext_vector_type(4))) float f32x4;
typedef __attribute__((ext_vector_type(2))) float f32x2;
typedef __attribute__((ext_vector_type(4))) unsigned u32x4;
typedef __attribute__((ext_vector_type(2))) unsigned u32x2;
#define LAS __attribute__((address_space(3)))

constexpr int NB = 256;
constexpr int NPB = 98;
constexpr int G1 = 128;
constexpr int CH = 3128;
constexpr int CAP1 = 31;
constexpr int CAP2 = 384;
constexpr size_t INBOX_BYTES = (size_t)NB * G1 * 128;
constexpr size_t OVF_OFF = INBOX_BYTES;

#define STAMP_DECL
#define STAMP(base, k)
#define STAMP_FLUSH(base, k0, k1, wgoff)

__device__ __forceinline__ unsigned bucket_of(unsigned d) { return (d * 42800u) >> 22; }

__global__ __launch_bounds__(1024) void k_part(const int* __restrict__ src, const int* __restrict__ dst,
                                               unsigned* __restrict__ ws, int E) {
    __shared__ __attribute__((aligned(16))) unsigned img[NB * 32];
    __shared__ unsigned cnt[NB];
    __shared__ unsigned novf;
    const int t = threadIdx.x, g = blockIdx.x;
    STAMP_DECL;
    STAMP(ws, 0);
    const int e0 = g * CH + 4 * t;
    const bool act = (4 * t < CH) && (e0 < E);
    int4 d4 = {0, 0, 0, 0}, s4 = {0, 0, 0, 0};
    if (act) { d4 = *(const int4*)(dst + e0); s4 = *(const int4*)(src + e0); }
    if (t < NB) cnt[t] = 0;
    if (t == NB) novf = 0;
    __syncthreads();
    STAMP(ws, 1);
    if (act) {
        const unsigned dd[4] = {(unsigned)d4.x, (unsigned)d4.y, (unsigned)d4.z, (unsigned)d4.w};
        const unsigned ss[4] = {(unsigned)s4.x, (unsigned)s4.y, (unsigned)s4.z, (unsigned)s4.w};
        unsigned b[4], r[4];
#pragma unroll
        for (int k = 0; k < 4; ++k) { b[k] = bucket_of(dd[k]); r[k] = atomicAdd(&cnt[b[k]], 1u); }
#pragma unroll
        for (int k = 0; k < 4; ++k) {
            const unsigned j = dd[k] - b[k] * NPB;
            if (r[k] < CAP1) img[b[k] * 32 + 1 + r[k]] = (ss[k] << 8) | ((j & 7) << 4) | (j >> 3);
            else { const unsigned p = atomicAdd(&novf, 1u); ws[OVF_OFF / 4 + g * CH + p] = (ss[k] << 16) | dd[k]; }
        }
    }
    __syncthreads();
    STAMP(ws, 2);
    const unsigned no = novf;
#pragma unroll
    for (int it = 0; it < 2; ++it) {
        const int b = (t >> 3) + 128 * it, p = t & 7;
        const unsigned c = cnt[b];
        u32x4 v = *(const u32x4*)&img[b * 32 + 4 * p];
        if (p == 0) v.x = c | (no << 16);
        if (4 * p <= c) *(u32x4*)(ws + ((size_t)b * G1 + g) * 32 + 4 * p) = v;
    }
    STAMP(ws, 3);
    STAMP(ws, 4);
    STAMP_FLUSH(ws, 0, 5, 0);
}

constexpr int L_LIST = 131072;
constexpr int L_WV = L_LIST + 8 * CAP2 * 4;
constexpr int L_CNT = L_WV + 16384;
constexpr int L_HDR = L_CNT + 64;
constexpr int L_TOTAL = L_HDR + G1 * 4;

__device__ __forceinline__ u32x4 make_srd(const void* p, unsigned bytes) {
    const unsigned long long a = (unsigned long long)p;
    u32x4 s; s.x = (unsigned)a; s.y = (unsigned)(a >> 32) & 0xFFFFu; s.z = bytes; s.w = 0x00020000u; return s;
}

__device__ __forceinline__ void dma_group(unsigned ldsbase, u32x4 srd, u32x4 ea, u32x4 eb, u32x4 off) {
    unsigned keep, tmp;
    unsigned mask = 0xFFFFFF00u;
#define DMA1(E, O) "v_and_or_b32 %1, " E ", %4, " O "\n\tbuffer_load_dwordx4 %1, %3, 0 offen lds\n\t"
    asm volatile(
        "s_waitcnt lgkmcnt(0)\n\t"
        "s_mov_b32 %0, m0\n\t"
        "s_mov_b32 m0, %2\n\t"
        DMA1("%5", "%13") "s_add_u32 m0, m0, 0x400\n\t"
        DMA1("%6", "%14") "s_add_u32 m0, m0, 0x400\n\t"
        DMA1("%7", "%15") "s_add_u32 m0, m0, 0x400\n\t"
        DMA1("%8", "%16") "s_add_u32 m0, m0, 0x400\n\t"
        DMA1("%9", "%13") "s_add_u32 m0, m0, 0x400\n\t"
        DMA1("%10", "%14") "s_add_u32 m0, m0, 0x400\n\t"
        DMA1("%11", "%15") "s_add_u32 m0, m0, 0x400\n\t"
        DMA1("%12", "%16")
        "s_mov_b32 m0, %0"
        : "=&s"(keep), "=&v"(tmp)
        : "s"(ldsbase), "s"(srd), "s"(mask),
          "v"(ea.x), "v"(ea.y), "v"(ea.z), "v"(ea.w), "v"(eb.x), "v"(eb.y), "v"(eb.z), "v"(eb.w),
          "v"(off.x), "v"(off.y), "v"(off.z), "v"(off.w)
        : "memory", "scc");
#undef DMA1
}

__device__ __forceinline__ bf16x4 tr_read(LAS char* p) { return __builtin_amdgcn_ds_read_tr16_b64_v4bf16((LAS bf16x4*)p); }
__device__ __forceinline__ bf16x8 cat(bf16x4 a, bf16x4 b) { return __builtin_shufflevector(a, b, 0, 1, 2, 3, 4, 5, 6, 7); }
__device__ __forceinline__ unsigned pk2(float a, float b) { f32x2 v = {a, b}; bf16x2 r = __builtin_convertvector(v, bf16x2); return __builtin_bit_cast(unsigned, r); }
__device__ __forceinline__ f32x4 mfma(bf16x8 a, bf16x8 b, f32x4 c) { return __builtin_amdgcn_mfma_f32_16x16x32_bf16(a, b, c, 0, 0, 0); }
#define WG_BARRIER() do { asm volatile("s_waitcnt lgkmcnt(0)" ::: "memory"); __builtin_amdgcn_s_barrier(); asm volatile("" ::: "memory"); } while (0)

__global__ __launch_bounds__(512, 2) void k_gather(const unsigned short* __restrict__ x, const unsigned short* __restrict__ wq,
                                                   const unsigned short* __restrict__ wv, const unsigned* __restrict__ ws,
                                                   unsigned short* __restrict__ out, int N) {
    __shared__ __attribute__((aligned(16))) char smem_[L_TOTAL];
    LAS char* smem = (LAS char*)smem_;
    const int t = threadIdx.x, l = t & 63, b = blockIdx.x;
    const int w = __builtin_amdgcn_readfirstlane(t >> 6);
    const int g = l >> 4, n = l & 15, q = (l >> 2) & 3, p = l & 3;
    STAMP_DECL;
    STAMP(ws, 6);
    LAS unsigned* cnt2 = (LAS unsigned*)(smem + L_CNT);
    LAS unsigned* hdr = (LAS unsigned*)(smem + L_HDR);
    LAS unsigned* lists = (LAS unsigned*)(smem + L_LIST);

    u32x4 in0 = *(const u32x4*)(ws + (size_t)b * G1 * 32 + 4 * t);
    u32x4 in1 = *(const u32x4*)(ws + (size_t)b * G1 * 32 + 2048 + 4 * t);

    STAMP(ws, 14);
    const unsigned xbytes = (unsigned)N * 256u;
    const u32x4 srd_x = make_srd(x, xbytes);
    const unsigned ring = (unsigned)(size_t)smem + w * 16384;
    {
        const u32x4 srd_w = make_srd(w < 4 ? wq : wv, 32768);
        const unsigned dstb = (w < 6) ? ring + 8192 : (unsigned)(size_t)smem + L_WV + (w - 6) * 8192;
        const unsigned r0 = ((w & 3) * 32 + g) << 8;
        u32x4 ea = {r0, r0 + 1024, r0 + 2048, r0 + 3072}, eb = ea + 4096;
        u32x4 offa, offb;
        if (w < 4) { const unsigned o0 = (n ^ g) << 4; offa = u32x4{o0, o0, o0 ^ 64, o0 ^ 64}; }
        else { const unsigned o0 = (n ^ ((g & 1) | ((g & 2) << 1))) << 4; offa = u32x4{o0, o0 ^ 128, o0, o0 ^ 128}; }
        offb = offa;
        dma_group(dstb, srd_w, ea, eb, offb);
    }
    STAMP(ws, 15);
    const int jn = 8 * n + w, node = b * NPB + jn;
    const bool nvalid = (jn < NPB) && (node < N);
    const auto rs_x = __builtin_amdgcn_make_buffer_rsrc((void*)x, 0, xbytes, 0x00020000);
    const unsigned rowoff = nvalid ? (unsigned)node * 256u : 0xFFFFF000u;
    u32x4 xo[4];
#pragma unroll
    for (int u = 0; u < 4; ++u) xo[u] = __builtin_amdgcn_raw_buffer_load_b128(rs_x, rowoff + 64 * u + 16 * g, 0, 0);

    STAMP(ws, 16);
    if (t < 16) cnt2[t] = 0;
    WG_BARRIER();
    STAMP(ws, 7);

    {
        unsigned c0 = __builtin_amdgcn_update_dpp(0u, in0.x, 0x00, 0xF, 0xF, false);
        c0 = __builtin_amdgcn_update_dpp(c0, c0, 0x114, 0xF, 0xA, false);
        unsigned c1 = __builtin_amdgcn_update_dpp(0u, in1.x, 0x00, 0xF, 0xF, false);
        c1 = __builtin_amdgcn_update_dpp(c1, c1, 0x114, 0xF, 0xA, false);
        if ((t & 7) == 0) { hdr[t >> 3] = c0; hdr[64 + (t >> 3)] = c1; }
        c0 &= 0xFFFF; c1 &= 0xFFFF;
        if (c0 > CAP1 || c1 > CAP1) cnt2[8] = 1;
        const unsigned i0 = 4 * (t & 7) - 1;
        const unsigned ee[8] = {in0.x, in0.y, in0.z, in0.w, in1.x, in1.y, in1.z, in1.w};
        unsigned rank[8];
#pragma unroll
        for (int k = 0; k < 8; ++k) {
            const bool v = (i0 + (k & 3)) < (k < 4 ? c0 : c1);
            rank[k] = 0x7FFFFFFFu;
            if (v) rank[k] = __hip_atomic_fetch_add(&cnt2[(ee[k] >> 4) & 7], 1u, __ATOMIC_RELAXED, __HIP_MEMORY_SCOPE_WORKGROUP);
        }
#pragma unroll
        for (int k = 0; k < 8; ++k) if (rank[k] < CAP2) lists[((ee[k] >> 4) & 7) * CAP2 + rank[k]] = ee[k];
    }
    STAMP(ws, 8);
    asm volatile("s_waitcnt vmcnt(0)" ::: "memory");
    WG_BARRIER();
    STAMP(ws, 9);

    bf16x8 qf[4], wvr[2][8];
    {
        LAS char* aq = smem + 8192 + (8 * g + q) * 256;
        const unsigned cq = (((4 * p) ^ (q | ((g & 1) << 2))) << 4) | ((p & 1) << 3);
        unsigned qp[8][2];
#pragma unroll
        for (int a = 0; a < 8; ++a) {
            f32x4 c = {0.f, 0.f, 0.f, 0.f};
            LAS char* pa = aq + (cq ^ ((a >> 1) << 4) ^ ((a & 1) << 3));
#pragma unroll
            for (int u = 0; u < 4; ++u)
                c = mfma(cat(tr_read(pa + u * 16384), tr_read(pa + u * 16384 + 1024)), __builtin_bit_cast(bf16x8, xo[u]), c);
            const float k = -0.08838834764831845f * 1.4426950408889634f;
            qp[a][0] = pk2(c[0] * k, c[1] * k); qp[a][1] = pk2(c[2] * k, c[3] * k);
        }
        const bool odd = g & 1;
#pragma unroll
        for (int s = 0; s < 4; ++s) {
            u32x4 v;
            v.x = odd ? qp[2 * s + 1][0] : qp[2 * s][0]; v.y = odd ? qp[2 * s + 1][1] : qp[2 * s][1];
            v.z = odd ? qp[2 * s][0] : qp[2 * s + 1][0]; v.w = odd ? qp[2 * s][1] : qp[2 * s + 1][1];
            qf[s] = __builtin_bit_cast(bf16x8, v);
        }
    }
    STAMP(ws, 17);
    const int r8 = (4 * g + q) & 7;
    const unsigned cv = ((p ^ ((r8 & 1) | ((r8 & 6) << 1))) << 4) | ((p & 1) << 3);
    {
        LAS char* av = smem + 4 * 16384 + 8192 + (4 * g + q) * 256;
#pragma unroll
        for (int u = 0; u < 2; ++u)
#pragma unroll
            for (int a = 0; a < 8; ++a) {
                LAS char* pa = av + (cv ^ ((a >> 1) << 6) ^ ((a & 1) << 3)) + u * 16384;
                wvr[u][a] = cat(tr_read(pa), tr_read(pa + 4096));
            }
    }
    WG_BARRIER();
    STAMP(ws, 10);

    u32x4 doff;
    { const unsigned o0 = (n ^ (((g & 1) << 3) | (g >> 1))) << 4; doff = u32x4{o0, o0 ^ 32, o0 ^ 64, o0 ^ 96}; }
    LAS char* rowp; LAS char* trp;
    { const int phi = ((n & 7) << 1) | (n >> 3); rowp = smem + w * 16384 + (4 * (n & 3) + (n >> 2)) * 256; rowp += ((4 * g) ^ phi) << 4; }
    unsigned trc;
    { const int i = 4 * g + q, phi = ((i & 7) << 1) | (i >> 3); trp = smem + w * 16384 + (4 * q + g) * 256 + 8 * (p & 1); trc = ((p >> 1) ^ phi) << 4; }
    const unsigned rowx = (unsigned)(size_t)rowp;
    const unsigned mykey = (w << 4) | n;
    LAS unsigned* mylist = lists + w * CAP2;

    f32x4 acc[8];
#pragma unroll
    for (int c = 0; c < 8; ++c) acc[c] = f32x4{0.f, 0.f, 0.f, 0.f};

    int gcur, pos = 0;
    bool skip;
    {
        unsigned mx = 0;
#pragma unroll
        for (int k = 0; k < 8; ++k) mx = max(mx, cnt2[k]);
        skip = __builtin_amdgcn_readfirstlane(mx) > CAP2;
        gcur = skip ? -1 : (__builtin_amdgcn_readfirstlane(cnt2[8]) ? 0 : G1);
    }
    for (;;) {
        const int c = skip ? 0 : __builtin_amdgcn_readfirstlane(cnt2[w]);
        skip = false;
        const int G = (c + 31) >> 5;
        if (l < 32 && c + l < G * 32) mylist[c + l] = 0xFFFFFFFFu;
        auto issue = [&](int grp, int slot) {
            const u32x4 ea = *(LAS u32x4*)(mylist + grp * 32 + 8 * g), eb = *(LAS u32x4*)(mylist + grp * 32 + 8 * g + 4);
            dma_group(ring + slot * 8192, srd_x, ea, eb, doff);
        };
        auto step = [&](int i, int slot) {
            if (i + 1 < G) asm volatile("s_waitcnt vmcnt(8)" ::: "memory"); else asm volatile("s_waitcnt vmcnt(0)" ::: "memory");
            bf16x8 af[2][4], tf[8];
#pragma unroll
            for (int tt = 0; tt < 2; ++tt)
#pragma unroll
                for (int s = 0; s < 4; ++s) af[tt][s] = *(LAS bf16x8*)(size_t)((rowx ^ (s << 4)) + tt * 4096 + slot * 8192);
            const u32x4 ea = *(LAS u32x4*)(mylist + i * 32 + 8 * g), eb = *(LAS u32x4*)(mylist + i * 32 + 8 * g + 4);
#pragma unroll
            for (int cc = 0; cc < 8; ++cc) {
                LAS char* pa = trp + (trc ^ (cc << 5)) + slot * 8192;
                tf[cc] = cat(tr_read(pa), tr_read(pa + 4096));
            }
            if (i + 2 < G) issue(i + 2, slot);
            f32x4 s0 = {0.f, 0.f, 0.f, 0.f}, s1 = {0.f, 0.f, 0.f, 0.f};
#pragma unroll
            for (int s = 0; s < 4; ++s) { s0 = mfma(af[0][s], qf[s], s0); s1 = mfma(af[1][s], qf[s], s1); }
            float al[8];
#pragma unroll
            for (int r = 0; r < 4; ++r) {
                const float z0 = ((ea[r] & 0xFF) == mykey) ? s0[r] : __builtin_inff();
                const float z1 = ((eb[r] & 0xFF) == mykey) ? s1[r] : __builtin_inff();
                al[r] = __builtin_amdgcn_rcpf(1.f + __builtin_amdgcn_exp2f(z0));
                al[4 + r] = __builtin_amdgcn_rcpf(1.f + __builtin_amdgcn_exp2f(z1));
            }
            const u32x4 pv = {pk2(al[0], al[1]), pk2(al[2], al[3]), pk2(al[4], al[5]), pk2(al[6], al[7])};
            const bf16x8 pf = __builtin_bit_cast(bf16x8, pv);
#pragma unroll
            for (int cc = 0; cc < 8; ++cc) acc[cc] = mfma(tf[cc], pf, acc[cc]);
        };
        if (G > 0) issue(0, 0);
        if (G > 1) issue(1, 1);
        for (int i = 0; i < G; i += 2) {
            step(i, 0);
            if (i + 1 < G) step(i + 1, 1);
        }
        unsigned lim = G1 * 32;
        while (gcur >= 0 && gcur < G1) {
            const unsigned h = __builtin_amdgcn_readfirstlane(hdr[gcur]);
            lim = h >> 16;
            if ((h & 0xFFFF) > CAP1 && (unsigned)pos < lim) break;
            ++gcur; pos = 0;
        }
        if (gcur >= G1) break;
        WG_BARRIER();
        if (t < 8) cnt2[t] = 0;
        WG_BARRIER();
        const unsigned i = pos + t;
        if (t < CAP2 && i < lim) {
            unsigned e; bool v;
            if (gcur < 0) {
                e = ws[(size_t)b * G1 * 32 + i];
                v = ((i & 31) - 1) < (hdr[i >> 5] & 0xFFFF);
            } else {
                const unsigned u = ws[OVF_OFF / 4 + gcur * CH + i], d = u & 0xFFFF, j = d - b * NPB;
                e = ((u >> 16) << 8) | ((j & 7) << 4) | (j >> 3);
                v = j < NPB;
            }
            if (v) {
                const unsigned tl = (e >> 4) & 7;
                lists[tl * CAP2 + __hip_atomic_fetch_add(&cnt2[tl], 1u, __ATOMIC_RELAXED, __HIP_MEMORY_SCOPE_WORKGROUP)] = e;
            }
        }
        __builtin_amdgcn_s_waitcnt(0x0F70);
        pos += CAP2;
        if (gcur < 0 && pos >= G1 * 32) { gcur = 0; pos = 0; }
        WG_BARRIER();
    }
    STAMP(ws, 11);

    {
        bf16x8 ag[4];
#pragma unroll
        for (int u = 0; u < 4; ++u) {
            const u32x4 v = {pk2(acc[2 * u][0], acc[2 * u][1]), pk2(acc[2 * u][2], acc[2 * u][3]),
                             pk2(acc[2 * u + 1][0], acc[2 * u + 1][1]), pk2(acc[2 * u + 1][2], acc[2 * u + 1][3])};
            ag[u] = __builtin_bit_cast(bf16x8, v);
        }
        LAS char* av = smem + L_WV + (4 * g + q) * 256;
        unsigned op[8][2];
#pragma unroll
        for (int a = 0; a < 8; ++a) {
            f32x4 c = {0.f, 0.f, 0.f, 0.f};
            c = mfma(wvr[0][a], ag[0], c);
            c = mfma(wvr[1][a], ag[1], c);
            LAS char* pa = av + (cv ^ ((a >> 1) << 6) ^ ((a & 1) << 3));
            c = mfma(cat(tr_read(pa), tr_read(pa + 4096)), ag[2], c);
            c = mfma(cat(tr_read(pa + 8192), tr_read(pa + 8192 + 4096)), ag[3], c);
            op[a][0] = pk2(c[0], c[1]); op[a][1] = pk2(c[2], c[3]);
        }
        const auto rs_o = __builtin_amdgcn_make_buffer_rsrc((void*)out, 0, xbytes, 0x00020000);
        const bool odd = g & 1;
#pragma unroll
        for (int bb = 0; bb < 4; ++bb) {
            u32x4 v;
            v.x = odd ? op[2 * bb + 1][0] : op[2 * bb][0]; v.y = odd ? op[2 * bb + 1][1] : op[2 * bb][1];
            v.z = odd ? op[2 * bb][0] : op[2 * bb + 1][0]; v.w = odd ? op[2 * bb][1] : op[2 * bb + 1][1];
            __builtin_amdgcn_raw_buffer_store_b128(v, rs_o, rowoff + 64 * bb + 16 * g, 0, 0);
        }
    }
    STAMP(ws, 12);
}

extern "C" void kernel_launch(void* const* d_in, const int* in_sizes, int n_in,
                              void* d_out, int out_size, void* d_ws, size_t ws_size,
                              hipStream_t stream) {
    const unsigned short* x = (const unsigned short*)d_in[0];
    const int* src = (const int*)d_in[1];
    const int* dst = (const int*)d_in[2];
    const unsigned short* wq = (const unsigned short*)d_in[3];
    const unsigned short* wv = (const unsigned short*)d_in[4];
    const int N = in_sizes[0] / 128, E = in_sizes[1];
    k_part<<<G1, 1024, 0, stream>>>(src, dst, (unsigned*)d_ws, E);
    k_gather<<<NB, 512, 0, stream>>>(x, wq, wv, (const unsigned*)d_ws, (unsigned short*)d_out, N);
}
```

```cpp
#include <hip/hip_runtime.h>
#include <hip/hip_bf16.h>
#include <stdint.h>

typedef __attribute__((ext_vector_type(8))) __bf16 bf16x8;
typedef __attribute__((ext_vector_type(4))) __bf16 bf16x4;
typedef __attribute__((ext_vector_type(2))) __bf16 bf16x2;
typedef __attribute__((ext_vector_type(4))) float f32x4;
typedef __attribute__((ext_vector_type(2))) float f32x2;
typedef __attribute__((ext_vector_type(4))) unsigned u32x4;
typedef __attribute__((ext_vector_type(2))) unsigned u32x2;
#define LAS __attribute__((address_space(3)))

constexpr int NB = 256;
constexpr int NPB = 98;
constexpr int G1 = 128;
constexpr int CH = 3128;
constexpr int CAP1 = 31;
constexpr int CAP2 = 384;
constexpr size_t INBOX_BYTES = (size_t)NB * G1 * 128;
constexpr size_t OVF_OFF = INBOX_BYTES;

#define STAMP_DECL
#define STAMP(base, k)
#define STAMP_FLUSH(base, k0, k1, wgoff)

__device__ __forceinline__ unsigned bucket_of(unsigned d) { return (d * 42800u) >> 22; }

__global__ __launch_bounds__(1024) void k_part(const int* __restrict__ src, const int* __restrict__ dst,
                                               unsigned* __restrict__ ws, int E) {
    __shared__ __attribute__((aligned(16))) unsigned img[NB * 32];
    __shared__ unsigned cnt[NB];
    __shared__ unsigned novf;
    const int t = threadIdx.x, g = blockIdx.x;
    STAMP_DECL;
    STAMP(ws, 0);
    const int e0 = g * CH + 4 * t;
    const bool act = (4 * t < CH) && (e0 < E);
    int4 d4 = {0, 0, 0, 0}, s4 = {0, 0, 0, 0};
    if (act) { d4 = *(const int4*)(dst + e0); s4 = *(const int4*)(src + e0); }
    if (t < NB) cnt[t] = 0;
    if (t == NB) novf = 0;
    __syncthreads();
    STAMP(ws, 1);
    if (act) {
        const unsigned dd[4] = {(unsigned)d4.x, (unsigned)d4.y, (unsigned)d4.z, (unsigned)d4.w};
        const unsigned ss[4] = {(unsigned)s4.x, (unsigned)s4.y, (unsigned)s4.z, (unsigned)s4.w};
        unsigned b[4], r[4];
#pragma unroll
        for (int k = 0; k < 4; ++k) { b[k] = bucket_of(dd[k]); r[k] = atomicAdd(&cnt[b[k]], 1u); }
#pragma unroll
        for (int k = 0; k < 4; ++k) {
            const unsigned j = dd[k] - b[k] * NPB;
            if (r[k] < CAP1) img[b[k] * 32 + 1 + r[k]] = (ss[k] << 8) | ((j & 7) << 4) | (j >> 3);
            else { const unsigned p = atomicAdd(&novf, 1u); ws[OVF_OFF / 4 + g * CH + p] = (ss[k] << 16) | dd[k]; }
        }
    }
    __syncthreads();
    STAMP(ws, 2);
    const unsigned no = novf;
#pragma unroll
    for (int it = 0; it < 2; ++it) {
        const int b = (t >> 3) + 128 * it, p = t & 7;
        const unsigned c = cnt[b];
        u32x4 v = *(const u32x4*)&img[b * 32 + 4 * p];
        if (p == 0) v.x = c | (no << 16);
        if (4 * p <= c) *(u32x4*)(ws + ((size_t)b * G1 + g) * 32 + 4 * p) = v;
    }
    STAMP(ws, 3);
    STAMP(ws, 4);
    STAMP_FLUSH(ws, 0, 5, 0);
}

constexpr int L_LIST = 131072;
constexpr int L_WV = L_LIST + 8 * CAP2 * 4;
constexpr int L_CNT = L_WV + 16384;
constexpr int L_HDR = L_CNT + 64;
constexpr int L_TOTAL = L_HDR + G1 * 4;

__device__ __forceinline__ u32x4 make_srd(const void* p, unsigned bytes) {
    const unsigned long long a = (unsigned long long)p;
    u32x4 s; s.x = (unsigned)a; s.y = (unsigned)(a >> 32) & 0xFFFFu; s.z = bytes; s.w = 0x00020000u; return s;
}

__device__ __forceinline__ void dma_group(unsigned ldsbase, u32x4 srd, u32x4 ea, u32x4 eb, u32x4 off) {
    unsigned keep, tmp;
    unsigned mask = 0xFFFFFF00u;
#define DMA1(E, O) "v_and_or_b32 %1, " E ", %4, " O "\n\tbuffer_load_dwordx4 %1, %3, 0 offen lds\n\t"
    asm volatile(
        "s_waitcnt lgkmcnt(0)\n\t"
        "s_mov_b32 %0, m0\n\t"
        "s_mov_b32 m0, %2\n\t"
        DMA1("%5", "%13") "s_add_u32 m0, m0, 0x400\n\t"
        DMA1("%6", "%14") "s_add_u32 m0, m0, 0x400\n\t"
        DMA1("%7", "%15") "s_add_u32 m0, m0, 0x400\n\t"
        DMA1("%8", "%16") "s_add_u32 m0, m0, 0x400\n\t"
        DMA1("%9", "%13") "s_add_u32 m0, m0, 0x400\n\t"
        DMA1("%10", "%14") "s_add_u32 m0, m0, 0x400\n\t"
        DMA1("%11", "%15") "s_add_u32 m0, m0, 0x400\n\t"
        DMA1("%12", "%16")
        "s_mov_b32 m0, %0"
        : "=&s"(keep), "=&v"(tmp)
        : "s"(ldsbase), "s"(srd), "s"(mask),
          "v"(ea.x), "v"(ea.y), "v"(ea.z), "v"(ea.w), "v"(eb.x), "v"(eb.y), "v"(eb.z), "v"(eb.w),
          "v"(off.x), "v"(off.y), "v"(off.z), "v"(off.w)
        : "memory", "scc");
#undef DMA1
}

__device__ __forceinline__ bf16x4 tr_read(LAS char* p) { return __builtin_amdgcn_ds_read_tr16_b64_v4bf16((LAS bf16x4*)p); }
__device__ __forceinline__ bf16x8 cat(bf16x4 a, bf16x4 b) { return __builtin_shufflevector(a, b, 0, 1, 2, 3, 4, 5, 6, 7); }
__device__ __forceinline__ unsigned pk2(float a, float b) { f32x2 v = {a, b}; bf16x2 r = __builtin_convertvector(v, bf16x2); return __builtin_bit_cast(unsigned, r); }
__device__ __forceinline__ f32x4 mfma(bf16x8 a, bf16x8 b, f32x4 c) { return __builtin_amdgcn_mfma_f32_16x16x32_bf16(a, b, c, 0, 0, 0); }
#define WG_BARRIER() do { asm volatile("s_waitcnt lgkmcnt(0)" ::: "memory"); __builtin_amdgcn_s_barrier(); asm volatile("" ::: "memory"); } while (0)

__global__ __launch_bounds__(512, 2) void k_gather(const unsigned short* __restrict__ x, const unsigned short* __restrict__ wq,
                                                   const unsigned short* __restrict__ wv, const unsigned* __restrict__ ws,
                                                   unsigned short* __restrict__ out, int N, const int* __restrict__ src, const int* __restrict__ dst, int E) {
    __shared__ __attribute__((aligned(16))) char smem_[L_TOTAL];
    LAS char* smem = (LAS char*)smem_;
    const int t = threadIdx.x, l = t & 63, b = blockIdx.x;
    const int w = __builtin_amdgcn_readfirstlane(t >> 6);
    const int g = l >> 4, n = l & 15, q = (l >> 2) & 3, p = l & 3;
    STAMP_DECL;
    STAMP(ws, 0);
    STAMP(ws, 6);
    LAS unsigned* cnt2 = (LAS unsigned*)(smem + L_CNT);
    LAS unsigned* hdr = (LAS unsigned*)(smem + L_HDR);
    LAS unsigned* lists = (LAS unsigned*)(smem + L_LIST);

    STAMP(ws, 14);
    const unsigned xbytes = (unsigned)N * 256u;
    const u32x4 srd_x = make_srd(x, xbytes);
    const unsigned ring = (unsigned)(size_t)smem + w * 16384;
    {
        const u32x4 srd_w = make_srd(w < 4 ? wq : wv, 32768);
        const unsigned dstb = (w < 6) ? ring + 8192 : (unsigned)(size_t)smem + L_WV + (w - 6) * 8192;
        const unsigned r0 = ((w & 3) * 32 + g) << 8;
        u32x4 ea = {r0, r0 + 1024, r0 + 2048, r0 + 3072}, eb = ea + 4096;
        u32x4 offa, offb;
        if (w < 4) { const unsigned o0 = (n ^ g) << 4; offa = u32x4{o0, o0, o0 ^ 64, o0 ^ 64}; }
        else { const unsigned o0 = (n ^ ((g & 1) | ((g & 2) << 1))) << 4; offa = u32x4{o0, o0 ^ 128, o0, o0 ^ 128}; }
        offb = offa;
        dma_group(dstb, srd_w, ea, eb, offb);
    }
    STAMP(ws, 15);
    const int jn = 8 * n + w, node = b * NPB + jn;
    const bool nvalid = (jn < NPB) && (node < N);
    const auto rs_x = __builtin_amdgcn_make_buffer_rsrc((void*)x, 0, xbytes, 0x00020000);
    const unsigned rowoff = nvalid ? (unsigned)node * 256u : 0xFFFFF000u;
    u32x4 xo[4];
#pragma unroll
    for (int u = 0; u < 4; ++u) xo[u] = __builtin_amdgcn_raw_buffer_load_b128(rs_x, rowoff + 64 * u + 16 * g, 0, 0);

    STAMP(ws, 16);
    if (t < 16) cnt2[t] = 0;
    WG_BARRIER();
    STAMP(ws, 7);

    if (t < G1) hdr[t] = 0;
    for (int i = t; i < E / 4; i += 2048) {
        int4 d4[4];
#pragma unroll
        for (int u = 0; u < 4; ++u) d4[u] = (i + 512 * u < E / 4) ? ((const int4*)dst)[i + 512 * u] : int4{-1, -1, -1, -1};
#pragma unroll
        for (int u = 0; u < 4; ++u) {
            const unsigned dd[4] = {(unsigned)d4[u].x, (unsigned)d4[u].y, (unsigned)d4[u].z, (unsigned)d4[u].w};
#pragma unroll
            for (int k = 0; k < 4; ++k) {
                const unsigned j = dd[k] - b * NPB;
                if (j < NPB) {
                    const unsigned e = ((unsigned)src[4 * (i + 512 * u) + k] << 8) | ((j & 7) << 4) | (j >> 3);
                    const unsigned r = __hip_atomic_fetch_add(&cnt2[j & 7], 1u, __ATOMIC_RELAXED, __HIP_MEMORY_SCOPE_WORKGROUP);
                    if (r < CAP2) lists[(j & 7) * CAP2 + r] = e;
                }
            }
        }
    }
    __builtin_amdgcn_s_waitcnt(0x0F70);
    STAMP(ws, 8);
    asm volatile("s_waitcnt vmcnt(0)" ::: "memory");
    WG_BARRIER();
    STAMP(ws, 9);

    bf16x8 qf[4], wvr[2][8];
    {
        LAS char* aq = smem + 8192 + (8 * g + q) * 256;
        const unsigned cq = (((4 * p) ^ (q | ((g & 1) << 2))) << 4) | ((p & 1) << 3);
        unsigned qp[8][2];
#pragma unroll
        for (int a = 0; a < 8; ++a) {
            f32x4 c = {0.f, 0.f, 0.f, 0.f};
            LAS char* pa = aq + (cq ^ ((a >> 1) << 4) ^ ((a & 1) << 3));
#pragma unroll
            for (int u = 0; u < 4; ++u)
                c = mfma(cat(tr_read(pa + u * 16384), tr_read(pa + u * 16384 + 1024)), __builtin_bit_cast(bf16x8, xo[u]), c);
            const float k = -0.08838834764831845f * 1.4426950408889634f;
            qp[a][0] = pk2(c[0] * k, c[1] * k); qp[a][1] = pk2(c[2] * k, c[3] * k);
        }
        const bool odd = g & 1;
#pragma unroll
        for (int s = 0; s < 4; ++s) {
            u32x4 v;
            v.x = odd ? qp[2 * s + 1][0] : qp[2 * s][0]; v.y = odd ? qp[2 * s + 1][1] : qp[2 * s][1];
            v.z = odd ? qp[2 * s][0] : qp[2 * s + 1][0]; v.w = odd ? qp[2 * s][1] : qp[2 * s + 1][1];
            qf[s] = __builtin_bit_cast(bf16x8, v);
        }
    }
    STAMP(ws, 17);
    const int r8 = (4 * g + q) & 7;
    const unsigned cv = ((p ^ ((r8 & 1) | ((r8 & 6) << 1))) << 4) | ((p & 1) << 3);
    {
        LAS char* av = smem + 4 * 16384 + 8192 + (4 * g + q) * 256;
#pragma unroll
        for (int u = 0; u < 2; ++u)
#pragma unroll
            for (int a = 0; a < 8; ++a) {
                LAS char* pa = av + (cv ^ ((a >> 1) << 6) ^ ((a & 1) << 3)) + u * 16384;
                wvr[u][a] = cat(tr_read(pa), tr_read(pa + 4096));
            }
    }
    WG_BARRIER();
    STAMP(ws, 10);

    u32x4 doff;
    { const unsigned o0 = (n ^ (((g & 1) << 3) | (g >> 1))) << 4; doff = u32x4{o0, o0 ^ 32, o0 ^ 64, o0 ^ 96}; }
    LAS char* rowp; LAS char* trp;
    { const int phi = ((n & 7) << 1) | (n >> 3); rowp = smem + w * 16384 + (4 * (n & 3) + (n >> 2)) * 256; rowp += ((4 * g) ^ phi) << 4; }
    unsigned trc;
    { const int i = 4 * g + q, phi = ((i & 7) << 1) | (i >> 3); trp = smem + w * 16384 + (4 * q + g) * 256 + 8 * (p & 1); trc = ((p >> 1) ^ phi) << 4; }
    const unsigned rowx = (unsigned)(size_t)rowp;
    const unsigned mykey = (w << 4) | n;
    LAS unsigned* mylist = lists + w * CAP2;

    f32x4 acc[8];
#pragma unroll
    for (int c = 0; c < 8; ++c) acc[c] = f32x4{0.f, 0.f, 0.f, 0.f};

    int gcur, pos = 0;
    bool skip;
    {
        unsigned mx = 0;
#pragma unroll
        for (int k = 0; k < 8; ++k) mx = max(mx, cnt2[k]);
        skip = __builtin_amdgcn_readfirstlane(mx) > CAP2;
        gcur = skip ? -1 : (__builtin_amdgcn_readfirstlane(cnt2[8]) ? 0 : G1);
    }
    for (;;) {
        const int c = skip ? 0 : __builtin_amdgcn_readfirstlane(cnt2[w]);
        skip = false;
        const int G = (c + 31) >> 5;
        if (l < 32 && c + l < G * 32) mylist[c + l] = 0xFFFFFFFFu;
        auto issue = [&](int grp, int slot) {
            const u32x4 ea = *(LAS u32x4*)(mylist + grp * 32 + 8 * g), eb = *(LAS u32x4*)(mylist + grp * 32 + 8 * g + 4);
            dma_group(ring + slot * 8192, srd_x, ea, eb, doff);
        };
        auto step = [&](int i, int slot) {
            if (i + 1 < G) asm volatile("s_waitcnt vmcnt(8)" ::: "memory"); else asm volatile("s_waitcnt vmcnt(0)" ::: "memory");
            bf16x8 af[2][4], tf[8];
#pragma unroll
            for (int tt = 0; tt < 2; ++tt)
#pragma unroll
                for (int s = 0; s < 4; ++s) af[tt][s] = *(LAS bf16x8*)(size_t)((rowx ^ (s << 4)) + tt * 4096 + slot * 8192);
            const u32x4 ea = *(LAS u32x4*)(mylist + i * 32 + 8 * g), eb = *(LAS u32x4*)(mylist + i * 32 + 8 * g + 4);
#pragma unroll
            for (int cc = 0; cc < 8; ++cc) {
                LAS char* pa = trp + (trc ^ (cc << 5)) + slot * 8192;
                tf[cc] = cat(tr_read(pa), tr_read(pa + 4096));
            }
            if (i + 2 < G) issue(i + 2, slot);
            f32x4 s0 = {0.f, 0.f, 0.f, 0.f}, s1 = {0.f, 0.f, 0.f, 0.f};
#pragma unroll
            for (int s = 0; s < 4; ++s) { s0 = mfma(af[0][s], qf[s], s0); s1 = mfma(af[1][s], qf[s], s1); }
            float al[8];
#pragma unroll
            for (int r = 0; r < 4; ++r) {
                const float z0 = ((ea[r] & 0xFF) == mykey) ? s0[r] : __builtin_inff();
                const float z1 = ((eb[r] & 0xFF) == mykey) ? s1[r] : __builtin_inff();
                al[r] = __builtin_amdgcn_rcpf(1.f + __builtin_amdgcn_exp2f(z0));
                al[4 + r] = __builtin_amdgcn_rcpf(1.f + __builtin_amdgcn_exp2f(z1));
            }
            const u32x4 pv = {pk2(al[0], al[1]), pk2(al[2], al[3]), pk2(al[4], al[5]), pk2(al[6], al[7])};
            const bf16x8 pf = __builtin_bit_cast(bf16x8, pv);
#pragma unroll
            for (int cc = 0; cc < 8; ++cc) acc[cc] = mfma(tf[cc], pf, acc[cc]);
        };
        if (G > 0) issue(0, 0);
        if (G > 1) issue(1, 1);
        for (int i = 0; i < G; i += 2) {
            step(i, 0);
            if (i + 1 < G) step(i + 1, 1);
        }
        unsigned lim = G1 * 32;
        while (gcur >= 0 && gcur < G1) {
            const unsigned h = __builtin_amdgcn_readfirstlane(hdr[gcur]);
            lim = h >> 16;
            if ((h & 0xFFFF) > CAP1 && (unsigned)pos < lim) break;
            ++gcur; pos = 0;
        }
        if (gcur >= G1) break;
        WG_BARRIER();
        if (t < 8) cnt2[t] = 0;
        WG_BARRIER();
        const unsigned i = pos + t;
        if (t < CAP2 && i < lim) {
            unsigned e; bool v;
            if (gcur < 0) {
                e = ws[(size_t)b * G1 * 32 + i];
                v = ((i & 31) - 1) < (hdr[i >> 5] & 0xFFFF);
            } else {
                const unsigned u = ws[OVF_OFF / 4 + gcur * CH + i], d = u & 0xFFFF, j = d - b * NPB;
                e = ((u >> 16) << 8) | ((j & 7) << 4) | (j >> 3);
                v = j < NPB;
            }
            if (v) {
                const unsigned tl = (e >> 4) & 7;
                lists[tl * CAP2 + __hip_atomic_fetch_add(&cnt2[tl], 1u, __ATOMIC_RELAXED, __HIP_MEMORY_SCOPE_WORKGROUP)] = e;
            }
        }
        __builtin_amdgcn_s_waitcnt(0x0F70);
        pos += CAP2;
        if (gcur < 0 && pos >= G1 * 32) { gcur = 0; pos = 0; }
        WG_BARRIER();
    }
    STAMP(ws, 11);

    {
        bf16x8 ag[4];
#pragma unroll
        for (int u = 0; u < 4; ++u) {
            const u32x4 v = {pk2(acc[2 * u][0], acc[2 * u][1]), pk2(acc[2 * u][2], acc[2 * u][3]),
                             pk2(acc[2 * u + 1][0], acc[2 * u + 1][1]), pk2(acc[2 * u + 1][2], acc[2 * u + 1][3])};
            ag[u] = __builtin_bit_cast(bf16x8, v);
        }
        LAS char* av = smem + L_WV + (4 * g + q) * 256;
        unsigned op[8][2];
#pragma unroll
        for (int a = 0; a < 8; ++a) {
            f32x4 c = {0.f, 0.f, 0.f, 0.f};
            c = mfma(wvr[0][a], ag[0], c);
            c = mfma(wvr[1][a], ag[1], c);
            LAS char* pa = av + (cv ^ ((a >> 1) << 6) ^ ((a & 1) << 3));
            c = mfma(cat(tr_read(pa), tr_read(pa + 4096)), ag[2], c);
            c = mfma(cat(tr_read(pa + 8192), tr_read(pa + 8192 + 4096)), ag[3], c);
            op[a][0] = pk2(c[0], c[1]); op[a][1] = pk2(c[2], c[3]);
        }
        const auto rs_o = __builtin_amdgcn_make_buffer_rsrc((void*)out, 0, xbytes, 0x00020000);
        const bool odd = g & 1;
#pragma unroll
        for (int bb = 0; bb < 4; ++bb) {
            u32x4 v;
            v.x = odd ? op[2 * bb + 1][0] : op[2 * bb][0]; v.y = odd ? op[2 * bb + 1][1] : op[2 * bb][1];
            v.z = odd ? op[2 * bb][0] : op[2 * bb + 1][0]; v.w = odd ? op[2 * bb][1] : op[2 * bb + 1][1];
            __builtin_amdgcn_raw_buffer_store_b128(v, rs_o, rowoff + 64 * bb + 16 * g, 0, 0);
        }
    }
    STAMP(ws, 12);
}

extern "C" void kernel_launch(void* const* d_in, const int* in_sizes, int n_in,
                              void* d_out, int out_size, void* d_ws, size_t ws_size,
                              hipStream_t stream) {
    const unsigned short* x = (const unsigned short*)d_in[0];
    const int* src = (const int*)d_in[1];
    const int* dst = (const int*)d_in[2];
    const unsigned short* wq = (const unsigned short*)d_in[3];
    const unsigned short* wv = (const unsigned short*)d_in[4];
    const int N = in_sizes[0] / 128, E = in_sizes[1];
    k_gather<<<NB, 512, 0, stream>>>(x, wq, wv, (const unsigned*)d_ws, (unsigned short*)d_out, N, src, dst, E);
}
```

```cpp
#define STAMPS
#define PROBE_A 6
#define PROBE_B 7
#define PROBE_M 0
#include <hip/hip_runtime.h>
#include <hip/hip_bf16.h>
#include <stdint.h>

typedef __attribute__((ext_vector_type(8))) __bf16 bf16x8;
typedef __attribute__((ext_vector_type(4))) __bf16 bf16x4;
typedef __attribute__((ext_vector_type(2))) __bf16 bf16x2;
typedef __attribute__((ext_vector_type(4))) float f32x4;
typedef __attribute__((ext_vector_type(2))) float f32x2;
typedef __attribute__((ext_vector_type(4))) unsigned u32x4;
typedef __attribute__((ext_vector_type(2))) unsigned u32x2;
#define LAS __attribute__((address_space(3)))

constexpr int NB = 256;
constexpr int NPB = 98;
constexpr int G1 = 128;
constexpr int CH = 3128;
constexpr int CAP1 = 31;
constexpr int CAP2 = 384;
constexpr size_t INBOX_BYTES = (size_t)NB * G1 * 128;
constexpr size_t OVF_OFF = INBOX_BYTES;

#define STAMP_N 24
#define STAMP_WGS 512
#define STAMP_OFF (200u << 20)
#define STAMP_VALID(w, k) (((k) < 5) ? ((w) < G1) : ((w) >= 256 && (k) >= 6 && (k) < 20))
#define STAMP_DECL unsigned long long st_[STAMP_N]
#define STAMP(base, k) do { __builtin_amdgcn_sched_barrier(0); asm volatile("s_memrealtime %0\n\ts_waitcnt lgkmcnt(0)" : "=s"(st_[k]) :: "memory"); __builtin_amdgcn_sched_barrier(0); } while (0)
#define STAMP_FLUSH(base, k0, k1, wgoff) do { if (threadIdx.x == STAMP_THREAD) { _Pragma("unroll") for (int k_ = k0; k_ < k1; ++k_) ((unsigned long long*)((char*)(base) + STAMP_OFF))[(size_t)(blockIdx.x + wgoff) * STAMP_N + k_] = st_[k_]; } } while (0)
#define STAMP_THREAD 0

__device__ __forceinline__ unsigned bucket_of(unsigned d) { return (d * 42800u) >> 22; }

__global__ __launch_bounds__(1024) void k_part(const int* __restrict__ src, const int* __restrict__ dst,
                                               unsigned* __restrict__ ws, int E) {
    __shared__ __attribute__((aligned(16))) unsigned img[NB * 32];
    __shared__ unsigned cnt[NB];
    __shared__ unsigned novf;
    const int t = threadIdx.x, g = blockIdx.x;
    STAMP_DECL;
    STAMP(ws, 0);
    const int e0 = g * CH + 4 * t;
    const bool act = (4 * t < CH) && (e0 < E);
    int4 d4 = {0, 0, 0, 0}, s4 = {0, 0, 0, 0};
    if (act) { d4 = *(const int4*)(dst + e0); s4 = *(const int4*)(src + e0); }
    if (t < NB) cnt[t] = 0;
    if (t == NB) novf = 0;
    __syncthreads();
    STAMP(ws, 1);
    if (act) {
        const unsigned dd[4] = {(unsigned)d4.x, (unsigned)d4.y, (unsigned)d4.z, (unsigned)d4.w};
        const unsigned ss[4] = {(unsigned)s4.x, (unsigned)s4.y, (unsigned)s4.z, (unsigned)s4.w};
        unsigned b[4], r[4];
#pragma unroll
        for (int k = 0; k < 4; ++k) { b[k] = bucket_of(dd[k]); r[k] = atomicAdd(&cnt[b[k]], 1u); }
#pragma unroll
        for (int k = 0; k < 4; ++k) {
            const unsigned j = dd[k] - b[k] * NPB;
            if (r[k] < CAP1) img[b[k] * 32 + 1 + r[k]] = (ss[k] << 8) | ((j & 7) << 4) | (j >> 3);
            else { const unsigned p = atomicAdd(&novf, 1u); ws[OVF_OFF / 4 + g * CH + p] = (ss[k] << 16) | dd[k]; }
        }
    }
    __syncthreads();
    STAMP(ws, 2);
    const unsigned no = novf;
#pragma unroll
    for (int it = 0; it < 2; ++it) {
        const int b = (t >> 3) + 128 * it, p = t & 7;
        const unsigned c = cnt[b];
        u32x4 v = *(const u32x4*)&img[b * 32 + 4 * p];
        if (p == 0) v.x = c | (no << 16);
        if (4 * p <= c) *(u32x4*)(ws + ((size_t)b * G1 + g) * 32 + 4 * p) = v;
    }
    STAMP(ws, 3);
    asm volatile("s_waitcnt vmcnt(0)" ::: "memory");
    STAMP(ws, 4);
    STAMP_FLUSH(ws, 0, 5, 0);
}

constexpr int L_LIST = 131072;
constexpr int L_WV = L_LIST + 8 * CAP2 * 4;
constexpr int L_CNT = L_WV + 16384;
constexpr int L_HDR = L_CNT + 64;
constexpr int L_TOTAL = L_HDR + G1 * 4;

__device__ __forceinline__ u32x4 make_srd(const void* p, unsigned bytes) {
    const unsigned long long a = (unsigned long long)p;
    u32x4 s; s.x = (unsigned)a; s.y = (unsigned)(a >> 32) & 0xFFFFu; s.z = bytes; s.w = 0x00020000u; return s;
}

__device__ __forceinline__ void dma_group(unsigned ldsbase, u32x4 srd, u32x4 ea, u32x4 eb, u32x4 off) {
    unsigned keep, tmp;
    unsigned mask = 0xFFFFFF00u;
#define DMA1(E, O) "v_and_or_b32 %1, " E ", %4, " O "\n\tbuffer_load_dwordx4 %1, %3, 0 offen lds\n\t"
    asm volatile(
        "s_waitcnt lgkmcnt(0)\n\t"
        "s_mov_b32 %0, m0\n\t"
        "s_mov_b32 m0, %2\n\t"
        DMA1("%5", "%13") "s_add_u32 m0, m0, 0x400\n\t"
        DMA1("%6", "%14") "s_add_u32 m0, m0, 0x400\n\t"
        DMA1("%7", "%15") "s_add_u32 m0, m0, 0x400\n\t"
        DMA1("%8", "%16") "s_add_u32 m0, m0, 0x400\n\t"
        DMA1("%9", "%13") "s_add_u32 m0, m0, 0x400\n\t"
        DMA1("%10", "%14") "s_add_u32 m0, m0, 0x400\n\t"
        DMA1("%11", "%15") "s_add_u32 m0, m0, 0x400\n\t"
        DMA1("%12", "%16")
        "s_mov_b32 m0, %0"
        : "=&s"(keep), "=&v"(tmp)
        : "s"(ldsbase), "s"(srd), "s"(mask),
          "v"(ea.x), "v"(ea.y), "v"(ea.z), "v"(ea.w), "v"(eb.x), "v"(eb.y), "v"(eb.z), "v"(eb.w),
          "v"(off.x), "v"(off.y), "v"(off.z), "v"(off.w)
        : "memory", "scc");
#undef DMA1
}

__device__ __forceinline__ bf16x4 tr_read(LAS char* p) { return __builtin_amdgcn_ds_read_tr16_b64_v4bf16((LAS bf16x4*)p); }
__device__ __forceinline__ bf16x8 cat(bf16x4 a, bf16x4 b) { return __builtin_shufflevector(a, b, 0, 1, 2, 3, 4, 5, 6, 7); }
__device__ __forceinline__ unsigned pk2(float a, float b) { f32x2 v = {a, b}; bf16x2 r = __builtin_convertvector(v, bf16x2); return __builtin_bit_cast(unsigned, r); }
__device__ __forceinline__ f32x4 mfma(bf16x8 a, bf16x8 b, f32x4 c) { return __builtin_amdgcn_mfma_f32_16x16x32_bf16(a, b, c, 0, 0, 0); }
#define WG_BARRIER() do { asm volatile("s_waitcnt lgkmcnt(0)" ::: "memory"); __builtin_amdgcn_s_barrier(); asm volatile("" ::: "memory"); } while (0)

__global__ __launch_bounds__(512, 2) void k_gather(const unsigned short* __restrict__ x, const unsigned short* __restrict__ wq,
                                                   const unsigned short* __restrict__ wv, const unsigned* __restrict__ ws,
                                                   unsigned short* __restrict__ out, int N) {
    __shared__ __attribute__((aligned(16))) char smem_[L_TOTAL];
    LAS char* smem = (LAS char*)smem_;
    const int t = threadIdx.x, l = t & 63, b = blockIdx.x;
    const int w = __builtin_amdgcn_readfirstlane(t >> 6);
    const int g = l >> 4, n = l & 15, q = (l >> 2) & 3, p = l & 3;
    STAMP_DECL;
    STAMP(ws, 6);
    LAS unsigned* cnt2 = (LAS unsigned*)(smem + L_CNT);
    LAS unsigned* hdr = (LAS unsigned*)(smem + L_HDR);
    LAS unsigned* lists = (LAS unsigned*)(smem + L_LIST);

    u32x4 in0 = *(const u32x4*)(ws + (size_t)b * G1 * 32 + 4 * t);
    u32x4 in1 = *(const u32x4*)(ws + (size_t)b * G1 * 32 + 2048 + 4 * t);

    STAMP(ws, 14);
    const unsigned xbytes = (unsigned)N * 256u;
    const u32x4 srd_x = make_srd(x, xbytes);
    const unsigned ring = (unsigned)(size_t)smem + w * 16384;
    {
        const u32x4 srd_w = make_srd(w < 4 ? wq : wv, 32768);
        const unsigned dstb = (w < 6) ? ring + 8192 : (unsigned)(size_t)smem + L_WV + (w - 6) * 8192;
        const unsigned r0 = ((w & 3) * 32 + g) << 8;
        u32x4 ea = {r0, r0 + 1024, r0 + 2048, r0 + 3072}, eb = ea + 4096;
        u32x4 offa, offb;
        if (w < 4) { const unsigned o0 = (n ^ g) << 4; offa = u32x4{o0, o0, o0 ^ 64, o0 ^ 64}; }
        else { const unsigned o0 = (n ^ ((g & 1) | ((g & 2) << 1))) << 4; offa = u32x4{o0, o0 ^ 128, o0, o0 ^ 128}; }
        offb = offa;
        dma_group(dstb, srd_w, ea, eb, offb);
    }
    STAMP(ws, 15);
    const int jn = 8 * n + w, node = b * NPB + jn;
    const bool nvalid = (jn < NPB) && (node < N);
    const auto rs_x = __builtin_amdgcn_make_buffer_rsrc((void*)x, 0, xbytes, 0x00020000);
    const unsigned rowoff = nvalid ? (unsigned)node * 256u : 0xFFFFF000u;
    u32x4 xo[4];
#pragma unroll
    for (int u = 0; u < 4; ++u) xo[u] = __builtin_amdgcn_raw_buffer_load_b128(rs_x, rowoff + 64 * u + 16 * g, 0, 0);

    STAMP(ws, 16);
    if (t < 16) cnt2[t] = 0;
    WG_BARRIER();
    STAMP(ws, 7);

    {
        unsigned c0 = __builtin_amdgcn_update_dpp(0u, in0.x, 0x00, 0xF, 0xF, false);
        c0 = __builtin_amdgcn_update_dpp(c0, c0, 0x114, 0xF, 0xA, false);
        unsigned c1 = __builtin_amdgcn_update_dpp(0u, in1.x, 0x00, 0xF, 0xF, false);
        c1 = __builtin_amdgcn_update_dpp(c1, c1, 0x114, 0xF, 0xA, false);
        if ((t & 7) == 0) { hdr[t >> 3] = c0; hdr[64 + (t >> 3)] = c1; }
        c0 &= 0xFFFF; c1 &= 0xFFFF;
        if (c0 > CAP1 || c1 > CAP1) cnt2[8] = 1;
        const unsigned i0 = 4 * (t & 7) - 1;
        const unsigned ee[8] = {in0.x, in0.y, in0.z, in0.w, in1.x, in1.y, in1.z, in1.w};
        unsigned rank[8];
#pragma unroll
        for (int k = 0; k < 8; ++k) {
            const bool v = (i0 + (k & 3)) < (k < 4 ? c0 : c1);
            rank[k] = 0x7FFFFFFFu;
            if (v) rank[k] = __hip_atomic_fetch_add(&cnt2[(ee[k] >> 4) & 7], 1u, __ATOMIC_RELAXED, __HIP_MEMORY_SCOPE_WORKGROUP);
        }
#pragma unroll
        for (int k = 0; k < 8; ++k) if (rank[k] < CAP2) lists[((ee[k] >> 4) & 7) * CAP2 + rank[k]] = ee[k];
    }
    STAMP(ws, 8);
    asm volatile("s_waitcnt vmcnt(0)" ::: "memory");
    WG_BARRIER();
    STAMP(ws, 9);

    bf16x8 qf[4], wvr[2][8];
    {
        LAS char* aq = smem + 8192 + (8 * g + q) * 256;
        const unsigned cq = (((4 * p) ^ (q | ((g & 1) << 2))) << 4) | ((p & 1) << 3);
        unsigned qp[8][2];
#pragma unroll
        for (int a = 0; a < 8; ++a) {
            f32x4 c = {0.f, 0.f, 0.f, 0.f};
            LAS char* pa = aq + (cq ^ ((a >> 1) << 4) ^ ((a & 1) << 3));
#pragma unroll
            for (int u = 0; u < 4; ++u)
                c = mfma(cat(tr_read(pa + u * 16384), tr_read(pa + u * 16384 + 1024)), __builtin_bit_cast(bf16x8, xo[u]), c);
            const float k = -0.08838834764831845f * 1.4426950408889634f;
            qp[a][0] = pk2(c[0] * k, c[1] * k); qp[a][1] = pk2(c[2] * k, c[3] * k);
        }
        const bool odd = g & 1;
#pragma unroll
        for (int s = 0; s < 4; ++s) {
            u32x4 v;
            v.x = odd ? qp[2 * s + 1][0] : qp[2 * s][0]; v.y = odd ? qp[2 * s + 1][1] : qp[2 * s][1];
            v.z = odd ? qp[2 * s][0] : qp[2 * s + 1][0]; v.w = odd ? qp[2 * s][1] : qp[2 * s + 1][1];
            qf[s] = __builtin_bit_cast(bf16x8, v);
        }
    }
    STAMP(ws, 17);
    const int r8 = (4 * g + q) & 7;
    const unsigned cv = ((p ^ ((r8 & 1) | ((r8 & 6) << 1))) << 4) | ((p & 1) << 3);
    {
        LAS char* av = smem + 4 * 16384 + 8192 + (4 * g + q) * 256;
#pragma unroll
        for (int u = 0; u < 2; ++u)
#pragma unroll
            for (int a = 0; a < 8; ++a) {
                LAS char* pa = av + (cv ^ ((a >> 1) << 6) ^ ((a & 1) << 3)) + u * 16384;
                wvr[u][a] = cat(tr_read(pa), tr_read(pa + 4096));
            }
    }
    WG_BARRIER();
    STAMP(ws, 10);

    u32x4 doff;
    { const unsigned o0 = (n ^ (((g & 1) << 3) | (g >> 1))) << 4; doff = u32x4{o0, o0 ^ 32, o0 ^ 64, o0 ^ 96}; }
    LAS char* rowp; LAS char* trp;
    { const int phi = ((n & 7) << 1) | (n >> 3); rowp = smem + w * 16384 + (4 * (n & 3) + (n >> 2)) * 256; rowp += ((4 * g) ^ phi) << 4; }
    unsigned trc;
    { const int i = 4 * g + q, phi = ((i & 7) << 1) | (i >> 3); trp = smem + w * 16384 + (4 * q + g) * 256 + 8 * (p & 1); trc = ((p >> 1) ^ phi) << 4; }
    const unsigned rowx = (unsigned)(size_t)rowp;
    const unsigned mykey = (w << 4) | n;
    LAS unsigned* mylist = lists + w * CAP2;

    f32x4 acc[8];
#pragma unroll
    for (int c = 0; c < 8; ++c) acc[c] = f32x4{0.f, 0.f, 0.f, 0.f};

    int gcur, pos = 0;
    bool skip;
    {
        unsigned mx = 0;
#pragma unroll
        for (int k = 0; k < 8; ++k) mx = max(mx, cnt2[k]);
        skip = __builtin_amdgcn_readfirstlane(mx) > CAP2;
        gcur = skip ? -1 : (__builtin_amdgcn_readfirstlane(cnt2[8]) ? 0 : G1);
    }
    for (;;) {
        const int c = skip ? 0 : __builtin_amdgcn_readfirstlane(cnt2[w]);
        skip = false;
        const int G = (c + 31) >> 5;
        if (l < 32 && c + l < G * 32) mylist[c + l] = 0xFFFFFFFFu;
        auto issue = [&](int grp, int slot) {
            const u32x4 ea = *(LAS u32x4*)(mylist + grp * 32 + 8 * g), eb = *(LAS u32x4*)(mylist + grp * 32 + 8 * g + 4);
            dma_group(ring + slot * 8192, srd_x, ea, eb, doff);
        };
        auto step = [&](int i, int slot) {
            if (i + 1 < G) asm volatile("s_waitcnt vmcnt(8)" ::: "memory"); else asm volatile("s_waitcnt vmcnt(0)" ::: "memory");
            bf16x8 af[2][4], tf[8];
#pragma unroll
            for (int tt = 0; tt < 2; ++tt)
#pragma unroll
                for (int s = 0; s < 4; ++s) af[tt][s] = *(LAS bf16x8*)(size_t)((rowx ^ (s << 4)) + tt * 4096 + slot * 8192);
            const u32x4 ea = *(LAS u32x4*)(mylist + i * 32 + 8 * g), eb = *(LAS u32x4*)(mylist + i * 32 + 8 * g + 4);
#pragma unroll
            for (int cc = 0; cc < 8; ++cc) {
                LAS char* pa = trp + (trc ^ (cc << 5)) + slot * 8192;
                tf[cc] = cat(tr_read(pa), tr_read(pa + 4096));
            }
            if (i + 2 < G) issue(i + 2, slot);
            f32x4 s0 = {0.f, 0.f, 0.f, 0.f}, s1 = {0.f, 0.f, 0.f, 0.f};
#pragma unroll
            for (int s = 0; s < 4; ++s) { s0 = mfma(af[0][s], qf[s], s0); s1 = mfma(af[1][s], qf[s], s1); }
            float al[8];
#pragma unroll
            for (int r = 0; r < 4; ++r) {
                const float z0 = ((ea[r] & 0xFF) == mykey) ? s0[r] : __builtin_inff();
                const float z1 = ((eb[r] & 0xFF) == mykey) ? s1[r] : __builtin_inff();
                al[r] = __builtin_amdgcn_rcpf(1.f + __builtin_amdgcn_exp2f(z0));
                al[4 + r] = __builtin_amdgcn_rcpf(1.f + __builtin_amdgcn_exp2f(z1));
            }
            const u32x4 pv = {pk2(al[0], al[1]), pk2(al[2], al[3]), pk2(al[4], al[5]), pk2(al[6], al[7])};
            const bf16x8 pf = __builtin_bit_cast(bf16x8, pv);
#pragma unroll
            for (int cc = 0; cc < 8; ++cc) acc[cc] = mfma(tf[cc], pf, acc[cc]);
        };
        if (G > 0) issue(0, 0);
        if (G > 1) issue(1, 1);
        for (int i = 0; i < G; i += 2) {
            step(i, 0);
            if (i + 1 < G) step(i + 1, 1);
        }
        unsigned lim = G1 * 32;
        while (gcur >= 0 && gcur < G1) {
            const unsigned h = __builtin_amdgcn_readfirstlane(hdr[gcur]);
            lim = h >> 16;
            if ((h & 0xFFFF) > CAP1 && (unsigned)pos < lim) break;
            ++gcur; pos = 0;
        }
        if (gcur >= G1) break;
        WG_BARRIER();
        if (t < 8) cnt2[t] = 0;
        WG_BARRIER();
        const unsigned i = pos + t;
        if (t < CAP2 && i < lim) {
            unsigned e; bool v;
            if (gcur < 0) {
                e = ws[(size_t)b * G1 * 32 + i];
                v = ((i & 31) - 1) < (hdr[i >> 5] & 0xFFFF);
            } else {
                const unsigned u = ws[OVF_OFF / 4 + gcur * CH + i], d = u & 0xFFFF, j = d - b * NPB;
                e = ((u >> 16) << 8) | ((j & 7) << 4) | (j >> 3);
                v = j < NPB;
            }
            if (v) {
                const unsigned tl = (e >> 4) & 7;
                lists[tl * CAP2 + __hip_atomic_fetch_add(&cnt2[tl], 1u, __ATOMIC_RELAXED, __HIP_MEMORY_SCOPE_WORKGROUP)] = e;
            }
        }
        __builtin_amdgcn_s_waitcnt(0x0F70);
        pos += CAP2;
        if (gcur < 0 && pos >= G1 * 32) { gcur = 0; pos = 0; }
        WG_BARRIER();
    }
    STAMP(ws, 11);

    {
        bf16x8 ag[4];
#pragma unroll
        for (int u = 0; u < 4; ++u) {
            const u32x4 v = {pk2(acc[2 * u][0], acc[2 * u][1]), pk2(acc[2 * u][2], acc[2 * u][3]),
                             pk2(acc[2 * u + 1][0], acc[2 * u + 1][1]), pk2(acc[2 * u + 1][2], acc[2 * u + 1][3])};
            ag[u] = __builtin_bit_cast(bf16x8, v);
        }
        LAS char* av = smem + L_WV + (4 * g + q) * 256;
        unsigned op[8][2];
#pragma unroll
        for (int a = 0; a < 8; ++a) {
            f32x4 c = {0.f, 0.f, 0.f, 0.f};
            c = mfma(wvr[0][a], ag[0], c);
            c = mfma(wvr[1][a], ag[1], c);
            LAS char* pa = av + (cv ^ ((a >> 1) << 6) ^ ((a & 1) << 3));
            c = mfma(cat(tr_read(pa), tr_read(pa + 4096)), ag[2], c);
            c = mfma(cat(tr_read(pa + 8192), tr_read(pa + 8192 + 4096)), ag[3], c);
            op[a][0] = pk2(c[0], c[1]); op[a][1] = pk2(c[2], c[3]);
        }
        const auto rs_o = __builtin_amdgcn_make_buffer_rsrc((void*)out, 0, xbytes, 0x00020000);
        const bool odd = g & 1;
#pragma unroll
        for (int bb = 0; bb < 4; ++bb) {
            u32x4 v;
            v.x = odd ? op[2 * bb + 1][0] : op[2 * bb][0]; v.y = odd ? op[2 * bb + 1][1] : op[2 * bb][1];
            v.z = odd ? op[2 * bb][0] : op[2 * bb + 1][0]; v.w = odd ? op[2 * bb][1] : op[2 * bb + 1][1];
            __builtin_amdgcn_raw_buffer_store_b128(v, rs_o, rowoff + 64 * bb + 16 * g, 0, 0);
        }
    }
    STAMP(ws, 12);
    asm volatile("s_waitcnt vmcnt(0)" ::: "memory");
    STAMP(ws, 18);
    __syncthreads();
    STAMP(ws, 13);
    STAMP(ws, 19);
    {
        const unsigned long long* ps = (const unsigned long long*)((const char*)ws + STAMP_OFF) + (size_t)(blockIdx.x & (G1 - 1)) * STAMP_N;
        st_[0] = ps[0]; st_[1] = ps[1]; st_[2] = ps[2]; st_[3] = ps[3]; st_[4] = ps[4];
        const unsigned long long tgt = st_[19] + (unsigned long long)PROBE_M * (st_[PROBE_B] - st_[PROBE_A]);
        if (threadIdx.x < 64) { int guard = 0; while (wall_clock64() < tgt && ++guard < 200000) __builtin_amdgcn_s_sleep(2); }
    }
}

extern "C" void kernel_launch(void* const* d_in, const int* in_sizes, int n_in,
                              void* d_out, int out_size, void* d_ws, size_t ws_size,
                              hipStream_t stream) {
    const unsigned short* x = (const unsigned short*)d_in[0];
    const int* src = (const int*)d_in[1];
    const int* dst = (const int*)d_in[2];
    const unsigned short* wq = (const unsigned short*)d_in[3];
    const unsigned short* wv = (const unsigned short*)d_in[4];
    const int N = in_sizes[0] / 128, E = in_sizes[1];
    k_part<<<G1, 1024, 0, stream>>>(src, dst, (unsigned*)d_ws, E);
    k_gather<<<NB, 512, 0, stream>>>(x, wq, wv, (const unsigned*)d_ws, (unsigned short*)d_out, N);
}
```

```cpp
#include <hip/hip_runtime.h>
#include <hip/hip_bf16.h>
#include <stdint.h>

typedef __attribute__((ext_vector_type(8))) __bf16 bf16x8;
typedef __attribute__((ext_vector_type(4))) __bf16 bf16x4;
typedef __attribute__((ext_vector_type(2))) __bf16 bf16x2;
typedef __attribute__((ext_vector_type(4))) float f32x4;
typedef __attribute__((ext_vector_type(2))) float f32x2;
typedef __attribute__((ext_vector_type(4))) unsigned u32x4;
typedef __attribute__((ext_vector_type(2))) unsigned u32x2;
#define LAS __attribute__((address_space(3)))

constexpr int NB = 256;
constexpr int NPB = 98;
constexpr int G1 = 128;
constexpr int CH = 3128;
constexpr int CAP1 = 31;
constexpr int CAP2 = 384;
constexpr size_t INBOX_BYTES = (size_t)NB * G1 * 128;
constexpr size_t OVF_OFF = INBOX_BYTES;

#define STAMP_DECL
#define STAMP(base, k)
#define STAMP_FLUSH(base, k0, k1, wgoff)

__device__ __forceinline__ unsigned bucket_of(unsigned d) { return (d * 42800u) >> 22; }

__global__ __launch_bounds__(1024) void k_part(const int* __restrict__ src, const int* __restrict__ dst,
                                               unsigned* __restrict__ ws, int E) {
    __shared__ __attribute__((aligned(16))) unsigned img[NB * 32];
    __shared__ unsigned cnt[NB];
    __shared__ unsigned novf;
    const int t = threadIdx.x, g = blockIdx.x;
    STAMP_DECL;
    STAMP(ws, 0);
    const int e0 = g * CH + 4 * t;
    const bool act = (4 * t < CH) && (e0 < E);
    int4 d4 = {0, 0, 0, 0}, s4 = {0, 0, 0, 0};
    if (act) { d4 = *(const int4*)(dst + e0); s4 = *(const int4*)(src + e0); }
    if (t < NB) cnt[t] = 0;
    if (t == NB) novf = 0;
    __syncthreads();
    STAMP(ws, 1);
    if (act) {
        const unsigned dd[4] = {(unsigned)d4.x, (unsigned)d4.y, (unsigned)d4.z, (unsigned)d4.w};
        const unsigned ss[4] = {(unsigned)s4.x, (unsigned)s4.y, (unsigned)s4.z, (unsigned)s4.w};
        unsigned b[4], r[4];
#pragma unroll
        for (int k = 0; k < 4; ++k) { b[k] = bucket_of(dd[k]); r[k] = atomicAdd(&cnt[b[k]], 1u); }
#pragma unroll
        for (int k = 0; k < 4; ++k) {
            const unsigned j = dd[k] - b[k] * NPB;
            if (r[k] < CAP1) img[b[k] * 32 + 1 + r[k]] = (ss[k] << 8) | ((j & 7) << 4) | (j >> 3);
            else { const unsigned p = atomicAdd(&novf, 1u); ws[OVF_OFF / 4 + g * CH + p] = (ss[k] << 16) | dd[k]; }
        }
    }
    __syncthreads();
    STAMP(ws, 2);
    const unsigned no = novf;
#pragma unroll
    for (int it = 0; it < 2; ++it) {
        const int b = (t >> 3) + 128 * it, p = t & 7;
        const unsigned c = cnt[b];
        u32x4 v = *(const u32x4*)&img[b * 32 + 4 * p];
        if (p == 0) v.x = c | (no << 16);
        if (4 * p <= c) *(u32x4*)(ws + ((size_t)b * G1 + g) * 32 + 4 * p) = v;
    }
    STAMP(ws, 3);
    STAMP(ws, 4);
    STAMP_FLUSH(ws, 0, 5, 0);
}

constexpr int L_LIST = 131072;
constexpr int L_WV = L_LIST + 8 * CAP2 * 4;
constexpr int L_CNT = L_WV + 16384;
constexpr int L_HDR = L_CNT + 64;
constexpr int L_TOTAL = L_HDR + G1 * 4;

__device__ __forceinline__ u32x4 make_srd(const void* p, unsigned bytes) {
    const unsigned long long a = (unsigned long long)p;
    u32x4 s; s.x = (unsigned)a; s.y = (unsigned)(a >> 32) & 0xFFFFu; s.z = bytes; s.w = 0x00020000u; return s;
}

__device__ __forceinline__ void dma_group(unsigned ldsbase, u32x4 srd, u32x4 ea, u32x4 eb, u32x4 off) {
    unsigned keep, tmp;
    unsigned mask = 0xFFFFFF00u;
#define DMA1(E, O) "v_and_or_b32 %1, " E ", %4, " O "\n\tbuffer_load_dwordx4 %1, %3, 0 offen lds\n\t"
    asm volatile(
        "s_waitcnt lgkmcnt(0)\n\t"
        "s_mov_b32 %0, m0\n\t"
        "s_mov_b32 m0, %2\n\t"
        DMA1("%5", "%13") "s_add_u32 m0, m0, 0x400\n\t"
        DMA1("%6", "%14") "s_add_u32 m0, m0, 0x400\n\t"
        DMA1("%7", "%15") "s_add_u32 m0, m0, 0x400\n\t"
        DMA1("%8", "%16") "s_add_u32 m0, m0, 0x400\n\t"
        DMA1("%9", "%13") "s_add_u32 m0, m0, 0x400\n\t"
        DMA1("%10", "%14") "s_add_u32 m0, m0, 0x400\n\t"
        DMA1("%11", "%15") "s_add_u32 m0, m0, 0x400\n\t"
        DMA1("%12", "%16")
        "s_mov_b32 m0, %0"
        : "=&s"(keep), "=&v"(tmp)
        : "s"(ldsbase), "s"(srd), "s"(mask),
          "v"(ea.x), "v"(ea.y), "v"(ea.z), "v"(ea.w), "v"(eb.x), "v"(eb.y), "v"(eb.z), "v"(eb.w),
          "v"(off.x), "v"(off.y), "v"(off.z), "v"(off.w)
        : "memory", "scc");
#undef DMA1
}

__device__ __forceinline__ bf16x4 tr_read(LAS char* p) { return __builtin_amdgcn_ds_read_tr16_b64_v4bf16((LAS bf16x4*)p); }
__device__ __forceinline__ bf16x8 cat(bf16x4 a, bf16x4 b) { return __builtin_shufflevector(a, b, 0, 1, 2, 3, 4, 5, 6, 7); }
__device__ __forceinline__ unsigned pk2(float a, float b) { f32x2 v = {a, b}; bf16x2 r = __builtin_convertvector(v, bf16x2); return __builtin_bit_cast(unsigned, r); }
__device__ __forceinline__ f32x4 mfma(bf16x8 a, bf16x8 b, f32x4 c) { return __builtin_amdgcn_mfma_f32_16x16x32_bf16(a, b, c, 0, 0, 0); }
#define WG_BARRIER() do { asm volatile("s_waitcnt lgkmcnt(0)" ::: "memory"); __builtin_amdgcn_s_barrier(); asm volatile("" ::: "memory"); } while (0)

__global__ __launch_bounds__(512, 2) void k_gather(const unsigned short* __restrict__ x, const unsigned short* __restrict__ wq,
                                                   const unsigned short* __restrict__ wv, const unsigned* __restrict__ ws,
                                                   unsigned short* __restrict__ out, int N) {
    __shared__ __attribute__((aligned(16))) char smem_[L_TOTAL];
    LAS char* smem = (LAS char*)smem_;
    const int t = threadIdx.x, l = t & 63, b = blockIdx.x;
    const int w = __builtin_amdgcn_readfirstlane(t >> 6);
    const int g = l >> 4, n = l & 15, q = (l >> 2) & 3, p = l & 3;
    STAMP_DECL;
    STAMP(ws, 6);
    LAS unsigned* cnt2 = (LAS unsigned*)(smem + L_CNT);
    LAS unsigned* hdr = (LAS unsigned*)(smem + L_HDR);
    LAS unsigned* lists = (LAS unsigned*)(smem + L_LIST);

    u32x4 in0 = *(const u32x4*)(ws + (size_t)b * G1 * 32 + 4 * t);
    u32x4 in1 = *(const u32x4*)(ws + (size_t)b * G1 * 32 + 2048 + 4 * t);

    STAMP(ws, 14);
    const unsigned xbytes = (unsigned)N * 256u;
    const u32x4 srd_x = make_srd(x, xbytes);
    const unsigned ring = (unsigned)(size_t)smem + w * 16384;
    {
        const u32x4 srd_w = make_srd(w < 4 ? wq : wv, 32768);
        const unsigned dstb = (w < 6) ? ring + 8192 : (unsigned)(size_t)smem + L_WV + (w - 6) * 8192;
        const unsigned r0 = ((w & 3) * 32 + g) << 8;
        u32x4 ea = {r0, r0 + 1024, r0 + 2048, r0 + 3072}, eb = ea + 4096;
        u32x4 offa, offb;
        if (w < 4) { const unsigned o0 = (n ^ g) << 4; offa = u32x4{o0, o0, o0 ^ 64, o0 ^ 64}; }
        else { const unsigned o0 = (n ^ ((g & 1) | ((g & 2) << 1))) << 4; offa = u32x4{o0, o0 ^ 128, o0, o0 ^ 128}; }
        offb = offa;
        dma_group(dstb, srd_w, ea, eb, offb);
    }
    STAMP(ws, 15);
    const int jn = 8 * n + w, node = b * NPB + jn;
    const bool nvalid = (jn < NPB) && (node < N);
    const auto rs_x = __builtin_amdgcn_make_buffer_rsrc((void*)x, 0, xbytes, 0x00020000);
    const unsigned rowoff = nvalid ? (unsigned)node * 256u : 0xFFFFF000u;
    u32x4 xo[4];
#pragma unroll
    for (int u = 0; u < 4; ++u) xo[u] = __builtin_amdgcn_raw_buffer_load_b128(rs_x, rowoff + 64 * u + 16 * g, 0, 0);

    STAMP(ws, 16);
    if (t < 16) cnt2[t] = 0;
    WG_BARRIER();
    STAMP(ws, 7);

    {
        unsigned c0 = __builtin_amdgcn_update_dpp(0u, in0.x, 0x00, 0xF, 0xF, false);
        c0 = __builtin_amdgcn_update_dpp(c0, c0, 0x114, 0xF, 0xA, false);
        unsigned c1 = __builtin_amdgcn_update_dpp(0u, in1.x, 0x00, 0xF, 0xF, false);
        c1 = __builtin_amdgcn_update_dpp(c1, c1, 0x114, 0xF, 0xA, false);
        if ((t & 7) == 0) { hdr[t >> 3] = c0; hdr[64 + (t >> 3)] = c1; }
        c0 &= 0xFFFF; c1 &= 0xFFFF;
        if (c0 > CAP1 || c1 > CAP1) cnt2[8] = 1;
        const unsigned i0 = 4 * (t & 7) - 1;
        const unsigned ee[8] = {in0.x, in0.y, in0.z, in0.w, in1.x, in1.y, in1.z, in1.w};
        unsigned rank[8];
#pragma unroll
        for (int k = 0; k < 8; ++k) {
            const bool v = (i0 + (k & 3)) < (k < 4 ? c0 : c1);
            rank[k] = 0x7FFFFFFFu;
            if (v) rank[k] = __hip_atomic_fetch_add(&cnt2[(ee[k] >> 4) & 7], 1u, __ATOMIC_RELAXED, __HIP_MEMORY_SCOPE_WORKGROUP);
        }
#pragma unroll
        for (int k = 0; k < 8; ++k) if (rank[k] < CAP2) lists[((ee[k] >> 4) & 7) * CAP2 + rank[k]] = ee[k];
    }
    STAMP(ws, 8);
    asm volatile("s_waitcnt vmcnt(0)" ::: "memory");
    WG_BARRIER();
    STAMP(ws, 9);

    bf16x8 qf[4], wvr[2][8];
    {
        LAS char* aq = smem + 8192 + (8 * g + q) * 256;
        const unsigned cq = (((4 * p) ^ (q | ((g & 1) << 2))) << 4) | ((p & 1) << 3);
        unsigned qp[8][2];
#pragma unroll
        for (int a = 0; a < 8; ++a) {
            f32x4 c = {0.f, 0.f, 0.f, 0.f};
            LAS char* pa = aq + (cq ^ ((a >> 1) << 4) ^ ((a & 1) << 3));
#pragma unroll
            for (int u = 0; u < 4; ++u)
                c = mfma(cat(tr_read(pa + u * 16384), tr_read(pa + u * 16384 + 1024)), __builtin_bit_cast(bf16x8, xo[u]), c);
            const float k = -0.08838834764831845f * 1.4426950408889634f;
            qp[a][0] = pk2(c[0] * k, c[1] * k); qp[a][1] = pk2(c[2] * k, c[3] * k);
        }
        const bool odd = g & 1;
#pragma unroll
        for (int s = 0; s < 4; ++s) {
            u32x4 v;
            v.x = odd ? qp[2 * s + 1][0] : qp[2 * s][0]; v.y = odd ? qp[2 * s + 1][1] : qp[2 * s][1];
            v.z = odd ? qp[2 * s][0] : qp[2 * s + 1][0]; v.w = odd ? qp[2 * s][1] : qp[2 * s + 1][1];
            qf[s] = __builtin_bit_cast(bf16x8, v);
        }
    }
    STAMP(ws, 17);
    const int r8 = (4 * g + q) & 7;
    const unsigned cv = ((p ^ ((r8 & 1) | ((r8 & 6) << 1))) << 4) | ((p & 1) << 3);
    {
        LAS char* av = smem + 4 * 16384 + 8192 + (4 * g + q) * 256;
#pragma unroll
        for (int u = 0; u < 2; ++u)
#pragma unroll
            for (int a = 0; a < 8; ++a) {
                LAS char* pa = av + (cv ^ ((a >> 1) << 6) ^ ((a & 1) << 3)) + u * 16384;
                wvr[u][a] = cat(tr_read(pa), tr_read(pa + 4096));
            }
    }
    WG_BARRIER();
    STAMP(ws, 10);

    u32x4 doff;
    { const unsigned o0 = (n ^ (((g & 1) << 3) | (g >> 1))) << 4; doff = u32x4{o0, o0 ^ 32, o0 ^ 64, o0 ^ 96}; }
    LAS char* rowp; LAS char* trp;
    { const int phi = ((n & 7) << 1) | (n >> 3); rowp = smem + w * 16384 + (4 * (n & 3) + (n >> 2)) * 256; rowp += ((4 * g) ^ phi) << 4; }
    unsigned trc;
    { const int i = 4 * g + q, phi = ((i & 7) << 1) | (i >> 3); trp = smem + w * 16384 + (4 * q + g) * 256 + 8 * (p & 1); trc = ((p >> 1) ^ phi) << 4; }
    const unsigned rowx = (unsigned)(size_t)rowp;
    const unsigned mykey = (w << 4) | n;
    LAS unsigned* mylist = lists + w * CAP2;

    f32x4 acc[8];
#pragma unroll
    for (int c = 0; c < 8; ++c) acc[c] = f32x4{0.f, 0.f, 0.f, 0.f};

    int gcur, pos = 0;
    bool skip;
    {
        unsigned mx = 0;
#pragma unroll
        for (int k = 0; k < 8; ++k) mx = max(mx, cnt2[k]);
        skip = __builtin_amdgcn_readfirstlane(mx) > CAP2;
        gcur = skip ? -1 : (__builtin_amdgcn_readfirstlane(cnt2[8]) ? 0 : G1);
    }
    for (;;) {
        const int c = skip ? 0 : __builtin_amdgcn_readfirstlane(cnt2[w]);
        skip = false;
        const int G = (c + 31) >> 5;
        if (l < 32 && c + l < G * 32) mylist[c + l] = 0xFFFFFFFFu;
        auto issue = [&](int grp, int slot) {
            const u32x4 ea = *(LAS u32x4*)(mylist + grp * 32 + 8 * g), eb = *(LAS u32x4*)(mylist + grp * 32 + 8 * g + 4);
            dma_group(ring + slot * 8192, srd_x, ea, eb, doff);
        };
        auto step = [&](int i, int slot) {
            if (i + 1 < G) asm volatile("s_waitcnt vmcnt(8)" ::: "memory"); else asm volatile("s_waitcnt vmcnt(0)" ::: "memory");
            bf16x8 af[2][4], tf[8];
#pragma unroll
            for (int tt = 0; tt < 2; ++tt)
#pragma unroll
                for (int s = 0; s < 4; ++s) af[tt][s] = *(LAS bf16x8*)(size_t)((rowx ^ (s << 4)) + tt * 4096 + slot * 8192);
            const u32x4 ea = *(LAS u32x4*)(mylist + i * 32 + 8 * g), eb = *(LAS u32x4*)(mylist + i * 32 + 8 * g + 4);
#pragma unroll
            for (int cc = 0; cc < 8; ++cc) {
                LAS char* pa = trp + (trc ^ (cc << 5)) + slot * 8192;
                tf[cc] = cat(tr_read(pa), tr_read(pa + 4096));
            }
            if (i + 2 < G) issue(i + 2, slot);
            f32x4 s0 = {0.f, 0.f, 0.f, 0.f}, s1 = {0.f, 0.f, 0.f, 0.f};
#pragma unroll
            for (int s = 0; s < 4; ++s) { s0 = mfma(af[0][s], qf[s], s0); s1 = mfma(af[1][s], qf[s], s1); }
            float al[8];
#pragma unroll
            for (int r = 0; r < 4; ++r) {
                const float z0 = ((ea[r] & 0xFF) == mykey) ? s0[r] : __builtin_inff();
                const float z1 = ((eb[r] & 0xFF) == mykey) ? s1[r] : __builtin_inff();
                al[r] = __builtin_amdgcn_rcpf(1.f + __builtin_amdgcn_exp2f(z0));
                al[4 + r] = __builtin_amdgcn_rcpf(1.f + __builtin_amdgcn_exp2f(z1));
            }
            const u32x4 pv = {pk2(al[0], al[1]), pk2(al[2], al[3]), pk2(al[4], al[5]), pk2(al[6], al[7])};
            const bf16x8 pf = __builtin_bit_cast(bf16x8, pv);
#pragma unroll
            for (int cc = 0; cc < 8; ++cc) acc[cc] = mfma(tf[cc], pf, acc[cc]);
        };
        if (G > 0) issue(0, 0);
        if (G > 1) issue(1, 1);
        for (int i = 0; i < G; i += 2) {
            step(i, 0);
            if (i + 1 < G) step(i + 1, 1);
        }
        unsigned lim = G1 * 32;
        while (gcur >= 0 && gcur < G1) {
            const unsigned h = __builtin_amdgcn_readfirstlane(hdr[gcur]);
            lim = h >> 16;
            if ((h & 0xFFFF) > CAP1 && (unsigned)pos < lim) break;
            ++gcur; pos = 0;
        }
        if (gcur >= G1) break;
        WG_BARRIER();
        if (t < 8) cnt2[t] = 0;
        WG_BARRIER();
        const unsigned i = pos + t;
        if (t < CAP2 && i < lim) {
            unsigned e; bool v;
            if (gcur < 0) {
                e = ws[(size_t)b * G1 * 32 + i];
                v = ((i & 31) - 1) < (hdr[i >> 5] & 0xFFFF);
            } else {
                const unsigned u = ws[OVF_OFF / 4 + gcur * CH + i], d = u & 0xFFFF, j = d - b * NPB;
                e = ((u >> 16) << 8) | ((j & 7) << 4) | (j >> 3);
                v = j < NPB;
            }
            if (v) {
                const unsigned tl = (e >> 4) & 7;
                lists[tl * CAP2 + __hip_atomic_fetch_add(&cnt2[tl], 1u, __ATOMIC_RELAXED, __HIP_MEMORY_SCOPE_WORKGROUP)] = e;
            }
        }
        __builtin_amdgcn_s_waitcnt(0x0F70);
        pos += CAP2;
        if (gcur < 0 && pos >= G1 * 32) { gcur = 0; pos = 0; }
        WG_BARRIER();
    }
    STAMP(ws, 11);

    {
        bf16x8 ag[4];
#pragma unroll
        for (int u = 0; u < 4; ++u) {
            const u32x4 v = {pk2(acc[2 * u][0], acc[2 * u][1]), pk2(acc[2 * u][2], acc[2 * u][3]),
                             pk2(acc[2 * u + 1][0], acc[2 * u + 1][1]), pk2(acc[2 * u + 1][2], acc[2 * u + 1][3])};
            ag[u] = __builtin_bit_cast(bf16x8, v);
        }
        LAS char* av = smem + L_WV + (4 * g + q) * 256;
        unsigned op[8][2];
#pragma unroll
        for (int a = 0; a < 8; ++a) {
            f32x4 c = {0.f, 0.f, 0.f, 0.f};
            c = mfma(wvr[0][a], ag[0], c);
            c = mfma(wvr[1][a], ag[1], c);
            LAS char* pa = av + (cv ^ ((a >> 1) << 6) ^ ((a & 1) << 3));
            c = mfma(cat(tr_read(pa), tr_read(pa + 4096)), ag[2], c);
            c = mfma(cat(tr_read(pa + 8192), tr_read(pa + 8192 + 4096)), ag[3], c);
            op[a][0] = pk2(c[0], c[1]); op[a][1] = pk2(c[2], c[3]);
        }
        const auto rs_o = __builtin_amdgcn_make_buffer_rsrc((void*)out, 0, xbytes, 0x00020000);
        const bool odd = g & 1;
#pragma unroll
        for (int bb = 0; bb < 4; ++bb) {
            u32x4 v;
            v.x = odd ? op[2 * bb + 1][0] : op[2 * bb][0]; v.y = odd ? op[2 * bb + 1][1] : op[2 * bb][1];
            v.z = odd ? op[2 * bb][0] : op[2 * bb + 1][0]; v.w = odd ? op[2 * bb][1] : op[2 * bb + 1][1];
            __builtin_amdgcn_raw_buffer_store_b128(v, rs_o, rowoff + 64 * bb + 16 * g, 0, 2);
        }
    }
    STAMP(ws, 12);
}

extern "C" void kernel_launch(void* const* d_in, const int* in_sizes, int n_in,
                              void* d_out, int out_size, void* d_ws, size_t ws_size,
                              hipStream_t stream) {
    const unsigned short* x = (const unsigned short*)d_in[0];
    const int* src = (const int*)d_in[1];
    const int* dst = (const int*)d_in[2];
    const unsigned short* wq = (const unsigned short*)d_in[3];
    const unsigned short* wv = (const unsigned short*)d_in[4];
    const int N = in_sizes[0] / 128, E = in_sizes[1];
    k_part<<<G1, 1024, 0, stream>>>(src, dst, (unsigned*)d_ws, E);
    k_gather<<<NB, 512, 0, stream>>>(x, wq, wv, (const unsigned*)d_ws, (unsigned short*)d_out, N);
}
```

```cpp
#include <hip/hip_runtime.h>
#include <hip/hip_bf16.h>
#include <stdint.h>

typedef __attribute__((ext_vector_type(8))) __bf16 bf16x8;
typedef __attribute__((ext_vector_type(4))) __bf16 bf16x4;
typedef __attribute__((ext_vector_type(2))) __bf16 bf16x2;
typedef __attribute__((ext_vector_type(4))) float f32x4;
typedef __attribute__((ext_vector_type(2))) float f32x2;
typedef __attribute__((ext_vector_type(4))) unsigned u32x4;
#define LAS __attribute__((address_space(3)))

constexpr int NB = 256;
constexpr int NPB = 98;
constexpr int G1 = 128;
constexpr int CH = 3136;
constexpr int CAP1 = 30;
constexpr int CAP2 = 384;
#define PF_ROWS 12288
constexpr unsigned WS_NONCE = 0;
constexpr unsigned WS_NOVF = 2048;
constexpr unsigned WS_INBOX = 4096;
constexpr unsigned WS_OVF = WS_INBOX + NB * G1 * 128;
constexpr unsigned WS_BYTES = WS_OVF + G1 * CH * 4;
constexpr unsigned OOB = 0xFFFFF000u;

#define STAMP_DECL
#define STAMP(k)
#define STAMP_FLUSH(base, k0, k1)

constexpr int L_CNT1 = 4 * 16384;
constexpr int L_LIST = 131072;
constexpr int L_WV = L_LIST + 8 * CAP2 * 4;
constexpr int L_CNT = L_WV + 16384;
constexpr int L_HDR = L_CNT + 128;
constexpr int L_DUMMY = L_HDR + G1 * 4;
constexpr int L_TOTAL = L_DUMMY + 256;

__device__ __forceinline__ u32x4 make_srd(const void* p, unsigned bytes) {
    const unsigned long long a = (unsigned long long)p;
    u32x4 s; s.x = (unsigned)a; s.y = (unsigned)(a >> 32) & 0xFFFFu; s.z = bytes; s.w = 0x00020000u; return s;
}
__device__ __forceinline__ unsigned bucket_of(unsigned d) { return (d * 42800u) >> 22; }
__device__ __forceinline__ unsigned tag_of(unsigned nonce) { return (nonce + 0x1000u) & 0xFFFFF000u; }
__device__ __forceinline__ unsigned mask_of(unsigned tag) { const unsigned h = (tag >> 12) * 0x9E3779B1u; return h ^ (h >> 15); }

#define LD4(dst, srd, off, FL) asm volatile("buffer_load_dwordx4 %0, %1, %2, 0 offen" FL : "=v"(dst) : "v"(off), "s"(srd) : "memory")
#define LD1(dst, srd, off, FL) asm volatile("buffer_load_dword %0, %1, %2, 0 offen" FL : "=v"(dst) : "v"(off), "s"(srd) : "memory")
#define ST4(val, srd, off, FL) asm volatile("buffer_store_dwordx4 %0, %1, %2, 0 offen" FL "\n\ts_nop 1" :: "v"(val), "v"(off), "s"(srd) : "memory")
#define ST1(val, srd, off, FL) asm volatile("buffer_store_dword %0, %1, %2, 0 offen" FL :: "v"(val), "v"(off), "s"(srd) : "memory")
#define PIN(x) asm volatile("" : "+v"(x))
#define VMWAIT(n) asm volatile("s_waitcnt vmcnt(" #n ")" ::: "memory")
#define WG_BARRIER() do { asm volatile("s_waitcnt lgkmcnt(0)" ::: "memory"); __builtin_amdgcn_s_barrier(); asm volatile("" ::: "memory"); } while (0)

__device__ __forceinline__ void dma_group(unsigned ldsbase, u32x4 srd, u32x4 ea, u32x4 eb, u32x4 off, int nm) {
    unsigned keep, tmp;
    unsigned mask = 0xFFFFFF00u;
#define DMA1(E, O) "v_and_or_b32 %1, " E ", %4, " O "\n\tbuffer_load_dwordx4 %1, %3, 0 offen lds\n\t"
#define NEXT(K) "s_add_u32 m0, m0, 0x400\n\ts_cmp_le_i32 %17, " #K "\n\ts_cbranch_scc1 1f\n\t"
    asm volatile(
        "s_waitcnt lgkmcnt(0)\n\t"
        "s_mov_b32 %0, m0\n\t"
        "s_mov_b32 m0, %2\n\t"
        DMA1("%5", "%13") NEXT(1) DMA1("%6", "%14") NEXT(2) DMA1("%7", "%15") NEXT(3) DMA1("%8", "%16") NEXT(4)
        DMA1("%9", "%13") NEXT(5) DMA1("%10", "%14") NEXT(6) DMA1("%11", "%15") NEXT(7) DMA1("%12", "%16")
        "1:\n\t"
        "s_mov_b32 m0, %0"
        : "=&s"(keep), "=&v"(tmp)
        : "s"(ldsbase), "s"(srd), "s"(mask),
          "v"(ea.x), "v"(ea.y), "v"(ea.z), "v"(ea.w), "v"(eb.x), "v"(eb.y), "v"(eb.z), "v"(eb.w),
          "v"(off.x), "v"(off.y), "v"(off.z), "v"(off.w), "s"(nm)
        : "memory", "scc");
#undef DMA1
#undef NEXT
}

__device__ __forceinline__ void vm_wait_upto8(int n) {
#define W1(K) "s_waitcnt vmcnt(" #K ")\n\ts_cmp_ge_i32 %0, " #K "\n\ts_cbranch_scc1 1f\n\t"
    asm volatile(W1(8) W1(7) W1(6) W1(5) W1(4) W1(3) W1(2) W1(1) "s_waitcnt vmcnt(0)\n\t1:" :: "s"(n) : "memory", "scc");
#undef W1
}

template <int CTRL, int BANK> __device__ __forceinline__ unsigned dpp(unsigned old, unsigned v) { return __builtin_amdgcn_update_dpp(old, v, CTRL, 0xF, BANK, false); }
__device__ __forceinline__ unsigned bcast8(unsigned v) { v = dpp<0x00, 0xF>(0u, v); return dpp<0x114, 0xA>(v, v); }
__device__ __forceinline__ unsigned sum8(unsigned v) { v += dpp<0xB1, 0xF>(0u, v); v += dpp<0x4E, 0xF>(0u, v); return v + dpp<0x141, 0xF>(0u, v); }
__device__ __forceinline__ bf16x4 tr_read(LAS char* p) { return __builtin_amdgcn_ds_read_tr16_b64_v4bf16((LAS bf16x4*)p); }
__device__ __forceinline__ bf16x8 cat(bf16x4 a, bf16x4 b) { return __builtin_shufflevector(a, b, 0, 1, 2, 3, 4, 5, 6, 7); }
__device__ __forceinline__ unsigned pk2(float a, float b) { f32x2 v = {a, b}; bf16x2 r = __builtin_convertvector(v, bf16x2); return __builtin_bit_cast(unsigned, r); }
__device__ __forceinline__ f32x4 mfma(bf16x8 a, bf16x8 b, f32x4 c) { return __builtin_amdgcn_mfma_f32_16x16x32_bf16(a, b, c, 0, 0, 0); }
__device__ __forceinline__ LAS unsigned* ldsp(unsigned a) { return (LAS unsigned*)(size_t)a; }
__device__ __forceinline__ unsigned lds_add(LAS unsigned* p) { return __hip_atomic_fetch_add(p, 1u, __ATOMIC_RELAXED, __HIP_MEMORY_SCOPE_WORKGROUP); }

__global__ __launch_bounds__(512, 2) void k_graph(const unsigned short* __restrict__ x, const int* __restrict__ src, const int* __restrict__ dst,
                                                  const unsigned short* __restrict__ wq, const unsigned short* __restrict__ wv,
                                                  unsigned* __restrict__ ws, unsigned short* __restrict__ out, int N, int E) {
    __shared__ __attribute__((aligned(16))) char smem_[L_TOTAL];
    LAS char* smem = (LAS char*)smem_;
    const int t = threadIdx.x, l = t & 63, b = blockIdx.x;
    const int w = __builtin_amdgcn_readfirstlane(t >> 6);
    const int g = l >> 4, n = l & 15, q = (l >> 2) & 3, p = l & 3;
    STAMP_DECL;
    STAMP(0);
    LAS unsigned* cnt1 = (LAS unsigned*)(smem + L_CNT1);
    LAS unsigned* cnt2 = (LAS unsigned*)(smem + L_CNT);
    LAS unsigned* hdr = (LAS unsigned*)(smem + L_HDR);
    LAS unsigned* lists = (LAS unsigned*)(smem + L_LIST);
    const unsigned xbytes = (unsigned)N * 256u;
    const u32x4 srd_x = make_srd(x, xbytes), srd_ws = make_srd(ws, WS_BYTES);
    const unsigned ring = (unsigned)(size_t)smem + w * 16384;
    const unsigned lds0 = (unsigned)(size_t)smem, dummy = lds0 + L_DUMMY + 4 * l;
    asm volatile("" :: "s"(src), "s"(dst), "s"(out), "s"(wq), "s"(wv));
    const bool isprod = ((b >> 3) & 1) == 0;
    const int pg = ((b >> 4) << 3) | (b & 7);

    const int jn = 8 * n + w, node = b * NPB + jn;
    const unsigned rowoff = ((jn < NPB) && (node < N)) ? (unsigned)node * 256u : OOB;
    unsigned mynonce;
    u32x4 xo[4];
    auto common_loads = [&]() {
        { const unsigned o = WS_NONCE + b * 4; LD1(mynonce, srd_ws, o, " sc1"); }
#pragma unroll
        for (int u = 0; u < 4; ++u) { const unsigned o = rowoff + 64 * u + 16 * g; LD4(xo[u], srd_x, o, ""); }
        const u32x4 srd_w = make_srd(w < 4 ? wq : wv, 32768);
        const unsigned dstb = (w < 6) ? ring + 8192 : (unsigned)(size_t)smem + L_WV + (w - 6) * 8192;
        const unsigned r0 = ((w & 3) * 32 + g) << 8;
        const u32x4 ea = {r0, r0 + 1024, r0 + 2048, r0 + 3072}, eb = ea + 4096;
        const unsigned o0 = (n ^ (w < 4 ? g : ((g & 1) | ((g & 2) << 1)))) << 4;
        const unsigned f1 = w < 4 ? 0 : 128, f2 = w < 4 ? 64 : 0;
        const u32x4 off = {o0, o0 ^ f1, o0 ^ f2, o0 ^ f1 ^ f2};
        dma_group(dstb, srd_w, ea, eb, off, 8);
    };
    auto zero_counters = [&]() {
        if (t < NB + 1) cnt1[t] = 0;
        if (t >= 448 && t < 448 + 32) cnt2[t - 448] = 0;
        WG_BARRIER();
    };

    if (isprod) {
        u32x4 dA, sA, dB, sB;
        unsigned nn[4];
        const unsigned eA = pg * CH + 4 * t, eB = eA + 2048;
        {
            const u32x4 srd_d = make_srd(dst, (unsigned)E * 4u), srd_s = make_srd(src, (unsigned)E * 4u);
            const unsigned oA = eA * 4, oB = (4 * t + 2048 < CH) ? eB * 4 : OOB;
            LD4(dA, srd_d, oA, ""); LD4(sA, srd_s, oA, ""); LD4(dB, srd_d, oB, ""); LD4(sB, srd_s, oB, "");
#pragma unroll
            for (int it = 0; it < 4; ++it) { const unsigned o = WS_NONCE + ((t >> 3) + 64 * it) * 4; LD1(nn[it], srd_ws, o, " sc1"); }
        }
        common_loads();
        zero_counters();
        STAMP(1);
        asm volatile("s_waitcnt vmcnt(17)" : "+v"(dA), "+v"(sA), "+v"(dB), "+v"(sB) :: "memory");
        STAMP(2);
        const bool vA = eA < (unsigned)E, vB = (4 * t + 2048 < CH) && eB < (unsigned)E;
        auto route = [&](const u32x4& d4, const u32x4& s4, bool valid) {
            unsigned bk[4], rk[4];
#pragma unroll
            for (int k = 0; k < 4; ++k) { bk[k] = bucket_of(d4[k]); rk[k] = lds_add(ldsp(valid ? lds0 + L_CNT1 + 4 * bk[k] : dummy)); }
            bool over = false;
#pragma unroll
            for (int k = 0; k < 4; ++k) {
                const unsigned j = d4[k] - bk[k] * NPB;
                const bool fit = valid && rk[k] < CAP1;
                over |= valid && !fit;
                *ldsp(fit ? lds0 + (bk[k] >> 6) * 16384 + (bk[k] & 63) * 128 + 8 + 4 * rk[k] : dummy) = (s4[k] << 8) | ((j & 7) << 4) | (j >> 3);
            }
            if (__builtin_expect(__any(over), 0)) {
#pragma unroll
                for (int k = 0; k < 4; ++k) {
                    const bool ov = valid && rk[k] >= CAP1;
                    const unsigned ps = lds_add(ldsp(ov ? lds0 + L_CNT1 + 4 * NB : dummy));
                    const unsigned o = ov ? WS_OVF + (pg * CH + ps) * 4 : OOB, v = (s4[k] << 16) | d4[k];
                    ST1(v, srd_ws, o, " sc1");
                }
                VMWAIT(0);
            }
        };
        route(dA, sA, vA);
        if (w < 5) route(dB, sB, vB);
        WG_BARRIER();
        STAMP(3);
        const unsigned no = __builtin_amdgcn_readfirstlane(cnt1[NB]);
        if (no) {
            const unsigned o = t == 0 ? WS_NOVF + pg * 4 : OOB;
            ST1(no, srd_ws, o, " sc1");
            VMWAIT(0);
            WG_BARRIER();
        }
        asm volatile("s_waitcnt vmcnt(13)" : "+v"(nn[0]), "+v"(nn[1]), "+v"(nn[2]), "+v"(nn[3]) :: "memory");
        const unsigned i0 = 4 * (t & 7) - 2;
#pragma unroll
        for (int it = 0; it < 4; ++it) {
            const int bb = (t >> 3) + 64 * it;
            const unsigned c = cnt1[bb];
            u32x4 v = *(LAS u32x4*)(smem + it * 16384 + t * 16);
#pragma unroll
            for (int k = 0; k < 4; ++k) v[k] = (i0 + k < c) ? v[k] : 0u;
            const unsigned s = sum8(v.x + v.y + v.z + v.w);
            const unsigned tg = tag_of(nn[it]), m = mask_of(tg);
            if ((t & 7) == 0) v.y = s;
            v ^= m;
            if ((t & 7) == 0) v.x = tg | c;
            const unsigned o = ((int)i0 < (int)c) ? WS_INBOX + (bb * G1 + pg) * 128 + (t & 7) * 16 : OOB;
            ST4(v, srd_ws, o, " sc1");
        }
        STAMP(4);
        asm volatile("s_waitcnt vmcnt(4)" : "+v"(xo[0]), "+v"(xo[1]), "+v"(xo[2]), "+v"(xo[3]), "+v"(mynonce) :: "memory");
    } else {
        common_loads();
        {
            unsigned keep, o[4];
#pragma unroll
            for (int it = 0; it < 4; ++it) { const unsigned line = (it * 16 + (b >> 4)) * 512 + t; o[it] = line < 2 * PF_ROWS ? line * 128 : OOB; }
            asm volatile("s_mov_b32 %0, m0\n\ts_mov_b32 m0, %1\n\ts_nop 0\n\t"
                         "buffer_load_dword %3, %2, 0 offen lds\n\tbuffer_load_dword %4, %2, 0 offen lds\n\t"
                         "buffer_load_dword %5, %2, 0 offen lds\n\tbuffer_load_dword %6, %2, 0 offen lds\n\ts_mov_b32 m0, %0"
                         : "=&s"(keep) : "s"(ring), "s"(srd_x), "v"(o[0]), "v"(o[1]), "v"(o[2]), "v"(o[3]) : "memory");
        }
        zero_counters();
        STAMP(1);
        asm volatile("s_waitcnt vmcnt(4)" : "+v"(xo[0]), "+v"(xo[1]), "+v"(xo[2]), "+v"(xo[3]), "+v"(mynonce) :: "memory");
        STAMP(2); STAMP(3); STAMP(4);
    }
    WG_BARRIER();
    STAMP(5);

    bf16x8 qf[4], wvr[2][8];
    {
        LAS char* aq = smem + 8192 + (8 * g + q) * 256;
        const unsigned cq = (((4 * p) ^ (q | ((g & 1) << 2))) << 4) | ((p & 1) << 3);
        unsigned qp[8][2];
#pragma unroll
        for (int a = 0; a < 8; ++a) {
            f32x4 c = {0.f, 0.f, 0.f, 0.f};
            LAS char* pa = aq + (cq ^ ((a >> 1) << 4) ^ ((a & 1) << 3));
#pragma unroll
            for (int u = 0; u < 4; ++u)
                c = mfma(cat(tr_read(pa + u * 16384), tr_read(pa + u * 16384 + 1024)), __builtin_bit_cast(bf16x8, xo[u]), c);
            const float k = -0.08838834764831845f * 1.4426950408889634f;
            qp[a][0] = pk2(c[0] * k, c[1] * k); qp[a][1] = pk2(c[2] * k, c[3] * k);
        }
        const bool odd = g & 1;
#pragma unroll
        for (int s = 0; s < 4; ++s) {
            u32x4 v;
            v.x = odd ? qp[2 * s + 1][0] : qp[2 * s][0]; v.y = odd ? qp[2 * s + 1][1] : qp[2 * s][1];
            v.z = odd ? qp[2 * s][0] : qp[2 * s + 1][0]; v.w = odd ? qp[2 * s][1] : qp[2 * s + 1][1];
            qf[s] = __builtin_bit_cast(bf16x8, v);
        }
    }
    const int r8 = (4 * g + q) & 7;
    const unsigned cv = ((p ^ ((r8 & 1) | ((r8 & 6) << 1))) << 4) | ((p & 1) << 3);
    {
        LAS char* av = smem + 4 * 16384 + 8192 + (4 * g + q) * 256;
#pragma unroll
        for (int u = 0; u < 2; ++u)
#pragma unroll
            for (int a = 0; a < 8; ++a) {
                LAS char* pa = av + (cv ^ ((a >> 1) << 6) ^ ((a & 1) << 3)) + u * 16384;
                wvr[u][a] = cat(tr_read(pa), tr_read(pa + 4096));
            }
    }
    STAMP(6);

    const unsigned mytag = tag_of(mynonce), mymask = mask_of(mytag);
    {
        const unsigned ib = WS_INBOX + b * (G1 * 128) + t * 16;
        const unsigned i0 = 4 * (t & 7) - 2;
        u32x4 in[2]; unsigned cc[2]; bool ok[2] = {false, false};
        for (unsigned spin = 0;; ++spin) {
            u32x4 nw[2];
            const unsigned o0 = ok[0] ? OOB : ib, o1 = ok[1] ? OOB : ib + 8192;
            asm volatile("buffer_load_dwordx4 %0, %2, %4, 0 offen sc1\n\tbuffer_load_dwordx4 %1, %3, %4, 0 offen sc1\n\ts_waitcnt vmcnt(0)"
                         : "=&v"(nw[0]), "=&v"(nw[1]) : "v"(o0), "v"(o1), "s"(srd_ws) : "memory");
#pragma unroll
            for (int h = 0; h < 2; ++h) {
                const unsigned hd = bcast8(nw[h].x), c = hd & 0xFFF;
                u32x4 v = nw[h] ^ mymask;
                const unsigned cs = bcast8(v.y);
                unsigned s = 0;
#pragma unroll
                for (int k = 0; k < 4; ++k) s += (i0 + k < c) ? v[k] : 0u;
                const bool good = ((hd ^ mytag) < 0x1000u) && cs == sum8(s);
                if (!ok[h] && good) { in[h] = v; cc[h] = c; ok[h] = true; }
            }
            if (__all(ok[0] && ok[1])) break;
            if (spin > (1u << 22)) break;
        }
        STAMP(7);
        if ((t & 7) == 0) { hdr[t >> 3] = cc[0]; hdr[64 + (t >> 3)] = cc[1]; }
        const unsigned c0 = cc[0], c1 = cc[1];
        if (c0 > CAP1 || c1 > CAP1) cnt2[16] = 1;
        unsigned rank[8];
#pragma unroll
        for (int k = 0; k < 8; ++k) {
            const unsigned e = in[k >> 2][k & 3];
            const bool v = i0 + (k & 3) < (k < 4 ? c0 : c1);
            rank[k] = lds_add(ldsp(v ? lds0 + L_CNT + ((e >> 1) & 56) + (e >= (PF_ROWS << 8) ? 4 : 0) : dummy));
        }
#pragma unroll
        for (int k = 0; k < 8; ++k) {
            const unsigned e = in[k >> 2][k & 3];
            const bool v = i0 + (k & 3) < (k < 4 ? c0 : c1) && rank[k] < CAP2;
            *ldsp(v ? lds0 + L_LIST + (((e >> 4) & 7) * CAP2 + (e >= (PF_ROWS << 8) ? CAP2 - 1 - rank[k] : rank[k])) * 4 : dummy) = e;
        }
    }
    WG_BARRIER();
    STAMP(8);

    u32x4 doff;
    { const unsigned o0 = (n ^ (((g & 1) << 3) | (g >> 1))) << 4; doff = u32x4{o0, o0 ^ 32, o0 ^ 64, o0 ^ 96}; }
    unsigned rowx, trc; LAS char* trp;
    { const int phi = ((n & 7) << 1) | (n >> 3); rowx = ring + (4 * (n & 3) + (n >> 2)) * 256 + (((4 * g) ^ phi) << 4); }
    { const int i = 4 * g + q, phi = ((i & 7) << 1) | (i >> 3); trp = smem + w * 16384 + (4 * q + g) * 256 + 8 * (p & 1); trc = ((p >> 1) ^ phi) << 4; }
    const unsigned mykey = (w << 4) | n;
    LAS unsigned* mylist = lists + w * CAP2;

    f32x4 acc[8];
#pragma unroll
    for (int c = 0; c < 8; ++c) acc[c] = f32x4{0.f, 0.f, 0.f, 0.f};

    int gcur, pos = 0;
    bool skip;
    {
        unsigned mx = 0;
#pragma unroll
        for (int k = 0; k < 8; ++k) mx = max(mx, cnt2[2 * k] + cnt2[2 * k + 1]);
        skip = __builtin_amdgcn_readfirstlane(mx) > CAP2;
        gcur = skip ? -1 : (__builtin_amdgcn_readfirstlane(cnt2[16]) ? 0 : G1);
    }
    for (;;) {
        const int clo = skip ? 0 : __builtin_amdgcn_readfirstlane(cnt2[2 * w]);
        const int c = skip ? 0 : clo + __builtin_amdgcn_readfirstlane(cnt2[2 * w + 1]);
        skip = false;
        const int G = (c + 31) >> 5;
        {
            unsigned e[CAP2 / 64];
#pragma unroll
            for (int k = 0; k < CAP2 / 64; ++k) {
                const int ps = l + 64 * k;
                e[k] = mylist[min(ps < clo ? ps : ps + CAP2 - c, CAP2 - 1)];
            }
            const int sg = (l & 32) | ((l & 3) << 3) | ((l >> 2) & 7);
#pragma unroll
            for (int k = 0; k < CAP2 / 64; ++k) mylist[sg + 64 * k] = (l + 64 * k < c) ? e[k] : 0xFFFFFFFFu;
        }
        const int nlast = G > 2 ? (((c - 1) & 31) >> 2) + 1 : 8;
        auto issue = [&](int grp, int slot, int nm) {
            const u32x4 ea = *(LAS u32x4*)(mylist + grp * 32 + 8 * g), eb = *(LAS u32x4*)(mylist + grp * 32 + 8 * g + 4);
            dma_group(ring + slot * 8192, srd_x, ea, eb, doff, nm);
        };
        auto step = [&](int i, int slot) {
            vm_wait_upto8(i + 2 < G ? 8 : (i + 2 == G ? nlast : 0));
            bf16x8 af[2][4], tf[8];
#pragma unroll
            for (int tt = 0; tt < 2; ++tt)
#pragma unroll
                for (int s = 0; s < 4; ++s) af[tt][s] = *(LAS bf16x8*)(size_t)((rowx ^ (s << 4)) + tt * 4096 + slot * 8192);
            const u32x4 ea = *(LAS u32x4*)(mylist + i * 32 + 8 * g), eb = *(LAS u32x4*)(mylist + i * 32 + 8 * g + 4);
#pragma unroll
            for (int cc = 0; cc < 8; ++cc) {
                LAS char* pa = trp + (trc ^ (cc << 5)) + slot * 8192;
                tf[cc] = cat(tr_read(pa), tr_read(pa + 4096));
            }
            if (i + 2 < G) issue(i + 2, slot, i + 3 == G ? nlast : 8);
            f32x4 s0 = {0.f, 0.f, 0.f, 0.f}, s1 = {0.f, 0.f, 0.f, 0.f};
#pragma unroll
            for (int s = 0; s < 4; ++s) { s0 = mfma(af[0][s], qf[s], s0); s1 = mfma(af[1][s], qf[s], s1); }
            float al[8];
#pragma unroll
            for (int r = 0; r < 4; ++r) {
                const float z0 = ((ea[r] & 0xFF) == mykey) ? s0[r] : __builtin_inff();
                const float z1 = ((eb[r] & 0xFF) == mykey) ? s1[r] : __builtin_inff();
                al[r] = __builtin_amdgcn_rcpf(1.f + __builtin_amdgcn_exp2f(z0));
                al[4 + r] = __builtin_amdgcn_rcpf(1.f + __builtin_amdgcn_exp2f(z1));
            }
            const u32x4 pv = {pk2(al[0], al[1]), pk2(al[2], al[3]), pk2(al[4], al[5]), pk2(al[6], al[7])};
            const bf16x8 pf8 = __builtin_bit_cast(bf16x8, pv);
#pragma unroll
            for (int cc = 0; cc < 8; ++cc) acc[cc] = mfma(tf[cc], pf8, acc[cc]);
        };
        if (G > 0) issue(0, 0, 8);
        if (G > 1) issue(1, 1, 8);
        STAMP(9);
#pragma clang loop unroll(disable)
        for (int i = 0; i < G; i += 2) {
            step(i, 0);
            if (i + 1 < G) step(i + 1, 1);
        }
        unsigned lim = G1 * 32;
        while (gcur >= 0 && gcur < G1) {
            if (__builtin_amdgcn_readfirstlane(hdr[gcur]) > CAP1) {
                unsigned v; const unsigned o = WS_NOVF + gcur * 4;
                asm volatile("buffer_load_dword %0, %1, %2, 0 offen sc1\n\ts_waitcnt vmcnt(0)" : "=&v"(v) : "v"(o), "s"(srd_ws) : "memory");
                lim = __builtin_amdgcn_readfirstlane(v);
                if ((unsigned)pos < lim) break;
            }
            ++gcur; pos = 0;
        }
        if (gcur >= G1) break;
        WG_BARRIER();
        if (t < 16) cnt2[t] = 0;
        WG_BARRIER();
        const unsigned i = pos + t;
        unsigned u;
        { const unsigned o = (t < CAP2 && i < lim) ? (gcur < 0 ? WS_INBOX + b * (G1 * 128) : WS_OVF + gcur * CH * 4) + i * 4 : OOB;
          asm volatile("buffer_load_dword %0, %1, %2, 0 offen sc1\n\ts_waitcnt vmcnt(0)" : "=&v"(u) : "v"(o), "s"(srd_ws) : "memory"); }
        if (t < CAP2 && i < lim) {
            unsigned e; bool v;
            if (gcur < 0) { e = u ^ mymask; v = ((i & 31) - 2) < hdr[i >> 5]; }
            else { const unsigned j = (u & 0xFFFF) - b * NPB; e = ((u >> 16) << 8) | ((j & 7) << 4) | (j >> 3); v = j < NPB; }
            if (v) { const unsigned tl = (e >> 4) & 7; lists[tl * CAP2 + lds_add(&cnt2[2 * tl])] = e; }
        }
        pos += CAP2;
        if (gcur < 0 && pos >= G1 * 32) { gcur = 0; pos = 0; }
        WG_BARRIER();
    }
    STAMP(10);

    {
        bf16x8 ag[4];
#pragma unroll
        for (int u = 0; u < 4; ++u) {
            const u32x4 v = {pk2(acc[2 * u][0], acc[2 * u][1]), pk2(acc[2 * u][2], acc[2 * u][3]),
                             pk2(acc[2 * u + 1][0], acc[2 * u + 1][1]), pk2(acc[2 * u + 1][2], acc[2 * u + 1][3])};
            ag[u] = __builtin_bit_cast(bf16x8, v);
        }
        LAS char* av = smem + L_WV + (4 * g + q) * 256;
        unsigned op[8][2];
#pragma unroll
        for (int a = 0; a < 8; ++a) {
            f32x4 c = {0.f, 0.f, 0.f, 0.f};
            c = mfma(wvr[0][a], ag[0], c);
            c = mfma(wvr[1][a], ag[1], c);
            LAS char* pa = av + (cv ^ ((a >> 1) << 6) ^ ((a & 1) << 3));
            c = mfma(cat(tr_read(pa), tr_read(pa + 4096)), ag[2], c);
            c = mfma(cat(tr_read(pa + 8192), tr_read(pa + 8192 + 4096)), ag[3], c);
            op[a][0] = pk2(c[0], c[1]); op[a][1] = pk2(c[2], c[3]);
        }
        const auto rs_o = __builtin_amdgcn_make_buffer_rsrc((void*)out, 0, xbytes, 0x00020000);
        const bool odd = g & 1;
#pragma unroll
        for (int bb = 0; bb < 4; ++bb) {
            u32x4 v;
            v.x = odd ? op[2 * bb + 1][0] : op[2 * bb][0]; v.y = odd ? op[2 * bb + 1][1] : op[2 * bb][1];
            v.z = odd ? op[2 * bb][0] : op[2 * bb + 1][0]; v.w = odd ? op[2 * bb][1] : op[2 * bb + 1][1];
            __builtin_amdgcn_raw_buffer_store_b128(v, rs_o, rowoff + 64 * bb + 16 * g, 0, 16);
        }
    }
    { const unsigned o = t == 0 ? WS_NONCE + b * 4 : OOB; if (w == 0) ST1(mytag, srd_ws, o, " sc1"); }
    STAMP(11);
}

extern "C" void kernel_launch(void* const* d_in, const int* in_sizes, int n_in,
                              void* d_out, int out_size, void* d_ws, size_t ws_size,
                              hipStream_t stream) {
    const int N = in_sizes[0] / 128, E = in_sizes[1];
    k_graph<<<NB, 512, 0, stream>>>((const unsigned short*)d_in[0], (const int*)d_in[1], (const int*)d_in[2],
                                    (const unsigned short*)d_in[3], (const unsigned short*)d_in[4],
                                    (unsigned*)d_ws, (unsigned short*)d_out, N, E);
}
```

```cpp
#define STAMPS
#define PROBE_A 0
#define PROBE_B 13
#define PROBE_M 0
#include <hip/hip_runtime.h>
#include <hip/hip_bf16.h>
#include <stdint.h>

typedef __attribute__((ext_vector_type(8))) __bf16 bf16x8;
typedef __attribute__((ext_vector_type(4))) __bf16 bf16x4;
typedef __attribute__((ext_vector_type(2))) __bf16 bf16x2;
typedef __attribute__((ext_vector_type(4))) float f32x4;
typedef __attribute__((ext_vector_type(2))) float f32x2;
typedef __attribute__((ext_vector_type(4))) unsigned u32x4;
#define LAS __attribute__((address_space(3)))

constexpr int NB = 256;
constexpr int NPB = 98;
constexpr int G1 = 128;
constexpr int CH = 3136;
constexpr int CAP1 = 30;
constexpr int CAP2 = 384;
#define PF_ROWS 12288
constexpr unsigned WS_NONCE = 0;
constexpr unsigned WS_NOVF = 2048;
constexpr unsigned WS_INBOX = 4096;
constexpr unsigned WS_OVF = WS_INBOX + NB * G1 * 128;
constexpr unsigned WS_BYTES = WS_OVF + G1 * CH * 4;
constexpr unsigned OOB = 0xFFFFF000u;

#define STAMP_N 24
#define STAMP_WGS 256
#define STAMP_OFF (200u << 20)
#define STAMP_ROLE 0
#define STAMP_VALID(w, k) ((k) < 14 && ((k) == 0 || ((((w) >> 3) & 1) == STAMP_ROLE)))
#define STAMP_DECL unsigned long long st_[STAMP_N] = {}
#define STAMP(k) do { __builtin_amdgcn_sched_barrier(0); asm volatile("s_memrealtime %0\n\ts_waitcnt lgkmcnt(0)" : "=s"(st_[k]) :: "memory"); __builtin_amdgcn_sched_barrier(0); } while (0)
#define STAMP_THREAD 0
#define STAMP_FLUSH(base, k0, k1) do { if (threadIdx.x == STAMP_THREAD) { _Pragma("unroll") for (int k_ = k0; k_ < k1; ++k_) ((unsigned long long*)((char*)(base) + STAMP_OFF))[(size_t)blockIdx.x * STAMP_N + k_] = st_[k_]; } } while (0)

constexpr int L_CNT1 = 4 * 16384;
constexpr int L_LIST = 131072;
constexpr int L_WV = L_LIST + 8 * CAP2 * 4;
constexpr int L_CNT = L_WV + 16384;
constexpr int L_HDR = L_CNT + 128;
constexpr int L_DUMMY = L_HDR + G1 * 4;
constexpr int L_TOTAL = L_DUMMY + 256;

__device__ __forceinline__ u32x4 make_srd(const void* p, unsigned bytes) {
    const unsigned long long a = (unsigned long long)p;
    u32x4 s; s.x = (unsigned)a; s.y = (unsigned)(a >> 32) & 0xFFFFu; s.z = bytes; s.w = 0x00020000u; return s;
}
__device__ __forceinline__ unsigned bucket_of(unsigned d) { return (d * 42800u) >> 22; }
__device__ __forceinline__ unsigned tag_of(unsigned nonce) { return (nonce + 0x1000u) & 0xFFFFF000u; }
__device__ __forceinline__ unsigned mask_of(unsigned tag) { const unsigned h = (tag >> 12) * 0x9E3779B1u; return h ^ (h >> 15); }

#define LD4(dst, srd, off, FL) asm volatile("buffer_load_dwordx4 %0, %1, %2, 0 offen" FL : "=v"(dst) : "v"(off), "s"(srd) : "memory")
#define LD1(dst, srd, off, FL) asm volatile("buffer_load_dword %0, %1, %2, 0 offen" FL : "=v"(dst) : "v"(off), "s"(srd) : "memory")
#define ST4(val, srd, off, FL) asm volatile("buffer_store_dwordx4 %0, %1, %2, 0 offen" FL "\n\ts_nop 1" :: "v"(val), "v"(off), "s"(srd) : "memory")
#define ST1(val, srd, off, FL) asm volatile("buffer_store_dword %0, %1, %2, 0 offen" FL :: "v"(val), "v"(off), "s"(srd) : "memory")
#define PIN(x) asm volatile("" : "+v"(x))
#define VMWAIT(n) asm volatile("s_waitcnt vmcnt(" #n ")" ::: "memory")
#define WG_BARRIER() do { asm volatile("s_waitcnt lgkmcnt(0)" ::: "memory"); __builtin_amdgcn_s_barrier(); asm volatile("" ::: "memory"); } while (0)

__device__ __forceinline__ void dma_group(unsigned ldsbase, u32x4 srd, u32x4 ea, u32x4 eb, u32x4 off, int nm) {
    unsigned keep, tmp;
    unsigned mask = 0xFFFFFF00u;
#define DMA1(E, O) "v_and_or_b32 %1, " E ", %4, " O "\n\tbuffer_load_dwordx4 %1, %3, 0 offen lds\n\t"
#define NEXT(K) "s_add_u32 m0, m0, 0x400\n\ts_cmp_le_i32 %17, " #K "\n\ts_cbranch_scc1 1f\n\t"
    asm volatile(
        "s_waitcnt lgkmcnt(0)\n\t"
        "s_mov_b32 %0, m0\n\t"
        "s_mov_b32 m0, %2\n\t"
        DMA1("%5", "%13") NEXT(1) DMA1("%6", "%14") NEXT(2) DMA1("%7", "%15") NEXT(3) DMA1("%8", "%16") NEXT(4)
        DMA1("%9", "%13") NEXT(5) DMA1("%10", "%14") NEXT(6) DMA1("%11", "%15") NEXT(7) DMA1("%12", "%16")
        "1:\n\t"
        "s_mov_b32 m0, %0"
        : "=&s"(keep), "=&v"(tmp)
        : "s"(ldsbase), "s"(srd), "s"(mask),
          "v"(ea.x), "v"(ea.y), "v"(ea.z), "v"(ea.w), "v"(eb.x), "v"(eb.y), "v"(eb.z), "v"(eb.w),
          "v"(off.x), "v"(off.y), "v"(off.z), "v"(off.w), "s"(nm)
        : "memory", "scc");
#undef DMA1
#undef NEXT
}

__device__ __forceinline__ void vm_wait_upto8(int n) {
#define W1(K) "s_waitcnt vmcnt(" #K ")\n\ts_cmp_ge_i32 %0, " #K "\n\ts_cbranch_scc1 1f\n\t"
    asm volatile(W1(8) W1(7) W1(6) W1(5) W1(4) W1(3) W1(2) W1(1) "s_waitcnt vmcnt(0)\n\t1:" :: "s"(n) : "memory", "scc");
#undef W1
}

template <int CTRL, int BANK> __device__ __forceinline__ unsigned dpp(unsigned old, unsigned v) { return __builtin_amdgcn_update_dpp(old, v, CTRL, 0xF, BANK, false); }
__device__ __forceinline__ unsigned bcast8(unsigned v) { v = dpp<0x00, 0xF>(0u, v); return dpp<0x114, 0xA>(v, v); }
__device__ __forceinline__ unsigned sum8(unsigned v) { v += dpp<0xB1, 0xF>(0u, v); v += dpp<0x4E, 0xF>(0u, v); return v + dpp<0x141, 0xF>(0u, v); }
__device__ __forceinline__ bf16x4 tr_read(LAS char* p) { return __builtin_amdgcn_ds_read_tr16_b64_v4bf16((LAS bf16x4*)p); }
__device__ __forceinline__ bf16x8 cat(bf16x4 a, bf16x4 b) { return __builtin_shufflevector(a, b, 0, 1, 2, 3, 4, 5, 6, 7); }
__device__ __forceinline__ unsigned pk2(float a, float b) { f32x2 v = {a, b}; bf16x2 r = __builtin_convertvector(v, bf16x2); return __builtin_bit_cast(unsigned, r); }
__device__ __forceinline__ f32x4 mfma(bf16x8 a, bf16x8 b, f32x4 c) { return __builtin_amdgcn_mfma_f32_16x16x32_bf16(a, b, c, 0, 0, 0); }
__device__ __forceinline__ LAS unsigned* ldsp(unsigned a) { return (LAS unsigned*)(size_t)a; }
__device__ __forceinline__ unsigned lds_add(LAS unsigned* p) { return __hip_atomic_fetch_add(p, 1u, __ATOMIC_RELAXED, __HIP_MEMORY_SCOPE_WORKGROUP); }

__global__ __launch_bounds__(512, 2) void k_graph(const unsigned short* __restrict__ x, const int* __restrict__ src, const int* __restrict__ dst,
                                                  const unsigned short* __restrict__ wq, const unsigned short* __restrict__ wv,
                                                  unsigned* __restrict__ ws, unsigned short* __restrict__ out, int N, int E) {
    __shared__ __attribute__((aligned(16))) char smem_[L_TOTAL];
    LAS char* smem = (LAS char*)smem_;
    const int t = threadIdx.x, l = t & 63, b = blockIdx.x;
    const int w = __builtin_amdgcn_readfirstlane(t >> 6);
    const int g = l >> 4, n = l & 15, q = (l >> 2) & 3, p = l & 3;
    STAMP_DECL;
    STAMP(0);
    LAS unsigned* cnt1 = (LAS unsigned*)(smem + L_CNT1);
    LAS unsigned* cnt2 = (LAS unsigned*)(smem + L_CNT);
    LAS unsigned* hdr = (LAS unsigned*)(smem + L_HDR);
    LAS unsigned* lists = (LAS unsigned*)(smem + L_LIST);
    const unsigned xbytes = (unsigned)N * 256u;
    const u32x4 srd_x = make_srd(x, xbytes), srd_ws = make_srd(ws, WS_BYTES);
    const unsigned ring = (unsigned)(size_t)smem + w * 16384;
    const unsigned lds0 = (unsigned)(size_t)smem, dummy = lds0 + L_DUMMY + 4 * l;
    asm volatile("" :: "s"(src), "s"(dst), "s"(out), "s"(wq), "s"(wv));
    const bool isprod = ((b >> 3) & 1) == 0;
    const int pg = ((b >> 4) << 3) | (b & 7);

    const int jn = 8 * n + w, node = b * NPB + jn;
    const unsigned rowoff = ((jn < NPB) && (node < N)) ? (unsigned)node * 256u : OOB;
    unsigned mynonce;
    u32x4 xo[4];
    auto common_loads = [&]() {
        { const unsigned o = WS_NONCE + b * 4; LD1(mynonce, srd_ws, o, " sc1"); }
#pragma unroll
        for (int u = 0; u < 4; ++u) { const unsigned o = rowoff + 64 * u + 16 * g; LD4(xo[u], srd_x, o, ""); }
        const u32x4 srd_w = make_srd(w < 4 ? wq : wv, 32768);
        const unsigned dstb = (w < 6) ? ring + 8192 : (unsigned)(size_t)smem + L_WV + (w - 6) * 8192;
        const unsigned r0 = ((w & 3) * 32 + g) << 8;
        const u32x4 ea = {r0, r0 + 1024, r0 + 2048, r0 + 3072}, eb = ea + 4096;
        const unsigned o0 = (n ^ (w < 4 ? g : ((g & 1) | ((g & 2) << 1)))) << 4;
        const unsigned f1 = w < 4 ? 0 : 128, f2 = w < 4 ? 64 : 0;
        const u32x4 off = {o0, o0 ^ f1, o0 ^ f2, o0 ^ f1 ^ f2};
        dma_group(dstb, srd_w, ea, eb, off, 8);
    };
    auto zero_counters = [&]() {
        if (t < NB + 1) cnt1[t] = 0;
        if (t >= 448 && t < 448 + 32) cnt2[t - 448] = 0;
        WG_BARRIER();
    };

    if (isprod) {
        u32x4 dA, sA, dB, sB;
        unsigned nn[4];
        const unsigned eA = pg * CH + 4 * t, eB = eA + 2048;
        {
            const u32x4 srd_d = make_srd(dst, (unsigned)E * 4u), srd_s = make_srd(src, (unsigned)E * 4u);
            const unsigned oA = eA * 4, oB = (4 * t + 2048 < CH) ? eB * 4 : OOB;
            LD4(dA, srd_d, oA, ""); LD4(sA, srd_s, oA, ""); LD4(dB, srd_d, oB, ""); LD4(sB, srd_s, oB, "");
#pragma unroll
            for (int it = 0; it < 4; ++it) { const unsigned o = WS_NONCE + ((t >> 3) + 64 * it) * 4; LD1(nn[it], srd_ws, o, " sc1"); }
        }
        common_loads();
        zero_counters();
        STAMP(1);
        asm volatile("s_waitcnt vmcnt(17)" : "+v"(dA), "+v"(sA), "+v"(dB), "+v"(sB) :: "memory");
        STAMP(2);
        const bool vA = eA < (unsigned)E, vB = (4 * t + 2048 < CH) && eB < (unsigned)E;
        auto route = [&](const u32x4& d4, const u32x4& s4, bool valid) {
            unsigned bk[4], rk[4];
#pragma unroll
            for (int k = 0; k < 4; ++k) { bk[k] = bucket_of(d4[k]); rk[k] = lds_add(ldsp(valid ? lds0 + L_CNT1 + 4 * bk[k] : dummy)); }
            bool over = false;
#pragma unroll
            for (int k = 0; k < 4; ++k) {
                const unsigned j = d4[k] - bk[k] * NPB;
                const bool fit = valid && rk[k] < CAP1;
                over |= valid && !fit;
                *ldsp(fit ? lds0 + (bk[k] >> 6) * 16384 + (bk[k] & 63) * 128 + 8 + 4 * rk[k] : dummy) = (s4[k] << 8) | ((j & 7) << 4) | (j >> 3);
            }
            if (__builtin_expect(__any(over), 0)) {
#pragma unroll
                for (int k = 0; k < 4; ++k) {
                    const bool ov = valid && rk[k] >= CAP1;
                    const unsigned ps = lds_add(ldsp(ov ? lds0 + L_CNT1 + 4 * NB : dummy));
                    const unsigned o = ov ? WS_OVF + (pg * CH + ps) * 4 : OOB, v = (s4[k] << 16) | d4[k];
                    ST1(v, srd_ws, o, " sc1");
                }
                VMWAIT(0);
            }
        };
        route(dA, sA, vA);
        if (w < 5) route(dB, sB, vB);
        WG_BARRIER();
        STAMP(3);
        const unsigned no = __builtin_amdgcn_readfirstlane(cnt1[NB]);
        if (no) {
            const unsigned o = t == 0 ? WS_NOVF + pg * 4 : OOB;
            ST1(no, srd_ws, o, " sc1");
            VMWAIT(0);
            WG_BARRIER();
        }
        asm volatile("s_waitcnt vmcnt(13)" : "+v"(nn[0]), "+v"(nn[1]), "+v"(nn[2]), "+v"(nn[3]) :: "memory");
        const unsigned i0 = 4 * (t & 7) - 2;
#pragma unroll
        for (int it = 0; it < 4; ++it) {
            const int bb = (t >> 3) + 64 * it;
            const unsigned c = cnt1[bb];
            u32x4 v = *(LAS u32x4*)(smem + it * 16384 + t * 16);
#pragma unroll
            for (int k = 0; k < 4; ++k) v[k] = (i0 + k < c) ? v[k] : 0u;
            const unsigned s = sum8(v.x + v.y + v.z + v.w);
            const unsigned tg = tag_of(nn[it]), m = mask_of(tg);
            if ((t & 7) == 0) v.y = s;
            v ^= m;
            if ((t & 7) == 0) v.x = tg | c;
            const unsigned o = ((int)i0 < (int)c) ? WS_INBOX + (bb * G1 + pg) * 128 + (t & 7) * 16 : OOB;
            ST4(v, srd_ws, o, " sc1");
        }
        STAMP(4);
        asm volatile("s_waitcnt vmcnt(4)" : "+v"(xo[0]), "+v"(xo[1]), "+v"(xo[2]), "+v"(xo[3]), "+v"(mynonce) :: "memory");
    } else {
        common_loads();
        {
            unsigned keep, o[4];
#pragma unroll
            for (int it = 0; it < 4; ++it) { const unsigned line = (it * 16 + (b >> 4)) * 512 + t; o[it] = line < 2 * PF_ROWS ? line * 128 : OOB; }
            asm volatile("s_mov_b32 %0, m0\n\ts_mov_b32 m0, %1\n\ts_nop 0\n\t"
                         "buffer_load_dword %3, %2, 0 offen lds\n\tbuffer_load_dword %4, %2, 0 offen lds\n\t"
                         "buffer_load_dword %5, %2, 0 offen lds\n\tbuffer_load_dword %6, %2, 0 offen lds\n\ts_mov_b32 m0, %0"
                         : "=&s"(keep) : "s"(ring), "s"(srd_x), "v"(o[0]), "v"(o[1]), "v"(o[2]), "v"(o[3]) : "memory");
        }
        zero_counters();
        STAMP(1);
        asm volatile("s_waitcnt vmcnt(4)" : "+v"(xo[0]), "+v"(xo[1]), "+v"(xo[2]), "+v"(xo[3]), "+v"(mynonce) :: "memory");
        STAMP(2); STAMP(3); STAMP(4);
    }
    WG_BARRIER();
    STAMP(5);

    bf16x8 qf[4], wvr[2][8];
    {
        LAS char* aq = smem + 8192 + (8 * g + q) * 256;
        const unsigned cq = (((4 * p) ^ (q | ((g & 1) << 2))) << 4) | ((p & 1) << 3);
        unsigned qp[8][2];
#pragma unroll
        for (int a = 0; a < 8; ++a) {
            f32x4 c = {0.f, 0.f, 0.f, 0.f};
            LAS char* pa = aq + (cq ^ ((a >> 1) << 4) ^ ((a & 1) << 3));
#pragma unroll
            for (int u = 0; u < 4; ++u)
                c = mfma(cat(tr_read(pa + u * 16384), tr_read(pa + u * 16384 + 1024)), __builtin_bit_cast(bf16x8, xo[u]), c);
            const float k = -0.08838834764831845f * 1.4426950408889634f;
            qp[a][0] = pk2(c[0] * k, c[1] * k); qp[a][1] = pk2(c[2] * k, c[3] * k);
        }
        const bool odd = g & 1;
#pragma unroll
        for (int s = 0; s < 4; ++s) {
            u32x4 v;
            v.x = odd ? qp[2 * s + 1][0] : qp[2 * s][0]; v.y = odd ? qp[2 * s + 1][1] : qp[2 * s][1];
            v.z = odd ? qp[2 * s][0] : qp[2 * s + 1][0]; v.w = odd ? qp[2 * s][1] : qp[2 * s + 1][1];
            qf[s] = __builtin_bit_cast(bf16x8, v);
        }
    }
    const int r8 = (4 * g + q) & 7;
    const unsigned cv = ((p ^ ((r8 & 1) | ((r8 & 6) << 1))) << 4) | ((p & 1) << 3);
    {
        LAS char* av = smem + 4 * 16384 + 8192 + (4 * g + q) * 256;
#pragma unroll
        for (int u = 0; u < 2; ++u)
#pragma unroll
            for (int a = 0; a < 8; ++a) {
                LAS char* pa = av + (cv ^ ((a >> 1) << 6) ^ ((a & 1) << 3)) + u * 16384;
                wvr[u][a] = cat(tr_read(pa), tr_read(pa + 4096));
            }
    }
    STAMP(6);

    const unsigned mytag = tag_of(mynonce), mymask = mask_of(mytag);
    {
        const unsigned ib = WS_INBOX + b * (G1 * 128) + t * 16;
        const unsigned i0 = 4 * (t & 7) - 2;
        u32x4 in[2]; unsigned cc[2]; bool ok[2] = {false, false};
        for (unsigned spin = 0;; ++spin) {
            u32x4 nw[2];
            const unsigned o0 = ok[0] ? OOB : ib, o1 = ok[1] ? OOB : ib + 8192;
            asm volatile("buffer_load_dwordx4 %0, %2, %4, 0 offen sc1\n\tbuffer_load_dwordx4 %1, %3, %4, 0 offen sc1\n\ts_waitcnt vmcnt(0)"
                         : "=&v"(nw[0]), "=&v"(nw[1]) : "v"(o0), "v"(o1), "s"(srd_ws) : "memory");
#pragma unroll
            for (int h = 0; h < 2; ++h) {
                const unsigned hd = bcast8(nw[h].x), c = hd & 0xFFF;
                u32x4 v = nw[h] ^ mymask;
                const unsigned cs = bcast8(v.y);
                unsigned s = 0;
#pragma unroll
                for (int k = 0; k < 4; ++k) s += (i0 + k < c) ? v[k] : 0u;
                const bool good = ((hd ^ mytag) < 0x1000u) && cs == sum8(s);
                if (!ok[h] && good) { in[h] = v; cc[h] = c; ok[h] = true; }
            }
            if (__all(ok[0] && ok[1])) break;
            if (spin > (1u << 22)) break;
        }
        STAMP(7);
        if ((t & 7) == 0) { hdr[t >> 3] = cc[0]; hdr[64 + (t >> 3)] = cc[1]; }
        const unsigned c0 = cc[0], c1 = cc[1];
        if (c0 > CAP1 || c1 > CAP1) cnt2[16] = 1;
        unsigned rank[8];
#pragma unroll
        for (int k = 0; k < 8; ++k) {
            const unsigned e = in[k >> 2][k & 3];
            const bool v = i0 + (k & 3) < (k < 4 ? c0 : c1);
            rank[k] = lds_add(ldsp(v ? lds0 + L_CNT + ((e >> 1) & 56) + (e >= (PF_ROWS << 8) ? 4 : 0) : dummy));
        }
#pragma unroll
        for (int k = 0; k < 8; ++k) {
            const unsigned e = in[k >> 2][k & 3];
            const bool v = i0 + (k & 3) < (k < 4 ? c0 : c1) && rank[k] < CAP2;
            *ldsp(v ? lds0 + L_LIST + (((e >> 4) & 7) * CAP2 + (e >= (PF_ROWS << 8) ? CAP2 - 1 - rank[k] : rank[k])) * 4 : dummy) = e;
        }
    }
    WG_BARRIER();
    STAMP(8);

    u32x4 doff;
    { const unsigned o0 = (n ^ (((g & 1) << 3) | (g >> 1))) << 4; doff = u32x4{o0, o0 ^ 32, o0 ^ 64, o0 ^ 96}; }
    unsigned rowx, trc; LAS char* trp;
    { const int phi = ((n & 7) << 1) | (n >> 3); rowx = ring + (4 * (n & 3) + (n >> 2)) * 256 + (((4 * g) ^ phi) << 4); }
    { const int i = 4 * g + q, phi = ((i & 7) << 1) | (i >> 3); trp = smem + w * 16384 + (4 * q + g) * 256 + 8 * (p & 1); trc = ((p >> 1) ^ phi) << 4; }
    const unsigned mykey = (w << 4) | n;
    LAS unsigned* mylist = lists + w * CAP2;

    f32x4 acc[8];
#pragma unroll
    for (int c = 0; c < 8; ++c) acc[c] = f32x4{0.f, 0.f, 0.f, 0.f};

    int gcur, pos = 0;
    bool skip;
    {
        unsigned mx = 0;
#pragma unroll
        for (int k = 0; k < 8; ++k) mx = max(mx, cnt2[2 * k] + cnt2[2 * k + 1]);
        skip = __builtin_amdgcn_readfirstlane(mx) > CAP2;
        gcur = skip ? -1 : (__builtin_amdgcn_readfirstlane(cnt2[16]) ? 0 : G1);
    }
    for (;;) {
        const int clo = skip ? 0 : __builtin_amdgcn_readfirstlane(cnt2[2 * w]);
        const int c = skip ? 0 : clo + __builtin_amdgcn_readfirstlane(cnt2[2 * w + 1]);
        skip = false;
        const int G = (c + 31) >> 5;
        {
            unsigned e[CAP2 / 64];
#pragma unroll
            for (int k = 0; k < CAP2 / 64; ++k) {
                const int ps = l + 64 * k;
                e[k] = mylist[min(ps < clo ? ps : ps + CAP2 - c, CAP2 - 1)];
            }
            const int sg = (l & 32) | ((l & 3) << 3) | ((l >> 2) & 7);
#pragma unroll
            for (int k = 0; k < CAP2 / 64; ++k) mylist[sg + 64 * k] = (l + 64 * k < c) ? e[k] : 0xFFFFFFFFu;
        }
        const int nlast = G > 2 ? (((c - 1) & 31) >> 2) + 1 : 8;
        auto issue = [&](int grp, int slot, int nm) {
            const u32x4 ea = *(LAS u32x4*)(mylist + grp * 32 + 8 * g), eb = *(LAS u32x4*)(mylist + grp * 32 + 8 * g + 4);
            dma_group(ring + slot * 8192, srd_x, ea, eb, doff, nm);
        };
        auto step = [&](int i, int slot) {
            vm_wait_upto8(i + 2 < G ? 8 : (i + 2 == G ? nlast : 0));
            bf16x8 af[2][4], tf[8];
#pragma unroll
            for (int tt = 0; tt < 2; ++tt)
#pragma unroll
                for (int s = 0; s < 4; ++s) af[tt][s] = *(LAS bf16x8*)(size_t)((rowx ^ (s << 4)) + tt * 4096 + slot * 8192);
            const u32x4 ea = *(LAS u32x4*)(mylist + i * 32 + 8 * g), eb = *(LAS u32x4*)(mylist + i * 32 + 8 * g + 4);
#pragma unroll
            for (int cc = 0; cc < 8; ++cc) {
                LAS char* pa = trp + (trc ^ (cc << 5)) + slot * 8192;
                tf[cc] = cat(tr_read(pa), tr_read(pa + 4096));
            }
            if (i + 2 < G) issue(i + 2, slot, i + 3 == G ? nlast : 8);
            f32x4 s0 = {0.f, 0.f, 0.f, 0.f}, s1 = {0.f, 0.f, 0.f, 0.f};
#pragma unroll
            for (int s = 0; s < 4; ++s) { s0 = mfma(af[0][s], qf[s], s0); s1 = mfma(af[1][s], qf[s], s1); }
            float al[8];
#pragma unroll
            for (int r = 0; r < 4; ++r) {
                const float z0 = ((ea[r] & 0xFF) == mykey) ? s0[r] : __builtin_inff();
                const float z1 = ((eb[r] & 0xFF) == mykey) ? s1[r] : __builtin_inff();
                al[r] = __builtin_amdgcn_rcpf(1.f + __builtin_amdgcn_exp2f(z0));
                al[4 + r] = __builtin_amdgcn_rcpf(1.f + __builtin_amdgcn_exp2f(z1));
            }
            const u32x4 pv = {pk2(al[0], al[1]), pk2(al[2], al[3]), pk2(al[4], al[5]), pk2(al[6], al[7])};
            const bf16x8 pf8 = __builtin_bit_cast(bf16x8, pv);
#pragma unroll
            for (int cc = 0; cc < 8; ++cc) acc[cc] = mfma(tf[cc], pf8, acc[cc]);
        };
        if (G > 0) issue(0, 0, 8);
        if (G > 1) issue(1, 1, 8);
        STAMP(9);
#pragma clang loop unroll(disable)
        for (int i = 0; i < G; i += 2) {
            step(i, 0);
            if (i + 1 < G) step(i + 1, 1);
        }
        unsigned lim = G1 * 32;
        while (gcur >= 0 && gcur < G1) {
            if (__builtin_amdgcn_readfirstlane(hdr[gcur]) > CAP1) {
                unsigned v; const unsigned o = WS_NOVF + gcur * 4;
                asm volatile("buffer_load_dword %0, %1, %2, 0 offen sc1\n\ts_waitcnt vmcnt(0)" : "=&v"(v) : "v"(o), "s"(srd_ws) : "memory");
                lim = __builtin_amdgcn_readfirstlane(v);
                if ((unsigned)pos < lim) break;
            }
            ++gcur; pos = 0;
        }
        if (gcur >= G1) break;
        WG_BARRIER();
        if (t < 16) cnt2[t] = 0;
        WG_BARRIER();
        const unsigned i = pos + t;
        unsigned u;
        { const unsigned o = (t < CAP2 && i < lim) ? (gcur < 0 ? WS_INBOX + b * (G1 * 128) : WS_OVF + gcur * CH * 4) + i * 4 : OOB;
          asm volatile("buffer_load_dword %0, %1, %2, 0 offen sc1\n\ts_waitcnt vmcnt(0)" : "=&v"(u) : "v"(o), "s"(srd_ws) : "memory"); }
        if (t < CAP2 && i < lim) {
            unsigned e; bool v;
            if (gcur < 0) { e = u ^ mymask; v = ((i & 31) - 2) < hdr[i >> 5]; }
            else { const unsigned j = (u & 0xFFFF) - b * NPB; e = ((u >> 16) << 8) | ((j & 7) << 4) | (j >> 3); v = j < NPB; }
            if (v) { const unsigned tl = (e >> 4) & 7; lists[tl * CAP2 + lds_add(&cnt2[2 * tl])] = e; }
        }
        pos += CAP2;
        if (gcur < 0 && pos >= G1 * 32) { gcur = 0; pos = 0; }
        WG_BARRIER();
    }
    STAMP(10);

    {
        bf16x8 ag[4];
#pragma unroll
        for (int u = 0; u < 4; ++u) {
            const u32x4 v = {pk2(acc[2 * u][0], acc[2 * u][1]), pk2(acc[2 * u][2], acc[2 * u][3]),
                             pk2(acc[2 * u + 1][0], acc[2 * u + 1][1]), pk2(acc[2 * u + 1][2], acc[2 * u + 1][3])};
            ag[u] = __builtin_bit_cast(bf16x8, v);
        }
        LAS char* av = smem + L_WV + (4 * g + q) * 256;
        unsigned op[8][2];
#pragma unroll
        for (int a = 0; a < 8; ++a) {
            f32x4 c = {0.f, 0.f, 0.f, 0.f};
            c = mfma(wvr[0][a], ag[0], c);
            c = mfma(wvr[1][a], ag[1], c);
            LAS char* pa = av + (cv ^ ((a >> 1) << 6) ^ ((a & 1) << 3));
            c = mfma(cat(tr_read(pa), tr_read(pa + 4096)), ag[2], c);
            c = mfma(cat(tr_read(pa + 8192), tr_read(pa + 8192 + 4096)), ag[3], c);
            op[a][0] = pk2(c[0], c[1]); op[a][1] = pk2(c[2], c[3]);
        }
        const auto rs_o = __builtin_amdgcn_make_buffer_rsrc((void*)out, 0, xbytes, 0x00020000);
        const bool odd = g & 1;
#pragma unroll
        for (int bb = 0; bb < 4; ++bb) {
            u32x4 v;
            v.x = odd ? op[2 * bb + 1][0] : op[2 * bb][0]; v.y = odd ? op[2 * bb + 1][1] : op[2 * bb][1];
            v.z = odd ? op[2 * bb][0] : op[2 * bb + 1][0]; v.w = odd ? op[2 * bb][1] : op[2 * bb + 1][1];
            __builtin_amdgcn_raw_buffer_store_b128(v, rs_o, rowoff + 64 * bb + 16 * g, 0, 16);
        }
    }
    { const unsigned o = t == 0 ? WS_NONCE + b * 4 : OOB; if (w == 0) ST1(mytag, srd_ws, o, " sc1"); }
    STAMP(11);
    asm volatile("s_waitcnt vmcnt(0)" ::: "memory");
    STAMP(12);
    __syncthreads();
    STAMP(13);
    {
        const unsigned long long tgt = st_[13] + (unsigned long long)PROBE_M * (st_[PROBE_B] - st_[PROBE_A]);
        if (threadIdx.x < 64) { int guard = 0; while (wall_clock64() < tgt && ++guard < 200000) __builtin_amdgcn_s_sleep(2); }
    }
}

extern "C" void kernel_launch(void* const* d_in, const int* in_sizes, int n_in,
                              void* d_out, int out_size, void* d_ws, size_t ws_size,
                              hipStream_t stream) {
    const int N = in_sizes[0] / 128, E = in_sizes[1];
    k_graph<<<NB, 512, 0, stream>>>((const unsigned short*)d_in[0], (const int*)d_in[1], (const int*)d_in[2],
                                    (const unsigned short*)d_in[3], (const unsigned short*)d_in[4],
                                    (unsigned*)d_ws, (unsigned short*)d_out, N, E);
}
```

```cpp
#include <hip/hip_runtime.h>
#include <hip/hip_bf16.h>
#include <stdint.h>
#include <type_traits>
#define ORD 1
#define SKIP 0
#define STR2(x) #x
#define STR(x) STR2(x)

typedef __attribute__((ext_vector_type(8))) __bf16 bf16x8;
typedef __attribute__((ext_vector_type(4))) __bf16 bf16x4;
typedef __attribute__((ext_vector_type(2))) __bf16 bf16x2;
typedef __attribute__((ext_vector_type(4))) float f32x4;
typedef __attribute__((ext_vector_type(2))) float f32x2;
typedef __attribute__((ext_vector_type(4))) unsigned u32x4;
typedef __attribute__((ext_vector_type(2))) unsigned u32x2;
typedef __attribute__((ext_vector_type(32))) float f32x32;
typedef __attribute__((ext_vector_type(16))) float f32x16;
typedef __attribute__((ext_vector_type(16))) unsigned u32x16;
typedef __attribute__((ext_vector_type(12))) unsigned u32x12;
typedef unsigned long long u64;
#define LAS __attribute__((address_space(3)))

constexpr int NB = 256;
constexpr int G1 = 128;
constexpr int CH = 3136;
constexpr int CAP1 = 30;
constexpr int CAP2 = 192;
constexpr int SUBB = 784;
#define POLL_AT 3
#define OUT_AUX 16
#define PF_R1 1
#define PF_R2 2
#define PF_D 0
#define NP_SLEEP 0
#define SPLIT 16384
constexpr unsigned KEY_STEP = 0x9E3779B1u;
constexpr unsigned WS_KEY = 0;
constexpr unsigned WS_NOVF = 2048;
constexpr unsigned WS_INBOX = 4096;
constexpr unsigned WS_OVF = WS_INBOX + NB * G1 * 128;
constexpr unsigned WS_BYTES = WS_OVF + G1 * CH * 4;
constexpr unsigned OOB = 0x80000000u;
constexpr unsigned PAD = OOB | 0xFFu;

constexpr int L_CNT = 0;
constexpr int L_HDR = 128;
constexpr int L_LIST = 768;
constexpr int L_RING = L_LIST + 16 * SUBB;
constexpr int L_IMG = L_RING;
constexpr int L_CNT1 = L_IMG + NB * 144;
constexpr int L_WQ = L_RING + 2 * 16384 + 8192;
constexpr int L_WV0 = L_RING + 6 * 16384 + 8192;
constexpr int L_WV1 = L_RING + 131072;
constexpr int L_DUMP = L_WV1 + 16384;
constexpr int L_TOTAL = L_DUMP + 2048;
static_assert(L_RING % 256 == 0 && L_TOTAL <= 163840, "LDS map");

#define STAMP_DECL
#define STAMP(k)
#define STAMP_FLUSH(base, k0, k1)

__device__ __forceinline__ u32x4 make_srd(const void* p, unsigned bytes, unsigned back = 0) {
    const u64 a = (u64)p - back;
    u32x4 s; s.x = (unsigned)a; s.y = (unsigned)(a >> 32) & 0xFFFFu; s.z = bytes + back; s.w = 0x00020000u; return s;
}
#define LD4(dst, srd, off, FL) asm volatile("buffer_load_dwordx4 %0, %1, %2, 0 offen" FL : "=v"(dst) : "v"(off), "s"(srd) : "memory")
#define LD4S(dst, srd, off, so, FL) asm volatile("buffer_load_dwordx4 %0, %1, %2, %3 offen" FL : "=v"(dst) : "v"(off), "s"(srd), "s"(so) : "memory")
#define LD1(dst, srd, off, FL) asm volatile("buffer_load_dword %0, %1, %2, 0 offen" FL : "=v"(dst) : "v"(off), "s"(srd) : "memory")
#define ST1(val, srd, off, FL) asm volatile("buffer_store_dword %0, %1, %2, 0 offen" FL :: "v"(val), "v"(off), "s"(srd) : "memory")
#define PIN(x) asm volatile("" : "+v"(x))
#define VMWAIT(n) asm volatile("s_waitcnt vmcnt(" #n ")" ::: "memory")
#define FENCE() __builtin_amdgcn_sched_barrier(0)
#define WG_BARRIER() do { asm volatile("s_waitcnt lgkmcnt(0)" ::: "memory"); __builtin_amdgcn_s_barrier(); asm volatile("" ::: "memory"); } while (0)

struct Srd4 { u32x4 s0, s1, s2, s3; };
__device__ __forceinline__ Srd4 make_srd4(const void* p, unsigned bytes) { return Srd4{make_srd(p, bytes), make_srd(p, bytes, 1024), make_srd(p, bytes, 2048), make_srd(p, bytes, 3072)}; }

__device__ __forceinline__ void set_m0(unsigned v) { asm volatile("s_mov_b32 m0, %0" :: "s"(v) : "memory"); }
template <int R> __device__ __forceinline__ void dma_rows(const Srd4& d, unsigned e, unsigned off) {
    unsigned tmp, mask = 0xFFFFFF00u;
    asm volatile("v_and_or_b32 %0, %1, %2, %3\n\tbuffer_load_dwordx4 %0, %4, 0 offen offset:%c5 lds"
                 : "=&v"(tmp) : "v"(e), "s"(mask), "v"(off), "s"(R == 0 ? d.s0 : R == 1 ? d.s1 : R == 2 ? d.s2 : d.s3), "i"(R * 1024) : "memory");
}
__device__ __forceinline__ void dma_half(unsigned ldsbase, const Srd4& d, const unsigned* e, const u32x4& off) {
    set_m0(ldsbase);
    dma_rows<0>(d, e[0], off.x); dma_rows<1>(d, e[1], off.y); dma_rows<2>(d, e[2], off.z); dma_rows<3>(d, e[3], off.w);
}
__device__ __forceinline__ void dma_group(unsigned ldsbase, const Srd4& d, const unsigned (&e)[8], const u32x4& off) {
    asm volatile("s_waitcnt lgkmcnt(0)" ::: "memory");
    dma_half(ldsbase, d, e, off); dma_half(ldsbase + 4096, d, e + 4, off);
}

__device__ __forceinline__ void gather_loop(f32x32& acc, const u32x16& qf, const u32x16& addr, u32x4 misc, f32x16 cc,
                                            const Srd4& d, unsigned ring, int G, int nlast) {
    f32x32 t0, t1; u32x12 t2; int tmp; unsigned mask = 0xFFFFFF00u;
    asm volatile(
        ".macro GWAIT k\n\ts_cmp_gt_i32 %[t], \\k\n\ts_cbranch_scc1 1f\n\ts_waitcnt vmcnt(\\k)\n\t.endm\n\t"
        ".macro GTR c, so\n\t"
        "ds_read_b64_tr_b16 v[96+4*\\c:97+4*\\c], v[180+\\c] offset:\\so\n\t"
        "ds_read_b64_tr_b16 v[98+4*\\c:99+4*\\c], v[180+\\c] offset:\\so+4096\n\t.endm\n\t"
        ".macro GS s, c\n\t"
        "v_mfma_f32_16x16x32_bf16 v[\\c:\\c+3], v[64+8*\\s:67+8*\\s], v[160+4*\\s:163+4*\\s], v[\\c:\\c+3]\n\t"
        "v_mfma_f32_16x16x32_bf16 v[\\c+4:\\c+7], v[68+8*\\s:71+8*\\s], v[160+4*\\s:163+4*\\s], v[\\c+4:\\c+7]\n\t.endm\n\t"
        ".macro GLOAD m, r, srd\n\t"
        "v_and_or_b32 v195, v[212+\\m], %[mask], v[188+\\r]\n\t"
        "buffer_load_dwordx4 v195, \\srd, 0 offen offset:1024*\\r lds\n\t.endm\n\t"
        ".macro GCMP m\n\tv_cmp_ne_u32_sdwa vcc, v[212+\\m], v193 src0_sel:BYTE_0 src1_sel:DWORD\n\t"
        ".if (" STR(SKIP) " & 4) == 0\n\tv_mfma_f32_16x16x32_bf16 v[128+4*\\m:131+4*\\m], v[96+4*\\m:99+4*\\m], v[220:223], v[128+4*\\m:131+4*\\m]\n\t.else\n\ts_nop 0\n\t.endif\n\t.endm\n\t"
        ".macro GEXP c\n\t.if (" STR(SKIP) " & 2) == 0\n\t.irp k, 0, 1, 2, 3, 4, 5, 6, 7\n\tv_exp_f32_e32 v[\\c+\\k], v[\\c+\\k]\n\t.endr\n\t.endif\n\t.endm\n\t"
        ".macro GLAST m, c\n\tGCMP \\m\n\ts_nop 0\n\tv_cndmask_b32_e32 v[\\c+\\m], 0, v194, vcc\n\t.endm\n\t"
        ".macro GPART m, r, srd\n\ts_cmp_le_i32 %[nl], \\m\n\ts_cbranch_scc1 4f\n\tGLOAD \\m, \\r, \\srd\n\t.endm\n\t"
        ".macro GSTEP so, c, eo\n\t"
        "s_waitcnt vmcnt(8)\n\t"
        "s_cmp_gt_i32 %[rem], 2\n\ts_cbranch_scc1 1f\n\t"
        "s_cmp_eq_u32 %[rem], 2\n\ts_cselect_b32 %[t], %[nl], 0\n\t"
        "GWAIT 7\n\tGWAIT 6\n\tGWAIT 5\n\tGWAIT 4\n\tGWAIT 3\n\tGWAIT 2\n\tGWAIT 1\n\tGWAIT 0\n\t"
        "1:\n\t"
        ".if (" STR(SKIP) " & 1) == 0\n\t"
        ".irp s, 0, 1, 2, 3\n\t"
        "ds_read_b128 v[64+8*\\s:67+8*\\s], v[176+\\s] offset:\\so\n\t"
        "ds_read_b128 v[68+8*\\s:71+8*\\s], v[176+\\s] offset:\\so+4096\n\t.endr\n\t"
        "GTR 0, \\so\n\tGTR 1, \\so\n\tGTR 2, \\so\n\t"
        "s_waitcnt lgkmcnt(12)\n\tGS 0, \\c\n\t"
        "GTR 3, \\so\n\t"
        "s_waitcnt lgkmcnt(12)\n\tGS 1, \\c\n\t"
        "GTR 4, \\so\n\tGTR 5, \\so\n\t"
        "s_waitcnt lgkmcnt(14)\n\tGS 2, \\c\n\t"
        "GTR 6, \\so\n\t"
        "s_waitcnt lgkmcnt(14)\n\tGS 3, \\c\n\t"
        "GTR 7, \\so\n\t"
        ".else\n\tGS 0, \\c\n\tGS 1, \\c\n\tGS 2, \\c\n\tGS 3, \\c\n\t.endif\n\t"
        "ds_read2_b32 v[212:213], v192 offset0:\\eo offset1:\\eo+4\n\t"
        "ds_read2_b32 v[214:215], v192 offset0:\\eo+8 offset1:\\eo+12\n\t"
        "ds_read2_b32 v[216:217], v192 offset0:\\eo+16 offset1:\\eo+20\n\t"
        "ds_read2_b32 v[218:219], v192 offset0:\\eo+24 offset1:\\eo+28\n\t"
        "s_add_u32 m0, %[ring], \\so\n\t"
        ".if " STR(ORD) " >= 2\n\ts_nop 0\n\tGEXP \\c\n\t.endif\n\t"
        "s_cmp_gt_i32 %[rem], 3\n\t"
        "s_waitcnt lgkmcnt(0)\n\t"
        "s_cbranch_scc0 2f\n\t"
        "GLOAD 0, 0, %[s0]\n\tGLOAD 1, 1, %[s1]\n\tGLOAD 2, 2, %[s2]\n\tGLOAD 3, 3, %[s3]\n\t"
        "s_add_u32 m0, m0, 0x1000\n\t"
        "GLOAD 4, 0, %[s0]\n\tGLOAD 5, 1, %[s1]\n\tGLOAD 6, 2, %[s2]\n\tGLOAD 7, 3, %[s3]\n\t"
        "s_branch 4f\n\t"
        "2:\n\t"
        "s_cmp_eq_u32 %[rem], 3\n\ts_cbranch_scc0 4f\n\t"
        "GLOAD 0, 0, %[s0]\n\tGPART 1, 1, %[s1]\n\tGPART 2, 2, %[s2]\n\tGPART 3, 3, %[s3]\n\t"
        "s_add_u32 m0, m0, 0x1000\n\t"
        "GPART 4, 0, %[s0]\n\tGPART 5, 1, %[s1]\n\tGPART 6, 2, %[s2]\n\tGPART 7, 3, %[s3]\n\t"
        "4:\n\t"
        ".if " STR(ORD) " < 2\n\tGEXP \\c\n\t.endif\n\t"
        ".if (" STR(SKIP) " & 2) == 0\n\t"
        ".irp k, 0, 1, 2, 3, 4, 5, 6, 7\n\tv_add_f32_e32 v[\\c+\\k], 1.0, v[\\c+\\k]\n\t.endr\n\t"
        ".irp k, 0, 1, 2, 3, 4, 5, 6, 7\n\tv_rcp_f32_e32 v[\\c+\\k], v[\\c+\\k]\n\t.endr\n\t.endif\n\t"
        ".irp k, 0, 1, 2, 3\n\tv_cvt_pk_bf16_f32 v[220+\\k], v[\\c+2*\\k], v[\\c+2*\\k+1]\n\t.endr\n\t"
        "s_add_i32 %[rem], %[rem], -1\n\t"
        ".irp m, 0, 1, 2, 3, 4, 5, 6, 7\n\tGLAST \\m, \\c\n\t.endr\n\t"
        "s_cmp_gt_i32 %[rem], 0\n\t"
        ".endm\n\t"
        "0:\n\t"
        "GSTEP 0, 196, 64\n\t"
        "s_cbranch_scc0 9f\n\t"
        "GSTEP 8192, 204, 96\n\t"
        "v_add_u32_e32 v192, 0x100, v192\n\t"
        "s_cbranch_scc1 0b\n\t"
        "9:\n\t"
        "s_nop 7\n\t"
        ".purgem GWAIT\n\t.purgem GTR\n\t.purgem GS\n\t.purgem GLOAD\n\t.purgem GCMP\n\t.purgem GEXP\n\t.purgem GLAST\n\t.purgem GPART\n\t.purgem GSTEP"
        : "+{v[128:159]}"(acc), "+{v[192:195]}"(misc), "+{v[196:211]}"(cc), "=&{v[64:95]}"(t0), "=&{v[96:127]}"(t1), "=&{v[212:223]}"(t2),
          [rem] "+s"(G), [t] "=&s"(tmp)
        : "{v[160:175]}"(qf), "{v[176:191]}"(addr), [s0] "s"(d.s0), [s1] "s"(d.s1), [s2] "s"(d.s2), [s3] "s"(d.s3),
          [mask] "s"(mask), [ring] "s"(ring), [nl] "s"(nlast)
        : "vcc", "scc", "memory");
}

template <int CTRL, int BANK> __device__ __forceinline__ unsigned dpp(unsigned old, unsigned v) { return __builtin_amdgcn_update_dpp(old, v, CTRL, 0xF, BANK, true); }
__device__ __forceinline__ unsigned bcast8(unsigned v) { v = dpp<0x00, 0xF>(0u, v); return __builtin_amdgcn_update_dpp(v, v, 0x114, 0xF, 0xA, false); }
__device__ __forceinline__ unsigned sum8(unsigned v) { v += dpp<0xB1, 0xF>(0u, v); v += dpp<0x4E, 0xF>(0u, v); return v + dpp<0x141, 0xF>(0u, v); }
__device__ __forceinline__ bf16x4 tr_read(LAS char* p) { return __builtin_amdgcn_ds_read_tr16_b64_v4bf16((LAS bf16x4*)p); }
__device__ __forceinline__ bf16x8 cat(bf16x4 a, bf16x4 b) { return __builtin_shufflevector(a, b, 0, 1, 2, 3, 4, 5, 6, 7); }
__device__ __forceinline__ unsigned pk2(float a, float b) { f32x2 v = {a, b}; bf16x2 r = __builtin_convertvector(v, bf16x2); return __builtin_bit_cast(unsigned, r); }
__device__ __forceinline__ f32x4 mfma(bf16x8 a, bf16x8 b, f32x4 c) { return __builtin_amdgcn_mfma_f32_16x16x32_bf16(a, b, c, 0, 0, 0); }
__device__ __forceinline__ unsigned lds_add(LAS char* p, unsigned v) { return __hip_atomic_fetch_add((LAS unsigned*)p, v, __ATOMIC_RELAXED, __HIP_MEMORY_SCOPE_WORKGROUP); }
__device__ __forceinline__ LAS unsigned* ldsp(unsigned a) { return (LAS unsigned*)(size_t)a; }
__device__ __forceinline__ unsigned byte0_x4(unsigned v) { unsigned r; asm("v_lshlrev_b32_sdwa %0, 2, %1 dst_sel:DWORD dst_unused:UNUSED_PAD src0_sel:DWORD src1_sel:BYTE_0" : "=v"(r) : "v"(v)); return r; }
__device__ __forceinline__ unsigned mad24(unsigned a, unsigned b, unsigned c) { unsigned r; asm("v_mad_u32_u24 %0, %1, %2, %3" : "=v"(r) : "v"(a), "s"(b), "v"(c)); return r; }
__device__ __forceinline__ unsigned rfl(unsigned v) { return __builtin_amdgcn_readfirstlane(v); }

__global__ __launch_bounds__(512, 2) void k_graph(const unsigned short* __restrict__ x, const int* __restrict__ src, const int* __restrict__ dst,
                                                  const unsigned short* __restrict__ wq, const unsigned short* __restrict__ wv,
                                                  unsigned* __restrict__ ws, unsigned short* __restrict__ out, int N, int E) {
    __shared__ __attribute__((aligned(16))) char smem_[L_TOTAL];
    LAS char* smem = (LAS char*)smem_;
    const int t = threadIdx.x, l = t & 63, b = blockIdx.x;
    const int w = rfl(t >> 6);
    const int g = l >> 4, n = l & 15, q = (l >> 2) & 3, p = l & 3, pc = t & 7;
    STAMP_DECL;
    STAMP(0);
    asm volatile("" :: "s"(src), "s"(dst), "s"(out), "s"(wq), "s"(wv), "s"(x), "s"(ws), "s"(N), "s"(E));
    const unsigned lds0 = (unsigned)(size_t)smem;
    const unsigned xbytes = (unsigned)N * 256u;
    const u32x4 srd_ws = make_srd(ws, WS_BYTES);
    const unsigned ring = lds0 + L_RING + w * 16384;
    const bool isprod = ((b >> 3) & 1) == 0;
    const int pg = ((b >> 4) << 3) | (b & 7);
    const int jn = 8 * n + w;
    const Srd4 srd_x = make_srd4(x, xbytes);

    unsigned mykey = 0;
    auto node_row = [&](int tile, int m) { const int nd = b + 256 * (8 * (4 * m + g) + tile); return nd < N ? (unsigned)nd << 8 : OOB; };
    auto zero_staging = [&]() {
        const u32x4 z = {0u, 0u, 0u, 0u};
#pragma unroll
        for (int k = 0; k < 5; ++k) *(LAS u32x4*)(smem + L_IMG + k * 8192 + t * 16) = z;
    };

    if (w < 4) {
        if (isprod) {
            u32x4 d4[3], s4[3];
            unsigned d1, s1, kb;
            const unsigned e0 = pg * CH + 4 * t, e1 = pg * CH + 3072 + t;
            {
                const u32x4 srd_d = make_srd(dst, (unsigned)E * 4u), srd_s = make_srd(src, (unsigned)E * 4u);
                const unsigned o = e0 * 4, o2 = o + 8192, o1 = w == 0 ? e1 * 4 : OOB;
                const unsigned k4 = 4096;
                LD4(d4[0], srd_d, o, ""); LD4S(d4[1], srd_d, o, k4, ""); LD4(d4[2], srd_d, o2, ""); LD1(d1, srd_d, o1, "");
                LD4(s4[0], srd_s, o, ""); LD4S(s4[1], srd_s, o, k4, ""); LD4(s4[2], srd_s, o2, ""); LD1(s1, srd_s, o1, "");
                const unsigned ok = WS_KEY + t * 4, om = WS_KEY + b * 4;
                LD1(kb, srd_ws, ok, " sc1"); LD1(mykey, srd_ws, om, " sc1");
            }
            zero_staging();
            if (t < 8) *(LAS u32x4*)(smem + L_CNT + t * 16) = u32x4{0u, 0u, 0u, 0u};
            unsigned ob = WS_INBOX + ((t >> 3) * G1 + pg) * 128 + pc * 16, ima = lds0 + L_IMG + (t >> 3) * 144 + pc * 16;
            PIN(ob); PIN(ima);
            WG_BARRIER();
            STAMP(1);
            unsigned b4[13], rk[13];
            const bool v1 = w == 0 && e1 < (unsigned)E;
            asm volatile("s_waitcnt vmcnt(6)" : "+v"(d4[0]), "+v"(d4[1]), "+v"(d4[2]), "+v"(d1) :: "memory");
            STAMP(2);
#pragma unroll
            for (int j = 0; j < 3; ++j)
                if (e0 + 1024 * j < (unsigned)E) {
#pragma unroll
                    for (int k = 0; k < 4; ++k) { b4[4 * j + k] = byte0_x4(d4[j][k]); rk[4 * j + k] = lds_add(smem + L_CNT1 + b4[4 * j + k], 4); }
                }
            if (v1) { b4[12] = byte0_x4(d1); rk[12] = lds_add(smem + L_CNT1 + b4[12], 4); }
            asm volatile("s_waitcnt vmcnt(1)" : "+v"(s4[0]), "+v"(s4[1]), "+v"(s4[2]), "+v"(s1), "+v"(kb) :: "memory");
            auto put = [&](unsigned d, unsigned sv, unsigned bb, unsigned r) {
                *(LAS unsigned*)(smem + L_IMG + 8 + mad24(bb, 36, min(r, 4u * CAP1))) = __builtin_amdgcn_perm(sv, d, 0x06050401u);
            };
            auto spill = [&](unsigned d, unsigned sv, unsigned r) {
                if (r >= 4u * CAP1) {
                    const unsigned o = WS_OVF + (pg * CH + lds_add(smem + L_CNT1 + 2048, 1)) * 4, v = (sv << 16) | d;
                    ST1(v, srd_ws, o, " sc1");
                }
            };
#pragma unroll
            for (int j = 0; j < 3; ++j)
                if (e0 + 1024 * j < (unsigned)E) {
#pragma unroll
                    for (int k = 0; k < 4; ++k) put(d4[j][k], s4[j][k], b4[4 * j + k], rk[4 * j + k]);
                    if (__builtin_expect(max(max(rk[4 * j], rk[4 * j + 1]), max(rk[4 * j + 2], rk[4 * j + 3])) >= 4u * CAP1, 0)) {
#pragma unroll
                        for (int k = 0; k < 4; ++k) spill(d4[j][k], s4[j][k], rk[4 * j + k]);
                    }
                }
            if (v1) { put(d1, s1, b4[12], rk[12]); spill(d1, s1, rk[12]); }
            *(LAS unsigned*)(smem + L_CNT1 + 1024 + t * 4) = kb + KEY_STEP;
            WG_BARRIER();
            STAMP(3);
            unsigned c4[8], kk[8], cs[8]; u32x4 v[8];
            const unsigned nov = *(LAS unsigned*)(smem + L_CNT1 + 2048);
#pragma unroll
            for (int it = 0; it < 8; ++it) {
                c4[it] = *(LAS unsigned*)(smem + L_CNT1 + (t >> 3) * 4 + it * 128);
                kk[it] = *(LAS unsigned*)(smem + L_CNT1 + 1024 + (t >> 3) * 4 + it * 128);
                v[it] = *(LAS u32x4*)(size_t)(ima + it * (32 * 144));
            }
#pragma unroll
            for (int it = 0; it < 8; ++it) cs[it] = sum8(v[it].x + v[it].y + v[it].z + v[it].w);
            const unsigned no = rfl(nov);
            if (__builtin_expect(no != 0, 0)) {
                const unsigned o = t == 0 ? WS_NOVF + pg * 4 : OOB;
                ST1(no, srd_ws, o, " sc1");
                VMWAIT(0);
                WG_BARRIER();
            }
#pragma unroll
            for (int it = 0; it < 8; ++it) {
                v[it].x = pc == 0 ? c4[it] : v[it].x; v[it].y = pc == 0 ? cs[it] : v[it].y;
                v[it] ^= kk[it];
                const unsigned o = (int)c4[it] > 16 * pc - 8 ? ob : OOB, so = it * 32 * G1 * 128;
                asm volatile("buffer_store_dwordx4 %0, %1, %2, %3 offen sc1\n\ts_nop 1" :: "v"(v[it]), "v"(o), "s"(srd_ws), "s"(so) : "memory");
            }
            asm volatile("s_waitcnt vmcnt(8)" : "+v"(mykey) :: "memory");
            STAMP(4);
            WG_BARRIER();
        } else {
            { const unsigned om = WS_KEY + b * 4; LD1(mykey, srd_ws, om, " sc1"); }
            if (t < 8) *(LAS u32x4*)(smem + L_CNT + t * 16) = u32x4{0u, 0u, 0u, 0u};
            asm volatile("s_waitcnt vmcnt(0)" : "+v"(mykey) :: "memory");
            if (NP_SLEEP) __builtin_amdgcn_s_sleep(NP_SLEEP);
            const unsigned o = ((b >> 4) * 256 + t) * 128;
            set_m0(lds0 + L_DUMP + w * 256);
            auto rounds = [&](int r0, int r1) {
                for (int r = r0; r < r1; ++r) {
                    const unsigned so = r << 19;
                    asm volatile("s_waitcnt vmcnt(%c3)\n\tbuffer_load_dword %1, %0, %2 offen lds" :: "s"(srd_x.s0), "v"(o), "s"(so), "i"(PF_D) : "memory");
                }
            };
            STAMP(1);
            rounds(0, PF_R1);
            STAMP(2);
            WG_BARRIER();
            STAMP(3);
            rounds(PF_R1, PF_R1 + PF_R2);
            STAMP(4);
        }
        STAMP(5);
        mykey += KEY_STEP;
        const unsigned ib = WS_INBOX + b * (G1 * 128) + t * 16, ib2 = ib + 8192, k4 = 4096;
        const int i4 = 16 * pc - 8;
        u32x4 raw[4], v[4]; unsigned h[4];
        LD4(raw[0], srd_ws, ib, " sc1"); LD4S(raw[1], srd_ws, ib, k4, " sc1"); LD4(raw[2], srd_ws, ib2, " sc1"); LD4S(raw[3], srd_ws, ib2, k4, " sc1");
        for (unsigned spin = 0;; ++spin) {
            asm volatile("s_waitcnt vmcnt(0)" : "+v"(raw[0]), "+v"(raw[1]), "+v"(raw[2]), "+v"(raw[3]) :: "memory");
            u64 pend[4];
#pragma unroll
            for (int j = 0; j < 4; ++j) {
                v[j] = raw[j] ^ mykey;
                h[j] = bcast8(v[j].x);
                const unsigned sm = (pc == 0 ? 0u - v[j].y : v[j].x + v[j].y) + v[j].z + v[j].w;
                pend[j] = __builtin_amdgcn_ballot_w64(sum8((int)h[j] > i4 ? sm : 0u) != 0 || h[j] >= 0x4000u);
            }
            if ((pend[0] | pend[1] | pend[2] | pend[3]) == 0 || spin > (1u << 22)) break;
            asm volatile("s_mov_b64 exec, %4\n\tbuffer_load_dwordx4 %0, %8, %10, 0 offen sc1\n\t"
                         "s_mov_b64 exec, %5\n\tbuffer_load_dwordx4 %1, %8, %10, %11 offen sc1\n\t"
                         "s_mov_b64 exec, %6\n\tbuffer_load_dwordx4 %2, %9, %10, 0 offen sc1\n\t"
                         "s_mov_b64 exec, %7\n\tbuffer_load_dwordx4 %3, %9, %10, %11 offen sc1\n\ts_mov_b64 exec, -1"
                         : "+v"(raw[0]), "+v"(raw[1]), "+v"(raw[2]), "+v"(raw[3])
                         : "s"(pend[0]), "s"(pend[1]), "s"(pend[2]), "s"(pend[3]), "v"(ib), "v"(ib2), "s"(srd_ws), "s"(k4) : "memory");
        }
        STAMP(7);
        unsigned key[16], rk[16]; u64 mk[16];
        const unsigned four = 4;
#pragma unroll
        for (int hf = 0; hf < 2; ++hf) {
            unsigned* ky = key + 8 * hf; unsigned* r = rk + 8 * hf; u64* m = mk + 8 * hf;
#pragma unroll
            for (int k = 0; k < 8; ++k) {
                const unsigned e = v[2 * hf + (k >> 2)][k & 3];
                m[k] = __builtin_amdgcn_ballot_w64((unsigned)(i4 + 4 * (k & 3)) < h[2 * hf + (k >> 2)]);
                ky[k] = ((e & 7) << 3) | (((e + ((16384 - SPLIT) << 8)) >> 20) & 4);
            }
#define A1(K, R, M) "s_mov_b64 exec, " M "\n\tds_add_rtn_u32 " R ", " K ", %24 offset:%c25\n\t"
            asm volatile(A1("%8", "%0", "%16") A1("%9", "%1", "%17") A1("%10", "%2", "%18") A1("%11", "%3", "%19")
                         A1("%12", "%4", "%20") A1("%13", "%5", "%21") A1("%14", "%6", "%22") A1("%15", "%7", "%23")
                         "s_mov_b64 exec, -1"
                         : "=&v"(r[0]), "=&v"(r[1]), "=&v"(r[2]), "=&v"(r[3]), "=&v"(r[4]), "=&v"(r[5]), "=&v"(r[6]), "=&v"(r[7])
                         : "v"(ky[0]), "v"(ky[1]), "v"(ky[2]), "v"(ky[3]), "v"(ky[4]), "v"(ky[5]), "v"(ky[6]), "v"(ky[7]),
                           "s"(m[0]), "s"(m[1]), "s"(m[2]), "s"(m[3]), "s"(m[4]), "s"(m[5]), "s"(m[6]), "s"(m[7]),
                           "v"(four), "i"(L_CNT) : "memory");
#undef A1
        }
#pragma unroll
        for (int hf = 0; hf < 2; ++hf) {
            unsigned* ky = key + 8 * hf; unsigned* r = rk + 8 * hf; u64* m = mk + 8 * hf;
            if (hf == 0) asm volatile("s_waitcnt lgkmcnt(8)" : "+v"(r[0]), "+v"(r[1]), "+v"(r[2]), "+v"(r[3]), "+v"(r[4]), "+v"(r[5]), "+v"(r[6]), "+v"(r[7]) :: "memory");
            else asm volatile("s_waitcnt lgkmcnt(8)" : "+v"(r[0]), "+v"(r[1]), "+v"(r[2]), "+v"(r[3]), "+v"(r[4]), "+v"(r[5]), "+v"(r[6]), "+v"(r[7]) :: "memory");
#pragma unroll
            for (int k = 0; k < 8; ++k) r[k] = mad24(ky[k], SUBB / 4, min(r[k], SUBB - 4u));
            const u32x4 va = v[2 * hf], vb = v[2 * hf + 1];
#define W1(A, V, M) "s_mov_b64 exec, " M "\n\tds_write_b32 " A ", " V " offset:%c24\n\t"
            asm volatile(W1("%0", "%8", "%16") W1("%1", "%9", "%17") W1("%2", "%10", "%18") W1("%3", "%11", "%19")
                         W1("%4", "%12", "%20") W1("%5", "%13", "%21") W1("%6", "%14", "%22") W1("%7", "%15", "%23")
                         "s_mov_b64 exec, -1"
                         :: "v"(r[0]), "v"(r[1]), "v"(r[2]), "v"(r[3]), "v"(r[4]), "v"(r[5]), "v"(r[6]), "v"(r[7]),
                            "v"(va.x), "v"(va.y), "v"(va.z), "v"(va.w), "v"(vb.x), "v"(vb.y), "v"(vb.z), "v"(vb.w),
                            "s"(m[0]), "s"(m[1]), "s"(m[2]), "s"(m[3]), "s"(m[4]), "s"(m[5]), "s"(m[6]), "s"(m[7]), "i"(L_LIST) : "memory");
#undef W1
        }
        if (pc == 0) {
#pragma unroll
            for (int j = 0; j < 4; ++j) *(LAS unsigned*)(smem + L_HDR + (t >> 3) * 4 + j * 128) = h[j];
        }
        if (max(max(h[0], h[1]), max(h[2], h[3])) > 4u * CAP1) *(LAS unsigned*)(smem + L_CNT + 64) = 1;
    } else {
        const int j = w - 4;
        if (isprod) { zero_staging(); WG_BARRIER(); }
        STAMP(1);
        u32x4 wr[8], xr[8];
        {
            const u32x4 srd_q = make_srd(wq, 32768);
            const unsigned o = j * 8192 + l * 16, k4 = 4096;
            LD4(wr[0], srd_q, o, ""); LD4(wr[1], srd_q, o, " offset:1024"); LD4(wr[2], srd_q, o, " offset:2048"); LD4(wr[3], srd_q, o, " offset:3072");
            LD4S(wr[4], srd_q, o, k4, ""); LD4S(wr[5], srd_q, o, k4, " offset:1024"); LD4S(wr[6], srd_q, o, k4, " offset:2048"); LD4S(wr[7], srd_q, o, k4, " offset:3072");
#pragma unroll
            for (int m = 0; m < 8; ++m) { const unsigned ox = node_row(m < 4 ? j : w, m & 3) + n * 16; LD4(xr[m], srd_x.s0, ox, ""); }
        }
        unsigned aqa[8], xfa[4];
        unsigned xa = ring + g * 256 + ((n ^ g) << 4), wa = lds0 + L_WQ + j * 16384 + g * 256 + ((n ^ g) << 4) + ((n & 4) << 1);
        {
            const unsigned aq = lds0 + L_WQ + (8 * g + q) * 256 + ((((4 * p) ^ (q | ((g & 1) << 2))) << 4) | ((p & 1) << 3));
#pragma unroll
            for (int s = 0; s < 4; ++s) { xfa[s] = ring + n * 256 + (((4 * s + g) ^ n) << 4); PIN(xfa[s]); }
#pragma unroll
            for (int c = 0; c < 8; ++c) { aqa[c] = aq ^ ((c >> 1) << 4) ^ ((c & 1) << 3); PIN(aqa[c]); }
            PIN(xa); PIN(wa);
        }
        STAMP(2);
        if (isprod) {
            WG_BARRIER();
            if (__builtin_expect(rfl(*(LAS unsigned*)(smem + L_CNT1 + 2048)) != 0, 0)) WG_BARRIER();
        }
        STAMP(3);
        asm volatile("s_waitcnt vmcnt(8)" : "+v"(wr[0]), "+v"(wr[1]), "+v"(wr[2]), "+v"(wr[3]), "+v"(wr[4]), "+v"(wr[5]), "+v"(wr[6]), "+v"(wr[7]) :: "memory");
#pragma unroll
        for (int m = 0; m < 8; ++m) {
            const unsigned a = (wa ^ ((m & 2) << 5)) + m * 1024;
            *(LAS u32x2*)(size_t)a = u32x2{wr[m].x, wr[m].y};
            *(LAS u32x2*)(size_t)(a ^ 8) = u32x2{wr[m].z, wr[m].w};
        }
        asm volatile("s_waitcnt vmcnt(0)" : "+v"(xr[0]), "+v"(xr[1]), "+v"(xr[2]), "+v"(xr[3]), "+v"(xr[4]), "+v"(xr[5]), "+v"(xr[6]), "+v"(xr[7]) :: "memory");
#pragma unroll
        for (int m = 0; m < 8; ++m) *(LAS u32x4*)(size_t)((xa ^ ((m & 3) << 6)) + m * 1024) = xr[m];
        STAMP(4);
        WG_BARRIER();
        STAMP(5);
        bf16x8 xo[2][4], fr[2][8];
        f32x4 qc[2][8];
        auto wq_frags = [&](int u, bf16x8 (&f)[8]) {
#pragma unroll
            for (int a = 0; a < 8; ++a) {
                LAS char* pa = (LAS char*)(size_t)aqa[a] + u * 16384;
                f[a] = cat(tr_read(pa), tr_read(pa + 1024));
            }
        };
#pragma unroll
        for (int u = 0; u < 4; ++u) { xo[0][u] = *(LAS bf16x8*)(size_t)xfa[u]; xo[1][u] = *(LAS bf16x8*)(size_t)(xfa[u] + 4096); }
        wq_frags(0, fr[0]);
        {
            const Srd4 srd_v = make_srd4(wv, 32768);
            const unsigned r0 = (j * 32 + g) << 8, o0 = (n ^ ((g & 1) | ((g & 2) << 1))) << 4;
            const unsigned e[8] = {r0, r0 + 1024, r0 + 2048, r0 + 3072, r0 + 4096, r0 + 5120, r0 + 6144, r0 + 7168};
            const unsigned base = lds0 + (j < 2 ? L_WV0 + j * 16384 : L_WV1 + (j - 2) * 8192);
            const u32x4 off = {o0, o0 ^ 128, o0, o0 ^ 128};
            dma_half(base, srd_v, e, off); dma_half(base + 4096, srd_v, e + 4, off);
        }
#pragma unroll
        for (int u = 0; u < 4; ++u) {
            if (u < 3) wq_frags(u + 1, fr[(u + 1) & 1]);
            FENCE();
#pragma unroll
            for (int a = 0; a < 8; ++a)
#pragma unroll
                for (int tt = 0; tt < 2; ++tt) qc[tt][a] = mfma(fr[u & 1][a], xo[tt][u], u ? qc[tt][a] : f32x4{0.f, 0.f, 0.f, 0.f});
            FENCE();
        }
        STAMP(6);
#pragma unroll
        for (int tt = 0; tt < 2; ++tt)
#pragma unroll
            for (int s = 0; s < 4; ++s) {
                const float k = -0.08838834764831845f * 1.4426950408889634f;
                const f32x4 c0 = qc[tt][2 * s] * k, c1 = qc[tt][2 * s + 1] * k;
                *(LAS u32x4*)(size_t)(ring - (tt ? 0 : 65536) + s * 1024 + l * 16) = u32x4{pk2(c0[0], c0[1]), pk2(c0[2], c0[3]), pk2(c1[0], c1[1]), pk2(c1[2], c1[3])};
            }
        VMWAIT(0);
        STAMP(7);
    }
    WG_BARRIER();
    STAMP(8);

    u32x16 addr, qf;
    bf16x8 wvr[2][8];
    f32x32 acc;
    unsigned cv;
    u32x4 doff;
    { const unsigned o0 = (n ^ (((g & 1) << 3) | (g >> 1))) << 4; doff = u32x4{o0, o0 ^ 32, o0 ^ 64, o0 ^ 96}; }
    LAS char* mylist = smem + L_LIST + w * (2 * SUBB);
    int gcur = G1, pos = 0;
    auto batch = [&](auto first_) {
        constexpr bool first = decltype(first_)::value;
        const unsigned cw = *(LAS unsigned*)(smem + L_CNT + (l & 31) * 4);
        int lo = __builtin_amdgcn_readlane(cw, 2 * w) >> 2, hi = __builtin_amdgcn_readlane(cw, 2 * w + 1) >> 2;
        if constexpr (first) {
            if (__builtin_amdgcn_readlane(cw, 16)) gcur = 0;
            if (__builtin_amdgcn_ballot_w64(cw > 4u * CAP2)) { gcur = -1; lo = 0; hi = 0; }
        }
        const int c = lo + hi, G = (c + 31) >> 5;
        const int nlast = G > 2 ? (((c - 1) & 31) >> 2) + 1 : 8;
        auto join = [&]() {
            for (int k = 0; k < G * 32 - lo; k += 64) {
                const unsigned e = *(LAS unsigned*)(mylist + SUBB + (l + k) * 4);
                if (l + k < G * 32 - lo) *(LAS unsigned*)(mylist + (lo + l + k) * 4) = l + k < hi ? e : PAD;
            }
        };
        auto entries = [&](int grp, unsigned (&e)[8]) {
#pragma unroll
            for (int m = 0; m < 8; ++m) e[m] = *(LAS unsigned*)(mylist + grp * 128 + g * 4 + m * 16);
        };
        const bool early = lo >= 64;
        if (!early) join();
        unsigned e0[8], e1[8];
        entries(0, e0); entries(1, e1);
        if constexpr (first) {
#pragma unroll
            for (int s = 0; s < 4; ++s) {
                const u32x4 v = *(LAS u32x4*)(size_t)(ring + s * 1024 + l * 16);
                qf[4 * s] = v.x; qf[4 * s + 1] = v.y; qf[4 * s + 2] = v.z; qf[4 * s + 3] = v.w;
            }
            asm volatile("s_waitcnt lgkmcnt(0)" : "+{v[160:175]}"(qf) :: "memory");
        }
        if (G > 0) dma_group(ring, srd_x, e0, doff);
        if constexpr (first) {
            if (G > 1 && w < 6) dma_group(ring + 8192, srd_x, e1, doff);
            {
                const unsigned ra = (4 * (n & 3) + (n >> 2)) * 256 + (((4 * g) ^ (((n & 7) << 1) | (n >> 3))) << 4);
                const int i = 4 * g + q, r8 = i & 7;
                const unsigned ta = (4 * q + g) * 256 + 8 * (p & 1) + (((p >> 1) ^ (((i & 7) << 1) | (i >> 3))) << 4);
                cv = i * 256 + (((p ^ ((r8 & 1) | ((r8 & 6) << 1))) << 4) | ((p & 1) << 3));
#pragma unroll
                for (int s = 0; s < 4; ++s) { addr[s] = ring + (ra ^ (s << 4)); addr[12 + s] = doff[s]; }
#pragma unroll
                for (int c = 0; c < 8; ++c) addr[4 + c] = ring + (ta ^ (c << 5));
                acc = 0.f;
            }
#pragma unroll
            for (int u = 0; u < 2; ++u)
#pragma unroll
                for (int a = 0; a < 8; ++a) {
                    LAS char* pa = smem + L_WV0 + (cv ^ ((a >> 1) << 6) ^ ((a & 1) << 3)) + u * 16384;
                    wvr[u][a] = cat(tr_read(pa), tr_read(pa + 4096));
                }
            WG_BARRIER();
            if (G > 1 && w >= 6) dma_group(ring + 8192, srd_x, e1, doff);
        } else if (G > 1) dma_group(ring + 8192, srd_x, e1, doff);
        f32x16 cc;
#pragma unroll
        for (int m = 0; m < 8; ++m) {
            cc[m] = (e0[m] & 0xFF) == (unsigned)jn ? 0.f : __builtin_inff();
            cc[8 + m] = (e1[m] & 0xFF) == (unsigned)jn ? 0.f : __builtin_inff();
        }
        if (early) join();
        STAMP(9);
        if (G > 0) gather_loop(acc, qf, addr, u32x4{(unsigned)(size_t)mylist + g * 4, (unsigned)jn, 0x7F800000u, 0u}, cc, srd_x, ring, G, nlast);
    };
    batch(std::true_type{});
    while (__builtin_expect(gcur < G1, 0)) {
        unsigned lim = G1 * 32;
        if (gcur >= 0) {
            const u64 m0 = __builtin_amdgcn_ballot_w64(l >= gcur && *(LAS unsigned*)(smem + L_HDR + l * 4) > 4u * CAP1);
            const u64 m1 = __builtin_amdgcn_ballot_w64(l + 64 >= gcur && *(LAS unsigned*)(smem + L_HDR + 256 + l * 4) > 4u * CAP1);
            if (!(m0 | m1)) break;
            gcur = m0 ? __builtin_ctzll(m0) : 64 + __builtin_ctzll(m1);
            unsigned v; const unsigned o = WS_NOVF + gcur * 4;
            asm volatile("buffer_load_dword %0, %1, %2, 0 offen sc1\n\ts_waitcnt vmcnt(0)" : "=&v"(v) : "v"(o), "s"(srd_ws) : "memory");
            lim = rfl(v);
        }
        const unsigned i = pos + t;
        const bool cand = t < CAP2 && i < lim;
        unsigned u;
        { const unsigned o = cand ? (gcur < 0 ? WS_INBOX + b * (G1 * 128) : WS_OVF + gcur * CH * 4) + i * 4 : OOB;
          asm volatile("buffer_load_dword %0, %1, %2, 0 offen sc1\n\ts_waitcnt vmcnt(0)" : "=&v"(u) : "v"(o), "s"(srd_ws) : "memory"); }
        WG_BARRIER();
        if (t < 16) *(LAS unsigned*)(smem + L_CNT + t * 4) = 0;
        WG_BARRIER();
        if (cand) {
            unsigned e; bool v;
            if (gcur < 0) { e = u ^ mykey; v = ((i & 31) - 2) * 4 < *(LAS unsigned*)(smem + L_HDR + (i >> 5) * 4); }
            else { e = ((u >> 16) << 8) | ((u >> 8) & 0xFF); v = (u & 0xFF) == (unsigned)b; }
            if (v) *(LAS unsigned*)(smem + L_LIST + (e & 7) * (2 * SUBB) + lds_add(smem + L_CNT + (e & 7) * 8, 4)) = e;
        }
        pos += CAP2;
        if ((unsigned)pos >= lim) { ++gcur; pos = 0; }
        WG_BARRIER();
        batch(std::false_type{});
    }
    STAMP(10);

    {
        bf16x8 wl[2][8], ag[4];
#pragma unroll
        for (int u = 0; u < 2; ++u)
#pragma unroll
            for (int a = 0; a < 8; ++a) {
                LAS char* pa = smem + L_WV1 + (cv ^ ((a >> 1) << 6) ^ ((a & 1) << 3)) + u * 8192;
                wl[u][a] = cat(tr_read(pa), tr_read(pa + 4096));
            }
#pragma unroll
        for (int u = 0; u < 4; ++u) {
            const u32x4 v = {pk2(acc[8 * u], acc[8 * u + 1]), pk2(acc[8 * u + 2], acc[8 * u + 3]),
                             pk2(acc[8 * u + 4], acc[8 * u + 5]), pk2(acc[8 * u + 6], acc[8 * u + 7])};
            ag[u] = __builtin_bit_cast(bf16x8, v);
        }
        FENCE();
        f32x4 oc[8];
#pragma unroll
        for (int a = 0; a < 8; ++a) oc[a] = mfma(wvr[0][a], ag[0], f32x4{0.f, 0.f, 0.f, 0.f});
#pragma unroll
        for (int a = 0; a < 8; ++a) oc[a] = mfma(wvr[1][a], ag[1], oc[a]);
#pragma unroll
        for (int a = 0; a < 8; ++a) oc[a] = mfma(wl[0][a], ag[2], oc[a]);
        FENCE();
        const unsigned ow = ring + n * 256 + ((g ^ n) << 4) + ((g & 1) << 3);
#pragma unroll
        for (int a = 0; a < 8; ++a) oc[a] = mfma(wl[1][a], ag[3], oc[a]);
#pragma unroll
        for (int a = 0; a < 8; ++a)
            *(LAS u32x2*)(size_t)(ow ^ ((a >> 1) << 6) ^ ((a & 1) << 3)) = u32x2{pk2(oc[a][0], oc[a][1]), pk2(oc[a][2], oc[a][3])};
        const auto rs_o = __builtin_amdgcn_make_buffer_rsrc((void*)out, 0, xbytes, 0x00020000);
#pragma unroll
        for (int m = 0; m < 4; ++m) {
            const u32x4 v = *(LAS u32x4*)(size_t)(ring + m * 1024 + l * 16);
            __builtin_amdgcn_raw_buffer_store_b128(v, rs_o, node_row(w, m) + ((n ^ g ^ (4 * m)) << 4), 0, OUT_AUX);
        }
    }
    if (w == 0) { const unsigned o = l == 0 ? WS_KEY + b * 4 : OOB; ST1(mykey, srd_ws, o, " sc1"); }
    STAMP(11);
}

extern "C" void kernel_launch(void* const* d_in, const int* in_sizes, int n_in,
                              void* d_out, int out_size, void* d_ws, size_t ws_size,
                              hipStream_t stream) {
    const int N = in_sizes[0] / 128, E = in_sizes[1];
    k_graph<<<NB, 512, 0, stream>>>((const unsigned short*)d_in[0], (const int*)d_in[1], (const int*)d_in[2],
                                    (const unsigned short*)d_in[3], (const unsigned short*)d_in[4],
                                    (unsigned*)d_ws, (unsigned short*)d_out, N, E);
}
```

```cpp
#include <hip/hip_runtime.h>
#include <hip/hip_bf16.h>
#include <stdint.h>
#include <type_traits>
#define ORD 1
#define SKIP 0
#define STR2(x) #x
#define STR(x) STR2(x)

typedef __attribute__((ext_vector_type(8))) __bf16 bf16x8;
typedef __attribute__((ext_vector_type(4))) __bf16 bf16x4;
typedef __attribute__((ext_vector_type(2))) __bf16 bf16x2;
typedef __attribute__((ext_vector_type(4))) float f32x4;
typedef __attribute__((ext_vector_type(2))) float f32x2;
typedef __attribute__((ext_vector_type(4))) unsigned u32x4;
typedef __attribute__((ext_vector_type(2))) unsigned u32x2;
typedef __attribute__((ext_vector_type(32))) float f32x32;
typedef __attribute__((ext_vector_type(16))) float f32x16;
typedef __attribute__((ext_vector_type(16))) unsigned u32x16;
typedef __attribute__((ext_vector_type(12))) unsigned u32x12;
typedef unsigned long long u64;
#define LAS __attribute__((address_space(3)))

constexpr int NB = 256;
constexpr int G1 = 128;
constexpr int CH = 3136;
constexpr int CAP1 = 30;
constexpr int CAP2 = 192;
constexpr int SUBB = 784;
#define POLL_AT 3
#define OUT_AUX 16
#define PF_R 0
#define PF_D 0
#define POLL_T 260
#define PF_T 200
#define WMODE 2
#define LPRIO 3
#define PKADD 1
#define NP_SLEEP 0
#define W_SLEEP 3
#define PRIO 1
#define SPLIT 16384
constexpr unsigned KEY_STEP = 0x9E3779B1u;
constexpr unsigned WS_KEY = 0;
constexpr unsigned WS_NOVF = 2048;
constexpr unsigned WS_INBOX = 4096;
constexpr unsigned WS_OVF = WS_INBOX + NB * G1 * 128;
constexpr unsigned WS_BYTES = WS_OVF + G1 * CH * 4;
constexpr unsigned OOB = 0x80000000u;
constexpr unsigned BIAS = 32768 - SPLIT;
constexpr unsigned PAD = 0x00FFFFFFu;

constexpr int L_CNT = 0;
constexpr int L_HDR = 128;
constexpr int L_LIST = 768;
constexpr int L_RING = L_LIST + 16 * SUBB;
constexpr int L_IMG = L_RING;
constexpr int L_CNT1 = L_IMG + NB * 144;
constexpr int L_WQ = L_RING + 2 * 16384 + 8192;
constexpr int L_WV0 = L_RING + 6 * 16384 + 8192;
constexpr int L_WV1 = L_RING + 131072;
constexpr int L_DUMP = L_WV1 + 16384;
constexpr int L_TOTAL = L_DUMP + 2048;
static_assert(L_RING % 256 == 0 && L_TOTAL <= 163840, "LDS map");

#define STAMP_DECL
#define STAMP(k)
#define STAMP_FLUSH(base, k0, k1)

__device__ __forceinline__ u32x4 make_srd(const void* p, unsigned bytes, unsigned back = 0) {
    const u64 a = (u64)p - back;
    u32x4 s; s.x = (unsigned)a; s.y = (unsigned)(a >> 32) & 0xFFFFu; s.z = bytes + back; s.w = 0x00020000u; return s;
}
#define LD4(dst, srd, off, FL) asm volatile("buffer_load_dwordx4 %0, %1, %2, 0 offen" FL : "=v"(dst) : "v"(off), "s"(srd) : "memory")
#define LD4S(dst, srd, off, so, FL) asm volatile("buffer_load_dwordx4 %0, %1, %2, %3 offen" FL : "=v"(dst) : "v"(off), "s"(srd), "s"(so) : "memory")
#define LD1(dst, srd, off, FL) asm volatile("buffer_load_dword %0, %1, %2, 0 offen" FL : "=v"(dst) : "v"(off), "s"(srd) : "memory")
#define ST1(val, srd, off, FL) asm volatile("buffer_store_dword %0, %1, %2, 0 offen" FL :: "v"(val), "v"(off), "s"(srd) : "memory")
#define PIN(x) asm volatile("" : "+v"(x))
#define VMWAIT(n) asm volatile("s_waitcnt vmcnt(" #n ")" ::: "memory")
#define FENCE() __builtin_amdgcn_sched_barrier(0)
#define WG_BARRIER() do { asm volatile("s_waitcnt lgkmcnt(0)" ::: "memory"); __builtin_amdgcn_s_barrier(); asm volatile("" ::: "memory"); } while (0)

struct Srd4 { u32x4 s0, s1, s2, s3; };
__device__ __forceinline__ Srd4 make_srd4(const void* p, unsigned bytes, unsigned back = 0) {
    return Srd4{make_srd(p, bytes, back), make_srd(p, bytes, back + 1024), make_srd(p, bytes, back + 2048), make_srd(p, bytes, back + 3072)};
}

__device__ __forceinline__ void set_m0(unsigned v) { asm volatile("s_mov_b32 m0, %0" :: "s"(v) : "memory"); }
template <int R> __device__ __forceinline__ void dma_rows(const Srd4& d, unsigned e, unsigned off) {
    unsigned tmp, mask = 0x00FFFF00u;
    asm volatile("v_and_or_b32 %0, %1, %2, %3\n\tbuffer_load_dwordx4 %0, %4, 0 offen offset:%c5 lds"
                 : "=&v"(tmp) : "v"(e), "s"(mask), "v"(off), "s"(R == 0 ? d.s0 : R == 1 ? d.s1 : R == 2 ? d.s2 : d.s3), "i"(R * 1024) : "memory");
}
__device__ __forceinline__ void dma_half(unsigned ldsbase, const Srd4& d, const unsigned* e, const u32x4& off) {
    set_m0(ldsbase);
    dma_rows<0>(d, e[0], off.x); dma_rows<1>(d, e[1], off.y); dma_rows<2>(d, e[2], off.z); dma_rows<3>(d, e[3], off.w);
}
__device__ __forceinline__ void dma_group(unsigned ldsbase, const Srd4& d, const unsigned (&e)[8], const u32x4& off) {
    asm volatile("s_waitcnt lgkmcnt(0)" ::: "memory");
    dma_half(ldsbase, d, e, off); dma_half(ldsbase + 4096, d, e + 4, off);
}

__device__ __forceinline__ void gather_loop(f32x32& acc, const u32x16& qf, const u32x16& addr, u32x4 misc, f32x16 cc,
                                            const Srd4& d, unsigned ring, int G, int nlast) {
    f32x32 t0, t1; u32x12 t2; int tmp, pr; u64 sm; unsigned mask = 0x00FFFF00u;
    asm volatile(
        ".macro GWAIT k\n\ts_cmp_gt_i32 %[t], \\k\n\ts_cbranch_scc1 1f\n\ts_waitcnt vmcnt(\\k)\n\t.endm\n\t"
        ".macro GROWS so\n\t.irp s, 0, 1, 2, 3\n\t"
        "ds_read_b128 v[64+8*\\s:67+8*\\s], v[176+\\s] offset:\\so\n\t"
        "ds_read_b128 v[68+8*\\s:71+8*\\s], v[176+\\s] offset:\\so+4096\n\t.endr\n\t.endm\n\t"
        ".macro GTR c, so\n\t"
        "ds_read_b64_tr_b16 v[96+4*\\c:97+4*\\c], v[180+\\c] offset:\\so\n\t"
        "ds_read_b64_tr_b16 v[98+4*\\c:99+4*\\c], v[180+\\c] offset:\\so+4096\n\t.endm\n\t"
        ".macro GS s, c\n\t"
        "v_mfma_f32_16x16x32_bf16 v[\\c:\\c+3], v[64+8*\\s:67+8*\\s], v[160+4*\\s:163+4*\\s], v[\\c:\\c+3]\n\t"
        "v_mfma_f32_16x16x32_bf16 v[\\c+4:\\c+7], v[68+8*\\s:71+8*\\s], v[160+4*\\s:163+4*\\s], v[\\c+4:\\c+7]\n\t.endm\n\t"
        ".macro GLOAD m, r, srd\n\t"
        "v_and_or_b32 v195, v[212+\\m], %[mask], v[188+\\r]\n\t"
        "buffer_load_dwordx4 v195, \\srd, 0 offen offset:1024*\\r lds\n\t.endm\n\t"
        ".macro GPART m, r, srd\n\ts_cmp_le_i32 %[nl], \\m\n\ts_cbranch_scc1 4f\n\tGLOAD \\m, \\r, \\srd\n\t.endm\n\t"
        ".macro GSUM m\n\tv_mfma_f32_16x16x32_bf16 v[128+4*(\\m):131+4*(\\m)], v[96+4*(\\m):99+4*(\\m)], v[220:223], v[128+4*(\\m):131+4*(\\m)]\n\t.endm\n\t"
        ".macro GCMP m, d\n\tv_cmp_ne_u32_sdwa \\d, v[212+\\m], v193 src0_sel:BYTE_0 src1_sel:DWORD\n\t.endm\n\t"
        ".macro GPAIR m, c\n\t"
        "GSUM \\m\n\tGCMP \\m+1, %[sm]\n\tv_cndmask_b32_e32 v[\\c+\\m], 0, v194, vcc\n\t"
        "GSUM \\m+1\n\t.if \\m < 6\n\tGCMP \\m+2, vcc\n\t.else\n\ts_cmp_gt_i32 %[rem], 0\n\t.endif\n\t"
        "v_cndmask_b32_e64 v[\\c+\\m+1], 0, v194, %[sm]\n\t.endm\n\t"
        ".macro GSTEP so, c, eo\n\t"
        "GTR 0, \\so\n\t"
        "s_waitcnt lgkmcnt(2)\n\tGS 0, \\c\n\t"
        "GTR 1, \\so\n\tGTR 2, \\so\n\tGS 1, \\c\n\t"
        "GTR 3, \\so\n\tGTR 4, \\so\n\tGS 2, \\c\n\t"
        "GTR 5, \\so\n\tGTR 6, \\so\n\tGS 3, \\c\n\t"
        "GTR 7, \\so\n\t"
        "ds_read2_b32 v[212:213], v192 offset0:\\eo offset1:\\eo+4\n\t"
        "ds_read2_b32 v[214:215], v192 offset0:\\eo+8 offset1:\\eo+12\n\t"
        "ds_read2_b32 v[216:217], v192 offset0:\\eo+16 offset1:\\eo+20\n\t"
        "ds_read2_b32 v[218:219], v192 offset0:\\eo+24 offset1:\\eo+28\n\t"
        "s_add_u32 m0, %[ring], \\so\n\t"
        "s_cmp_gt_i32 %[rem], 3\n\t"
        ".irp k, 0, 1, 2, 3, 4, 5, 6, 7\n\tv_exp_f32_e32 v[\\c+\\k], v[\\c+\\k]\n\t.endr\n\t"
        "s_waitcnt lgkmcnt(0)\n\t"
        "s_cbranch_scc0 2f\n\t"
        "GLOAD 0, 0, %[s0]\n\tGLOAD 1, 1, %[s1]\n\tGLOAD 2, 2, %[s2]\n\tGLOAD 3, 3, %[s3]\n\t"
        "s_add_u32 m0, m0, 0x1000\n\t"
        "GLOAD 4, 0, %[s0]\n\tGLOAD 5, 1, %[s1]\n\tGLOAD 6, 2, %[s2]\n\tGLOAD 7, 3, %[s3]\n\t"
        "GSIG \\c\n\t"
        "s_waitcnt vmcnt(8)\n\t"
        "s_branch 1f\n\t"
        "2:\n\t"
        "s_mov_b32 %[t], 0\n\t"
        "s_cmp_eq_u32 %[rem], 3\n\ts_cbranch_scc0 4f\n\t"
        "s_mov_b32 %[t], %[nl]\n\t"
        "GLOAD 0, 0, %[s0]\n\tGPART 1, 1, %[s1]\n\tGPART 2, 2, %[s2]\n\tGPART 3, 3, %[s3]\n\t"
        "s_add_u32 m0, m0, 0x1000\n\t"
        "GPART 4, 0, %[s0]\n\tGPART 5, 1, %[s1]\n\tGPART 6, 2, %[s2]\n\tGPART 7, 3, %[s3]\n\t"
        "4:\n\t"
        "GSIG \\c\n\t"
        "s_waitcnt vmcnt(8)\n\t"
        "GWAIT 7\n\tGWAIT 6\n\tGWAIT 5\n\tGWAIT 4\n\tGWAIT 3\n\tGWAIT 2\n\tGWAIT 1\n\tGWAIT 0\n\t"
        "1:\n\t"
        "GROWS 8192-\\so\n\t"
        "GCMP 0, vcc\n\t"
        "s_add_i32 %[rem], %[rem], -1\n\t"
        "GPAIR 0, \\c\n\tGPAIR 2, \\c\n\tGPAIR 4, \\c\n\tGPAIR 6, \\c\n\t"
        ".endm\n\t"
        ".macro GSIG c\n\t"
        ".if " STR(PKADD) "\n\t.irp k, 0, 2, 4, 6\n\tv_pk_add_f32 v[\\c+\\k:\\c+\\k+1], v[\\c+\\k:\\c+\\k+1], 1.0 op_sel_hi:[1,0]\n\t.endr\n\t.else\n\t"
        ".irp k, 0, 1, 2, 3, 4, 5, 6, 7\n\tv_add_f32_e32 v[\\c+\\k], 1.0, v[\\c+\\k]\n\t.endr\n\t.endif\n\t"
        ".irp k, 0, 1, 2, 3, 4, 5, 6, 7\n\tv_rcp_f32_e32 v[\\c+\\k], v[\\c+\\k]\n\t.endr\n\t"
        ".irp k, 0, 1, 2, 3\n\tv_cvt_pk_bf16_f32 v[220+\\k], v[\\c+2*\\k], v[\\c+2*\\k+1]\n\t.endr\n\t.endm\n\t"
        "s_waitcnt vmcnt(8)\n\t"
        "s_cmp_gt_i32 %[rem], 1\n\ts_cbranch_scc1 1f\n\ts_waitcnt vmcnt(0)\n\t1:\n\t"
        "GROWS 0\n\t"
        ".if " STR(LPRIO) " >= 3\n\ts_mov_b32 %[pr], 0\n\ts_setprio 2\n\t.endif\n\t"
        "0:\n\t"
        ".if " STR(LPRIO) " == 3\n\t"
        "s_cmp_eq_u32 %[pr], 1\n\ts_cbranch_scc0 5f\n\ts_setprio 1\n\t5:\n\t"
        "s_cmp_eq_u32 %[pr], 2\n\ts_cbranch_scc0 5f\n\ts_setprio 0\n\t5:\n\t"
        "s_add_u32 %[pr], %[pr], 1\n\t.endif\n\t"
        "GSTEP 0, 196, 64\n\t"
        "s_cbranch_scc0 9f\n\t"
        "GSTEP 8192, 204, 96\n\t"
        "v_add_u32_e32 v192, 0x100, v192\n\t"
        "s_cbranch_scc1 0b\n\t"
        "9:\n\t"
        "s_waitcnt lgkmcnt(0)\n\ts_nop 6\n\t"
        ".purgem GWAIT\n\t.purgem GROWS\n\t.purgem GTR\n\t.purgem GS\n\t.purgem GLOAD\n\t.purgem GPART\n\t.purgem GSUM\n\t.purgem GCMP\n\t.purgem GPAIR\n\t.purgem GSTEP\n\t.purgem GSIG"
        : "+{v[128:159]}"(acc), "+{v[192:195]}"(misc), "+{v[196:211]}"(cc), "=&{v[64:95]}"(t0), "=&{v[96:127]}"(t1), "=&{v[212:223]}"(t2),
          [rem] "+s"(G), [t] "=&s"(tmp), [pr] "=&s"(pr), [sm] "=&s"(sm)
        : "{v[160:175]}"(qf), "{v[176:191]}"(addr), [s0] "s"(d.s0), [s1] "s"(d.s1), [s2] "s"(d.s2), [s3] "s"(d.s3),
          [mask] "s"(mask), [ring] "s"(ring), [nl] "s"(nlast)
        : "vcc", "scc", "memory");
}

__device__ __forceinline__ void inbox_request(u32x16& raw, const u32x4& srd, unsigned ib) {
    unsigned o1 = 0x1000, o2 = 0x2000, o3 = 0x3000;
    asm volatile("buffer_load_dwordx4 v[64:67], %1, %2, 0 offen sc1\n\tbuffer_load_dwordx4 v[68:71], %1, %2, %3 offen sc1\n\t"
                 "buffer_load_dwordx4 v[72:75], %1, %2, %4 offen sc1\n\tbuffer_load_dwordx4 v[76:79], %1, %2, %5 offen sc1"
                 : "=&{v[64:79]}"(raw) : "v"(ib), "s"(srd), "s"(o1), "s"(o2), "s"(o3) : "memory");
}
__device__ __forceinline__ void inbox_stage(u32x16 raw, const u32x4& srd, unsigned ib, unsigned key, int i4, unsigned hdra) {
    u32x16 t0; u32x4 t1; u64 m0, m1, m2, m3; unsigned todo, fresh, spin, o1, o2, o3;
    u64 pc0 = 0x0101010101010101ull;
    asm volatile(
        ".macro IVAL j, so\n\t"
        "s_bitcmp1_b32 %[todo], \\j\n\ts_cbranch_scc0 2f\n\t"
        ".irp k, 0, 1, 2, 3\n\tv_xor_b32_e32 v[64+4*\\j+\\k], %[key], v[64+4*\\j+\\k]\n\t.endr\n\t"
        "v_mov_b32_dpp v[80+\\j], v[64+4*\\j] quad_perm:[0,0,0,0] row_mask:0xf bank_mask:0xf\n\t"
        "v_add_u32_e32 v96, v[64+4*\\j], v[65+4*\\j]\n\t"
        "v_sub_u32_e32 v97, 0, v[65+4*\\j]\n\t"
        "v_mov_b32_dpp v[80+\\j], v[80+\\j] row_shr:4 row_mask:0xf bank_mask:0xa\n\t"
        "v_cndmask_b32_e64 v96, v96, v97, %[pc0]\n\t"
        "v_cmp_gt_i32_e32 vcc, v[80+\\j], v92\n\t"
        "v_add3_u32 v96, v96, v[66+4*\\j], v[67+4*\\j]\n\t"
        "v_lshrrev_b32_e32 v97, 14, v[80+\\j]\n\t"
        "v_cndmask_b32_e32 v96, 0, v96, vcc\n\t"
        "s_nop 1\n\tv_add_u32_dpp v96, v96, v96 quad_perm:[1,0,3,2] row_mask:0xf bank_mask:0xf\n\t"
        "s_nop 1\n\tv_add_u32_dpp v96, v96, v96 quad_perm:[2,3,0,1] row_mask:0xf bank_mask:0xf\n\t"
        "s_nop 1\n\tv_add_u32_dpp v96, v96, v96 row_half_mirror row_mask:0xf bank_mask:0xf\n\t"
        "v_or_b32_e32 v96, v96, v97\n\t"
        "v_cmp_ne_u32_e32 vcc, 0, v96\n\t"
        "s_cbranch_vccz 1f\n\t"
        "buffer_load_dwordx4 v[64+4*\\j:67+4*\\j], %[ib], %[srd], \\so offen sc1\n\t"
        "s_branch 2f\n\t"
        "1:\n\ts_bitset1_b32 %[fresh], \\j\n\t"
        "2:\n\t.endm\n\t"
        ".macro IRANK j\n\t"
        "s_bitcmp1_b32 %[fresh], \\j\n\ts_cbranch_scc0 2f\n\t"
        "v_cmp_lt_u32_e64 %[m0], v92, v[80+\\j]\n\tv_cmp_lt_u32_e64 %[m1], v93, v[80+\\j]\n\t"
        "v_cmp_lt_u32_e64 %[m2], v94, v[80+\\j]\n\tv_cmp_lt_u32_e64 %[m3], v95, v[80+\\j]\n\t"
        ".irp k, 0, 1, 2, 3\n\tv_lshrrev_b32_e32 v[84+\\k], 21, v[64+4*\\j+\\k]\n\t.endr\n\t"
        ".irp k, 0, 1, 2, 3\n\tv_and_b32_e32 v[84+\\k], 60, v[84+\\k]\n\t.endr\n\t"
        "v_cmp_lt_u32_e32 vcc, %c[cap], v[80+\\j]\n\t"
        "s_mov_b64 exec, %[m0]\n\tds_add_rtn_u32 v88, v84, v98 offset:%c[cnt]\n\t"
        "s_mov_b64 exec, %[m1]\n\tds_add_rtn_u32 v89, v85, v98 offset:%c[cnt]\n\t"
        "s_mov_b64 exec, %[m2]\n\tds_add_rtn_u32 v90, v86, v98 offset:%c[cnt]\n\t"
        "s_mov_b64 exec, %[m3]\n\tds_add_rtn_u32 v91, v87, v98 offset:%c[cnt]\n\t"
        "s_mov_b64 exec, %[pc0]\n\tds_write_b32 %[hdra], v[80+\\j] offset:128*\\j\n\t"
        "s_mov_b64 exec, -1\n\t"
        "s_cbranch_vccz 1f\n\t"
        "v_mov_b32_e32 v96, 0\n\tds_write_b32 v96, v98 offset:%c[cnt]+64\n\ts_waitcnt lgkmcnt(0)\n\t"
        "1:\n\ts_waitcnt lgkmcnt(1)\n\t"
        ".irp k, 0, 1, 2, 3\n\tv_min_u32_e32 v[88+\\k], %c[sub]-4, v[88+\\k]\n\t.endr\n\t"
        ".irp k, 0, 1, 2, 3\n\tv_mad_u32_u24 v[88+\\k], v[84+\\k], %[sub4], v[88+\\k]\n\t.endr\n\t"
        "s_mov_b64 exec, %[m0]\n\tds_write_b32 v88, v[64+4*\\j] offset:%c[list]\n\t"
        "s_mov_b64 exec, %[m1]\n\tds_write_b32 v89, v[65+4*\\j] offset:%c[list]\n\t"
        "s_mov_b64 exec, %[m2]\n\tds_write_b32 v90, v[66+4*\\j] offset:%c[list]\n\t"
        "s_mov_b64 exec, %[m3]\n\tds_write_b32 v91, v[67+4*\\j] offset:%c[list]\n\t"
        "s_mov_b64 exec, -1\n\t"
        "2:\n\t.endm\n\t"
        "s_movk_i32 %[o1], 0x1000\n\ts_movk_i32 %[o2], 0x2000\n\ts_movk_i32 %[o3], 0x3000\n\t"
        "v_mov_b32_e32 v92, %[i4]\n\tv_add_u32_e32 v93, 4, %[i4]\n\tv_add_u32_e32 v94, 8, %[i4]\n\tv_add_u32_e32 v95, 12, %[i4]\n\t"
        "v_mov_b32_e32 v98, 4\n\t"
        "s_mov_b32 %[todo], 15\n\ts_mov_b32 %[spin], 0x400000\n\t"
        "0:\n\t"
        "s_mov_b32 %[fresh], 0\n\t"
        "s_waitcnt vmcnt(3)\n\ts_cmp_eq_u32 %[todo], 15\n\ts_cbranch_scc1 3f\n\ts_waitcnt vmcnt(0)\n\t3:\n\t"
        "IVAL 0, 0\n\tIRANK 0\n\t"
        "s_waitcnt vmcnt(2)\n\tIVAL 1, %[o1]\n\tIRANK 1\n\t"
        "s_waitcnt vmcnt(1)\n\tIVAL 2, %[o2]\n\tIRANK 2\n\t"
        "s_cmp_eq_u32 %[todo], 15\n\ts_cbranch_scc0 3f\n\ts_waitcnt vmcnt(0)\n\t3:\n\tIVAL 3, %[o3]\n\tIRANK 3\n\t"
        "s_andn2_b32 %[todo], %[todo], %[fresh]\n\t"
        "s_cbranch_scc0 9f\n\t"
        "s_sub_u32 %[spin], %[spin], 1\n\t"
        "s_cbranch_scc0 0b\n\t"
        "s_waitcnt vmcnt(0)\n\t"
        "9:\n\t"
        ".purgem IVAL\n\t.purgem IRANK"
        : "+{v[64:79]}"(raw), "=&{v[80:95]}"(t0), "=&{v[96:99]}"(t1), [m0] "=&s"(m0), [m1] "=&s"(m1), [m2] "=&s"(m2), [m3] "=&s"(m3),
          [todo] "=&s"(todo), [fresh] "=&s"(fresh), [spin] "=&s"(spin), [o1] "=&s"(o1), [o2] "=&s"(o2), [o3] "=&s"(o3)
        : [srd] "s"(srd), [ib] "v"(ib), [key] "s"(key), [i4] "v"(i4), [hdra] "v"(hdra), [pc0] "s"(pc0), [sub4] "s"(SUBB / 4),
          [cnt] "i"(L_CNT), [list] "i"(L_LIST), [sub] "i"(SUBB), [cap] "i"(4 * CAP1)
        : "vcc", "scc", "memory");
}

template <int CTRL, int BANK> __device__ __forceinline__ unsigned dpp(unsigned old, unsigned v) { return __builtin_amdgcn_update_dpp(old, v, CTRL, 0xF, BANK, true); }
__device__ __forceinline__ unsigned bcast8(unsigned v) { v = dpp<0x00, 0xF>(0u, v); return __builtin_amdgcn_update_dpp(v, v, 0x114, 0xF, 0xA, false); }
__device__ __forceinline__ unsigned sum8(unsigned v) { v += dpp<0xB1, 0xF>(0u, v); v += dpp<0x4E, 0xF>(0u, v); return v + dpp<0x141, 0xF>(0u, v); }
__device__ __forceinline__ bf16x4 tr_read(LAS char* p) { return __builtin_amdgcn_ds_read_tr16_b64_v4bf16((LAS bf16x4*)p); }
__device__ __forceinline__ bf16x8 cat(bf16x4 a, bf16x4 b) { return __builtin_shufflevector(a, b, 0, 1, 2, 3, 4, 5, 6, 7); }
__device__ __forceinline__ unsigned pk2(float a, float b) { f32x2 v = {a, b}; bf16x2 r = __builtin_convertvector(v, bf16x2); return __builtin_bit_cast(unsigned, r); }
__device__ __forceinline__ f32x4 mfma(bf16x8 a, bf16x8 b, f32x4 c) { return __builtin_amdgcn_mfma_f32_16x16x32_bf16(a, b, c, 0, 0, 0); }
__device__ __forceinline__ unsigned lds_add(LAS char* p, unsigned v) { return __hip_atomic_fetch_add((LAS unsigned*)p, v, __ATOMIC_RELAXED, __HIP_MEMORY_SCOPE_WORKGROUP); }
__device__ __forceinline__ LAS unsigned* ldsp(unsigned a) { return (LAS unsigned*)(size_t)a; }
__device__ __forceinline__ unsigned byte0_x4(unsigned v) { unsigned r; asm("v_lshlrev_b32_sdwa %0, 2, %1 dst_sel:DWORD dst_unused:UNUSED_PAD src0_sel:DWORD src1_sel:BYTE_0" : "=v"(r) : "v"(v)); return r; }
__device__ __forceinline__ unsigned mad24(unsigned a, unsigned b, unsigned c) { unsigned r; asm("v_mad_u32_u24 %0, %1, %2, %3" : "=v"(r) : "v"(a), "s"(b), "v"(c)); return r; }
__device__ __forceinline__ void lds_arrive(LAS char* p, int lane) {
    asm volatile("s_waitcnt lgkmcnt(0)" ::: "memory");
    if (lane == 0) __hip_atomic_fetch_add((LAS unsigned*)p, 1u, __ATOMIC_RELAXED, __HIP_MEMORY_SCOPE_WORKGROUP);
}
__device__ __forceinline__ void lds_await(LAS char* p, unsigned n) {
    while (__builtin_amdgcn_readfirstlane(*(volatile LAS unsigned*)p) < n) __builtin_amdgcn_s_sleep(1);
    asm volatile("" ::: "memory");
}
__device__ __forceinline__ unsigned rfl(unsigned v) { return __builtin_amdgcn_readfirstlane(v); }

__global__ __launch_bounds__(512, 2) void k_graph(const unsigned short* __restrict__ x, const int* __restrict__ src, const int* __restrict__ dst,
                                                  const unsigned short* __restrict__ wq, const unsigned short* __restrict__ wv,
                                                  unsigned* __restrict__ ws, unsigned short* __restrict__ out, int N, int E) {
    __shared__ __attribute__((aligned(16))) char smem_[L_TOTAL];
    LAS char* smem = (LAS char*)smem_;
    const int t = threadIdx.x, l = t & 63, b = blockIdx.x;
    const int w = rfl(t >> 6);
    const int g = l >> 4, n = l & 15, q = (l >> 2) & 3, p = l & 3, pc = t & 7;
    STAMP_DECL;
    STAMP(0);
    const u64 t_start = __builtin_amdgcn_s_memrealtime();
    asm volatile("" :: "s"(src), "s"(dst), "s"(out), "s"(wq), "s"(wv), "s"(x), "s"(ws), "s"(N), "s"(E));
    const unsigned lds0 = (unsigned)(size_t)smem;
    const unsigned xbytes = (unsigned)N * 256u;
    const u32x4 srd_ws = make_srd(ws, WS_BYTES);
    const unsigned ring = lds0 + L_RING + w * 16384;
    const bool isprod = ((b >> 3) & 1) == 0;
    const int pg = ((b & 7) << 4) | (b >> 4);
    const int jn = 8 * n + w;
    const u32x4 srd_x0 = make_srd(x, xbytes);

    unsigned mykey = 0;
    auto node_row = [&](int tile, int m) { const int nd = b + 256 * (8 * (4 * m + g) + tile); return nd < N ? (unsigned)nd << 8 : OOB; };
    auto zero_staging = [&]() {
        const u32x4 z = {0u, 0u, 0u, 0u};
#pragma unroll
        for (int k = 0; k < 5; ++k) *(LAS u32x4*)(smem + L_IMG + k * 8192 + t * 16) = z;
    };

    u32x16 addr;
    f32x32 acc, wva, wvb;
    unsigned cv, lista, cnta, qa, keyn, inf;
    Srd4 srd_x;
    auto loop_constants = [&]() {
        srd_x = make_srd4(x, xbytes, BIAS * 256);
        lista = lds0 + L_LIST + w * (2 * SUBB) + g * 4; cnta = lds0 + L_CNT + (l & 31) * 4; qa = ring + l * 16; keyn = jn; inf = 0x7F800000u;
        asm volatile("" : "+s"(srd_x.s0), "+s"(srd_x.s1), "+s"(srd_x.s2), "+s"(srd_x.s3));
        asm volatile("" : "+v"(lista), "+v"(cnta), "+v"(qa), "+v"(keyn), "+v"(inf));
        const unsigned o0 = (n ^ (((g & 1) << 3) | (g >> 1))) << 4;
        const unsigned ra = (4 * (n & 3) + (n >> 2)) * 256 + (((4 * g) ^ (((n & 7) << 1) | (n >> 3))) << 4);
        const int i = 4 * g + q, r8 = i & 7;
        const unsigned ta = (4 * q + g) * 256 + 8 * (p & 1) + (((p >> 1) ^ (((i & 7) << 1) | (i >> 3))) << 4);
        cv = i * 256 + (((p ^ ((r8 & 1) | ((r8 & 6) << 1))) << 4) | ((p & 1) << 3));
#pragma unroll
        for (int s = 0; s < 4; ++s) { addr[s] = ring + (ra ^ (s << 4)); addr[12 + s] = o0 ^ (s << 5); }
#pragma unroll
        for (int c = 0; c < 8; ++c) addr[4 + c] = ring + (ta ^ (c << 5));
        acc = 0.f;
        asm volatile("" : "+{v[128:159]}"(acc), "+{v[176:191]}"(addr), "+v"(cv));
    };
    auto wv_resident = [&]() {
#pragma unroll
        for (int a = 0; a < 8; ++a) {
            LAS char* pa = smem + L_WV0 + (cv ^ ((a >> 1) << 6) ^ ((a & 1) << 3));
            const f32x4 f0 = __builtin_bit_cast(f32x4, cat(tr_read(pa), tr_read(pa + 4096)));
            const f32x4 f1 = __builtin_bit_cast(f32x4, cat(tr_read(pa + 16384), tr_read(pa + 16384 + 4096)));
#pragma unroll
            for (int k = 0; k < 4; ++k) { wva[4 * a + k] = f0[k]; wvb[4 * a + k] = f1[k]; }
        }
        asm volatile("" : "+{v[224:255]}"(wva), "+{v[32:63]}"(wvb));
    };

    if (w < 4) {
        if (isprod) {
            u32x4 d4[3], s4[3];
            unsigned d1, s1, kb;
            const unsigned e0 = pg * CH + 4 * t, e1 = pg * CH + 3072 + t;
            {
                const u32x4 srd_d = make_srd(dst, (unsigned)E * 4u), srd_s = make_srd(src, (unsigned)E * 4u);
                const unsigned o = e0 * 4, o2 = o + 8192, o1 = w == 0 ? e1 * 4 : OOB;
                const unsigned k4 = 4096;
                LD4(d4[0], srd_d, o, ""); LD4S(d4[1], srd_d, o, k4, ""); LD4(d4[2], srd_d, o2, ""); LD1(d1, srd_d, o1, "");
                LD4(s4[0], srd_s, o, ""); LD4S(s4[1], srd_s, o, k4, ""); LD4(s4[2], srd_s, o2, ""); LD1(s1, srd_s, o1, "");
                const unsigned ok = WS_KEY + t * 4, om = WS_KEY + b * 4;
                LD1(kb, srd_ws, ok, " sc1"); LD1(mykey, srd_ws, om, " sc1");
            }
            zero_staging();
            if (t < 8) *(LAS u32x4*)(smem + L_CNT + t * 16) = u32x4{0u, 0u, 0u, 0u};
            unsigned ob = WS_INBOX + ((t >> 3) * G1 + pg) * 128 + pc * 16, ima = lds0 + L_IMG + (t >> 3) * 144 + pc * 16;
            PIN(ob); PIN(ima);
            WG_BARRIER();
            if (PRIO) __builtin_amdgcn_s_setprio(2);
            STAMP(1);
            unsigned b4[13], rk[13];
            const bool v1 = w == 0 && e1 < (unsigned)E;
            asm volatile("s_waitcnt vmcnt(6)" : "+v"(d4[0]), "+v"(d4[1]), "+v"(d4[2]), "+v"(d1) :: "memory");
            STAMP(2);
#pragma unroll
            for (int j = 0; j < 3; ++j)
                if (e0 + 1024 * j < (unsigned)E) {
#pragma unroll
                    for (int k = 0; k < 4; ++k) { b4[4 * j + k] = byte0_x4(d4[j][k]); rk[4 * j + k] = lds_add(smem + L_CNT1 + b4[4 * j + k], 4); }
                }
            if (v1) { b4[12] = byte0_x4(d1); rk[12] = lds_add(smem + L_CNT1 + b4[12], 4); }
            asm volatile("s_waitcnt vmcnt(1)" : "+v"(s4[0]), "+v"(s4[1]), "+v"(s4[2]), "+v"(s1), "+v"(kb) :: "memory");
            auto put = [&](unsigned d, unsigned sv, unsigned bb, unsigned r) {
                *(LAS unsigned*)(smem + L_IMG + 8 + mad24(bb, 36, min(r, 4u * CAP1))) = __builtin_amdgcn_perm(sv + BIAS, d, 0x01050401u);
            };
            auto spill = [&](unsigned d, unsigned sv, unsigned r) {
                if (r >= 4u * CAP1) {
                    const unsigned o = WS_OVF + (pg * CH + lds_add(smem + L_CNT1 + 2048, 1)) * 4, v = (sv << 16) | d;
                    ST1(v, srd_ws, o, " sc1");
                }
            };
#pragma unroll
            for (int j = 0; j < 3; ++j)
                if (e0 + 1024 * j < (unsigned)E) {
#pragma unroll
                    for (int k = 0; k < 4; ++k) put(d4[j][k], s4[j][k], b4[4 * j + k], rk[4 * j + k]);
                    if (__builtin_expect(max(max(rk[4 * j], rk[4 * j + 1]), max(rk[4 * j + 2], rk[4 * j + 3])) >= 4u * CAP1, 0)) {
#pragma unroll
                        for (int k = 0; k < 4; ++k) spill(d4[j][k], s4[j][k], rk[4 * j + k]);
                    }
                }
            if (v1) { put(d1, s1, b4[12], rk[12]); spill(d1, s1, rk[12]); }
            *(LAS unsigned*)(smem + L_CNT1 + 1024 + t * 4) = kb + KEY_STEP;
            WG_BARRIER();
            STAMP(3);
            unsigned c4[8], kk[8], cs[8]; u32x4 v[8];
            const unsigned nov = *(LAS unsigned*)(smem + L_CNT1 + 2048);
#pragma unroll
            for (int it = 0; it < 8; ++it) {
                c4[it] = *(LAS unsigned*)(smem + L_CNT1 + (t >> 3) * 4 + it * 128);
                kk[it] = *(LAS unsigned*)(smem + L_CNT1 + 1024 + (t >> 3) * 4 + it * 128);
                v[it] = *(LAS u32x4*)(size_t)(ima + it * (32 * 144));
            }
#pragma unroll
            for (int it = 0; it < 8; ++it) cs[it] = sum8(v[it].x + v[it].y + v[it].z + v[it].w);
            lds_arrive(smem + L_CNT + 84, l);
            const unsigned no = rfl(nov);
            if (__builtin_expect(no != 0, 0)) {
                const unsigned o = t == 0 ? WS_NOVF + pg * 4 : OOB;
                ST1(no, srd_ws, o, " sc1");
                VMWAIT(0);
                WG_BARRIER();
            }
#pragma unroll
            for (int it = 0; it < 8; ++it) {
                v[it].x = pc == 0 ? c4[it] : v[it].x; v[it].y = pc == 0 ? cs[it] : v[it].y;
                v[it] ^= kk[it];
                const unsigned o = (WMODE == 2 || (int)c4[it] > 16 * pc - 8) ? ob : OOB, so = it * 32 * G1 * 128;
                asm volatile("buffer_store_dwordx4 %0, %1, %2, %3 offen sc1\n\ts_nop 1" :: "v"(v[it]), "v"(o), "s"(srd_ws), "s"(so) : "memory");
            }
            asm volatile("s_waitcnt vmcnt(8)" : "+v"(mykey) :: "memory");
            STAMP(4);
        } else {
            { const unsigned om = WS_KEY + b * 4; LD1(mykey, srd_ws, om, " sc1"); }
            if (t < 8) *(LAS u32x4*)(smem + L_CNT + t * 16) = u32x4{0u, 0u, 0u, 0u};
            WG_BARRIER();
            asm volatile("s_waitcnt vmcnt(0)" : "+v"(mykey) :: "memory");
            if (NP_SLEEP) __builtin_amdgcn_s_sleep(NP_SLEEP);
            const unsigned o = ((b >> 4) * 256 + t) * 128;
            set_m0(lds0 + L_DUMP + w * 256);
            STAMP(1);
            for (int r = 0; r < PF_R; ++r) {
                asm volatile("s_waitcnt vmcnt(%c0)" :: "i"(PF_D) : "memory");
                if (__builtin_amdgcn_s_memrealtime() > t_start + PF_T) break;
                const unsigned so = r << 19;
                asm volatile("buffer_load_dword %1, %0, %2 offen lds" :: "s"(srd_x0), "v"(o), "s"(so) : "memory");
            }
            if (PRIO) __builtin_amdgcn_s_setprio(2);
            STAMP(2); STAMP(3); STAMP(4);
        }
        STAMP(5);
        mykey = rfl(mykey) + KEY_STEP;
        loop_constants();
        unsigned ib = WS_INBOX + b * (G1 * 128) + t * 16;
        PIN(ib);
        while (__builtin_amdgcn_s_memrealtime() < t_start + POLL_T) __builtin_amdgcn_s_sleep(1);
        STAMP(6);
        u32x16 raw;
        inbox_request(raw, srd_ws, ib);
        inbox_stage(raw, srd_ws, ib, mykey, 16 * pc - 8, lds0 + L_HDR + (t >> 3) * 4);
        if (PRIO) __builtin_amdgcn_s_setprio(0);
        STAMP(7);
    } else {
        const int j = w - 4;
        if (isprod) zero_staging();
        WG_BARRIER();
        STAMP(1);
        u32x4 wr[8], xr[8];
        {
            const u32x4 srd_q = make_srd(wq, 32768);
            const unsigned o = j * 8192 + l * 16, k4 = 4096;
            LD4(wr[0], srd_q, o, ""); LD4(wr[1], srd_q, o, " offset:1024"); LD4(wr[2], srd_q, o, " offset:2048"); LD4(wr[3], srd_q, o, " offset:3072");
            LD4S(wr[4], srd_q, o, k4, ""); LD4S(wr[5], srd_q, o, k4, " offset:1024"); LD4S(wr[6], srd_q, o, k4, " offset:2048"); LD4S(wr[7], srd_q, o, k4, " offset:3072");
#pragma unroll
            for (int m = 0; m < 8; ++m) { const unsigned ox = node_row(m < 4 ? j : w, m & 3) + n * 16; LD4(xr[m], srd_x0, ox, ""); }
        }
        unsigned aqa[8], xfa[4];
        unsigned xa = ring + g * 256 + ((n ^ g) << 4), wa = lds0 + L_WQ + j * 16384 + g * 256 + ((n ^ g) << 4) + ((n & 4) << 1);
        {
            const unsigned aq = lds0 + L_WQ + (8 * g + q) * 256 + ((((4 * p) ^ (q | ((g & 1) << 2))) << 4) | ((p & 1) << 3));
#pragma unroll
            for (int s = 0; s < 4; ++s) { xfa[s] = ring + n * 256 + (((4 * s + g) ^ n) << 4); PIN(xfa[s]); }
#pragma unroll
            for (int c = 0; c < 8; ++c) { aqa[c] = aq ^ ((c >> 1) << 4) ^ ((c & 1) << 3); PIN(aqa[c]); }
            PIN(xa); PIN(wa);
        }
        STAMP(2);
        if (isprod) {
            WG_BARRIER();
            if (__builtin_expect(rfl(*(LAS unsigned*)(smem + L_CNT1 + 2048)) != 0, 0)) WG_BARRIER();
            if (W_SLEEP) __builtin_amdgcn_s_sleep(W_SLEEP);
        }
        STAMP(3);
        asm volatile("s_waitcnt vmcnt(8)" : "+v"(wr[0]), "+v"(wr[1]), "+v"(wr[2]), "+v"(wr[3]), "+v"(wr[4]), "+v"(wr[5]), "+v"(wr[6]), "+v"(wr[7]) :: "memory");
#pragma unroll
        for (int m = 0; m < 8; ++m) {
            const unsigned a = (wa ^ ((m & 2) << 5)) + m * 1024;
            *(LAS u32x2*)(size_t)a = u32x2{wr[m].x, wr[m].y};
            *(LAS u32x2*)(size_t)(a ^ 8) = u32x2{wr[m].z, wr[m].w};
        }
        asm volatile("s_waitcnt vmcnt(0)" : "+v"(xr[0]), "+v"(xr[1]), "+v"(xr[2]), "+v"(xr[3]), "+v"(xr[4]), "+v"(xr[5]), "+v"(xr[6]), "+v"(xr[7]) :: "memory");
#pragma unroll
        for (int m = 0; m < 8; ++m) *(LAS u32x4*)(size_t)((xa ^ ((m & 3) << 6)) + m * 1024) = xr[m];
        STAMP(4);
        lds_arrive(smem + L_CNT + 80, l);
        lds_await(smem + L_CNT + 80, 4);
        STAMP(5);
        bf16x8 xo[2][4], fr[2][8];
        f32x4 qc[2][8];
        auto wq_frags = [&](int u, bf16x8 (&f)[8]) {
#pragma unroll
            for (int a = 0; a < 8; ++a) {
                LAS char* pa = (LAS char*)(size_t)aqa[a] + u * 16384;
                f[a] = cat(tr_read(pa), tr_read(pa + 1024));
            }
        };
#pragma unroll
        for (int u = 0; u < 4; ++u) { xo[0][u] = *(LAS bf16x8*)(size_t)xfa[u]; xo[1][u] = *(LAS bf16x8*)(size_t)(xfa[u] + 4096); }
        wq_frags(0, fr[0]);
        {
            const Srd4 srd_v = make_srd4(wv, 32768);
            const unsigned r0 = (j * 32 + g) << 8, o0 = (n ^ ((g & 1) | ((g & 2) << 1))) << 4;
            const unsigned e[8] = {r0, r0 + 1024, r0 + 2048, r0 + 3072, r0 + 4096, r0 + 5120, r0 + 6144, r0 + 7168};
            const unsigned base = lds0 + (j < 2 ? L_WV0 + j * 16384 : L_WV1 + (j - 2) * 8192);
            const u32x4 off = {o0, o0 ^ 128, o0, o0 ^ 128};
            dma_half(base, srd_v, e, off); dma_half(base + 4096, srd_v, e + 4, off);
        }
#pragma unroll
        for (int u = 0; u < 4; ++u) {
            if (u < 3) wq_frags(u + 1, fr[(u + 1) & 1]);
            FENCE();
#pragma unroll
            for (int a = 0; a < 8; ++a)
#pragma unroll
                for (int tt = 0; tt < 2; ++tt) qc[tt][a] = mfma(fr[u & 1][a], xo[tt][u], u ? qc[tt][a] : f32x4{0.f, 0.f, 0.f, 0.f});
            FENCE();
            if (u == 1 && j < 2) { VMWAIT(0); lds_arrive(smem + L_CNT + 88, l); }
        }
        STAMP(6);
        if (isprod) lds_await(smem + L_CNT + 84, 4);
#pragma unroll
        for (int tt = 0; tt < 2; ++tt)
#pragma unroll
            for (int s = 0; s < 4; ++s) {
                const float k = -0.08838834764831845f * 1.4426950408889634f;
                const f32x4 c0 = qc[tt][2 * s] * k, c1 = qc[tt][2 * s + 1] * k;
                *(LAS u32x4*)(size_t)(ring - (tt ? 0 : 65536) + s * 1024 + l * 16) = u32x4{pk2(c0[0], c0[1]), pk2(c0[2], c0[3]), pk2(c1[0], c1[1]), pk2(c1[2], c1[3])};
            }
        loop_constants();
        lds_await(smem + L_CNT + 88, 2);
        wv_resident();
        VMWAIT(0);
        STAMP(7);
    }
    WG_BARRIER();
    STAMP(8);
    if (LPRIO >= 3) __builtin_amdgcn_s_setprio(3);
    if (LPRIO == 1 && w >= 4) __builtin_amdgcn_s_setprio(1);
    if (LPRIO == 2) { if (w >= 6) __builtin_amdgcn_s_setprio(3); else if (w >= 4) __builtin_amdgcn_s_setprio(2); else if (w >= 2) __builtin_amdgcn_s_setprio(1); }

    u32x16 qf;
    const u32x4 doff = {addr[12], addr[13], addr[14], addr[15]};
    LAS char* mylist = smem + L_LIST + w * (2 * SUBB);
    int gcur = G1, pos = 0;
    auto batch = [&](auto first_) {
        constexpr bool first = decltype(first_)::value;
        auto entries = [&](int grp, unsigned (&e)[8]) {
#pragma unroll
            for (int m = 0; m < 8; ++m) e[m] = *(LAS unsigned*)(size_t)(lista + grp * 128 + m * 16);
        };
        unsigned cw = *(LAS unsigned*)(size_t)cnta, e0[8], e1[8];
        entries(0, e0);
        if constexpr (first) {
#pragma unroll
            for (int s = 0; s < 4; ++s) {
                const u32x4 v = *(LAS u32x4*)(size_t)(qa + s * 1024);
                qf[4 * s] = v.x; qf[4 * s + 1] = v.y; qf[4 * s + 2] = v.z; qf[4 * s + 3] = v.w;
            }
        }
        entries(1, e1);
        FENCE();
        int lo = __builtin_amdgcn_readlane(cw, 2 * w) >> 2, hi = __builtin_amdgcn_readlane(cw, 2 * w + 1) >> 2;
        if constexpr (first) {
            if (__builtin_amdgcn_readlane(cw, 16)) gcur = 0;
            if (__builtin_amdgcn_ballot_w64(cw > 4u * CAP2)) { gcur = -1; lo = 0; hi = 0; }
        }
        const int c = lo + hi, G = (c + 31) >> 5;
        const int nlast = G > 2 ? (((c - 1) & 31) >> 2) + 1 : 8;
        auto join = [&]() {
            for (int k = 0; k < G * 32 - lo; k += 64) {
                const unsigned e = *(LAS unsigned*)(mylist + SUBB + (l + k) * 4);
                if (l + k < G * 32 - lo) *(LAS unsigned*)(mylist + (lo + l + k) * 4) = l + k < hi ? e : PAD;
            }
        };
        const bool early = lo >= 64;
        if (__builtin_expect(!early, 0)) { join(); entries(0, e0); entries(1, e1); }
        if constexpr (first) asm volatile("s_waitcnt lgkmcnt(0)" : "+{v[160:175]}"(qf) :: "memory");
        if (G > 0) dma_group(ring, srd_x, e0, doff);
        if constexpr (first) {
            if (w < 4) {
                if (G > 1) dma_group(ring + 8192, srd_x, e1, doff);
                wv_resident();
                lds_arrive(smem + L_CNT + 92, l);
            } else {
                if (w >= 6) lds_await(smem + L_CNT + 92, 4);
                if (G > 1) dma_group(ring + 8192, srd_x, e1, doff);
            }
        } else if (G > 1) dma_group(ring + 8192, srd_x, e1, doff);
        f32x16 cc;
#pragma unroll
        for (int m = 0; m < 8; ++m) {
            cc[m] = (e0[m] & 0xFF) == keyn ? 0.f : __uint_as_float(inf);
            cc[8 + m] = (e1[m] & 0xFF) == keyn ? 0.f : __uint_as_float(inf);
        }
        if (early) join();
        STAMP(9);
        if (G > 0) gather_loop(acc, qf, addr, u32x4{lista, keyn, inf, 0u}, cc, srd_x, ring, G, nlast);
    };
    batch(std::true_type{});
    while (__builtin_expect(gcur < G1, 0)) {
        unsigned lim = G1 * 32;
        if (gcur >= 0) {
            const u64 m0 = __builtin_amdgcn_ballot_w64(l >= gcur && *(LAS unsigned*)(smem + L_HDR + l * 4) > 4u * CAP1);
            const u64 m1 = __builtin_amdgcn_ballot_w64(l + 64 >= gcur && *(LAS unsigned*)(smem + L_HDR + 256 + l * 4) > 4u * CAP1);
            if (!(m0 | m1)) break;
            gcur = m0 ? __builtin_ctzll(m0) : 64 + __builtin_ctzll(m1);
            unsigned v; const unsigned o = WS_NOVF + gcur * 4;
            asm volatile("buffer_load_dword %0, %1, %2, 0 offen sc1\n\ts_waitcnt vmcnt(0)" : "=&v"(v) : "v"(o), "s"(srd_ws) : "memory");
            lim = rfl(v);
        }
        const unsigned i = pos + t;
        const bool cand = t < CAP2 && i < lim;
        unsigned u;
        { const unsigned o = cand ? (gcur < 0 ? WS_INBOX + b * (G1 * 128) : WS_OVF + gcur * CH * 4) + i * 4 : OOB;
          asm volatile("buffer_load_dword %0, %1, %2, 0 offen sc1\n\ts_waitcnt vmcnt(0)" : "=&v"(u) : "v"(o), "s"(srd_ws) : "memory"); }
        WG_BARRIER();
        if (t < 16) *(LAS unsigned*)(smem + L_CNT + t * 4) = 0;
        WG_BARRIER();
        if (cand) {
            unsigned e; bool v;
            if (gcur < 0) { e = u ^ mykey; v = ((i & 31) - 2) * 4 < *(LAS unsigned*)(smem + L_HDR + (i >> 5) * 4); }
            else { e = (((u >> 16) + BIAS) << 8) | ((u >> 8) & 0xFF); v = (u & 0xFF) == (unsigned)b; }
            if (v) *(LAS unsigned*)(smem + L_LIST + (e & 7) * (2 * SUBB) + lds_add(smem + L_CNT + (e & 7) * 8, 4)) = e;
        }
        pos += CAP2;
        if ((unsigned)pos >= lim) { ++gcur; pos = 0; }
        WG_BARRIER();
        batch(std::false_type{});
    }
    STAMP(10);

    {
        bf16x8 wl[2][8], ag[4];
#pragma unroll
        for (int u = 0; u < 2; ++u)
#pragma unroll
            for (int a = 0; a < 8; ++a) {
                LAS char* pa = smem + L_WV1 + (cv ^ ((a >> 1) << 6) ^ ((a & 1) << 3)) + u * 8192;
                wl[u][a] = cat(tr_read(pa), tr_read(pa + 4096));
            }
#pragma unroll
        for (int u = 0; u < 4; ++u) {
            const u32x4 v = {pk2(acc[8 * u], acc[8 * u + 1]), pk2(acc[8 * u + 2], acc[8 * u + 3]),
                             pk2(acc[8 * u + 4], acc[8 * u + 5]), pk2(acc[8 * u + 6], acc[8 * u + 7])};
            ag[u] = __builtin_bit_cast(bf16x8, v);
        }
        FENCE();
        f32x4 oc[8];
#pragma unroll
        for (int a = 0; a < 8; ++a) oc[a] = mfma(__builtin_bit_cast(bf16x8, f32x4{wva[4 * a], wva[4 * a + 1], wva[4 * a + 2], wva[4 * a + 3]}), ag[0], f32x4{0.f, 0.f, 0.f, 0.f});
#pragma unroll
        for (int a = 0; a < 8; ++a) oc[a] = mfma(__builtin_bit_cast(bf16x8, f32x4{wvb[4 * a], wvb[4 * a + 1], wvb[4 * a + 2], wvb[4 * a + 3]}), ag[1], oc[a]);
#pragma unroll
        for (int a = 0; a < 8; ++a) oc[a] = mfma(wl[0][a], ag[2], oc[a]);
        FENCE();
        const unsigned ow = ring + n * 256 + ((g ^ n) << 4) + ((g & 1) << 3);
#pragma unroll
        for (int a = 0; a < 8; ++a) oc[a] = mfma(wl[1][a], ag[3], oc[a]);
#pragma unroll
        for (int a = 0; a < 8; ++a)
            *(LAS u32x2*)(size_t)(ow ^ ((a >> 1) << 6) ^ ((a & 1) << 3)) = u32x2{pk2(oc[a][0], oc[a][1]), pk2(oc[a][2], oc[a][3])};
        const auto rs_o = __builtin_amdgcn_make_buffer_rsrc((void*)out, 0, xbytes, 0x00020000);
#pragma unroll
        for (int m = 0; m < 4; ++m) {
            const u32x4 v = *(LAS u32x4*)(size_t)(ring + m * 1024 + l * 16);
            __builtin_amdgcn_raw_buffer_store_b128(v, rs_o, node_row(w, m) + ((n ^ g ^ (4 * m)) << 4), 0, OUT_AUX);
        }
    }
    if (w == 0) { const unsigned o = l == 0 ? WS_KEY + b * 4 : OOB; ST1(mykey, srd_ws, o, " sc1"); }
    STAMP(11);
}

extern "C" void kernel_launch(void* const* d_in, const int* in_sizes, int n_in,
                              void* d_out, int out_size, void* d_ws, size_t ws_size,
                              hipStream_t stream) {
    const int N = in_sizes[0] / 128, E = in_sizes[1];
    k_graph<<<NB, 512, 0, stream>>>((const unsigned short*)d_in[0], (const int*)d_in[1], (const int*)d_in[2],
                                    (const unsigned short*)d_in[3], (const unsigned short*)d_in[4],
                                    (unsigned*)d_ws, (unsigned short*)d_out, N, E);
}
```

```cpp
#define STAMPS
#include <hip/hip_runtime.h>
#include <hip/hip_bf16.h>
#include <stdint.h>
#include <type_traits>
#define ORD 1
#define SKIP 0
#define STR2(x) #x
#define STR(x) STR2(x)

typedef __attribute__((ext_vector_type(8))) __bf16 bf16x8;
typedef __attribute__((ext_vector_type(4))) __bf16 bf16x4;
typedef __attribute__((ext_vector_type(2))) __bf16 bf16x2;
typedef __attribute__((ext_vector_type(4))) float f32x4;
typedef __attribute__((ext_vector_type(2))) float f32x2;
typedef __attribute__((ext_vector_type(4))) unsigned u32x4;
typedef __attribute__((ext_vector_type(2))) unsigned u32x2;
typedef __attribute__((ext_vector_type(32))) float f32x32;
typedef __attribute__((ext_vector_type(16))) float f32x16;
typedef __attribute__((ext_vector_type(16))) unsigned u32x16;
typedef __attribute__((ext_vector_type(12))) unsigned u32x12;
typedef unsigned long long u64;
#define LAS __attribute__((address_space(3)))

constexpr int NB = 256;
constexpr int G1 = 128;
constexpr int CH = 3136;
constexpr int CAP1 = 30;
constexpr int CAP2 = 192;
constexpr int SUBB = 784;
#define POLL_AT 3
#define OUT_AUX 16
#define PF_R 0
#define PF_D 0
#define POLL_T 260
#define PF_T 200
#define WMODE 2
#define LPRIO 3
#define PKADD 1
#define NP_SLEEP 0
#define W_SLEEP 3
#define PRIO 1
#define SPLIT 16384
constexpr unsigned KEY_STEP = 0x9E3779B1u;
constexpr unsigned WS_KEY = 0;
constexpr unsigned WS_NOVF = 2048;
constexpr unsigned WS_INBOX = 4096;
constexpr unsigned WS_OVF = WS_INBOX + NB * G1 * 128;
constexpr unsigned WS_BYTES = WS_OVF + G1 * CH * 4;
constexpr unsigned OOB = 0x80000000u;
constexpr unsigned BIAS = 32768 - SPLIT;
constexpr unsigned PAD = 0x00FFFFFFu;

constexpr int L_CNT = 0;
constexpr int L_HDR = 128;
constexpr int L_LIST = 768;
constexpr int L_RING = L_LIST + 16 * SUBB;
constexpr int L_IMG = L_RING;
constexpr int L_CNT1 = L_IMG + NB * 144;
constexpr int L_WQ = L_RING + 2 * 16384 + 8192;
constexpr int L_WV0 = L_RING + 6 * 16384 + 8192;
constexpr int L_WV1 = L_RING + 131072;
constexpr int L_DUMP = L_WV1 + 16384;
constexpr int L_TOTAL = L_DUMP + 2048;
static_assert(L_RING % 256 == 0 && L_TOTAL <= 163840, "LDS map");

#define STAMP_N 24
#define STAMP_WGS 256
#define STAMP_OFF (200u << 20)
#define STAMP_ROLE 0
#define STAMP_VALID(w, k) ((k) < 14 && ((STAMP_MASK >> (k)) & 1) && ((k) == 0 || STAMP_ROLE == 2 || ((((w) >> 3) & 1) == STAMP_ROLE)))
#define STAMP_DECL unsigned long long st_[STAMP_N] = {}
#define STAMP_MASK 0xFFFFFF
#define STAMP(k) do { if (!((STAMP_MASK >> (k)) & 1)) break; __builtin_amdgcn_sched_barrier(0); asm volatile("s_memrealtime %0\n\ts_waitcnt lgkmcnt(0)" : "=s"(st_[k]) :: "memory"); __builtin_amdgcn_sched_barrier(0); } while (0)
#define STAMP_THREAD 0
#define STAMP_FLUSH(base, k0, k1) do { if (threadIdx.x == STAMP_THREAD) { _Pragma("unroll") for (int k_ = k0; k_ < k1; ++k_) ((unsigned long long*)((char*)(base) + STAMP_OFF))[(size_t)blockIdx.x * STAMP_N + k_] = st_[k_]; } } while (0)

__device__ __forceinline__ u32x4 make_srd(const void* p, unsigned bytes, unsigned back = 0) {
    const u64 a = (u64)p - back;
    u32x4 s; s.x = (unsigned)a; s.y = (unsigned)(a >> 32) & 0xFFFFu; s.z = bytes + back; s.w = 0x00020000u; return s;
}
#define LD4(dst, srd, off, FL) asm volatile("buffer_load_dwordx4 %0, %1, %2, 0 offen" FL : "=v"(dst) : "v"(off), "s"(srd) : "memory")
#define LD4S(dst, srd, off, so, FL) asm volatile("buffer_load_dwordx4 %0, %1, %2, %3 offen" FL : "=v"(dst) : "v"(off), "s"(srd), "s"(so) : "memory")
#define LD1(dst, srd, off, FL) asm volatile("buffer_load_dword %0, %1, %2, 0 offen" FL : "=v"(dst) : "v"(off), "s"(srd) : "memory")
#define ST1(val, srd, off, FL) asm volatile("buffer_store_dword %0, %1, %2, 0 offen" FL :: "v"(val), "v"(off), "s"(srd) : "memory")
#define PIN(x) asm volatile("" : "+v"(x))
#define VMWAIT(n) asm volatile("s_waitcnt vmcnt(" #n ")" ::: "memory")
#define FENCE() __builtin_amdgcn_sched_barrier(0)
#define WG_BARRIER() do { asm volatile("s_waitcnt lgkmcnt(0)" ::: "memory"); __builtin_amdgcn_s_barrier(); asm volatile("" ::: "memory"); } while (0)

struct Srd4 { u32x4 s0, s1, s2, s3; };
__device__ __forceinline__ Srd4 make_srd4(const void* p, unsigned bytes, unsigned back = 0) {
    return Srd4{make_srd(p, bytes, back), make_srd(p, bytes, back + 1024), make_srd(p, bytes, back + 2048), make_srd(p, bytes, back + 3072)};
}

__device__ __forceinline__ void set_m0(unsigned v) { asm volatile("s_mov_b32 m0, %0" :: "s"(v) : "memory"); }
template <int R> __device__ __forceinline__ void dma_rows(const Srd4& d, unsigned e, unsigned off) {
    unsigned tmp, mask = 0x00FFFF00u;
    asm volatile("v_and_or_b32 %0, %1, %2, %3\n\tbuffer_load_dwordx4 %0, %4, 0 offen offset:%c5 lds"
                 : "=&v"(tmp) : "v"(e), "s"(mask), "v"(off), "s"(R == 0 ? d.s0 : R == 1 ? d.s1 : R == 2 ? d.s2 : d.s3), "i"(R * 1024) : "memory");
}
__device__ __forceinline__ void dma_half(unsigned ldsbase, const Srd4& d, const unsigned* e, const u32x4& off) {
    set_m0(ldsbase);
    dma_rows<0>(d, e[0], off.x); dma_rows<1>(d, e[1], off.y); dma_rows<2>(d, e[2], off.z); dma_rows<3>(d, e[3], off.w);
}
__device__ __forceinline__ void dma_group(unsigned ldsbase, const Srd4& d, const unsigned (&e)[8], const u32x4& off) {
    asm volatile("s_waitcnt lgkmcnt(0)" ::: "memory");
    dma_half(ldsbase, d, e, off); dma_half(ldsbase + 4096, d, e + 4, off);
}

__device__ __forceinline__ void gather_loop(f32x32& acc, const u32x16& qf, const u32x16& addr, u32x4 misc, f32x16 cc,
                                            const Srd4& d, unsigned ring, int G, int nlast) {
    f32x32 t0, t1; u32x12 t2; int tmp, pr; u64 sm; unsigned mask = 0x00FFFF00u;
    asm volatile(
        ".macro GWAIT k\n\ts_cmp_gt_i32 %[t], \\k\n\ts_cbranch_scc1 1f\n\ts_waitcnt vmcnt(\\k)\n\t.endm\n\t"
        ".macro GROWS so\n\t.irp s, 0, 1, 2, 3\n\t"
        "ds_read_b128 v[64+8*\\s:67+8*\\s], v[176+\\s] offset:\\so\n\t"
        "ds_read_b128 v[68+8*\\s:71+8*\\s], v[176+\\s] offset:\\so+4096\n\t.endr\n\t.endm\n\t"
        ".macro GTR c, so\n\t"
        "ds_read_b64_tr_b16 v[96+4*\\c:97+4*\\c], v[180+\\c] offset:\\so\n\t"
        "ds_read_b64_tr_b16 v[98+4*\\c:99+4*\\c], v[180+\\c] offset:\\so+4096\n\t.endm\n\t"
        ".macro GS s, c\n\t"
        "v_mfma_f32_16x16x32_bf16 v[\\c:\\c+3], v[64+8*\\s:67+8*\\s], v[160+4*\\s:163+4*\\s], v[\\c:\\c+3]\n\t"
        "v_mfma_f32_16x16x32_bf16 v[\\c+4:\\c+7], v[68+8*\\s:71+8*\\s], v[160+4*\\s:163+4*\\s], v[\\c+4:\\c+7]\n\t.endm\n\t"
        ".macro GLOAD m, r, srd\n\t"
        "v_and_or_b32 v195, v[212+\\m], %[mask], v[188+\\r]\n\t"
        "buffer_load_dwordx4 v195, \\srd, 0 offen offset:1024*\\r lds\n\t.endm\n\t"
        ".macro GPART m, r, srd\n\ts_cmp_le_i32 %[nl], \\m\n\ts_cbranch_scc1 4f\n\tGLOAD \\m, \\r, \\srd\n\t.endm\n\t"
        ".macro GSUM m\n\tv_mfma_f32_16x16x32_bf16 v[128+4*(\\m):131+4*(\\m)], v[96+4*(\\m):99+4*(\\m)], v[220:223], v[128+4*(\\m):131+4*(\\m)]\n\t.endm\n\t"
        ".macro GCMP m, d\n\tv_cmp_ne_u32_sdwa \\d, v[212+\\m], v193 src0_sel:BYTE_0 src1_sel:DWORD\n\t.endm\n\t"
        ".macro GPAIR m, c\n\t"
        "GSUM \\m\n\tGCMP \\m+1, %[sm]\n\tv_cndmask_b32_e32 v[\\c+\\m], 0, v194, vcc\n\t"
        "GSUM \\m+1\n\t.if \\m < 6\n\tGCMP \\m+2, vcc\n\t.else\n\ts_cmp_gt_i32 %[rem], 0\n\t.endif\n\t"
        "v_cndmask_b32_e64 v[\\c+\\m+1], 0, v194, %[sm]\n\t.endm\n\t"
        ".macro GSTEP so, c, eo\n\t"
        "GTR 0, \\so\n\t"
        "s_waitcnt lgkmcnt(2)\n\tGS 0, \\c\n\t"
        "GTR 1, \\so\n\tGTR 2, \\so\n\tGS 1, \\c\n\t"
        "GTR 3, \\so\n\tGTR 4, \\so\n\tGS 2, \\c\n\t"
        "GTR 5, \\so\n\tGTR 6, \\so\n\tGS 3, \\c\n\t"
        "GTR 7, \\so\n\t"
        "ds_read2_b32 v[212:213], v192 offset0:\\eo offset1:\\eo+4\n\t"
        "ds_read2_b32 v[214:215], v192 offset0:\\eo+8 offset1:\\eo+12\n\t"
        "ds_read2_b32 v[216:217], v192 offset0:\\eo+16 offset1:\\eo+20\n\t"
        "ds_read2_b32 v[218:219], v192 offset0:\\eo+24 offset1:\\eo+28\n\t"
        "s_add_u32 m0, %[ring], \\so\n\t"
        "s_cmp_gt_i32 %[rem], 3\n\t"
        ".irp k, 0, 1, 2, 3, 4, 5, 6, 7\n\tv_exp_f32_e32 v[\\c+\\k], v[\\c+\\k]\n\t.endr\n\t"
        "s_waitcnt lgkmcnt(0)\n\t"
        "s_cbranch_scc0 2f\n\t"
        "GLOAD 0, 0, %[s0]\n\tGLOAD 1, 1, %[s1]\n\tGLOAD 2, 2, %[s2]\n\tGLOAD 3, 3, %[s3]\n\t"
        "s_add_u32 m0, m0, 0x1000\n\t"
        "GLOAD 4, 0, %[s0]\n\tGLOAD 5, 1, %[s1]\n\tGLOAD 6, 2, %[s2]\n\tGLOAD 7, 3, %[s3]\n\t"
        "GSIG \\c\n\t"
        "s_waitcnt vmcnt(8)\n\t"
        "s_branch 1f\n\t"
        "2:\n\t"
        "s_mov_b32 %[t], 0\n\t"
        "s_cmp_eq_u32 %[rem], 3\n\ts_cbranch_scc0 4f\n\t"
        "s_mov_b32 %[t], %[nl]\n\t"
        "GLOAD 0, 0, %[s0]\n\tGPART 1, 1, %[s1]\n\tGPART 2, 2, %[s2]\n\tGPART 3, 3, %[s3]\n\t"
        "s_add_u32 m0, m0, 0x1000\n\t"
        "GPART 4, 0, %[s0]\n\tGPART 5, 1, %[s1]\n\tGPART 6, 2, %[s2]\n\tGPART 7, 3, %[s3]\n\t"
        "4:\n\t"
        "GSIG \\c\n\t"
        "s_waitcnt vmcnt(8)\n\t"
        "GWAIT 7\n\tGWAIT 6\n\tGWAIT 5\n\tGWAIT 4\n\tGWAIT 3\n\tGWAIT 2\n\tGWAIT 1\n\tGWAIT 0\n\t"
        "1:\n\t"
        "GROWS 8192-\\so\n\t"
        "GCMP 0, vcc\n\t"
        "s_add_i32 %[rem], %[rem], -1\n\t"
        "GPAIR 0, \\c\n\tGPAIR 2, \\c\n\tGPAIR 4, \\c\n\tGPAIR 6, \\c\n\t"
        ".endm\n\t"
        ".macro GSIG c\n\t"
        ".if " STR(PKADD) "\n\t.irp k, 0, 2, 4, 6\n\tv_pk_add_f32 v[\\c+\\k:\\c+\\k+1], v[\\c+\\k:\\c+\\k+1], 1.0 op_sel_hi:[1,0]\n\t.endr\n\t.else\n\t"
        ".irp k, 0, 1, 2, 3, 4, 5, 6, 7\n\tv_add_f32_e32 v[\\c+\\k], 1.0, v[\\c+\\k]\n\t.endr\n\t.endif\n\t"
        ".irp k, 0, 1, 2, 3, 4, 5, 6, 7\n\tv_rcp_f32_e32 v[\\c+\\k], v[\\c+\\k]\n\t.endr\n\t"
        ".irp k, 0, 1, 2, 3\n\tv_cvt_pk_bf16_f32 v[220+\\k], v[\\c+2*\\k], v[\\c+2*\\k+1]\n\t.endr\n\t.endm\n\t"
        "s_waitcnt vmcnt(8)\n\t"
        "s_cmp_gt_i32 %[rem], 1\n\ts_cbranch_scc1 1f\n\ts_waitcnt vmcnt(0)\n\t1:\n\t"
        "GROWS 0\n\t"
        ".if " STR(LPRIO) " >= 3\n\ts_mov_b32 %[pr], 0\n\ts_setprio 2\n\t.endif\n\t"
        "0:\n\t"
        ".if " STR(LPRIO) " == 3\n\t"
        "s_cmp_eq_u32 %[pr], 1\n\ts_cbranch_scc0 5f\n\ts_setprio 1\n\t5:\n\t"
        "s_cmp_eq_u32 %[pr], 2\n\ts_cbranch_scc0 5f\n\ts_setprio 0\n\t5:\n\t"
        "s_add_u32 %[pr], %[pr], 1\n\t.endif\n\t"
        "GSTEP 0, 196, 64\n\t"
        "s_cbranch_scc0 9f\n\t"
        "GSTEP 8192, 204, 96\n\t"
        "v_add_u32_e32 v192, 0x100, v192\n\t"
        "s_cbranch_scc1 0b\n\t"
        "9:\n\t"
        "s_waitcnt lgkmcnt(0)\n\ts_nop 6\n\t"
        ".purgem GWAIT\n\t.purgem GROWS\n\t.purgem GTR\n\t.purgem GS\n\t.purgem GLOAD\n\t.purgem GPART\n\t.purgem GSUM\n\t.purgem GCMP\n\t.purgem GPAIR\n\t.purgem GSTEP\n\t.purgem GSIG"
        : "+{v[128:159]}"(acc), "+{v[192:195]}"(misc), "+{v[196:211]}"(cc), "=&{v[64:95]}"(t0), "=&{v[96:127]}"(t1), "=&{v[212:223]}"(t2),
          [rem] "+s"(G), [t] "=&s"(tmp), [pr] "=&s"(pr), [sm] "=&s"(sm)
        : "{v[160:175]}"(qf), "{v[176:191]}"(addr), [s0] "s"(d.s0), [s1] "s"(d.s1), [s2] "s"(d.s2), [s3] "s"(d.s3),
          [mask] "s"(mask), [ring] "s"(ring), [nl] "s"(nlast)
        : "vcc", "scc", "memory");
}

__device__ __forceinline__ void inbox_request(u32x16& raw, const u32x4& srd, unsigned ib) {
    unsigned o1 = 0x1000, o2 = 0x2000, o3 = 0x3000;
    asm volatile("buffer_load_dwordx4 v[64:67], %1, %2, 0 offen sc1\n\tbuffer_load_dwordx4 v[68:71], %1, %2, %3 offen sc1\n\t"
                 "buffer_load_dwordx4 v[72:75], %1, %2, %4 offen sc1\n\tbuffer_load_dwordx4 v[76:79], %1, %2, %5 offen sc1"
                 : "=&{v[64:79]}"(raw) : "v"(ib), "s"(srd), "s"(o1), "s"(o2), "s"(o3) : "memory");
}
__device__ __forceinline__ void inbox_stage(u32x16 raw, const u32x4& srd, unsigned ib, unsigned key, int i4, unsigned hdra) {
    u32x16 t0; u32x4 t1; u64 m0, m1, m2, m3; unsigned todo, fresh, spin, o1, o2, o3;
    u64 pc0 = 0x0101010101010101ull;
    asm volatile(
        ".macro IVAL j, so\n\t"
        "s_bitcmp1_b32 %[todo], \\j\n\ts_cbranch_scc0 2f\n\t"
        ".irp k, 0, 1, 2, 3\n\tv_xor_b32_e32 v[64+4*\\j+\\k], %[key], v[64+4*\\j+\\k]\n\t.endr\n\t"
        "v_mov_b32_dpp v[80+\\j], v[64+4*\\j] quad_perm:[0,0,0,0] row_mask:0xf bank_mask:0xf\n\t"
        "v_add_u32_e32 v96, v[64+4*\\j], v[65+4*\\j]\n\t"
        "v_sub_u32_e32 v97, 0, v[65+4*\\j]\n\t"
        "v_mov_b32_dpp v[80+\\j], v[80+\\j] row_shr:4 row_mask:0xf bank_mask:0xa\n\t"
        "v_cndmask_b32_e64 v96, v96, v97, %[pc0]\n\t"
        "v_cmp_gt_i32_e32 vcc, v[80+\\j], v92\n\t"
        "v_add3_u32 v96, v96, v[66+4*\\j], v[67+4*\\j]\n\t"
        "v_lshrrev_b32_e32 v97, 14, v[80+\\j]\n\t"
        "v_cndmask_b32_e32 v96, 0, v96, vcc\n\t"
        "s_nop 1\n\tv_add_u32_dpp v96, v96, v96 quad_perm:[1,0,3,2] row_mask:0xf bank_mask:0xf\n\t"
        "s_nop 1\n\tv_add_u32_dpp v96, v96, v96 quad_perm:[2,3,0,1] row_mask:0xf bank_mask:0xf\n\t"
        "s_nop 1\n\tv_add_u32_dpp v96, v96, v96 row_half_mirror row_mask:0xf bank_mask:0xf\n\t"
        "v_or_b32_e32 v96, v96, v97\n\t"
        "v_cmp_ne_u32_e32 vcc, 0, v96\n\t"
        "s_cbranch_vccz 1f\n\t"
        "buffer_load_dwordx4 v[64+4*\\j:67+4*\\j], %[ib], %[srd], \\so offen sc1\n\t"
        "s_branch 2f\n\t"
        "1:\n\ts_bitset1_b32 %[fresh], \\j\n\t"
        "2:\n\t.endm\n\t"
        ".macro IRANK j\n\t"
        "s_bitcmp1_b32 %[fresh], \\j\n\ts_cbranch_scc0 2f\n\t"
        "v_cmp_lt_u32_e64 %[m0], v92, v[80+\\j]\n\tv_cmp_lt_u32_e64 %[m1], v93, v[80+\\j]\n\t"
        "v_cmp_lt_u32_e64 %[m2], v94, v[80+\\j]\n\tv_cmp_lt_u32_e64 %[m3], v95, v[80+\\j]\n\t"
        ".irp k, 0, 1, 2, 3\n\tv_lshrrev_b32_e32 v[84+\\k], 21, v[64+4*\\j+\\k]\n\t.endr\n\t"
        ".irp k, 0, 1, 2, 3\n\tv_and_b32_e32 v[84+\\k], 60, v[84+\\k]\n\t.endr\n\t"
        "v_cmp_lt_u32_e32 vcc, %c[cap], v[80+\\j]\n\t"
        "s_mov_b64 exec, %[m0]\n\tds_add_rtn_u32 v88, v84, v98 offset:%c[cnt]\n\t"
        "s_mov_b64 exec, %[m1]\n\tds_add_rtn_u32 v89, v85, v98 offset:%c[cnt]\n\t"
        "s_mov_b64 exec, %[m2]\n\tds_add_rtn_u32 v90, v86, v98 offset:%c[cnt]\n\t"
        "s_mov_b64 exec, %[m3]\n\tds_add_rtn_u32 v91, v87, v98 offset:%c[cnt]\n\t"
        "s_mov_b64 exec, %[pc0]\n\tds_write_b32 %[hdra], v[80+\\j] offset:128*\\j\n\t"
        "s_mov_b64 exec, -1\n\t"
        "s_cbranch_vccz 1f\n\t"
        "v_mov_b32_e32 v96, 0\n\tds_write_b32 v96, v98 offset:%c[cnt]+64\n\ts_waitcnt lgkmcnt(0)\n\t"
        "1:\n\ts_waitcnt lgkmcnt(1)\n\t"
        ".irp k, 0, 1, 2, 3\n\tv_min_u32_e32 v[88+\\k], %c[sub]-4, v[88+\\k]\n\t.endr\n\t"
        ".irp k, 0, 1, 2, 3\n\tv_mad_u32_u24 v[88+\\k], v[84+\\k], %[sub4], v[88+\\k]\n\t.endr\n\t"
        "s_mov_b64 exec, %[m0]\n\tds_write_b32 v88, v[64+4*\\j] offset:%c[list]\n\t"
        "s_mov_b64 exec, %[m1]\n\tds_write_b32 v89, v[65+4*\\j] offset:%c[list]\n\t"
        "s_mov_b64 exec, %[m2]\n\tds_write_b32 v90, v[66+4*\\j] offset:%c[list]\n\t"
        "s_mov_b64 exec, %[m3]\n\tds_write_b32 v91, v[67+4*\\j] offset:%c[list]\n\t"
        "s_mov_b64 exec, -1\n\t"
        "2:\n\t.endm\n\t"
        "s_movk_i32 %[o1], 0x1000\n\ts_movk_i32 %[o2], 0x2000\n\ts_movk_i32 %[o3], 0x3000\n\t"
        "v_mov_b32_e32 v92, %[i4]\n\tv_add_u32_e32 v93, 4, %[i4]\n\tv_add_u32_e32 v94, 8, %[i4]\n\tv_add_u32_e32 v95, 12, %[i4]\n\t"
        "v_mov_b32_e32 v98, 4\n\t"
        "s_mov_b32 %[todo], 15\n\ts_mov_b32 %[spin], 0x400000\n\t"
        "0:\n\t"
        "s_mov_b32 %[fresh], 0\n\t"
        "s_waitcnt vmcnt(3)\n\ts_cmp_eq_u32 %[todo], 15\n\ts_cbranch_scc1 3f\n\ts_waitcnt vmcnt(0)\n\t3:\n\t"
        "IVAL 0, 0\n\tIRANK 0\n\t"
        "s_waitcnt vmcnt(2)\n\tIVAL 1, %[o1]\n\tIRANK 1\n\t"
        "s_waitcnt vmcnt(1)\n\tIVAL 2, %[o2]\n\tIRANK 2\n\t"
        "s_cmp_eq_u32 %[todo], 15\n\ts_cbranch_scc0 3f\n\ts_waitcnt vmcnt(0)\n\t3:\n\tIVAL 3, %[o3]\n\tIRANK 3\n\t"
        "s_andn2_b32 %[todo], %[todo], %[fresh]\n\t"
        "s_cbranch_scc0 9f\n\t"
        "s_sub_u32 %[spin], %[spin], 1\n\t"
        "s_cbranch_scc0 0b\n\t"
        "s_waitcnt vmcnt(0)\n\t"
        "9:\n\t"
        ".purgem IVAL\n\t.purgem IRANK"
        : "+{v[64:79]}"(raw), "=&{v[80:95]}"(t0), "=&{v[96:99]}"(t1), [m0] "=&s"(m0), [m1] "=&s"(m1), [m2] "=&s"(m2), [m3] "=&s"(m3),
          [todo] "=&s"(todo), [fresh] "=&s"(fresh), [spin] "=&s"(spin), [o1] "=&s"(o1), [o2] "=&s"(o2), [o3] "=&s"(o3)
        : [srd] "s"(srd), [ib] "v"(ib), [key] "s"(key), [i4] "v"(i4), [hdra] "v"(hdra), [pc0] "s"(pc0), [sub4] "s"(SUBB / 4),
          [cnt] "i"(L_CNT), [list] "i"(L_LIST), [sub] "i"(SUBB), [cap] "i"(4 * CAP1)
        : "vcc", "scc", "memory");
}

template <int CTRL, int BANK> __device__ __forceinline__ unsigned dpp(unsigned old, unsigned v) { return __builtin_amdgcn_update_dpp(old, v, CTRL, 0xF, BANK, true); }
__device__ __forceinline__ unsigned bcast8(unsigned v) { v = dpp<0x00, 0xF>(0u, v); return __builtin_amdgcn_update_dpp(v, v, 0x114, 0xF, 0xA, false); }
__device__ __forceinline__ unsigned sum8(unsigned v) { v += dpp<0xB1, 0xF>(0u, v); v += dpp<0x4E, 0xF>(0u, v); return v + dpp<0x141, 0xF>(0u, v); }
__device__ __forceinline__ bf16x4 tr_read(LAS char* p) { return __builtin_amdgcn_ds_read_tr16_b64_v4bf16((LAS bf16x4*)p); }
__device__ __forceinline__ bf16x8 cat(bf16x4 a, bf16x4 b) { return __builtin_shufflevector(a, b, 0, 1, 2, 3, 4, 5, 6, 7); }
__device__ __forceinline__ unsigned pk2(float a, float b) { f32x2 v = {a, b}; bf16x2 r = __builtin_convertvector(v, bf16x2); return __builtin_bit_cast(unsigned, r); }
__device__ __forceinline__ f32x4 mfma(bf16x8 a, bf16x8 b, f32x4 c) { return __builtin_amdgcn_mfma_f32_16x16x32_bf16(a, b, c, 0, 0, 0); }
__device__ __forceinline__ unsigned lds_add(LAS char* p, unsigned v) { return __hip_atomic_fetch_add((LAS unsigned*)p, v, __ATOMIC_RELAXED, __HIP_MEMORY_SCOPE_WORKGROUP); }
__device__ __forceinline__ LAS unsigned* ldsp(unsigned a) { return (LAS unsigned*)(size_t)a; }
__device__ __forceinline__ unsigned byte0_x4(unsigned v) { unsigned r; asm("v_lshlrev_b32_sdwa %0, 2, %1 dst_sel:DWORD dst_unused:UNUSED_PAD src0_sel:DWORD src1_sel:BYTE_0" : "=v"(r) : "v"(v)); return r; }
__device__ __forceinline__ unsigned mad24(unsigned a, unsigned b, unsigned c) { unsigned r; asm("v_mad_u32_u24 %0, %1, %2, %3" : "=v"(r) : "v"(a), "s"(b), "v"(c)); return r; }
__device__ __forceinline__ void lds_arrive(LAS char* p, int lane) {
    asm volatile("s_waitcnt lgkmcnt(0)" ::: "memory");
    if (lane == 0) __hip_atomic_fetch_add((LAS unsigned*)p, 1u, __ATOMIC_RELAXED, __HIP_MEMORY_SCOPE_WORKGROUP);
}
__device__ __forceinline__ void lds_await(LAS char* p, unsigned n) {
    while (__builtin_amdgcn_readfirstlane(*(volatile LAS unsigned*)p) < n) __builtin_amdgcn_s_sleep(1);
    asm volatile("" ::: "memory");
}
__device__ __forceinline__ unsigned rfl(unsigned v) { return __builtin_amdgcn_readfirstlane(v); }

__global__ __launch_bounds__(512, 2) void k_graph(const unsigned short* __restrict__ x, const int* __restrict__ src, const int* __restrict__ dst,
                                                  const unsigned short* __restrict__ wq, const unsigned short* __restrict__ wv,
                                                  unsigned* __restrict__ ws, unsigned short* __restrict__ out, int N, int E) {
    __shared__ __attribute__((aligned(16))) char smem_[L_TOTAL];
    LAS char* smem = (LAS char*)smem_;
    const int t = threadIdx.x, l = t & 63, b = blockIdx.x;
    const int w = rfl(t >> 6);
    const int g = l >> 4, n = l & 15, q = (l >> 2) & 3, p = l & 3, pc = t & 7;
    STAMP_DECL;
    STAMP(0);
    if (threadIdx.x == 0) __hip_atomic_store((unsigned long long*)((char*)ws + STAMP_OFF) + blockIdx.x, st_[0], __ATOMIC_RELAXED, __HIP_MEMORY_SCOPE_AGENT);
    const u64 t_start = __builtin_amdgcn_s_memrealtime();
    asm volatile("" :: "s"(src), "s"(dst), "s"(out), "s"(wq), "s"(wv), "s"(x), "s"(ws), "s"(N), "s"(E));
    const unsigned lds0 = (unsigned)(size_t)smem;
    const unsigned xbytes = (unsigned)N * 256u;
    const u32x4 srd_ws = make_srd(ws, WS_BYTES);
    const unsigned ring = lds0 + L_RING + w * 16384;
    const bool isprod = ((b >> 3) & 1) == 0;
    const int pg = ((b & 7) << 4) | (b >> 4);
    const int jn = 8 * n + w;
    const u32x4 srd_x0 = make_srd(x, xbytes);

    unsigned mykey = 0;
    auto node_row = [&](int tile, int m) { const int nd = b + 256 * (8 * (4 * m + g) + tile); return nd < N ? (unsigned)nd << 8 : OOB; };
    auto zero_staging = [&]() {
        const u32x4 z = {0u, 0u, 0u, 0u};
#pragma unroll
        for (int k = 0; k < 5; ++k) *(LAS u32x4*)(smem + L_IMG + k * 8192 + t * 16) = z;
    };

    u32x16 addr;
    f32x32 acc, wva, wvb;
    unsigned cv, lista, cnta, qa, keyn, inf;
    Srd4 srd_x;
    auto loop_constants = [&]() {
        srd_x = make_srd4(x, xbytes, BIAS * 256);
        lista = lds0 + L_LIST + w * (2 * SUBB) + g * 4; cnta = lds0 + L_CNT + (l & 31) * 4; qa = ring + l * 16; keyn = jn; inf = 0x7F800000u;
        asm volatile("" : "+s"(srd_x.s0), "+s"(srd_x.s1), "+s"(srd_x.s2), "+s"(srd_x.s3));
        asm volatile("" : "+v"(lista), "+v"(cnta), "+v"(qa), "+v"(keyn), "+v"(inf));
        const unsigned o0 = (n ^ (((g & 1) << 3) | (g >> 1))) << 4;
        const unsigned ra = (4 * (n & 3) + (n >> 2)) * 256 + (((4 * g) ^ (((n & 7) << 1) | (n >> 3))) << 4);
        const int i = 4 * g + q, r8 = i & 7;
        const unsigned ta = (4 * q + g) * 256 + 8 * (p & 1) + (((p >> 1) ^ (((i & 7) << 1) | (i >> 3))) << 4);
        cv = i * 256 + (((p ^ ((r8 & 1) | ((r8 & 6) << 1))) << 4) | ((p & 1) << 3));
#pragma unroll
        for (int s = 0; s < 4; ++s) { addr[s] = ring + (ra ^ (s << 4)); addr[12 + s] = o0 ^ (s << 5); }
#pragma unroll
        for (int c = 0; c < 8; ++c) addr[4 + c] = ring + (ta ^ (c << 5));
        acc = 0.f;
        asm volatile("" : "+{v[128:159]}"(acc), "+{v[176:191]}"(addr), "+v"(cv));
    };
    auto wv_resident = [&]() {
#pragma unroll
        for (int a = 0; a < 8; ++a) {
            LAS char* pa = smem + L_WV0 + (cv ^ ((a >> 1) << 6) ^ ((a & 1) << 3));
            const f32x4 f0 = __builtin_bit_cast(f32x4, cat(tr_read(pa), tr_read(pa + 4096)));
            const f32x4 f1 = __builtin_bit_cast(f32x4, cat(tr_read(pa + 16384), tr_read(pa + 16384 + 4096)));
#pragma unroll
            for (int k = 0; k < 4; ++k) { wva[4 * a + k] = f0[k]; wvb[4 * a + k] = f1[k]; }
        }
        asm volatile("" : "+{v[224:255]}"(wva), "+{v[32:63]}"(wvb));
    };

    if (w < 4) {
        if (isprod) {
            u32x4 d4[3], s4[3];
            unsigned d1, s1, kb;
            const unsigned e0 = pg * CH + 4 * t, e1 = pg * CH + 3072 + t;
            {
                const u32x4 srd_d = make_srd(dst, (unsigned)E * 4u), srd_s = make_srd(src, (unsigned)E * 4u);
                const unsigned o = e0 * 4, o2 = o + 8192, o1 = w == 0 ? e1 * 4 : OOB;
                const unsigned k4 = 4096;
                LD4(d4[0], srd_d, o, ""); LD4S(d4[1], srd_d, o, k4, ""); LD4(d4[2], srd_d, o2, ""); LD1(d1, srd_d, o1, "");
                LD4(s4[0], srd_s, o, ""); LD4S(s4[1], srd_s, o, k4, ""); LD4(s4[2], srd_s, o2, ""); LD1(s1, srd_s, o1, "");
                const unsigned ok = WS_KEY + t * 4, om = WS_KEY + b * 4;
                LD1(kb, srd_ws, ok, " sc1"); LD1(mykey, srd_ws, om, " sc1");
            }
            zero_staging();
            if (t < 8) *(LAS u32x4*)(smem + L_CNT + t * 16) = u32x4{0u, 0u, 0u, 0u};
            unsigned ob = WS_INBOX + ((t >> 3) * G1 + pg) * 128 + pc * 16, ima = lds0 + L_IMG + (t >> 3) * 144 + pc * 16;
            PIN(ob); PIN(ima);
            WG_BARRIER();
            if (PRIO) __builtin_amdgcn_s_setprio(2);
            STAMP(1);
            unsigned b4[13], rk[13];
            const bool v1 = w == 0 && e1 < (unsigned)E;
            asm volatile("s_waitcnt vmcnt(6)" : "+v"(d4[0]), "+v"(d4[1]), "+v"(d4[2]), "+v"(d1) :: "memory");
            STAMP(2);
#pragma unroll
            for (int j = 0; j < 3; ++j)
                if (e0 + 1024 * j < (unsigned)E) {
#pragma unroll
                    for (int k = 0; k < 4; ++k) { b4[4 * j + k] = byte0_x4(d4[j][k]); rk[4 * j + k] = lds_add(smem + L_CNT1 + b4[4 * j + k], 4); }
                }
            if (v1) { b4[12] = byte0_x4(d1); rk[12] = lds_add(smem + L_CNT1 + b4[12], 4); }
            asm volatile("s_waitcnt vmcnt(1)" : "+v"(s4[0]), "+v"(s4[1]), "+v"(s4[2]), "+v"(s1), "+v"(kb) :: "memory");
            auto put = [&](unsigned d, unsigned sv, unsigned bb, unsigned r) {
                *(LAS unsigned*)(smem + L_IMG + 8 + mad24(bb, 36, min(r, 4u * CAP1))) = __builtin_amdgcn_perm(sv + BIAS, d, 0x01050401u);
            };
            auto spill = [&](unsigned d, unsigned sv, unsigned r) {
                if (r >= 4u * CAP1) {
                    const unsigned o = WS_OVF + (pg * CH + lds_add(smem + L_CNT1 + 2048, 1)) * 4, v = (sv << 16) | d;
                    ST1(v, srd_ws, o, " sc1");
                }
            };
#pragma unroll
            for (int j = 0; j < 3; ++j)
                if (e0 + 1024 * j < (unsigned)E) {
#pragma unroll
                    for (int k = 0; k < 4; ++k) put(d4[j][k], s4[j][k], b4[4 * j + k], rk[4 * j + k]);
                    if (__builtin_expect(max(max(rk[4 * j], rk[4 * j + 1]), max(rk[4 * j + 2], rk[4 * j + 3])) >= 4u * CAP1, 0)) {
#pragma unroll
                        for (int k = 0; k < 4; ++k) spill(d4[j][k], s4[j][k], rk[4 * j + k]);
                    }
                }
            if (v1) { put(d1, s1, b4[12], rk[12]); spill(d1, s1, rk[12]); }
            *(LAS unsigned*)(smem + L_CNT1 + 1024 + t * 4) = kb + KEY_STEP;
            WG_BARRIER();
            STAMP(3);
            unsigned c4[8], kk[8], cs[8]; u32x4 v[8];
            const unsigned nov = *(LAS unsigned*)(smem + L_CNT1 + 2048);
#pragma unroll
            for (int it = 0; it < 8; ++it) {
                c4[it] = *(LAS unsigned*)(smem + L_CNT1 + (t >> 3) * 4 + it * 128);
                kk[it] = *(LAS unsigned*)(smem + L_CNT1 + 1024 + (t >> 3) * 4 + it * 128);
                v[it] = *(LAS u32x4*)(size_t)(ima + it * (32 * 144));
            }
#pragma unroll
            for (int it = 0; it < 8; ++it) cs[it] = sum8(v[it].x + v[it].y + v[it].z + v[it].w);
            lds_arrive(smem + L_CNT + 84, l);
            const unsigned no = rfl(nov);
            if (__builtin_expect(no != 0, 0)) {
                const unsigned o = t == 0 ? WS_NOVF + pg * 4 : OOB;
                ST1(no, srd_ws, o, " sc1");
                VMWAIT(0);
                WG_BARRIER();
            }
#pragma unroll
            for (int it = 0; it < 8; ++it) {
                v[it].x = pc == 0 ? c4[it] : v[it].x; v[it].y = pc == 0 ? cs[it] : v[it].y;
                v[it] ^= kk[it];
                const unsigned o = (WMODE == 2 || (int)c4[it] > 16 * pc - 8) ? ob : OOB, so = it * 32 * G1 * 128;
                asm volatile("buffer_store_dwordx4 %0, %1, %2, %3 offen sc1\n\ts_nop 1" :: "v"(v[it]), "v"(o), "s"(srd_ws), "s"(so) : "memory");
            }
            asm volatile("s_waitcnt vmcnt(8)" : "+v"(mykey) :: "memory");
            STAMP(4);
        } else {
            { const unsigned om = WS_KEY + b * 4; LD1(mykey, srd_ws, om, " sc1"); }
            if (t < 8) *(LAS u32x4*)(smem + L_CNT + t * 16) = u32x4{0u, 0u, 0u, 0u};
            WG_BARRIER();
            asm volatile("s_waitcnt vmcnt(0)" : "+v"(mykey) :: "memory");
            if (NP_SLEEP) __builtin_amdgcn_s_sleep(NP_SLEEP);
            const unsigned o = ((b >> 4) * 256 + t) * 128;
            set_m0(lds0 + L_DUMP + w * 256);
            STAMP(1);
            for (int r = 0; r < PF_R; ++r) {
                asm volatile("s_waitcnt vmcnt(%c0)" :: "i"(PF_D) : "memory");
                if (__builtin_amdgcn_s_memrealtime() > t_start + PF_T) break;
                const unsigned so = r << 19;
                asm volatile("buffer_load_dword %1, %0, %2 offen lds" :: "s"(srd_x0), "v"(o), "s"(so) : "memory");
            }
            if (PRIO) __builtin_amdgcn_s_setprio(2);
            STAMP(2); STAMP(3); STAMP(4);
        }
        STAMP(5);
        mykey = rfl(mykey) + KEY_STEP;
        loop_constants();
        unsigned ib = WS_INBOX + b * (G1 * 128) + t * 16;
        PIN(ib);
        while (__builtin_amdgcn_s_memrealtime() < t_start + POLL_T) __builtin_amdgcn_s_sleep(1);
        STAMP(6);
        u32x16 raw;
        inbox_request(raw, srd_ws, ib);
        inbox_stage(raw, srd_ws, ib, mykey, 16 * pc - 8, lds0 + L_HDR + (t >> 3) * 4);
        if (PRIO) __builtin_amdgcn_s_setprio(0);
        STAMP(7);
    } else {
        const int j = w - 4;
        if (isprod) zero_staging();
        WG_BARRIER();
        STAMP(1);
        u32x4 wr[8], xr[8];
        {
            const u32x4 srd_q = make_srd(wq, 32768);
            const unsigned o = j * 8192 + l * 16, k4 = 4096;
            LD4(wr[0], srd_q, o, ""); LD4(wr[1], srd_q, o, " offset:1024"); LD4(wr[2], srd_q, o, " offset:2048"); LD4(wr[3], srd_q, o, " offset:3072");
            LD4S(wr[4], srd_q, o, k4, ""); LD4S(wr[5], srd_q, o, k4, " offset:1024"); LD4S(wr[6], srd_q, o, k4, " offset:2048"); LD4S(wr[7], srd_q, o, k4, " offset:3072");
#pragma unroll
            for (int m = 0; m < 8; ++m) { const unsigned ox = node_row(m < 4 ? j : w, m & 3) + n * 16; LD4(xr[m], srd_x0, ox, ""); }
        }
        unsigned aqa[8], xfa[4];
        unsigned xa = ring + g * 256 + ((n ^ g) << 4), wa = lds0 + L_WQ + j * 16384 + g * 256 + ((n ^ g) << 4) + ((n & 4) << 1);
        {
            const unsigned aq = lds0 + L_WQ + (8 * g + q) * 256 + ((((4 * p) ^ (q | ((g & 1) << 2))) << 4) | ((p & 1) << 3));
#pragma unroll
            for (int s = 0; s < 4; ++s) { xfa[s] = ring + n * 256 + (((4 * s + g) ^ n) << 4); PIN(xfa[s]); }
#pragma unroll
            for (int c = 0; c < 8; ++c) { aqa[c] = aq ^ ((c >> 1) << 4) ^ ((c & 1) << 3); PIN(aqa[c]); }
            PIN(xa); PIN(wa);
        }
        STAMP(2);
        if (isprod) {
            WG_BARRIER();
            if (__builtin_expect(rfl(*(LAS unsigned*)(smem + L_CNT1 + 2048)) != 0, 0)) WG_BARRIER();
            if (W_SLEEP) __builtin_amdgcn_s_sleep(W_SLEEP);
        }
        STAMP(3);
        asm volatile("s_waitcnt vmcnt(8)" : "+v"(wr[0]), "+v"(wr[1]), "+v"(wr[2]), "+v"(wr[3]), "+v"(wr[4]), "+v"(wr[5]), "+v"(wr[6]), "+v"(wr[7]) :: "memory");
#pragma unroll
        for (int m = 0; m < 8; ++m) {
            const unsigned a = (wa ^ ((m & 2) << 5)) + m * 1024;
            *(LAS u32x2*)(size_t)a = u32x2{wr[m].x, wr[m].y};
            *(LAS u32x2*)(size_t)(a ^ 8) = u32x2{wr[m].z, wr[m].w};
        }
        asm volatile("s_waitcnt vmcnt(0)" : "+v"(xr[0]), "+v"(xr[1]), "+v"(xr[2]), "+v"(xr[3]), "+v"(xr[4]), "+v"(xr[5]), "+v"(xr[6]), "+v"(xr[7]) :: "memory");
#pragma unroll
        for (int m = 0; m < 8; ++m) *(LAS u32x4*)(size_t)((xa ^ ((m & 3) << 6)) + m * 1024) = xr[m];
        STAMP(4);
        lds_arrive(smem + L_CNT + 80, l);
        lds_await(smem + L_CNT + 80, 4);
        STAMP(5);
        bf16x8 xo[2][4], fr[2][8];
        f32x4 qc[2][8];
        auto wq_frags = [&](int u, bf16x8 (&f)[8]) {
#pragma unroll
            for (int a = 0; a < 8; ++a) {
                LAS char* pa = (LAS char*)(size_t)aqa[a] + u * 16384;
                f[a] = cat(tr_read(pa), tr_read(pa + 1024));
            }
        };
#pragma unroll
        for (int u = 0; u < 4; ++u) { xo[0][u] = *(LAS bf16x8*)(size_t)xfa[u]; xo[1][u] = *(LAS bf16x8*)(size_t)(xfa[u] + 4096); }
        wq_frags(0, fr[0]);
        {
            const Srd4 srd_v = make_srd4(wv, 32768);
            const unsigned r0 = (j * 32 + g) << 8, o0 = (n ^ ((g & 1) | ((g & 2) << 1))) << 4;
            const unsigned e[8] = {r0, r0 + 1024, r0 + 2048, r0 + 3072, r0 + 4096, r0 + 5120, r0 + 6144, r0 + 7168};
            const unsigned base = lds0 + (j < 2 ? L_WV0 + j * 16384 : L_WV1 + (j - 2) * 8192);
            const u32x4 off = {o0, o0 ^ 128, o0, o0 ^ 128};
            dma_half(base, srd_v, e, off); dma_half(base + 4096, srd_v, e + 4, off);
        }
#pragma unroll
        for (int u = 0; u < 4; ++u) {
            if (u < 3) wq_frags(u + 1, fr[(u + 1) & 1]);
            FENCE();
#pragma unroll
            for (int a = 0; a < 8; ++a)
#pragma unroll
                for (int tt = 0; tt < 2; ++tt) qc[tt][a] = mfma(fr[u & 1][a], xo[tt][u], u ? qc[tt][a] : f32x4{0.f, 0.f, 0.f, 0.f});
            FENCE();
            if (u == 1 && j < 2) { VMWAIT(0); lds_arrive(smem + L_CNT + 88, l); }
        }
        STAMP(6);
        if (isprod) lds_await(smem + L_CNT + 84, 4);
#pragma unroll
        for (int tt = 0; tt < 2; ++tt)
#pragma unroll
            for (int s = 0; s < 4; ++s) {
                const float k = -0.08838834764831845f * 1.4426950408889634f;
                const f32x4 c0 = qc[tt][2 * s] * k, c1 = qc[tt][2 * s + 1] * k;
                *(LAS u32x4*)(size_t)(ring - (tt ? 0 : 65536) + s * 1024 + l * 16) = u32x4{pk2(c0[0], c0[1]), pk2(c0[2], c0[3]), pk2(c1[0], c1[1]), pk2(c1[2], c1[3])};
            }
        loop_constants();
        lds_await(smem + L_CNT + 88, 2);
        wv_resident();
        VMWAIT(0);
        STAMP(7);
    }
    WG_BARRIER();
    STAMP(8);
    if (LPRIO >= 3) __builtin_amdgcn_s_setprio(3);
    if (LPRIO == 1 && w >= 4) __builtin_amdgcn_s_setprio(1);
    if (LPRIO == 2) { if (w >= 6) __builtin_amdgcn_s_setprio(3); else if (w >= 4) __builtin_amdgcn_s_setprio(2); else if (w >= 2) __builtin_amdgcn_s_setprio(1); }

    u32x16 qf;
    const u32x4 doff = {addr[12], addr[13], addr[14], addr[15]};
    LAS char* mylist = smem + L_LIST + w * (2 * SUBB);
    int gcur = G1, pos = 0;
    auto batch = [&](auto first_) {
        constexpr bool first = decltype(first_)::value;
        auto entries = [&](int grp, unsigned (&e)[8]) {
#pragma unroll
            for (int m = 0; m < 8; ++m) e[m] = *(LAS unsigned*)(size_t)(lista + grp * 128 + m * 16);
        };
        unsigned cw = *(LAS unsigned*)(size_t)cnta, e0[8], e1[8];
        entries(0, e0);
        if constexpr (first) {
#pragma unroll
            for (int s = 0; s < 4; ++s) {
                const u32x4 v = *(LAS u32x4*)(size_t)(qa + s * 1024);
                qf[4 * s] = v.x; qf[4 * s + 1] = v.y; qf[4 * s + 2] = v.z; qf[4 * s + 3] = v.w;
            }
        }
        entries(1, e1);
        FENCE();
        int lo = __builtin_amdgcn_readlane(cw, 2 * w) >> 2, hi = __builtin_amdgcn_readlane(cw, 2 * w + 1) >> 2;
        if constexpr (first) {
            if (__builtin_amdgcn_readlane(cw, 16)) gcur = 0;
            if (__builtin_amdgcn_ballot_w64(cw > 4u * CAP2)) { gcur = -1; lo = 0; hi = 0; }
        }
        const int c = lo + hi, G = (c + 31) >> 5;
        const int nlast = G > 2 ? (((c - 1) & 31) >> 2) + 1 : 8;
        auto join = [&]() {
            for (int k = 0; k < G * 32 - lo; k += 64) {
                const unsigned e = *(LAS unsigned*)(mylist + SUBB + (l + k) * 4);
                if (l + k < G * 32 - lo) *(LAS unsigned*)(mylist + (lo + l + k) * 4) = l + k < hi ? e : PAD;
            }
        };
        const bool early = lo >= 64;
        if (__builtin_expect(!early, 0)) { join(); entries(0, e0); entries(1, e1); }
        if constexpr (first) asm volatile("s_waitcnt lgkmcnt(0)" : "+{v[160:175]}"(qf) :: "memory");
        if (G > 0) dma_group(ring, srd_x, e0, doff);
        if constexpr (first) {
            if (w < 4) {
                if (G > 1) dma_group(ring + 8192, srd_x, e1, doff);
                wv_resident();
                lds_arrive(smem + L_CNT + 92, l);
            } else {
                if (w >= 6) lds_await(smem + L_CNT + 92, 4);
                if (G > 1) dma_group(ring + 8192, srd_x, e1, doff);
            }
        } else if (G > 1) dma_group(ring + 8192, srd_x, e1, doff);
        f32x16 cc;
#pragma unroll
        for (int m = 0; m < 8; ++m) {
            cc[m] = (e0[m] & 0xFF) == keyn ? 0.f : __uint_as_float(inf);
            cc[8 + m] = (e1[m] & 0xFF) == keyn ? 0.f : __uint_as_float(inf);
        }
        if (early) join();
        STAMP(9);
        if (G > 0) gather_loop(acc, qf, addr, u32x4{lista, keyn, inf, 0u}, cc, srd_x, ring, G, nlast);
    };
    batch(std::true_type{});
    while (__builtin_expect(gcur < G1, 0)) {
        unsigned lim = G1 * 32;
        if (gcur >= 0) {
            const u64 m0 = __builtin_amdgcn_ballot_w64(l >= gcur && *(LAS unsigned*)(smem + L_HDR + l * 4) > 4u * CAP1);
            const u64 m1 = __builtin_amdgcn_ballot_w64(l + 64 >= gcur && *(LAS unsigned*)(smem + L_HDR + 256 + l * 4) > 4u * CAP1);
            if (!(m0 | m1)) break;
            gcur = m0 ? __builtin_ctzll(m0) : 64 + __builtin_ctzll(m1);
            unsigned v; const unsigned o = WS_NOVF + gcur * 4;
            asm volatile("buffer_load_dword %0, %1, %2, 0 offen sc1\n\ts_waitcnt vmcnt(0)" : "=&v"(v) : "v"(o), "s"(srd_ws) : "memory");
            lim = rfl(v);
        }
        const unsigned i = pos + t;
        const bool cand = t < CAP2 && i < lim;
        unsigned u;
        { const unsigned o = cand ? (gcur < 0 ? WS_INBOX + b * (G1 * 128) : WS_OVF + gcur * CH * 4) + i * 4 : OOB;
          asm volatile("buffer_load_dword %0, %1, %2, 0 offen sc1\n\ts_waitcnt vmcnt(0)" : "=&v"(u) : "v"(o), "s"(srd_ws) : "memory"); }
        WG_BARRIER();
        if (t < 16) *(LAS unsigned*)(smem + L_CNT + t * 4) = 0;
        WG_BARRIER();
        if (cand) {
            unsigned e; bool v;
            if (gcur < 0) { e = u ^ mykey; v = ((i & 31) - 2) * 4 < *(LAS unsigned*)(smem + L_HDR + (i >> 5) * 4); }
            else { e = (((u >> 16) + BIAS) << 8) | ((u >> 8) & 0xFF); v = (u & 0xFF) == (unsigned)b; }
            if (v) *(LAS unsigned*)(smem + L_LIST + (e & 7) * (2 * SUBB) + lds_add(smem + L_CNT + (e & 7) * 8, 4)) = e;
        }
        pos += CAP2;
        if ((unsigned)pos >= lim) { ++gcur; pos = 0; }
        WG_BARRIER();
        batch(std::false_type{});
    }
    STAMP(10);

    {
        bf16x8 wl[2][8], ag[4];
#pragma unroll
        for (int u = 0; u < 2; ++u)
#pragma unroll
            for (int a = 0; a < 8; ++a) {
                LAS char* pa = smem + L_WV1 + (cv ^ ((a >> 1) << 6) ^ ((a & 1) << 3)) + u * 8192;
                wl[u][a] = cat(tr_read(pa), tr_read(pa + 4096));
            }
#pragma unroll
        for (int u = 0; u < 4; ++u) {
            const u32x4 v = {pk2(acc[8 * u], acc[8 * u + 1]), pk2(acc[8 * u + 2], acc[8 * u + 3]),
                             pk2(acc[8 * u + 4], acc[8 * u + 5]), pk2(acc[8 * u + 6], acc[8 * u + 7])};
            ag[u] = __builtin_bit_cast(bf16x8, v);
        }
        FENCE();
        f32x4 oc[8];
#pragma unroll
        for (int a = 0; a < 8; ++a) oc[a] = mfma(__builtin_bit_cast(bf16x8, f32x4{wva[4 * a], wva[4 * a + 1], wva[4 * a + 2], wva[4 * a + 3]}), ag[0], f32x4{0.f, 0.f, 0.f, 0.f});
#pragma unroll
        for (int a = 0; a < 8; ++a) oc[a] = mfma(__builtin_bit_cast(bf16x8, f32x4{wvb[4 * a], wvb[4 * a + 1], wvb[4 * a + 2], wvb[4 * a + 3]}), ag[1], oc[a]);
#pragma unroll
        for (int a = 0; a < 8; ++a) oc[a] = mfma(wl[0][a], ag[2], oc[a]);
        FENCE();
        const unsigned ow = ring + n * 256 + ((g ^ n) << 4) + ((g & 1) << 3);
#pragma unroll
        for (int a = 0; a < 8; ++a) oc[a] = mfma(wl[1][a], ag[3], oc[a]);
#pragma unroll
        for (int a = 0; a < 8; ++a)
            *(LAS u32x2*)(size_t)(ow ^ ((a >> 1) << 6) ^ ((a & 1) << 3)) = u32x2{pk2(oc[a][0], oc[a][1]), pk2(oc[a][2], oc[a][3])};
        const auto rs_o = __builtin_amdgcn_make_buffer_rsrc((void*)out, 0, xbytes, 0x00020000);
#pragma unroll
        for (int m = 0; m < 4; ++m) {
            const u32x4 v = *(LAS u32x4*)(size_t)(ring + m * 1024 + l * 16);
            __builtin_amdgcn_raw_buffer_store_b128(v, rs_o, node_row(w, m) + ((n ^ g ^ (4 * m)) << 4), 0, OUT_AUX);
        }
    }
    if (w == 0) { const unsigned o = l == 0 ? WS_KEY + b * 4 : OOB; ST1(mykey, srd_ws, o, " sc1"); }
    STAMP(11);
    asm volatile("s_waitcnt vmcnt(0)" ::: "memory");
    STAMP(12);
    __syncthreads();
    STAMP(13);
    if (threadIdx.x < 64) {
        unsigned long long mn = ~0ull, mx = 0;
        for (int k = 0; k < 4; ++k) { const unsigned long long v = __hip_atomic_load((unsigned long long*)((char*)ws + STAMP_OFF) + threadIdx.x + 64 * k, __ATOMIC_RELAXED, __HIP_MEMORY_SCOPE_AGENT); mn = v < mn ? v : mn; mx = v > mx ? v : mx; }
        for (int o = 32; o; o >>= 1) { unsigned long long a = __shfl_xor(mn, o), c = __shfl_xor(mx, o); mn = a < mn ? a : mn; mx = c > mx ? c : mx; }
        const unsigned long long tgt = st_[13] + 10 * (mx - mn);
        int guard = 0; while (wall_clock64() < tgt && ++guard < 200000) __builtin_amdgcn_s_sleep(2);
    }
}

extern "C" void kernel_launch(void* const* d_in, const int* in_sizes, int n_in,
                              void* d_out, int out_size, void* d_ws, size_t ws_size,
                              hipStream_t stream) {
    const int N = in_sizes[0] / 128, E = in_sizes[1];
    k_graph<<<NB, 512, 0, stream>>>((const unsigned short*)d_in[0], (const int*)d_in[1], (const int*)d_in[2],
                                    (const unsigned short*)d_in[3], (const unsigned short*)d_in[4],
                                    (unsigned*)d_ws, (unsigned short*)d_out, N, E);
}
```

```cpp
#define STAMPS
#define PROBE_A 0
#define PROBE_B 13
#define PROBE_M 0
#include <hip/hip_runtime.h>
#include <hip/hip_bf16.h>
#include <stdint.h>
#include <type_traits>
#define ORD 1
#define SKIP 0
#define STR2(x) #x
#define STR(x) STR2(x)

typedef __attribute__((ext_vector_type(8))) __bf16 bf16x8;
typedef __attribute__((ext_vector_type(4))) __bf16 bf16x4;
typedef __attribute__((ext_vector_type(2))) __bf16 bf16x2;
typedef __attribute__((ext_vector_type(4))) float f32x4;
typedef __attribute__((ext_vector_type(2))) float f32x2;
typedef __attribute__((ext_vector_type(4))) unsigned u32x4;
typedef __attribute__((ext_vector_type(2))) unsigned u32x2;
typedef __attribute__((ext_vector_type(32))) float f32x32;
typedef __attribute__((ext_vector_type(16))) float f32x16;
typedef __attribute__((ext_vector_type(16))) unsigned u32x16;
typedef __attribute__((ext_vector_type(12))) unsigned u32x12;
typedef unsigned long long u64;
#define LAS __attribute__((address_space(3)))

constexpr int NB = 256;
constexpr int G1 = 128;
constexpr int CH = 3136;
constexpr int CAP1 = 30;
constexpr int CAP2 = 192;
constexpr int SUBB = 784;
#define POLL_AT 3
#define OUT_AUX 16
#define PF_R 0
#define PF_D 0
#define POLL_T 260
#define PF_T 200
#define WMODE 2
#define LPRIO 3
#define PKADD 1
#define NP_SLEEP 0
#define W_SLEEP 3
#define PRIO 1
#define SPLIT 16384
constexpr unsigned KEY_STEP = 0x9E3779B1u;
constexpr unsigned WS_KEY = 0;
constexpr unsigned WS_NOVF = 2048;
constexpr unsigned WS_INBOX = 4096;
constexpr unsigned WS_OVF = WS_INBOX + NB * G1 * 128;
constexpr unsigned WS_BYTES = WS_OVF + G1 * CH * 4;
constexpr unsigned OOB = 0x80000000u;
constexpr unsigned BIAS = 32768 - SPLIT;
constexpr unsigned PAD = 0x00FFFFFFu;

constexpr int L_CNT = 0;
constexpr int L_HDR = 128;
constexpr int L_LIST = 768;
constexpr int L_RING = L_LIST + 16 * SUBB;
constexpr int L_IMG = L_RING;
constexpr int L_CNT1 = L_IMG + NB * 144;
constexpr int L_WQ = L_RING + 2 * 16384 + 8192;
constexpr int L_WV0 = L_RING + 6 * 16384 + 8192;
constexpr int L_WV1 = L_RING + 131072;
constexpr int L_DUMP = L_WV1 + 16384;
constexpr int L_TOTAL = L_DUMP + 2048;
static_assert(L_RING % 256 == 0 && L_TOTAL <= 163840, "LDS map");

#define STAMP_N 24
#define STAMP_WGS 256
#define STAMP_OFF (200u << 20)
#define STAMP_ROLE 0
#define STAMP_VALID(w, k) ((k) < 14 && ((STAMP_MASK >> (k)) & 1) && ((k) == 0 || STAMP_ROLE == 2 || ((((w) >> 3) & 1) == STAMP_ROLE)))
#define STAMP_DECL unsigned long long st_[STAMP_N] = {}
#define STAMP_MASK 0xFFFFFF
#define STAMP(k) do { if (!((STAMP_MASK >> (k)) & 1)) break; __builtin_amdgcn_sched_barrier(0); asm volatile("s_memrealtime %0\n\ts_waitcnt lgkmcnt(0)" : "=s"(st_[k]) :: "memory"); __builtin_amdgcn_sched_barrier(0); } while (0)
#define STAMP_THREAD 0
#define STAMP_FLUSH(base, k0, k1) do { if (threadIdx.x == STAMP_THREAD) { _Pragma("unroll") for (int k_ = k0; k_ < k1; ++k_) ((unsigned long long*)((char*)(base) + STAMP_OFF))[(size_t)blockIdx.x * STAMP_N + k_] = st_[k_]; } } while (0)

__device__ __forceinline__ u32x4 make_srd(const void* p, unsigned bytes, unsigned back = 0) {
    const u64 a = (u64)p - back;
    u32x4 s; s.x = (unsigned)a; s.y = (unsigned)(a >> 32) & 0xFFFFu; s.z = bytes + back; s.w = 0x00020000u; return s;
}
#define LD4(dst, srd, off, FL) asm volatile("buffer_load_dwordx4 %0, %1, %2, 0 offen" FL : "=v"(dst) : "v"(off), "s"(srd) : "memory")
#define LD4S(dst, srd, off, so, FL) asm volatile("buffer_load_dwordx4 %0, %1, %2, %3 offen" FL : "=v"(dst) : "v"(off), "s"(srd), "s"(so) : "memory")
#define LD1(dst, srd, off, FL) asm volatile("buffer_load_dword %0, %1, %2, 0 offen" FL : "=v"(dst) : "v"(off), "s"(srd) : "memory")
#define ST1(val, srd, off, FL) asm volatile("buffer_store_dword %0, %1, %2, 0 offen" FL :: "v"(val), "v"(off), "s"(srd) : "memory")
#define PIN(x) asm volatile("" : "+v"(x))
#define VMWAIT(n) asm volatile("s_waitcnt vmcnt(" #n ")" ::: "memory")
#define FENCE() __builtin_amdgcn_sched_barrier(0)
#define WG_BARRIER() do { asm volatile("s_waitcnt lgkmcnt(0)" ::: "memory"); __builtin_amdgcn_s_barrier(); asm volatile("" ::: "memory"); } while (0)

struct Srd4 { u32x4 s0, s1, s2, s3; };
__device__ __forceinline__ Srd4 make_srd4(const void* p, unsigned bytes, unsigned back = 0) {
    return Srd4{make_srd(p, bytes, back), make_srd(p, bytes, back + 1024), make_srd(p, bytes, back + 2048), make_srd(p, bytes, back + 3072)};
}

__device__ __forceinline__ void set_m0(unsigned v) { asm volatile("s_mov_b32 m0, %0" :: "s"(v) : "memory"); }
template <int R> __device__ __forceinline__ void dma_rows(const Srd4& d, unsigned e, unsigned off) {
    unsigned tmp, mask = 0x00FFFF00u;
    asm volatile("v_and_or_b32 %0, %1, %2, %3\n\tbuffer_load_dwordx4 %0, %4, 0 offen offset:%c5 lds"
                 : "=&v"(tmp) : "v"(e), "s"(mask), "v"(off), "s"(R == 0 ? d.s0 : R == 1 ? d.s1 : R == 2 ? d.s2 : d.s3), "i"(R * 1024) : "memory");
}
__device__ __forceinline__ void dma_half(unsigned ldsbase, const Srd4& d, const unsigned* e, const u32x4& off) {
    set_m0(ldsbase);
    dma_rows<0>(d, e[0], off.x); dma_rows<1>(d, e[1], off.y); dma_rows<2>(d, e[2], off.z); dma_rows<3>(d, e[3], off.w);
}
__device__ __forceinline__ void dma_group(unsigned ldsbase, const Srd4& d, const unsigned (&e)[8], const u32x4& off) {
    asm volatile("s_waitcnt lgkmcnt(0)" ::: "memory");
    dma_half(ldsbase, d, e, off); dma_half(ldsbase + 4096, d, e + 4, off);
}

__device__ __forceinline__ void gather_loop(f32x32& acc, const u32x16& qf, const u32x16& addr, u32x4 misc, f32x16 cc,
                                            const Srd4& d, unsigned ring, int G, int nlast) {
    f32x32 t0, t1; u32x12 t2; int tmp, pr; u64 sm; unsigned mask = 0x00FFFF00u;
    asm volatile(
        ".macro GWAIT k\n\ts_cmp_gt_i32 %[t], \\k\n\ts_cbranch_scc1 1f\n\ts_waitcnt vmcnt(\\k)\n\t.endm\n\t"
        ".macro GROWS so\n\t.irp s, 0, 1, 2, 3\n\t"
        "ds_read_b128 v[64+8*\\s:67+8*\\s], v[176+\\s] offset:\\so\n\t"
        "ds_read_b128 v[68+8*\\s:71+8*\\s], v[176+\\s] offset:\\so+4096\n\t.endr\n\t.endm\n\t"
        ".macro GTR c, so\n\t"
        "ds_read_b64_tr_b16 v[96+4*\\c:97+4*\\c], v[180+\\c] offset:\\so\n\t"
        "ds_read_b64_tr_b16 v[98+4*\\c:99+4*\\c], v[180+\\c] offset:\\so+4096\n\t.endm\n\t"
        ".macro GS s, c\n\t"
        "v_mfma_f32_16x16x32_bf16 v[\\c:\\c+3], v[64+8*\\s:67+8*\\s], v[160+4*\\s:163+4*\\s], v[\\c:\\c+3]\n\t"
        "v_mfma_f32_16x16x32_bf16 v[\\c+4:\\c+7], v[68+8*\\s:71+8*\\s], v[160+4*\\s:163+4*\\s], v[\\c+4:\\c+7]\n\t.endm\n\t"
        ".macro GLOAD m, r, srd\n\t"
        "v_and_or_b32 v195, v[212+\\m], %[mask], v[188+\\r]\n\t"
        "buffer_load_dwordx4 v195, \\srd, 0 offen offset:1024*\\r lds\n\t.endm\n\t"
        ".macro GPART m, r, srd\n\ts_cmp_le_i32 %[nl], \\m\n\ts_cbranch_scc1 4f\n\tGLOAD \\m, \\r, \\srd\n\t.endm\n\t"
        ".macro GSUM m\n\tv_mfma_f32_16x16x32_bf16 v[128+4*(\\m):131+4*(\\m)], v[96+4*(\\m):99+4*(\\m)], v[220:223], v[128+4*(\\m):131+4*(\\m)]\n\t.endm\n\t"
        ".macro GCMP m, d\n\tv_cmp_ne_u32_sdwa \\d, v[212+\\m], v193 src0_sel:BYTE_0 src1_sel:DWORD\n\t.endm\n\t"
        ".macro GPAIR m, c\n\t"
        "GSUM \\m\n\tGCMP \\m+1, %[sm]\n\tv_cndmask_b32_e32 v[\\c+\\m], 0, v194, vcc\n\t"
        "GSUM \\m+1\n\t.if \\m < 6\n\tGCMP \\m+2, vcc\n\t.else\n\ts_cmp_gt_i32 %[rem], 0\n\t.endif\n\t"
        "v_cndmask_b32_e64 v[\\c+\\m+1], 0, v194, %[sm]\n\t.endm\n\t"
        ".macro GSTEP so, c, eo\n\t"
        "GTR 0, \\so\n\t"
        "s_waitcnt lgkmcnt(2)\n\tGS 0, \\c\n\t"
        "GTR 1, \\so\n\tGTR 2, \\so\n\tGS 1, \\c\n\t"
        "GTR 3, \\so\n\tGTR 4, \\so\n\tGS 2, \\c\n\t"
        "GTR 5, \\so\n\tGTR 6, \\so\n\tGS 3, \\c\n\t"
        "GTR 7, \\so\n\t"
        "ds_read2_b32 v[212:213], v192 offset0:\\eo offset1:\\eo+4\n\t"
        "ds_read2_b32 v[214:215], v192 offset0:\\eo+8 offset1:\\eo+12\n\t"
        "ds_read2_b32 v[216:217], v192 offset0:\\eo+16 offset1:\\eo+20\n\t"
        "ds_read2_b32 v[218:219], v192 offset0:\\eo+24 offset1:\\eo+28\n\t"
        "s_add_u32 m0, %[ring], \\so\n\t"
        "s_cmp_gt_i32 %[rem], 3\n\t"
        ".irp k, 0, 1, 2, 3, 4, 5, 6, 7\n\tv_exp_f32_e32 v[\\c+\\k], v[\\c+\\k]\n\t.endr\n\t"
        "s_waitcnt lgkmcnt(0)\n\t"
        "s_cbranch_scc0 2f\n\t"
        "GLOAD 0, 0, %[s0]\n\tGLOAD 1, 1, %[s1]\n\tGLOAD 2, 2, %[s2]\n\tGLOAD 3, 3, %[s3]\n\t"
        "s_add_u32 m0, m0, 0x1000\n\t"
        "GLOAD 4, 0, %[s0]\n\tGLOAD 5, 1, %[s1]\n\tGLOAD 6, 2, %[s2]\n\tGLOAD 7, 3, %[s3]\n\t"
        "GSIG \\c\n\t"
        "s_waitcnt vmcnt(8)\n\t"
        "s_branch 1f\n\t"
        "2:\n\t"
        "s_mov_b32 %[t], 0\n\t"
        "s_cmp_eq_u32 %[rem], 3\n\ts_cbranch_scc0 4f\n\t"
        "s_mov_b32 %[t], %[nl]\n\t"
        "GLOAD 0, 0, %[s0]\n\tGPART 1, 1, %[s1]\n\tGPART 2, 2, %[s2]\n\tGPART 3, 3, %[s3]\n\t"
        "s_add_u32 m0, m0, 0x1000\n\t"
        "GPART 4, 0, %[s0]\n\tGPART 5, 1, %[s1]\n\tGPART 6, 2, %[s2]\n\tGPART 7, 3, %[s3]\n\t"
        "4:\n\t"
        "GSIG \\c\n\t"
        "s_waitcnt vmcnt(8)\n\t"
        "GWAIT 7\n\tGWAIT 6\n\tGWAIT 5\n\tGWAIT 4\n\tGWAIT 3\n\tGWAIT 2\n\tGWAIT 1\n\tGWAIT 0\n\t"
        "1:\n\t"
        "GROWS 8192-\\so\n\t"
        "GCMP 0, vcc\n\t"
        "s_add_i32 %[rem], %[rem], -1\n\t"
        "GPAIR 0, \\c\n\tGPAIR 2, \\c\n\tGPAIR 4, \\c\n\tGPAIR 6, \\c\n\t"
        ".endm\n\t"
        ".macro GSIG c\n\t"
        ".if " STR(PKADD) "\n\t.irp k, 0, 2, 4, 6\n\tv_pk_add_f32 v[\\c+\\k:\\c+\\k+1], v[\\c+\\k:\\c+\\k+1], 1.0 op_sel_hi:[1,0]\n\t.endr\n\t.else\n\t"
        ".irp k, 0, 1, 2, 3, 4, 5, 6, 7\n\tv_add_f32_e32 v[\\c+\\k], 1.0, v[\\c+\\k]\n\t.endr\n\t.endif\n\t"
        ".irp k, 0, 1, 2, 3, 4, 5, 6, 7\n\tv_rcp_f32_e32 v[\\c+\\k], v[\\c+\\k]\n\t.endr\n\t"
        ".irp k, 0, 1, 2, 3\n\tv_cvt_pk_bf16_f32 v[220+\\k], v[\\c+2*\\k], v[\\c+2*\\k+1]\n\t.endr\n\t.endm\n\t"
        "s_waitcnt vmcnt(8)\n\t"
        "s_cmp_gt_i32 %[rem], 1\n\ts_cbranch_scc1 1f\n\ts_waitcnt vmcnt(0)\n\t1:\n\t"
        "GROWS 0\n\t"
        ".if " STR(LPRIO) " >= 3\n\ts_mov_b32 %[pr], 0\n\ts_setprio 2\n\t.endif\n\t"
        "0:\n\t"
        ".if " STR(LPRIO) " == 3\n\t"
        "s_cmp_eq_u32 %[pr], 1\n\ts_cbranch_scc0 5f\n\ts_setprio 1\n\t5:\n\t"
        "s_cmp_eq_u32 %[pr], 2\n\ts_cbranch_scc0 5f\n\ts_setprio 0\n\t5:\n\t"
        "s_add_u32 %[pr], %[pr], 1\n\t.endif\n\t"
        "GSTEP 0, 196, 64\n\t"
        "s_cbranch_scc0 9f\n\t"
        "GSTEP 8192, 204, 96\n\t"
        "v_add_u32_e32 v192, 0x100, v192\n\t"
        "s_cbranch_scc1 0b\n\t"
        "9:\n\t"
        "s_waitcnt lgkmcnt(0)\n\ts_nop 6\n\t"
        ".purgem GWAIT\n\t.purgem GROWS\n\t.purgem GTR\n\t.purgem GS\n\t.purgem GLOAD\n\t.purgem GPART\n\t.purgem GSUM\n\t.purgem GCMP\n\t.purgem GPAIR\n\t.purgem GSTEP\n\t.purgem GSIG"
        : "+{v[128:159]}"(acc), "+{v[192:195]}"(misc), "+{v[196:211]}"(cc), "=&{v[64:95]}"(t0), "=&{v[96:127]}"(t1), "=&{v[212:223]}"(t2),
          [rem] "+s"(G), [t] "=&s"(tmp), [pr] "=&s"(pr), [sm] "=&s"(sm)
        : "{v[160:175]}"(qf), "{v[176:191]}"(addr), [s0] "s"(d.s0), [s1] "s"(d.s1), [s2] "s"(d.s2), [s3] "s"(d.s3),
          [mask] "s"(mask), [ring] "s"(ring), [nl] "s"(nlast)
        : "vcc", "scc", "memory");
}

__device__ __forceinline__ void inbox_request(u32x16& raw, const u32x4& srd, unsigned ib) {
    unsigned o1 = 0x1000, o2 = 0x2000, o3 = 0x3000;
    asm volatile("buffer_load_dwordx4 v[64:67], %1, %2, 0 offen sc1\n\tbuffer_load_dwordx4 v[68:71], %1, %2, %3 offen sc1\n\t"
                 "buffer_load_dwordx4 v[72:75], %1, %2, %4 offen sc1\n\tbuffer_load_dwordx4 v[76:79], %1, %2, %5 offen sc1"
                 : "=&{v[64:79]}"(raw) : "v"(ib), "s"(srd), "s"(o1), "s"(o2), "s"(o3) : "memory");
}
__device__ __forceinline__ void inbox_stage(u32x16 raw, const u32x4& srd, unsigned ib, unsigned key, int i4, unsigned hdra) {
    u32x16 t0; u32x4 t1; u64 m0, m1, m2, m3; unsigned todo, fresh, spin, o1, o2, o3;
    u64 pc0 = 0x0101010101010101ull;
    asm volatile(
        ".macro IVAL j, so\n\t"
        "s_bitcmp1_b32 %[todo], \\j\n\ts_cbranch_scc0 2f\n\t"
        ".irp k, 0, 1, 2, 3\n\tv_xor_b32_e32 v[64+4*\\j+\\k], %[key], v[64+4*\\j+\\k]\n\t.endr\n\t"
        "v_mov_b32_dpp v[80+\\j], v[64+4*\\j] quad_perm:[0,0,0,0] row_mask:0xf bank_mask:0xf\n\t"
        "v_add_u32_e32 v96, v[64+4*\\j], v[65+4*\\j]\n\t"
        "v_sub_u32_e32 v97, 0, v[65+4*\\j]\n\t"
        "v_mov_b32_dpp v[80+\\j], v[80+\\j] row_shr:4 row_mask:0xf bank_mask:0xa\n\t"
        "v_cndmask_b32_e64 v96, v96, v97, %[pc0]\n\t"
        "v_cmp_gt_i32_e32 vcc, v[80+\\j], v92\n\t"
        "v_add3_u32 v96, v96, v[66+4*\\j], v[67+4*\\j]\n\t"
        "v_lshrrev_b32_e32 v97, 14, v[80+\\j]\n\t"
        "v_cndmask_b32_e32 v96, 0, v96, vcc\n\t"
        "s_nop 1\n\tv_add_u32_dpp v96, v96, v96 quad_perm:[1,0,3,2] row_mask:0xf bank_mask:0xf\n\t"
        "s_nop 1\n\tv_add_u32_dpp v96, v96, v96 quad_perm:[2,3,0,1] row_mask:0xf bank_mask:0xf\n\t"
        "s_nop 1\n\tv_add_u32_dpp v96, v96, v96 row_half_mirror row_mask:0xf bank_mask:0xf\n\t"
        "v_or_b32_e32 v96, v96, v97\n\t"
        "v_cmp_ne_u32_e32 vcc, 0, v96\n\t"
        "s_cbranch_vccz 1f\n\t"
        "buffer_load_dwordx4 v[64+4*\\j:67+4*\\j], %[ib], %[srd], \\so offen sc1\n\t"
        "s_branch 2f\n\t"
        "1:\n\ts_bitset1_b32 %[fresh], \\j\n\t"
        "2:\n\t.endm\n\t"
        ".macro IRANK j\n\t"
        "s_bitcmp1_b32 %[fresh], \\j\n\ts_cbranch_scc0 2f\n\t"
        "v_cmp_lt_u32_e64 %[m0], v92, v[80+\\j]\n\tv_cmp_lt_u32_e64 %[m1], v93, v[80+\\j]\n\t"
        "v_cmp_lt_u32_e64 %[m2], v94, v[80+\\j]\n\tv_cmp_lt_u32_e64 %[m3], v95, v[80+\\j]\n\t"
        ".irp k, 0, 1, 2, 3\n\tv_lshrrev_b32_e32 v[84+\\k], 21, v[64+4*\\j+\\k]\n\t.endr\n\t"
        ".irp k, 0, 1, 2, 3\n\tv_and_b32_e32 v[84+\\k], 60, v[84+\\k]\n\t.endr\n\t"
        "v_cmp_lt_u32_e32 vcc, %c[cap], v[80+\\j]\n\t"
        "s_mov_b64 exec, %[m0]\n\tds_add_rtn_u32 v88, v84, v98 offset:%c[cnt]\n\t"
        "s_mov_b64 exec, %[m1]\n\tds_add_rtn_u32 v89, v85, v98 offset:%c[cnt]\n\t"
        "s_mov_b64 exec, %[m2]\n\tds_add_rtn_u32 v90, v86, v98 offset:%c[cnt]\n\t"
        "s_mov_b64 exec, %[m3]\n\tds_add_rtn_u32 v91, v87, v98 offset:%c[cnt]\n\t"
        "s_mov_b64 exec, %[pc0]\n\tds_write_b32 %[hdra], v[80+\\j] offset:128*\\j\n\t"
        "s_mov_b64 exec, -1\n\t"
        "s_cbranch_vccz 1f\n\t"
        "v_mov_b32_e32 v96, 0\n\tds_write_b32 v96, v98 offset:%c[cnt]+64\n\ts_waitcnt lgkmcnt(0)\n\t"
        "1:\n\ts_waitcnt lgkmcnt(1)\n\t"
        ".irp k, 0, 1, 2, 3\n\tv_min_u32_e32 v[88+\\k], %c[sub]-4, v[88+\\k]\n\t.endr\n\t"
        ".irp k, 0, 1, 2, 3\n\tv_mad_u32_u24 v[88+\\k], v[84+\\k], %[sub4], v[88+\\k]\n\t.endr\n\t"
        "s_mov_b64 exec, %[m0]\n\tds_write_b32 v88, v[64+4*\\j] offset:%c[list]\n\t"
        "s_mov_b64 exec, %[m1]\n\tds_write_b32 v89, v[65+4*\\j] offset:%c[list]\n\t"
        "s_mov_b64 exec, %[m2]\n\tds_write_b32 v90, v[66+4*\\j] offset:%c[list]\n\t"
        "s_mov_b64 exec, %[m3]\n\tds_write_b32 v91, v[67+4*\\j] offset:%c[list]\n\t"
        "s_mov_b64 exec, -1\n\t"
        "2:\n\t.endm\n\t"
        "s_movk_i32 %[o1], 0x1000\n\ts_movk_i32 %[o2], 0x2000\n\ts_movk_i32 %[o3], 0x3000\n\t"
        "v_mov_b32_e32 v92, %[i4]\n\tv_add_u32_e32 v93, 4, %[i4]\n\tv_add_u32_e32 v94, 8, %[i4]\n\tv_add_u32_e32 v95, 12, %[i4]\n\t"
        "v_mov_b32_e32 v98, 4\n\t"
        "s_mov_b32 %[todo], 15\n\ts_mov_b32 %[spin], 0x400000\n\t"
        "0:\n\t"
        "s_mov_b32 %[fresh], 0\n\t"
        "s_waitcnt vmcnt(3)\n\ts_cmp_eq_u32 %[todo], 15\n\ts_cbranch_scc1 3f\n\ts_waitcnt vmcnt(0)\n\t3:\n\t"
        "IVAL 0, 0\n\tIRANK 0\n\t"
        "s_waitcnt vmcnt(2)\n\tIVAL 1, %[o1]\n\tIRANK 1\n\t"
        "s_waitcnt vmcnt(1)\n\tIVAL 2, %[o2]\n\tIRANK 2\n\t"
        "s_cmp_eq_u32 %[todo], 15\n\ts_cbranch_scc0 3f\n\ts_waitcnt vmcnt(0)\n\t3:\n\tIVAL 3, %[o3]\n\tIRANK 3\n\t"
        "s_andn2_b32 %[todo], %[todo], %[fresh]\n\t"
        "s_cbranch_scc0 9f\n\t"
        "s_sub_u32 %[spin], %[spin], 1\n\t"
        "s_cbranch_scc0 0b\n\t"
        "s_waitcnt vmcnt(0)\n\t"
        "9:\n\t"
        ".purgem IVAL\n\t.purgem IRANK"
        : "+{v[64:79]}"(raw), "=&{v[80:95]}"(t0), "=&{v[96:99]}"(t1), [m0] "=&s"(m0), [m1] "=&s"(m1), [m2] "=&s"(m2), [m3] "=&s"(m3),
          [todo] "=&s"(todo), [fresh] "=&s"(fresh), [spin] "=&s"(spin), [o1] "=&s"(o1), [o2] "=&s"(o2), [o3] "=&s"(o3)
        : [srd] "s"(srd), [ib] "v"(ib), [key] "s"(key), [i4] "v"(i4), [hdra] "v"(hdra), [pc0] "s"(pc0), [sub4] "s"(SUBB / 4),
          [cnt] "i"(L_CNT), [list] "i"(L_LIST), [sub] "i"(SUBB), [cap] "i"(4 * CAP1)
        : "vcc", "scc", "memory");
}

template <int CTRL, int BANK> __device__ __forceinline__ unsigned dpp(unsigned old, unsigned v) { return __builtin_amdgcn_update_dpp(old, v, CTRL, 0xF, BANK, true); }
__device__ __forceinline__ unsigned bcast8(unsigned v) { v = dpp<0x00, 0xF>(0u, v); return __builtin_amdgcn_update_dpp(v, v, 0x114, 0xF, 0xA, false); }
__device__ __forceinline__ unsigned sum8(unsigned v) { v += dpp<0xB1, 0xF>(0u, v); v += dpp<0x4E, 0xF>(0u, v); return v + dpp<0x141, 0xF>(0u, v); }
__device__ __forceinline__ bf16x4 tr_read(LAS char* p) { return __builtin_amdgcn_ds_read_tr16_b64_v4bf16((LAS bf16x4*)p); }
__device__ __forceinline__ bf16x8 cat(bf16x4 a, bf16x4 b) { return __builtin_shufflevector(a, b, 0, 1, 2, 3, 4, 5, 6, 7); }
__device__ __forceinline__ unsigned pk2(float a, float b) { f32x2 v = {a, b}; bf16x2 r = __builtin_convertvector(v, bf16x2); return __builtin_bit_cast(unsigned, r); }
__device__ __forceinline__ f32x4 mfma(bf16x8 a, bf16x8 b, f32x4 c) { return __builtin_amdgcn_mfma_f32_16x16x32_bf16(a, b, c, 0, 0, 0); }
__device__ __forceinline__ unsigned lds_add(LAS char* p, unsigned v) { return __hip_atomic_fetch_add((LAS unsigned*)p, v, __ATOMIC_RELAXED, __HIP_MEMORY_SCOPE_WORKGROUP); }
__device__ __forceinline__ LAS unsigned* ldsp(unsigned a) { return (LAS unsigned*)(size_t)a; }
__device__ __forceinline__ unsigned byte0_x4(unsigned v) { unsigned r; asm("v_lshlrev_b32_sdwa %0, 2, %1 dst_sel:DWORD dst_unused:UNUSED_PAD src0_sel:DWORD src1_sel:BYTE_0" : "=v"(r) : "v"(v)); return r; }
__device__ __forceinline__ unsigned mad24(unsigned a, unsigned b, unsigned c) { unsigned r; asm("v_mad_u32_u24 %0, %1, %2, %3" : "=v"(r) : "v"(a), "s"(b), "v"(c)); return r; }
__device__ __forceinline__ void lds_arrive(LAS char* p, int lane) {
    asm volatile("s_waitcnt lgkmcnt(0)" ::: "memory");
    if (lane == 0) __hip_atomic_fetch_add((LAS unsigned*)p, 1u, __ATOMIC_RELAXED, __HIP_MEMORY_SCOPE_WORKGROUP);
}
__device__ __forceinline__ void lds_await(LAS char* p, unsigned n) {
    while (__builtin_amdgcn_readfirstlane(*(volatile LAS unsigned*)p) < n) __builtin_amdgcn_s_sleep(1);
    asm volatile("" ::: "memory");
}
__device__ __forceinline__ unsigned rfl(unsigned v) { return __builtin_amdgcn_readfirstlane(v); }

__global__ __launch_bounds__(512, 2) void k_graph(const unsigned short* __restrict__ x, const int* __restrict__ src, const int* __restrict__ dst,
                                                  const unsigned short* __restrict__ wq, const unsigned short* __restrict__ wv,
                                                  unsigned* __restrict__ ws, unsigned short* __restrict__ out, int N, int E) {
    __shared__ __attribute__((aligned(16))) char smem_[L_TOTAL];
    LAS char* smem = (LAS char*)smem_;
    const int t = threadIdx.x, l = t & 63, b = blockIdx.x;
    const int w = rfl(t >> 6);
    const int g = l >> 4, n = l & 15, q = (l >> 2) & 3, p = l & 3, pc = t & 7;
    STAMP_DECL;
    STAMP(0);
    const u64 t_start = __builtin_amdgcn_s_memrealtime();
    asm volatile("" :: "s"(src), "s"(dst), "s"(out), "s"(wq), "s"(wv), "s"(x), "s"(ws), "s"(N), "s"(E));
    const unsigned lds0 = (unsigned)(size_t)smem;
    const unsigned xbytes = (unsigned)N * 256u;
    const u32x4 srd_ws = make_srd(ws, WS_BYTES);
    const unsigned ring = lds0 + L_RING + w * 16384;
    const bool isprod = ((b >> 3) & 1) == 0;
    const int pg = ((b & 7) << 4) | (b >> 4);
    const int jn = 8 * n + w;
    const u32x4 srd_x0 = make_srd(x, xbytes);

    unsigned mykey = 0;
    auto node_row = [&](int tile, int m) { const int nd = b + 256 * (8 * (4 * m + g) + tile); return nd < N ? (unsigned)nd << 8 : OOB; };
    auto zero_staging = [&]() {
        const u32x4 z = {0u, 0u, 0u, 0u};
#pragma unroll
        for (int k = 0; k < 5; ++k) *(LAS u32x4*)(smem + L_IMG + k * 8192 + t * 16) = z;
    };

    u32x16 addr;
    f32x32 acc, wva, wvb;
    unsigned cv, lista, cnta, qa, keyn, inf;
    Srd4 srd_x;
    auto loop_constants = [&]() {
        srd_x = make_srd4(x, xbytes, BIAS * 256);
        lista = lds0 + L_LIST + w * (2 * SUBB) + g * 4; cnta = lds0 + L_CNT + (l & 31) * 4; qa = ring + l * 16; keyn = jn; inf = 0x7F800000u;
        asm volatile("" : "+s"(srd_x.s0), "+s"(srd_x.s1), "+s"(srd_x.s2), "+s"(srd_x.s3));
        asm volatile("" : "+v"(lista), "+v"(cnta), "+v"(qa), "+v"(keyn), "+v"(inf));
        const unsigned o0 = (n ^ (((g & 1) << 3) | (g >> 1))) << 4;
        const unsigned ra = (4 * (n & 3) + (n >> 2)) * 256 + (((4 * g) ^ (((n & 7) << 1) | (n >> 3))) << 4);
        const int i = 4 * g + q, r8 = i & 7;
        const unsigned ta = (4 * q + g) * 256 + 8 * (p & 1) + (((p >> 1) ^ (((i & 7) << 1) | (i >> 3))) << 4);
        cv = i * 256 + (((p ^ ((r8 & 1) | ((r8 & 6) << 1))) << 4) | ((p & 1) << 3));
#pragma unroll
        for (int s = 0; s < 4; ++s) { addr[s] = ring + (ra ^ (s << 4)); addr[12 + s] = o0 ^ (s << 5); }
#pragma unroll
        for (int c = 0; c < 8; ++c) addr[4 + c] = ring + (ta ^ (c << 5));
        acc = 0.f;
        asm volatile("" : "+{v[128:159]}"(acc), "+{v[176:191]}"(addr), "+v"(cv));
    };
    auto wv_resident = [&]() {
#pragma unroll
        for (int a = 0; a < 8; ++a) {
            LAS char* pa = smem + L_WV0 + (cv ^ ((a >> 1) << 6) ^ ((a & 1) << 3));
            const f32x4 f0 = __builtin_bit_cast(f32x4, cat(tr_read(pa), tr_read(pa + 4096)));
            const f32x4 f1 = __builtin_bit_cast(f32x4, cat(tr_read(pa + 16384), tr_read(pa + 16384 + 4096)));
#pragma unroll
            for (int k = 0; k < 4; ++k) { wva[4 * a + k] = f0[k]; wvb[4 * a + k] = f1[k]; }
        }
        asm volatile("" : "+{v[224:255]}"(wva), "+{v[32:63]}"(wvb));
    };

    if (w < 4) {
        if (isprod) {
            u32x4 d4[3], s4[3];
            unsigned d1, s1, kb;
            const unsigned e0 = pg * CH + 4 * t, e1 = pg * CH + 3072 + t;
            {
                const u32x4 srd_d = make_srd(dst, (unsigned)E * 4u), srd_s = make_srd(src, (unsigned)E * 4u);
                const unsigned o = e0 * 4, o2 = o + 8192, o1 = w == 0 ? e1 * 4 : OOB;
                const unsigned k4 = 4096;
                LD4(d4[0], srd_d, o, ""); LD4S(d4[1], srd_d, o, k4, ""); LD4(d4[2], srd_d, o2, ""); LD1(d1, srd_d, o1, "");
                LD4(s4[0], srd_s, o, ""); LD4S(s4[1], srd_s, o, k4, ""); LD4(s4[2], srd_s, o2, ""); LD1(s1, srd_s, o1, "");
                const unsigned ok = WS_KEY + t * 4, om = WS_KEY + b * 4;
                LD1(kb, srd_ws, ok, " sc1"); LD1(mykey, srd_ws, om, " sc1");
            }
            zero_staging();
            if (t < 8) *(LAS u32x4*)(smem + L_CNT + t * 16) = u32x4{0u, 0u, 0u, 0u};
            unsigned ob = WS_INBOX + ((t >> 3) * G1 + pg) * 128 + pc * 16, ima = lds0 + L_IMG + (t >> 3) * 144 + pc * 16;
            PIN(ob); PIN(ima);
            WG_BARRIER();
            if (PRIO) __builtin_amdgcn_s_setprio(2);
            STAMP(1);
            unsigned b4[13], rk[13];
            const bool v1 = w == 0 && e1 < (unsigned)E;
            asm volatile("s_waitcnt vmcnt(6)" : "+v"(d4[0]), "+v"(d4[1]), "+v"(d4[2]), "+v"(d1) :: "memory");
            STAMP(2);
#pragma unroll
            for (int j = 0; j < 3; ++j)
                if (e0 + 1024 * j < (unsigned)E) {
#pragma unroll
                    for (int k = 0; k < 4; ++k) { b4[4 * j + k] = byte0_x4(d4[j][k]); rk[4 * j + k] = lds_add(smem + L_CNT1 + b4[4 * j + k], 4); }
                }
            if (v1) { b4[12] = byte0_x4(d1); rk[12] = lds_add(smem + L_CNT1 + b4[12], 4); }
            asm volatile("s_waitcnt vmcnt(1)" : "+v"(s4[0]), "+v"(s4[1]), "+v"(s4[2]), "+v"(s1), "+v"(kb) :: "memory");
            auto put = [&](unsigned d, unsigned sv, unsigned bb, unsigned r) {
                *(LAS unsigned*)(smem + L_IMG + 8 + mad24(bb, 36, min(r, 4u * CAP1))) = __builtin_amdgcn_perm(sv + BIAS, d, 0x01050401u);
            };
            auto spill = [&](unsigned d, unsigned sv, unsigned r) {
                if (r >= 4u * CAP1) {
                    const unsigned o = WS_OVF + (pg * CH + lds_add(smem + L_CNT1 + 2048, 1)) * 4, v = (sv << 16) | d;
                    ST1(v, srd_ws, o, " sc1");
                }
            };
#pragma unroll
            for (int j = 0; j < 3; ++j)
                if (e0 + 1024 * j < (unsigned)E) {
#pragma unroll
                    for (int k = 0; k < 4; ++k) put(d4[j][k], s4[j][k], b4[4 * j + k], rk[4 * j + k]);
                    if (__builtin_expect(max(max(rk[4 * j], rk[4 * j + 1]), max(rk[4 * j + 2], rk[4 * j + 3])) >= 4u * CAP1, 0)) {
#pragma unroll
                        for (int k = 0; k < 4; ++k) spill(d4[j][k], s4[j][k], rk[4 * j + k]);
                    }
                }
            if (v1) { put(d1, s1, b4[12], rk[12]); spill(d1, s1, rk[12]); }
            *(LAS unsigned*)(smem + L_CNT1 + 1024 + t * 4) = kb + KEY_STEP;
            WG_BARRIER();
            STAMP(3);
            unsigned c4[8], kk[8], cs[8]; u32x4 v[8];
            const unsigned nov = *(LAS unsigned*)(smem + L_CNT1 + 2048);
#pragma unroll
            for (int it = 0; it < 8; ++it) {
                c4[it] = *(LAS unsigned*)(smem + L_CNT1 + (t >> 3) * 4 + it * 128);
                kk[it] = *(LAS unsigned*)(smem + L_CNT1 + 1024 + (t >> 3) * 4 + it * 128);
                v[it] = *(LAS u32x4*)(size_t)(ima + it * (32 * 144));
            }
#pragma unroll
            for (int it = 0; it < 8; ++it) cs[it] = sum8(v[it].x + v[it].y + v[it].z + v[it].w);
            lds_arrive(smem + L_CNT + 84, l);
            const unsigned no = rfl(nov);
            if (__builtin_expect(no != 0, 0)) {
                const unsigned o = t == 0 ? WS_NOVF + pg * 4 : OOB;
                ST1(no, srd_ws, o, " sc1");
                VMWAIT(0);
                WG_BARRIER();
            }
#pragma unroll
            for (int it = 0; it < 8; ++it) {
                v[it].x = pc == 0 ? c4[it] : v[it].x; v[it].y = pc == 0 ? cs[it] : v[it].y;
                v[it] ^= kk[it];
                const unsigned o = (WMODE == 2 || (int)c4[it] > 16 * pc - 8) ? ob : OOB, so = it * 32 * G1 * 128;
                asm volatile("buffer_store_dwordx4 %0, %1, %2, %3 offen sc1\n\ts_nop 1" :: "v"(v[it]), "v"(o), "s"(srd_ws), "s"(so) : "memory");
            }
            asm volatile("s_waitcnt vmcnt(8)" : "+v"(mykey) :: "memory");
            STAMP(4);
        } else {
            { const unsigned om = WS_KEY + b * 4; LD1(mykey, srd_ws, om, " sc1"); }
            if (t < 8) *(LAS u32x4*)(smem + L_CNT + t * 16) = u32x4{0u, 0u, 0u, 0u};
            WG_BARRIER();
            asm volatile("s_waitcnt vmcnt(0)" : "+v"(mykey) :: "memory");
            if (NP_SLEEP) __builtin_amdgcn_s_sleep(NP_SLEEP);
            const unsigned o = ((b >> 4) * 256 + t) * 128;
            set_m0(lds0 + L_DUMP + w * 256);
            STAMP(1);
            for (int r = 0; r < PF_R; ++r) {
                asm volatile("s_waitcnt vmcnt(%c0)" :: "i"(PF_D) : "memory");
                if (__builtin_amdgcn_s_memrealtime() > t_start + PF_T) break;
                const unsigned so = r << 19;
                asm volatile("buffer_load_dword %1, %0, %2 offen lds" :: "s"(srd_x0), "v"(o), "s"(so) : "memory");
            }
            if (PRIO) __builtin_amdgcn_s_setprio(2);
            STAMP(2); STAMP(3); STAMP(4);
        }
        STAMP(5);
        mykey = rfl(mykey) + KEY_STEP;
        loop_constants();
        unsigned ib = WS_INBOX + b * (G1 * 128) + t * 16;
        PIN(ib);
        while (__builtin_amdgcn_s_memrealtime() < t_start + POLL_T) __builtin_amdgcn_s_sleep(1);
        STAMP(6);
        u32x16 raw;
        inbox_request(raw, srd_ws, ib);
        inbox_stage(raw, srd_ws, ib, mykey, 16 * pc - 8, lds0 + L_HDR + (t >> 3) * 4);
        if (PRIO) __builtin_amdgcn_s_setprio(0);
        STAMP(7);
    } else {
        const int j = w - 4;
        if (isprod) zero_staging();
        WG_BARRIER();
        STAMP(1);
        u32x4 wr[8], xr[8];
        {
            const u32x4 srd_q = make_srd(wq, 32768);
            const unsigned o = j * 8192 + l * 16, k4 = 4096;
            LD4(wr[0], srd_q, o, ""); LD4(wr[1], srd_q, o, " offset:1024"); LD4(wr[2], srd_q, o, " offset:2048"); LD4(wr[3], srd_q, o, " offset:3072");
            LD4S(wr[4], srd_q, o, k4, ""); LD4S(wr[5], srd_q, o, k4, " offset:1024"); LD4S(wr[6], srd_q, o, k4, " offset:2048"); LD4S(wr[7], srd_q, o, k4, " offset:3072");
#pragma unroll
            for (int m = 0; m < 8; ++m) { const unsigned ox = node_row(m < 4 ? j : w, m & 3) + n * 16; LD4(xr[m], srd_x0, ox, ""); }
        }
        unsigned aqa[8], xfa[4];
        unsigned xa = ring + g * 256 + ((n ^ g) << 4), wa = lds0 + L_WQ + j * 16384 + g * 256 + ((n ^ g) << 4) + ((n & 4) << 1);
        {
            const unsigned aq = lds0 + L_WQ + (8 * g + q) * 256 + ((((4 * p) ^ (q | ((g & 1) << 2))) << 4) | ((p & 1) << 3));
#pragma unroll
            for (int s = 0; s < 4; ++s) { xfa[s] = ring + n * 256 + (((4 * s + g) ^ n) << 4); PIN(xfa[s]); }
#pragma unroll
            for (int c = 0; c < 8; ++c) { aqa[c] = aq ^ ((c >> 1) << 4) ^ ((c & 1) << 3); PIN(aqa[c]); }
            PIN(xa); PIN(wa);
        }
        STAMP(2);
        if (isprod) {
            WG_BARRIER();
            if (__builtin_expect(rfl(*(LAS unsigned*)(smem + L_CNT1 + 2048)) != 0, 0)) WG_BARRIER();
            if (W_SLEEP) __builtin_amdgcn_s_sleep(W_SLEEP);
        }
        STAMP(3);
        asm volatile("s_waitcnt vmcnt(8)" : "+v"(wr[0]), "+v"(wr[1]), "+v"(wr[2]), "+v"(wr[3]), "+v"(wr[4]), "+v"(wr[5]), "+v"(wr[6]), "+v"(wr[7]) :: "memory");
#pragma unroll
        for (int m = 0; m < 8; ++m) {
            const unsigned a = (wa ^ ((m & 2) << 5)) + m * 1024;
            *(LAS u32x2*)(size_t)a = u32x2{wr[m].x, wr[m].y};
            *(LAS u32x2*)(size_t)(a ^ 8) = u32x2{wr[m].z, wr[m].w};
        }
        asm volatile("s_waitcnt vmcnt(0)" : "+v"(xr[0]), "+v"(xr[1]), "+v"(xr[2]), "+v"(xr[3]), "+v"(xr[4]), "+v"(xr[5]), "+v"(xr[6]), "+v"(xr[7]) :: "memory");
#pragma unroll
        for (int m = 0; m < 8; ++m) *(LAS u32x4*)(size_t)((xa ^ ((m & 3) << 6)) + m * 1024) = xr[m];
        STAMP(4);
        lds_arrive(smem + L_CNT + 80, l);
        lds_await(smem + L_CNT + 80, 4);
        STAMP(5);
        bf16x8 xo[2][4], fr[2][8];
        f32x4 qc[2][8];
        auto wq_frags = [&](int u, bf16x8 (&f)[8]) {
#pragma unroll
            for (int a = 0; a < 8; ++a) {
                LAS char* pa = (LAS char*)(size_t)aqa[a] + u * 16384;
                f[a] = cat(tr_read(pa), tr_read(pa + 1024));
            }
        };
#pragma unroll
        for (int u = 0; u < 4; ++u) { xo[0][u] = *(LAS bf16x8*)(size_t)xfa[u]; xo[1][u] = *(LAS bf16x8*)(size_t)(xfa[u] + 4096); }
        wq_frags(0, fr[0]);
        {
            const Srd4 srd_v = make_srd4(wv, 32768);
            const unsigned r0 = (j * 32 + g) << 8, o0 = (n ^ ((g & 1) | ((g & 2) << 1))) << 4;
            const unsigned e[8] = {r0, r0 + 1024, r0 + 2048, r0 + 3072, r0 + 4096, r0 + 5120, r0 + 6144, r0 + 7168};
            const unsigned base = lds0 + (j < 2 ? L_WV0 + j * 16384 : L_WV1 + (j - 2) * 8192);
            const u32x4 off = {o0, o0 ^ 128, o0, o0 ^ 128};
            dma_half(base, srd_v, e, off); dma_half(base + 4096, srd_v, e + 4, off);
        }
#pragma unroll
        for (int u = 0; u < 4; ++u) {
            if (u < 3) wq_frags(u + 1, fr[(u + 1) & 1]);
            FENCE();
#pragma unroll
            for (int a = 0; a < 8; ++a)
#pragma unroll
                for (int tt = 0; tt < 2; ++tt) qc[tt][a] = mfma(fr[u & 1][a], xo[tt][u], u ? qc[tt][a] : f32x4{0.f, 0.f, 0.f, 0.f});
            FENCE();
            if (u == 1 && j < 2) { VMWAIT(0); lds_arrive(smem + L_CNT + 88, l); }
        }
        STAMP(6);
        if (isprod) lds_await(smem + L_CNT + 84, 4);
#pragma unroll
        for (int tt = 0; tt < 2; ++tt)
#pragma unroll
            for (int s = 0; s < 4; ++s) {
                const float k = -0.08838834764831845f * 1.4426950408889634f;
                const f32x4 c0 = qc[tt][2 * s] * k, c1 = qc[tt][2 * s + 1] * k;
                *(LAS u32x4*)(size_t)(ring - (tt ? 0 : 65536) + s * 1024 + l * 16) = u32x4{pk2(c0[0], c0[1]), pk2(c0[2], c0[3]), pk2(c1[0], c1[1]), pk2(c1[2], c1[3])};
            }
        loop_constants();
        lds_await(smem + L_CNT + 88, 2);
        wv_resident();
        VMWAIT(0);
        STAMP(7);
    }
    WG_BARRIER();
    STAMP(8);
    if (LPRIO >= 3) __builtin_amdgcn_s_setprio(3);
    if (LPRIO == 1 && w >= 4) __builtin_amdgcn_s_setprio(1);
    if (LPRIO == 2) { if (w >= 6) __builtin_amdgcn_s_setprio(3); else if (w >= 4) __builtin_amdgcn_s_setprio(2); else if (w >= 2) __builtin_amdgcn_s_setprio(1); }

    u32x16 qf;
    const u32x4 doff = {addr[12], addr[13], addr[14], addr[15]};
    LAS char* mylist = smem + L_LIST + w * (2 * SUBB);
    int gcur = G1, pos = 0;
    auto batch = [&](auto first_) {
        constexpr bool first = decltype(first_)::value;
        auto entries = [&](int grp, unsigned (&e)[8]) {
#pragma unroll
            for (int m = 0; m < 8; ++m) e[m] = *(LAS unsigned*)(size_t)(lista + grp * 128 + m * 16);
        };
        unsigned cw = *(LAS unsigned*)(size_t)cnta, e0[8], e1[8];
        entries(0, e0);
        if constexpr (first) {
#pragma unroll
            for (int s = 0; s < 4; ++s) {
                const u32x4 v = *(LAS u32x4*)(size_t)(qa + s * 1024);
                qf[4 * s] = v.x; qf[4 * s + 1] = v.y; qf[4 * s + 2] = v.z; qf[4 * s + 3] = v.w;
            }
        }
        entries(1, e1);
        FENCE();
        int lo = __builtin_amdgcn_readlane(cw, 2 * w) >> 2, hi = __builtin_amdgcn_readlane(cw, 2 * w + 1) >> 2;
        if constexpr (first) {
            if (__builtin_amdgcn_readlane(cw, 16)) gcur = 0;
            if (__builtin_amdgcn_ballot_w64(cw > 4u * CAP2)) { gcur = -1; lo = 0; hi = 0; }
        }
        const int c = lo + hi, G = (c + 31) >> 5;
        const int nlast = G > 2 ? (((c - 1) & 31) >> 2) + 1 : 8;
        auto join = [&]() {
            for (int k = 0; k < G * 32 - lo; k += 64) {
                const unsigned e = *(LAS unsigned*)(mylist + SUBB + (l + k) * 4);
                if (l + k < G * 32 - lo) *(LAS unsigned*)(mylist + (lo + l + k) * 4) = l + k < hi ? e : PAD;
            }
        };
        const bool early = lo >= 64;
        if (__builtin_expect(!early, 0)) { join(); entries(0, e0); entries(1, e1); }
        if constexpr (first) asm volatile("s_waitcnt lgkmcnt(0)" : "+{v[160:175]}"(qf) :: "memory");
        if (G > 0) dma_group(ring, srd_x, e0, doff);
        if constexpr (first) {
            if (w < 4) {
                if (G > 1) dma_group(ring + 8192, srd_x, e1, doff);
                wv_resident();
                lds_arrive(smem + L_CNT + 92, l);
            } else {
                if (w >= 6) lds_await(smem + L_CNT + 92, 4);
                if (G > 1) dma_group(ring + 8192, srd_x, e1, doff);
            }
        } else if (G > 1) dma_group(ring + 8192, srd_x, e1, doff);
        f32x16 cc;
#pragma unroll
        for (int m = 0; m < 8; ++m) {
            cc[m] = (e0[m] & 0xFF) == keyn ? 0.f : __uint_as_float(inf);
            cc[8 + m] = (e1[m] & 0xFF) == keyn ? 0.f : __uint_as_float(inf);
        }
        if (early) join();
        STAMP(9);
        if (G > 0) gather_loop(acc, qf, addr, u32x4{lista, keyn, inf, 0u}, cc, srd_x, ring, G, nlast);
    };
    batch(std::true_type{});
    while (__builtin_expect(gcur < G1, 0)) {
        unsigned lim = G1 * 32;
        if (gcur >= 0) {
            const u64 m0 = __builtin_amdgcn_ballot_w64(l >= gcur && *(LAS unsigned*)(smem + L_HDR + l * 4) > 4u * CAP1);
            const u64 m1 = __builtin_amdgcn_ballot_w64(l + 64 >= gcur && *(LAS unsigned*)(smem + L_HDR + 256 + l * 4) > 4u * CAP1);
            if (!(m0 | m1)) break;
            gcur = m0 ? __builtin_ctzll(m0) : 64 + __builtin_ctzll(m1);
            unsigned v; const unsigned o = WS_NOVF + gcur * 4;
            asm volatile("buffer_load_dword %0, %1, %2, 0 offen sc1\n\ts_waitcnt vmcnt(0)" : "=&v"(v) : "v"(o), "s"(srd_ws) : "memory");
            lim = rfl(v);
        }
        const unsigned i = pos + t;
        const bool cand = t < CAP2 && i < lim;
        unsigned u;
        { const unsigned o = cand ? (gcur < 0 ? WS_INBOX + b * (G1 * 128) : WS_OVF + gcur * CH * 4) + i * 4 : OOB;
          asm volatile("buffer_load_dword %0, %1, %2, 0 offen sc1\n\ts_waitcnt vmcnt(0)" : "=&v"(u) : "v"(o), "s"(srd_ws) : "memory"); }
        WG_BARRIER();
        if (t < 16) *(LAS unsigned*)(smem + L_CNT + t * 4) = 0;
        WG_BARRIER();
        if (cand) {
            unsigned e; bool v;
            if (gcur < 0) { e = u ^ mykey; v = ((i & 31) - 2) * 4 < *(LAS unsigned*)(smem + L_HDR + (i >> 5) * 4); }
            else { e = (((u >> 16) + BIAS) << 8) | ((u >> 8) & 0xFF); v = (u & 0xFF) == (unsigned)b; }
            if (v) *(LAS unsigned*)(smem + L_LIST + (e & 7) * (2 * SUBB) + lds_add(smem + L_CNT + (e & 7) * 8, 4)) = e;
        }
        pos += CAP2;
        if ((unsigned)pos >= lim) { ++gcur; pos = 0; }
        WG_BARRIER();
        batch(std::false_type{});
    }
    STAMP(10);

    {
        bf16x8 wl[2][8], ag[4];
#pragma unroll
        for (int u = 0; u < 2; ++u)
#pragma unroll
            for (int a = 0; a < 8; ++a) {
                LAS char* pa = smem + L_WV1 + (cv ^ ((a >> 1) << 6) ^ ((a & 1) << 3)) + u * 8192;
                wl[u][a] = cat(tr_read(pa), tr_read(pa + 4096));
            }
#pragma unroll
        for (int u = 0; u < 4; ++u) {
            const u32x4 v = {pk2(acc[8 * u], acc[8 * u + 1]), pk2(acc[8 * u + 2], acc[8 * u + 3]),
                             pk2(acc[8 * u + 4], acc[8 * u + 5]), pk2(acc[8 * u + 6], acc[8 * u + 7])};
            ag[u] = __builtin_bit_cast(bf16x8, v);
        }
        FENCE();
        f32x4 oc[8];
#pragma unroll
        for (int a = 0; a < 8; ++a) oc[a] = mfma(__builtin_bit_cast(bf16x8, f32x4{wva[4 * a], wva[4 * a + 1], wva[4 * a + 2], wva[4 * a + 3]}), ag[0], f32x4{0.f, 0.f, 0.f, 0.f});
#pragma unroll
        for (int a = 0; a < 8; ++a) oc[a] = mfma(__builtin_bit_cast(bf16x8, f32x4{wvb[4 * a], wvb[4 * a + 1], wvb[4 * a + 2], wvb[4 * a + 3]}), ag[1], oc[a]);
#pragma unroll
        for (int a = 0; a < 8; ++a) oc[a] = mfma(wl[0][a], ag[2], oc[a]);
        FENCE();
        const unsigned ow = ring + n * 256 + ((g ^ n) << 4) + ((g & 1) << 3);
#pragma unroll
        for (int a = 0; a < 8; ++a) oc[a] = mfma(wl[1][a], ag[3], oc[a]);
#pragma unroll
        for (int a = 0; a < 8; ++a)
            *(LAS u32x2*)(size_t)(ow ^ ((a >> 1) << 6) ^ ((a & 1) << 3)) = u32x2{pk2(oc[a][0], oc[a][1]), pk2(oc[a][2], oc[a][3])};
        const auto rs_o = __builtin_amdgcn_make_buffer_rsrc((void*)out, 0, xbytes, 0x00020000);
#pragma unroll
        for (int m = 0; m < 4; ++m) {
            const u32x4 v = *(LAS u32x4*)(size_t)(ring + m * 1024 + l * 16);
            __builtin_amdgcn_raw_buffer_store_b128(v, rs_o, node_row(w, m) + ((n ^ g ^ (4 * m)) << 4), 0, OUT_AUX);
        }
    }
    if (w == 0) { const unsigned o = l == 0 ? WS_KEY + b * 4 : OOB; ST1(mykey, srd_ws, o, " sc1"); }
    STAMP(11);
    asm volatile("s_waitcnt vmcnt(0)" ::: "memory");
    STAMP(12);
    __syncthreads();
    STAMP(13);
    {
        const unsigned long long tgt = st_[13] + (unsigned long long)PROBE_M * (st_[PROBE_B] - st_[PROBE_A]);
        if (threadIdx.x < 64) { int guard = 0; while (wall_clock64() < tgt && ++guard < 200000) __builtin_amdgcn_s_sleep(2); }
    }
}

extern "C" void kernel_launch(void* const* d_in, const int* in_sizes, int n_in,
                              void* d_out, int out_size, void* d_ws, size_t ws_size,
                              hipStream_t stream) {
    const int N = in_sizes[0] / 128, E = in_sizes[1];
    k_graph<<<NB, 512, 0, stream>>>((const unsigned short*)d_in[0], (const int*)d_in[1], (const int*)d_in[2],
                                    (const unsigned short*)d_in[3], (const unsigned short*)d_in[4],
                                    (unsigned*)d_ws, (unsigned short*)d_out, N, E);
}
```

```cpp
#define POLL_T 400
#include <hip/hip_runtime.h>
#include <hip/hip_bf16.h>
#include <stdint.h>
#include <type_traits>
#define ORD 1
#define SKIP 0
#define STR2(x) #x
#define STR(x) STR2(x)

typedef __attribute__((ext_vector_type(8))) __bf16 bf16x8;
typedef __attribute__((ext_vector_type(4))) __bf16 bf16x4;
typedef __attribute__((ext_vector_type(2))) __bf16 bf16x2;
typedef __attribute__((ext_vector_type(4))) float f32x4;
typedef __attribute__((ext_vector_type(2))) float f32x2;
typedef __attribute__((ext_vector_type(4))) unsigned u32x4;
typedef __attribute__((ext_vector_type(2))) unsigned u32x2;
typedef __attribute__((ext_vector_type(32))) float f32x32;
typedef __attribute__((ext_vector_type(16))) float f32x16;
typedef __attribute__((ext_vector_type(16))) unsigned u32x16;
typedef __attribute__((ext_vector_type(12))) unsigned u32x12;
typedef unsigned long long u64;
#define LAS __attribute__((address_space(3)))

constexpr int NB = 256;
constexpr int G1 = 128;
constexpr int CH = 3136;
constexpr int CAP1 = 30;
constexpr int CAP2 = 192;
constexpr int SUBB = 784;
#define POLL_AT 3
#define OUT_AUX 16
#define PF_R 0
#define PF_D 0
#define PF_T 200
#define WMODE 2
#define LPRIO 3
#define PKADD 1
#define WS_BASE 0
#define TOUCH 0
#define NP_SLEEP 0
#define W_SLEEP 3
#define PRIO 1
#define SPLIT 16384
constexpr unsigned KEY_STEP = 0x9E3779B1u;
constexpr unsigned WS_KEY = 0;
constexpr unsigned WS_NOVF = 2048;
constexpr unsigned WS_INBOX = 4096;
constexpr unsigned WS_OVF = WS_INBOX + NB * G1 * 128;
constexpr unsigned WS_BYTES = WS_OVF + G1 * CH * 4;
constexpr unsigned OOB = 0x80000000u;
constexpr unsigned BIAS = 32768 - SPLIT;
constexpr unsigned PAD = 0x00FFFFFFu;

constexpr int L_CNT = 0;
constexpr int L_HDR = 128;
constexpr int L_LIST = 768;
constexpr int L_RING = L_LIST + 16 * SUBB;
constexpr int L_IMG = L_RING;
constexpr int L_CNT1 = L_IMG + NB * 144;
constexpr int L_WQ = L_RING + 2 * 16384 + 8192;
constexpr int L_WV0 = L_RING + 6 * 16384 + 8192;
constexpr int L_WV1 = L_RING + 131072;
constexpr int L_DUMP = L_WV1 + 16384;
constexpr int L_TOTAL = L_DUMP + 2048;
static_assert(L_RING % 256 == 0 && L_TOTAL <= 163840, "LDS map");

#define STAMP_DECL
#define STAMP(k)
#define STAMP_FLUSH(base, k0, k1)

__device__ __forceinline__ u32x4 make_srd(const void* p, unsigned bytes, unsigned back = 0) {
    const u64 a = (u64)p - back;
    u32x4 s; s.x = (unsigned)a; s.y = (unsigned)(a >> 32) & 0xFFFFu; s.z = bytes + back; s.w = 0x00020000u; return s;
}
#define LD4(dst, srd, off, FL) asm volatile("buffer_load_dwordx4 %0, %1, %2, 0 offen" FL : "=v"(dst) : "v"(off), "s"(srd) : "memory")
#define LD4S(dst, srd, off, so, FL) asm volatile("buffer_load_dwordx4 %0, %1, %2, %3 offen" FL : "=v"(dst) : "v"(off), "s"(srd), "s"(so) : "memory")
#define LD1(dst, srd, off, FL) asm volatile("buffer_load_dword %0, %1, %2, 0 offen" FL : "=v"(dst) : "v"(off), "s"(srd) : "memory")
#define ST1(val, srd, off, FL) asm volatile("buffer_store_dword %0, %1, %2, 0 offen" FL :: "v"(val), "v"(off), "s"(srd) : "memory")
#define PIN(x) asm volatile("" : "+v"(x))
#define VMWAIT(n) asm volatile("s_waitcnt vmcnt(" #n ")" ::: "memory")
#define FENCE() __builtin_amdgcn_sched_barrier(0)
#define WG_BARRIER() do { asm volatile("s_waitcnt lgkmcnt(0)" ::: "memory"); __builtin_amdgcn_s_barrier(); asm volatile("" ::: "memory"); } while (0)

struct Srd4 { u32x4 s0, s1, s2, s3; };
__device__ __forceinline__ Srd4 make_srd4(const void* p, unsigned bytes, unsigned back = 0) {
    return Srd4{make_srd(p, bytes, back), make_srd(p, bytes, back + 1024), make_srd(p, bytes, back + 2048), make_srd(p, bytes, back + 3072)};
}

__device__ __forceinline__ void set_m0(unsigned v) { asm volatile("s_mov_b32 m0, %0" :: "s"(v) : "memory"); }
template <int R> __device__ __forceinline__ void dma_rows(const Srd4& d, unsigned e, unsigned off) {
    unsigned tmp, mask = 0x00FFFF00u;
    asm volatile("v_and_or_b32 %0, %1, %2, %3\n\tbuffer_load_dwordx4 %0, %4, 0 offen offset:%c5 lds"
                 : "=&v"(tmp) : "v"(e), "s"(mask), "v"(off), "s"(R == 0 ? d.s0 : R == 1 ? d.s1 : R == 2 ? d.s2 : d.s3), "i"(R * 1024) : "memory");
}
__device__ __forceinline__ void dma_half(unsigned ldsbase, const Srd4& d, const unsigned* e, const u32x4& off) {
    set_m0(ldsbase);
    dma_rows<0>(d, e[0], off.x); dma_rows<1>(d, e[1], off.y); dma_rows<2>(d, e[2], off.z); dma_rows<3>(d, e[3], off.w);
}
__device__ __forceinline__ void dma_group(unsigned ldsbase, const Srd4& d, const unsigned (&e)[8], const u32x4& off) {
    asm volatile("s_waitcnt lgkmcnt(0)" ::: "memory");
    dma_half(ldsbase, d, e, off); dma_half(ldsbase + 4096, d, e + 4, off);
}

__device__ __forceinline__ void gather_loop(f32x32& acc, const u32x16& qf, const u32x16& addr, u32x4 misc, f32x16 cc,
                                            const Srd4& d, unsigned ring, int G, int nlast) {
    f32x32 t0, t1; u32x12 t2; int tmp, pr; u64 sm; unsigned mask = 0x00FFFF00u;
    asm volatile(
        ".macro GWAIT k\n\ts_cmp_gt_i32 %[t], \\k\n\ts_cbranch_scc1 1f\n\ts_waitcnt vmcnt(\\k)\n\t.endm\n\t"
        ".macro GROWS so\n\t.irp s, 0, 1, 2, 3\n\t"
        "ds_read_b128 v[64+8*\\s:67+8*\\s], v[176+\\s] offset:\\so\n\t"
        "ds_read_b128 v[68+8*\\s:71+8*\\s], v[176+\\s] offset:\\so+4096\n\t.endr\n\t.endm\n\t"
        ".macro GTR c, so\n\t"
        "ds_read_b64_tr_b16 v[96+4*\\c:97+4*\\c], v[180+\\c] offset:\\so\n\t"
        "ds_read_b64_tr_b16 v[98+4*\\c:99+4*\\c], v[180+\\c] offset:\\so+4096\n\t.endm\n\t"
        ".macro GS s, c\n\t"
        "v_mfma_f32_16x16x32_bf16 v[\\c:\\c+3], v[64+8*\\s:67+8*\\s], v[160+4*\\s:163+4*\\s], v[\\c:\\c+3]\n\t"
        "v_mfma_f32_16x16x32_bf16 v[\\c+4:\\c+7], v[68+8*\\s:71+8*\\s], v[160+4*\\s:163+4*\\s], v[\\c+4:\\c+7]\n\t.endm\n\t"
        ".macro GLOAD m, r, srd\n\t"
        "v_and_or_b32 v195, v[212+\\m], %[mask], v[188+\\r]\n\t"
        "buffer_load_dwordx4 v195, \\srd, 0 offen offset:1024*\\r lds\n\t.endm\n\t"
        ".macro GPART m, r, srd\n\ts_cmp_le_i32 %[nl], \\m\n\ts_cbranch_scc1 4f\n\tGLOAD \\m, \\r, \\srd\n\t.endm\n\t"
        ".macro GSUM m\n\tv_mfma_f32_16x16x32_bf16 v[128+4*(\\m):131+4*(\\m)], v[96+4*(\\m):99+4*(\\m)], v[220:223], v[128+4*(\\m):131+4*(\\m)]\n\t.endm\n\t"
        ".macro GCMP m, d\n\tv_cmp_ne_u32_sdwa \\d, v[212+\\m], v193 src0_sel:BYTE_0 src1_sel:DWORD\n\t.endm\n\t"
        ".macro GPAIR m, c\n\t"
        "GSUM \\m\n\tGCMP \\m+1, %[sm]\n\tv_cndmask_b32_e32 v[\\c+\\m], 0, v194, vcc\n\t"
        "GSUM \\m+1\n\t.if \\m < 6\n\tGCMP \\m+2, vcc\n\t.else\n\ts_cmp_gt_i32 %[rem], 0\n\t.endif\n\t"
        "v_cndmask_b32_e64 v[\\c+\\m+1], 0, v194, %[sm]\n\t.endm\n\t"
        ".macro GSTEP so, c, eo\n\t"
        "GTR 0, \\so\n\t"
        "s_waitcnt lgkmcnt(2)\n\tGS 0, \\c\n\t"
        "GTR 1, \\so\n\tGTR 2, \\so\n\tGS 1, \\c\n\t"
        "GTR 3, \\so\n\tGTR 4, \\so\n\tGS 2, \\c\n\t"
        "GTR 5, \\so\n\tGTR 6, \\so\n\tGS 3, \\c\n\t"
        "GTR 7, \\so\n\t"
        "ds_read2_b32 v[212:213], v192 offset0:\\eo offset1:\\eo+4\n\t"
        "ds_read2_b32 v[214:215], v192 offset0:\\eo+8 offset1:\\eo+12\n\t"
        "ds_read2_b32 v[216:217], v192 offset0:\\eo+16 offset1:\\eo+20\n\t"
        "ds_read2_b32 v[218:219], v192 offset0:\\eo+24 offset1:\\eo+28\n\t"
        "s_add_u32 m0, %[ring], \\so\n\t"
        "s_cmp_gt_i32 %[rem], 3\n\t"
        ".irp k, 0, 1, 2, 3, 4, 5, 6, 7\n\tv_exp_f32_e32 v[\\c+\\k], v[\\c+\\k]\n\t.endr\n\t"
        "s_waitcnt lgkmcnt(0)\n\t"
        "s_cbranch_scc0 2f\n\t"
        "GLOAD 0, 0, %[s0]\n\tGLOAD 1, 1, %[s1]\n\tGLOAD 2, 2, %[s2]\n\tGLOAD 3, 3, %[s3]\n\t"
        "s_add_u32 m0, m0, 0x1000\n\t"
        "GLOAD 4, 0, %[s0]\n\tGLOAD 5, 1, %[s1]\n\tGLOAD 6, 2, %[s2]\n\tGLOAD 7, 3, %[s3]\n\t"
        "GSIG \\c\n\t"
        "s_waitcnt vmcnt(8)\n\t"
        "s_branch 1f\n\t"
        "2:\n\t"
        "s_mov_b32 %[t], 0\n\t"
        "s_cmp_eq_u32 %[rem], 3\n\ts_cbranch_scc0 4f\n\t"
        "s_mov_b32 %[t], %[nl]\n\t"
        "GLOAD 0, 0, %[s0]\n\tGPART 1, 1, %[s1]\n\tGPART 2, 2, %[s2]\n\tGPART 3, 3, %[s3]\n\t"
        "s_add_u32 m0, m0, 0x1000\n\t"
        "GPART 4, 0, %[s0]\n\tGPART 5, 1, %[s1]\n\tGPART 6, 2, %[s2]\n\tGPART 7, 3, %[s3]\n\t"
        "4:\n\t"
        "GSIG \\c\n\t"
        "s_waitcnt vmcnt(8)\n\t"
        "GWAIT 7\n\tGWAIT 6\n\tGWAIT 5\n\tGWAIT 4\n\tGWAIT 3\n\tGWAIT 2\n\tGWAIT 1\n\tGWAIT 0\n\t"
        "1:\n\t"
        "GROWS 8192-\\so\n\t"
        "GCMP 0, vcc\n\t"
        "s_add_i32 %[rem], %[rem], -1\n\t"
        "GPAIR 0, \\c\n\tGPAIR 2, \\c\n\tGPAIR 4, \\c\n\tGPAIR 6, \\c\n\t"
        ".endm\n\t"
        ".macro GSIG c\n\t"
        ".if " STR(PKADD) "\n\t.irp k, 0, 2, 4, 6\n\tv_pk_add_f32 v[\\c+\\k:\\c+\\k+1], v[\\c+\\k:\\c+\\k+1], 1.0 op_sel_hi:[1,0]\n\t.endr\n\t.else\n\t"
        ".irp k, 0, 1, 2, 3, 4, 5, 6, 7\n\tv_add_f32_e32 v[\\c+\\k], 1.0, v[\\c+\\k]\n\t.endr\n\t.endif\n\t"
        ".irp k, 0, 1, 2, 3, 4, 5, 6, 7\n\tv_rcp_f32_e32 v[\\c+\\k], v[\\c+\\k]\n\t.endr\n\t"
        ".irp k, 0, 1, 2, 3\n\tv_cvt_pk_bf16_f32 v[220+\\k], v[\\c+2*\\k], v[\\c+2*\\k+1]\n\t.endr\n\t.endm\n\t"
        "s_waitcnt vmcnt(8)\n\t"
        "s_cmp_gt_i32 %[rem], 1\n\ts_cbranch_scc1 1f\n\ts_waitcnt vmcnt(0)\n\t1:\n\t"
        "GROWS 0\n\t"
        ".if " STR(LPRIO) " >= 3\n\ts_mov_b32 %[pr], 0\n\ts_setprio 2\n\t.endif\n\t"
        "0:\n\t"
        ".if " STR(LPRIO) " == 3\n\t"
        "s_cmp_eq_u32 %[pr], 1\n\ts_cbranch_scc0 5f\n\ts_setprio 1\n\t5:\n\t"
        "s_cmp_eq_u32 %[pr], 2\n\ts_cbranch_scc0 5f\n\ts_setprio 0\n\t5:\n\t"
        "s_add_u32 %[pr], %[pr], 1\n\t.endif\n\t"
        "GSTEP 0, 196, 64\n\t"
        "s_cbranch_scc0 9f\n\t"
        "GSTEP 8192, 204, 96\n\t"
        "v_add_u32_e32 v192, 0x100, v192\n\t"
        "s_cbranch_scc1 0b\n\t"
        "9:\n\t"
        "s_waitcnt lgkmcnt(0)\n\ts_nop 6\n\t"
        ".purgem GWAIT\n\t.purgem GROWS\n\t.purgem GTR\n\t.purgem GS\n\t.purgem GLOAD\n\t.purgem GPART\n\t.purgem GSUM\n\t.purgem GCMP\n\t.purgem GPAIR\n\t.purgem GSTEP\n\t.purgem GSIG"
        : "+{v[128:159]}"(acc), "+{v[192:195]}"(misc), "+{v[196:211]}"(cc), "=&{v[64:95]}"(t0), "=&{v[96:127]}"(t1), "=&{v[212:223]}"(t2),
          [rem] "+s"(G), [t] "=&s"(tmp), [pr] "=&s"(pr), [sm] "=&s"(sm)
        : "{v[160:175]}"(qf), "{v[176:191]}"(addr), [s0] "s"(d.s0), [s1] "s"(d.s1), [s2] "s"(d.s2), [s3] "s"(d.s3),
          [mask] "s"(mask), [ring] "s"(ring), [nl] "s"(nlast)
        : "vcc", "scc", "memory");
}

__device__ __forceinline__ void inbox_request(u32x16& raw, const u32x4& srd, unsigned ib) {
    unsigned o1 = 0x1000, o2 = 0x2000, o3 = 0x3000;
    asm volatile("buffer_load_dwordx4 v[64:67], %1, %2, 0 offen sc1\n\tbuffer_load_dwordx4 v[68:71], %1, %2, %3 offen sc1\n\t"
                 "buffer_load_dwordx4 v[72:75], %1, %2, %4 offen sc1\n\tbuffer_load_dwordx4 v[76:79], %1, %2, %5 offen sc1"
                 : "=&{v[64:79]}"(raw) : "v"(ib), "s"(srd), "s"(o1), "s"(o2), "s"(o3) : "memory");
}
__device__ __forceinline__ void inbox_stage(u32x16 raw, const u32x4& srd, unsigned ib, unsigned key, int i4, unsigned hdra) {
    u32x16 t0; u32x4 t1; u64 m0, m1, m2, m3; unsigned todo, fresh, spin, o1, o2, o3;
    u64 pc0 = 0x0101010101010101ull;
    asm volatile(
        ".macro IVAL j, so\n\t"
        "s_bitcmp1_b32 %[todo], \\j\n\ts_cbranch_scc0 2f\n\t"
        ".irp k, 0, 1, 2, 3\n\tv_xor_b32_e32 v[64+4*\\j+\\k], %[key], v[64+4*\\j+\\k]\n\t.endr\n\t"
        "v_mov_b32_dpp v[80+\\j], v[64+4*\\j] quad_perm:[0,0,0,0] row_mask:0xf bank_mask:0xf\n\t"
        "v_add_u32_e32 v96, v[64+4*\\j], v[65+4*\\j]\n\t"
        "v_sub_u32_e32 v97, 0, v[65+4*\\j]\n\t"
        "v_mov_b32_dpp v[80+\\j], v[80+\\j] row_shr:4 row_mask:0xf bank_mask:0xa\n\t"
        "v_cndmask_b32_e64 v96, v96, v97, %[pc0]\n\t"
        "v_cmp_gt_i32_e32 vcc, v[80+\\j], v92\n\t"
        "v_add3_u32 v96, v96, v[66+4*\\j], v[67+4*\\j]\n\t"
        "v_lshrrev_b32_e32 v97, 14, v[80+\\j]\n\t"
        "v_cndmask_b32_e32 v96, 0, v96, vcc\n\t"
        "s_nop 1\n\tv_add_u32_dpp v96, v96, v96 quad_perm:[1,0,3,2] row_mask:0xf bank_mask:0xf\n\t"
        "s_nop 1\n\tv_add_u32_dpp v96, v96, v96 quad_perm:[2,3,0,1] row_mask:0xf bank_mask:0xf\n\t"
        "s_nop 1\n\tv_add_u32_dpp v96, v96, v96 row_half_mirror row_mask:0xf bank_mask:0xf\n\t"
        "v_or_b32_e32 v96, v96, v97\n\t"
        "v_cmp_ne_u32_e32 vcc, 0, v96\n\t"
        "s_cbranch_vccz 1f\n\t"
        "buffer_load_dwordx4 v[64+4*\\j:67+4*\\j], %[ib], %[srd], \\so offen sc1\n\t"
        "s_branch 2f\n\t"
        "1:\n\ts_bitset1_b32 %[fresh], \\j\n\t"
        "2:\n\t.endm\n\t"
        ".macro IRANK j\n\t"
        "s_bitcmp1_b32 %[fresh], \\j\n\ts_cbranch_scc0 2f\n\t"
        "v_cmp_lt_u32_e64 %[m0], v92, v[80+\\j]\n\tv_cmp_lt_u32_e64 %[m1], v93, v[80+\\j]\n\t"
        "v_cmp_lt_u32_e64 %[m2], v94, v[80+\\j]\n\tv_cmp_lt_u32_e64 %[m3], v95, v[80+\\j]\n\t"
        ".irp k, 0, 1, 2, 3\n\tv_lshrrev_b32_e32 v[84+\\k], 21, v[64+4*\\j+\\k]\n\t.endr\n\t"
        ".irp k, 0, 1, 2, 3\n\tv_and_b32_e32 v[84+\\k], 60, v[84+\\k]\n\t.endr\n\t"
        "v_cmp_lt_u32_e32 vcc, %c[cap], v[80+\\j]\n\t"
        "s_mov_b64 exec, %[m0]\n\tds_add_rtn_u32 v88, v84, v98 offset:%c[cnt]\n\t"
        "s_mov_b64 exec, %[m1]\n\tds_add_rtn_u32 v89, v85, v98 offset:%c[cnt]\n\t"
        "s_mov_b64 exec, %[m2]\n\tds_add_rtn_u32 v90, v86, v98 offset:%c[cnt]\n\t"
        "s_mov_b64 exec, %[m3]\n\tds_add_rtn_u32 v91, v87, v98 offset:%c[cnt]\n\t"
        "s_mov_b64 exec, %[pc0]\n\tds_write_b32 %[hdra], v[80+\\j] offset:128*\\j\n\t"
        "s_mov_b64 exec, -1\n\t"
        "s_cbranch_vccz 1f\n\t"
        "v_mov_b32_e32 v96, 0\n\tds_write_b32 v96, v98 offset:%c[cnt]+64\n\ts_waitcnt lgkmcnt(0)\n\t"
        "1:\n\ts_waitcnt lgkmcnt(1)\n\t"
        ".irp k, 0, 1, 2, 3\n\tv_min_u32_e32 v[88+\\k], %c[sub]-4, v[88+\\k]\n\t.endr\n\t"
        ".irp k, 0, 1, 2, 3\n\tv_mad_u32_u24 v[88+\\k], v[84+\\k], %[sub4], v[88+\\k]\n\t.endr\n\t"
        "s_mov_b64 exec, %[m0]\n\tds_write_b32 v88, v[64+4*\\j] offset:%c[list]\n\t"
        "s_mov_b64 exec, %[m1]\n\tds_write_b32 v89, v[65+4*\\j] offset:%c[list]\n\t"
        "s_mov_b64 exec, %[m2]\n\tds_write_b32 v90, v[66+4*\\j] offset:%c[list]\n\t"
        "s_mov_b64 exec, %[m3]\n\tds_write_b32 v91, v[67+4*\\j] offset:%c[list]\n\t"
        "s_mov_b64 exec, -1\n\t"
        "2:\n\t.endm\n\t"
        "s_movk_i32 %[o1], 0x1000\n\ts_movk_i32 %[o2], 0x2000\n\ts_movk_i32 %[o3], 0x3000\n\t"
        "v_mov_b32_e32 v92, %[i4]\n\tv_add_u32_e32 v93, 4, %[i4]\n\tv_add_u32_e32 v94, 8, %[i4]\n\tv_add_u32_e32 v95, 12, %[i4]\n\t"
        "v_mov_b32_e32 v98, 4\n\t"
        "s_mov_b32 %[todo], 15\n\ts_mov_b32 %[spin], 0x400000\n\t"
        "0:\n\t"
        "s_mov_b32 %[fresh], 0\n\t"
        "s_waitcnt vmcnt(3)\n\ts_cmp_eq_u32 %[todo], 15\n\ts_cbranch_scc1 3f\n\ts_waitcnt vmcnt(0)\n\t3:\n\t"
        "IVAL 0, 0\n\tIRANK 0\n\t"
        "s_waitcnt vmcnt(2)\n\tIVAL 1, %[o1]\n\tIRANK 1\n\t"
        "s_waitcnt vmcnt(1)\n\tIVAL 2, %[o2]\n\tIRANK 2\n\t"
        "s_cmp_eq_u32 %[todo], 15\n\ts_cbranch_scc0 3f\n\ts_waitcnt vmcnt(0)\n\t3:\n\tIVAL 3, %[o3]\n\tIRANK 3\n\t"
        "s_andn2_b32 %[todo], %[todo], %[fresh]\n\t"
        "s_cbranch_scc0 9f\n\t"
        "s_sub_u32 %[spin], %[spin], 1\n\t"
        "s_cbranch_scc0 0b\n\t"
        "s_waitcnt vmcnt(0)\n\t"
        "9:\n\t"
        ".purgem IVAL\n\t.purgem IRANK"
        : "+{v[64:79]}"(raw), "=&{v[80:95]}"(t0), "=&{v[96:99]}"(t1), [m0] "=&s"(m0), [m1] "=&s"(m1), [m2] "=&s"(m2), [m3] "=&s"(m3),
          [todo] "=&s"(todo), [fresh] "=&s"(fresh), [spin] "=&s"(spin), [o1] "=&s"(o1), [o2] "=&s"(o2), [o3] "=&s"(o3)
        : [srd] "s"(srd), [ib] "v"(ib), [key] "s"(key), [i4] "v"(i4), [hdra] "v"(hdra), [pc0] "s"(pc0), [sub4] "s"(SUBB / 4),
          [cnt] "i"(L_CNT), [list] "i"(L_LIST), [sub] "i"(SUBB), [cap] "i"(4 * CAP1)
        : "vcc", "scc", "memory");
}

template <int CTRL, int BANK> __device__ __forceinline__ unsigned dpp(unsigned old, unsigned v) { return __builtin_amdgcn_update_dpp(old, v, CTRL, 0xF, BANK, true); }
__device__ __forceinline__ unsigned bcast8(unsigned v) { v = dpp<0x00, 0xF>(0u, v); return __builtin_amdgcn_update_dpp(v, v, 0x114, 0xF, 0xA, false); }
__device__ __forceinline__ unsigned sum8(unsigned v) { v += dpp<0xB1, 0xF>(0u, v); v += dpp<0x4E, 0xF>(0u, v); return v + dpp<0x141, 0xF>(0u, v); }
__device__ __forceinline__ bf16x4 tr_read(LAS char* p) { return __builtin_amdgcn_ds_read_tr16_b64_v4bf16((LAS bf16x4*)p); }
__device__ __forceinline__ bf16x8 cat(bf16x4 a, bf16x4 b) { return __builtin_shufflevector(a, b, 0, 1, 2, 3, 4, 5, 6, 7); }
__device__ __forceinline__ unsigned pk2(float a, float b) { f32x2 v = {a, b}; bf16x2 r = __builtin_convertvector(v, bf16x2); return __builtin_bit_cast(unsigned, r); }
__device__ __forceinline__ f32x4 mfma(bf16x8 a, bf16x8 b, f32x4 c) { return __builtin_amdgcn_mfma_f32_16x16x32_bf16(a, b, c, 0, 0, 0); }
__device__ __forceinline__ unsigned lds_add(LAS char* p, unsigned v) { return __hip_atomic_fetch_add((LAS unsigned*)p, v, __ATOMIC_RELAXED, __HIP_MEMORY_SCOPE_WORKGROUP); }
__device__ __forceinline__ LAS unsigned* ldsp(unsigned a) { return (LAS unsigned*)(size_t)a; }
__device__ __forceinline__ unsigned byte0_x4(unsigned v) { unsigned r; asm("v_lshlrev_b32_sdwa %0, 2, %1 dst_sel:DWORD dst_unused:UNUSED_PAD src0_sel:DWORD src1_sel:BYTE_0" : "=v"(r) : "v"(v)); return r; }
__device__ __forceinline__ unsigned mad24(unsigned a, unsigned b, unsigned c) { unsigned r; asm("v_mad_u32_u24 %0, %1, %2, %3" : "=v"(r) : "v"(a), "s"(b), "v"(c)); return r; }
__device__ __forceinline__ void lds_arrive(LAS char* p, int lane) {
    asm volatile("s_waitcnt lgkmcnt(0)" ::: "memory");
    if (lane == 0) __hip_atomic_fetch_add((LAS unsigned*)p, 1u, __ATOMIC_RELAXED, __HIP_MEMORY_SCOPE_WORKGROUP);
}
__device__ __forceinline__ void lds_await(LAS char* p, unsigned n) {
    while (__builtin_amdgcn_readfirstlane(*(volatile LAS unsigned*)p) < n) __builtin_amdgcn_s_sleep(1);
    asm volatile("" ::: "memory");
}
__device__ __forceinline__ unsigned rfl(unsigned v) { return __builtin_amdgcn_readfirstlane(v); }

__global__ __launch_bounds__(512, 2) void k_graph(const unsigned short* __restrict__ x, const int* __restrict__ src, const int* __restrict__ dst,
                                                  const unsigned short* __restrict__ wq, const unsigned short* __restrict__ wv,
                                                  unsigned* __restrict__ ws, unsigned short* __restrict__ out, int N, int E) {
    __shared__ __attribute__((aligned(16))) char smem_[L_TOTAL];
    LAS char* smem = (LAS char*)smem_;
    const int t = threadIdx.x, l = t & 63, b = blockIdx.x;
    const int w = rfl(t >> 6);
    const int g = l >> 4, n = l & 15, q = (l >> 2) & 3, p = l & 3, pc = t & 7;
    STAMP_DECL;
    STAMP(0);
    const u64 t_start = __builtin_amdgcn_s_memrealtime();
    asm volatile("" :: "s"(src), "s"(dst), "s"(out), "s"(wq), "s"(wv), "s"(x), "s"(ws), "s"(N), "s"(E));
    const unsigned lds0 = (unsigned)(size_t)smem;
    const unsigned xbytes = (unsigned)N * 256u;
    const u32x4 srd_ws = make_srd(ws, WS_BYTES);
    const unsigned ring = lds0 + L_RING + w * 16384;
    const bool isprod = ((b >> 3) & 1) == 0;
    const int pg = ((b & 7) << 4) | (b >> 4);
    const int jn = 8 * n + w;
    const u32x4 srd_x0 = make_srd(x, xbytes);

    unsigned mykey = 0;
    auto node_row = [&](int tile, int m) { const int nd = b + 256 * (8 * (4 * m + g) + tile); return nd < N ? (unsigned)nd << 8 : OOB; };
    auto zero_staging = [&]() {
        const u32x4 z = {0u, 0u, 0u, 0u};
#pragma unroll
        for (int k = 0; k < 5; ++k) *(LAS u32x4*)(smem + L_IMG + k * 8192 + t * 16) = z;
    };

    u32x16 addr;
    f32x32 acc, wva, wvb;
    unsigned cv, lista, cnta, qa, keyn, inf;
    Srd4 srd_x;
    auto loop_constants = [&]() {
        srd_x = make_srd4(x, xbytes, BIAS * 256);
        lista = lds0 + L_LIST + w * (2 * SUBB) + g * 4; cnta = lds0 + L_CNT + (l & 31) * 4; qa = ring + l * 16; keyn = jn; inf = 0x7F800000u;
        asm volatile("" : "+s"(srd_x.s0), "+s"(srd_x.s1), "+s"(srd_x.s2), "+s"(srd_x.s3));
        asm volatile("" : "+v"(lista), "+v"(cnta), "+v"(qa), "+v"(keyn), "+v"(inf));
        const unsigned o0 = (n ^ (((g & 1) << 3) | (g >> 1))) << 4;
        const unsigned ra = (4 * (n & 3) + (n >> 2)) * 256 + (((4 * g) ^ (((n & 7) << 1) | (n >> 3))) << 4);
        const int i = 4 * g + q, r8 = i & 7;
        const unsigned ta = (4 * q + g) * 256 + 8 * (p & 1) + (((p >> 1) ^ (((i & 7) << 1) | (i >> 3))) << 4);
        cv = i * 256 + (((p ^ ((r8 & 1) | ((r8 & 6) << 1))) << 4) | ((p & 1) << 3));
#pragma unroll
        for (int s = 0; s < 4; ++s) { addr[s] = ring + (ra ^ (s << 4)); addr[12 + s] = o0 ^ (s << 5); }
#pragma unroll
        for (int c = 0; c < 8; ++c) addr[4 + c] = ring + (ta ^ (c << 5));
        acc = 0.f;
        asm volatile("" : "+{v[128:159]}"(acc), "+{v[176:191]}"(addr), "+v"(cv));
    };
    auto wv_resident = [&]() {
#pragma unroll
        for (int a = 0; a < 8; ++a) {
            LAS char* pa = smem + L_WV0 + (cv ^ ((a >> 1) << 6) ^ ((a & 1) << 3));
            const f32x4 f0 = __builtin_bit_cast(f32x4, cat(tr_read(pa), tr_read(pa + 4096)));
            const f32x4 f1 = __builtin_bit_cast(f32x4, cat(tr_read(pa + 16384), tr_read(pa + 16384 + 4096)));
#pragma unroll
            for (int k = 0; k < 4; ++k) { wva[4 * a + k] = f0[k]; wvb[4 * a + k] = f1[k]; }
        }
        asm volatile("" : "+{v[224:255]}"(wva), "+{v[32:63]}"(wvb));
    };

    if (w < 4) {
        if (isprod) {
            u32x4 d4[3], s4[3];
            unsigned d1, s1, kb;
            const unsigned e0 = pg * CH + 4 * t, e1 = pg * CH + 3072 + t;
            {
                const u32x4 srd_d = make_srd(dst, (unsigned)E * 4u), srd_s = make_srd(src, (unsigned)E * 4u);
                const unsigned o = e0 * 4, o2 = o + 8192, o1 = w == 0 ? e1 * 4 : OOB;
                const unsigned k4 = 4096;
                LD4(d4[0], srd_d, o, ""); LD4S(d4[1], srd_d, o, k4, ""); LD4(d4[2], srd_d, o2, ""); LD1(d1, srd_d, o1, "");
                LD4(s4[0], srd_s, o, ""); LD4S(s4[1], srd_s, o, k4, ""); LD4(s4[2], srd_s, o2, ""); LD1(s1, srd_s, o1, "");
                const unsigned ok = WS_KEY + t * 4, om = WS_KEY + b * 4;
                LD1(kb, srd_ws, ok, " sc1"); LD1(mykey, srd_ws, om, " sc1");
            }
            zero_staging();
            if (t < 8) *(LAS u32x4*)(smem + L_CNT + t * 16) = u32x4{0u, 0u, 0u, 0u};
            unsigned ob = WS_INBOX + ((t >> 3) * G1 + pg) * 128 + pc * 16, ima = lds0 + L_IMG + (t >> 3) * 144 + pc * 16;
            PIN(ob); PIN(ima);
            WG_BARRIER();
            if (PRIO) __builtin_amdgcn_s_setprio(2);
            STAMP(1);
            unsigned b4[13], rk[13];
            const bool v1 = w == 0 && e1 < (unsigned)E;
            asm volatile("s_waitcnt vmcnt(6)" : "+v"(d4[0]), "+v"(d4[1]), "+v"(d4[2]), "+v"(d1) :: "memory");
            STAMP(2);
#pragma unroll
            for (int j = 0; j < 3; ++j)
                if (e0 + 1024 * j < (unsigned)E) {
#pragma unroll
                    for (int k = 0; k < 4; ++k) { b4[4 * j + k] = byte0_x4(d4[j][k]); rk[4 * j + k] = lds_add(smem + L_CNT1 + b4[4 * j + k], 4); }
                }
            if (v1) { b4[12] = byte0_x4(d1); rk[12] = lds_add(smem + L_CNT1 + b4[12], 4); }
            asm volatile("s_waitcnt vmcnt(1)" : "+v"(s4[0]), "+v"(s4[1]), "+v"(s4[2]), "+v"(s1), "+v"(kb) :: "memory");
            auto put = [&](unsigned d, unsigned sv, unsigned bb, unsigned r) {
                *(LAS unsigned*)(smem + L_IMG + 8 + mad24(bb, 36, min(r, 4u * CAP1))) = __builtin_amdgcn_perm(sv + BIAS, d, 0x01050401u);
            };
            auto spill = [&](unsigned d, unsigned sv, unsigned r) {
                if (r >= 4u * CAP1) {
                    const unsigned o = WS_OVF + (pg * CH + lds_add(smem + L_CNT1 + 2048, 1)) * 4, v = (sv << 16) | d;
                    ST1(v, srd_ws, o, " sc1");
                }
            };
#pragma unroll
            for (int j = 0; j < 3; ++j)
                if (e0 + 1024 * j < (unsigned)E) {
#pragma unroll
                    for (int k = 0; k < 4; ++k) put(d4[j][k], s4[j][k], b4[4 * j + k], rk[4 * j + k]);
                    if (__builtin_expect(max(max(rk[4 * j], rk[4 * j + 1]), max(rk[4 * j + 2], rk[4 * j + 3])) >= 4u * CAP1, 0)) {
#pragma unroll
                        for (int k = 0; k < 4; ++k) spill(d4[j][k], s4[j][k], rk[4 * j + k]);
                    }
                }
            if (v1) { put(d1, s1, b4[12], rk[12]); spill(d1, s1, rk[12]); }
            *(LAS unsigned*)(smem + L_CNT1 + 1024 + t * 4) = kb + KEY_STEP;
            WG_BARRIER();
            STAMP(3);
            unsigned c4[8], kk[8], cs[8]; u32x4 v[8];
            const unsigned nov = *(LAS unsigned*)(smem + L_CNT1 + 2048);
#pragma unroll
            for (int it = 0; it < 8; ++it) {
                c4[it] = *(LAS unsigned*)(smem + L_CNT1 + (t >> 3) * 4 + it * 128);
                kk[it] = *(LAS unsigned*)(smem + L_CNT1 + 1024 + (t >> 3) * 4 + it * 128);
                v[it] = *(LAS u32x4*)(size_t)(ima + it * (32 * 144));
            }
#pragma unroll
            for (int it = 0; it < 8; ++it) cs[it] = sum8(v[it].x + v[it].y + v[it].z + v[it].w);
            lds_arrive(smem + L_CNT + 84, l);
            const unsigned no = rfl(nov);
            if (__builtin_expect(no != 0, 0)) {
                const unsigned o = t == 0 ? WS_NOVF + pg * 4 : OOB;
                ST1(no, srd_ws, o, " sc1");
                VMWAIT(0);
                WG_BARRIER();
            }
#pragma unroll
            for (int it = 0; it < 8; ++it) {
                v[it].x = pc == 0 ? c4[it] : v[it].x; v[it].y = pc == 0 ? cs[it] : v[it].y;
                v[it] ^= kk[it];
                const unsigned o = (WMODE == 2 || (int)c4[it] > 16 * pc - 8) ? ob : OOB, so = it * 32 * G1 * 128;
                asm volatile("buffer_store_dwordx4 %0, %1, %2, %3 offen sc1\n\ts_nop 1" :: "v"(v[it]), "v"(o), "s"(srd_ws), "s"(so) : "memory");
            }
            asm volatile("s_waitcnt vmcnt(8)" : "+v"(mykey) :: "memory");
            STAMP(4);
        } else {
            { const unsigned om = WS_KEY + b * 4; LD1(mykey, srd_ws, om, " sc1"); }
            if (t < 8) *(LAS u32x4*)(smem + L_CNT + t * 16) = u32x4{0u, 0u, 0u, 0u};
            WG_BARRIER();
            asm volatile("s_waitcnt vmcnt(0)" : "+v"(mykey) :: "memory");
            if (NP_SLEEP) __builtin_amdgcn_s_sleep(NP_SLEEP);
            const unsigned o = ((b >> 4) * 256 + t) * 128;
            set_m0(lds0 + L_DUMP + w * 256);
            STAMP(1);
            for (int r = 0; r < PF_R; ++r) {
                asm volatile("s_waitcnt vmcnt(%c0)" :: "i"(PF_D) : "memory");
                if (__builtin_amdgcn_s_memrealtime() > t_start + PF_T) break;
                const unsigned so = r << 19;
                asm volatile("buffer_load_dword %1, %0, %2 offen lds" :: "s"(srd_x0), "v"(o), "s"(so) : "memory");
            }
            if (PRIO) __builtin_amdgcn_s_setprio(2);
            STAMP(2); STAMP(3); STAMP(4);
        }
        STAMP(5);
        mykey = rfl(mykey) + KEY_STEP;
        loop_constants();
        unsigned ib = WS_INBOX + b * (G1 * 128) + t * 16;
        PIN(ib);
        while (__builtin_amdgcn_s_memrealtime() < t_start + POLL_T) __builtin_amdgcn_s_sleep(1);
        STAMP(6);
        u32x16 raw;
        inbox_request(raw, srd_ws, ib);
        inbox_stage(raw, srd_ws, ib, mykey, 16 * pc - 8, lds0 + L_HDR + (t >> 3) * 4);
        if (PRIO) __builtin_amdgcn_s_setprio(0);
        STAMP(7);
    } else {
        const int j = w - 4;
        if (TOUCH && j == 3) {
            const u32x4 srd_o = make_srd(out, xbytes);
            const unsigned o1 = WS_INBOX + l * 65536 + (b & 127) * 128, o2 = l * 102400 + b * 256;
            set_m0(lds0 + L_DUMP + 1024);
            if (TOUCH & 1) asm volatile("s_nop 0\n\tbuffer_load_dword %1, %0, 0 offen lds" :: "s"(srd_ws), "v"(o1) : "memory");
            if (TOUCH & 2) asm volatile("s_nop 0\n\tbuffer_load_dword %1, %0, 0 offen lds" :: "s"(srd_o), "v"(o2) : "memory");
        }
        if (isprod) zero_staging();
        WG_BARRIER();
        STAMP(1);
        u32x4 wr[8], xr[8];
        {
            const u32x4 srd_q = make_srd(wq, 32768);
            const unsigned o = j * 8192 + l * 16, k4 = 4096;
            LD4(wr[0], srd_q, o, ""); LD4(wr[1], srd_q, o, " offset:1024"); LD4(wr[2], srd_q, o, " offset:2048"); LD4(wr[3], srd_q, o, " offset:3072");
            LD4S(wr[4], srd_q, o, k4, ""); LD4S(wr[5], srd_q, o, k4, " offset:1024"); LD4S(wr[6], srd_q, o, k4, " offset:2048"); LD4S(wr[7], srd_q, o, k4, " offset:3072");
#pragma unroll
            for (int m = 0; m < 8; ++m) { const unsigned ox = node_row(m < 4 ? j : w, m & 3) + n * 16; LD4(xr[m], srd_x0, ox, ""); }
        }
        unsigned aqa[8], xfa[4];
        unsigned xa = ring + g * 256 + ((n ^ g) << 4), wa = lds0 + L_WQ + j * 16384 + g * 256 + ((n ^ g) << 4) + ((n & 4) << 1);
        {
            const unsigned aq = lds0 + L_WQ + (8 * g + q) * 256 + ((((4 * p) ^ (q | ((g & 1) << 2))) << 4) | ((p & 1) << 3));
#pragma unroll
            for (int s = 0; s < 4; ++s) { xfa[s] = ring + n * 256 + (((4 * s + g) ^ n) << 4); PIN(xfa[s]); }
#pragma unroll
            for (int c = 0; c < 8; ++c) { aqa[c] = aq ^ ((c >> 1) << 4) ^ ((c & 1) << 3); PIN(aqa[c]); }
            PIN(xa); PIN(wa);
        }
        STAMP(2);
        if (isprod) {
            WG_BARRIER();
            if (__builtin_expect(rfl(*(LAS unsigned*)(smem + L_CNT1 + 2048)) != 0, 0)) WG_BARRIER();
            if (W_SLEEP) __builtin_amdgcn_s_sleep(W_SLEEP);
        }
        STAMP(3);
        asm volatile("s_waitcnt vmcnt(8)" : "+v"(wr[0]), "+v"(wr[1]), "+v"(wr[2]), "+v"(wr[3]), "+v"(wr[4]), "+v"(wr[5]), "+v"(wr[6]), "+v"(wr[7]) :: "memory");
#pragma unroll
        for (int m = 0; m < 8; ++m) {
            const unsigned a = (wa ^ ((m & 2) << 5)) + m * 1024;
            *(LAS u32x2*)(size_t)a = u32x2{wr[m].x, wr[m].y};
            *(LAS u32x2*)(size_t)(a ^ 8) = u32x2{wr[m].z, wr[m].w};
        }
        asm volatile("s_waitcnt vmcnt(0)" : "+v"(xr[0]), "+v"(xr[1]), "+v"(xr[2]), "+v"(xr[3]), "+v"(xr[4]), "+v"(xr[5]), "+v"(xr[6]), "+v"(xr[7]) :: "memory");
#pragma unroll
        for (int m = 0; m < 8; ++m) *(LAS u32x4*)(size_t)((xa ^ ((m & 3) << 6)) + m * 1024) = xr[m];
        STAMP(4);
        lds_arrive(smem + L_CNT + 80, l);
        lds_await(smem + L_CNT + 80, 4);
        STAMP(5);
        bf16x8 xo[2][4], fr[2][8];
        f32x4 qc[2][8];
        auto wq_frags = [&](int u, bf16x8 (&f)[8]) {
#pragma unroll
            for (int a = 0; a < 8; ++a) {
                LAS char* pa = (LAS char*)(size_t)aqa[a] + u * 16384;
                f[a] = cat(tr_read(pa), tr_read(pa + 1024));
            }
        };
#pragma unroll
        for (int u = 0; u < 4; ++u) { xo[0][u] = *(LAS bf16x8*)(size_t)xfa[u]; xo[1][u] = *(LAS bf16x8*)(size_t)(xfa[u] + 4096); }
        wq_frags(0, fr[0]);
        {
            const Srd4 srd_v = make_srd4(wv, 32768);
            const unsigned r0 = (j * 32 + g) << 8, o0 = (n ^ ((g & 1) | ((g & 2) << 1))) << 4;
            const unsigned e[8] = {r0, r0 + 1024, r0 + 2048, r0 + 3072, r0 + 4096, r0 + 5120, r0 + 6144, r0 + 7168};
            const unsigned base = lds0 + (j < 2 ? L_WV0 + j * 16384 : L_WV1 + (j - 2) * 8192);
            const u32x4 off = {o0, o0 ^ 128, o0, o0 ^ 128};
            dma_half(base, srd_v, e, off); dma_half(base + 4096, srd_v, e + 4, off);
        }
#pragma unroll
        for (int u = 0; u < 4; ++u) {
            if (u < 3) wq_frags(u + 1, fr[(u + 1) & 1]);
            FENCE();
#pragma unroll
            for (int a = 0; a < 8; ++a)
#pragma unroll
                for (int tt = 0; tt < 2; ++tt) qc[tt][a] = mfma(fr[u & 1][a], xo[tt][u], u ? qc[tt][a] : f32x4{0.f, 0.f, 0.f, 0.f});
            FENCE();
            if (u == 1 && j < 2) { VMWAIT(0); lds_arrive(smem + L_CNT + 88, l); }
        }
        STAMP(6);
        if (isprod) lds_await(smem + L_CNT + 84, 4);
#pragma unroll
        for (int tt = 0; tt < 2; ++tt)
#pragma unroll
            for (int s = 0; s < 4; ++s) {
                const float k = -0.08838834764831845f * 1.4426950408889634f;
                const f32x4 c0 = qc[tt][2 * s] * k, c1 = qc[tt][2 * s + 1] * k;
                *(LAS u32x4*)(size_t)(ring - (tt ? 0 : 65536) + s * 1024 + l * 16) = u32x4{pk2(c0[0], c0[1]), pk2(c0[2], c0[3]), pk2(c1[0], c1[1]), pk2(c1[2], c1[3])};
            }
        loop_constants();
        lds_await(smem + L_CNT + 88, 2);
        wv_resident();
        VMWAIT(0);
        STAMP(7);
    }
    WG_BARRIER();
    STAMP(8);
    if (LPRIO >= 3) __builtin_amdgcn_s_setprio(3);
    if (LPRIO == 1 && w >= 4) __builtin_amdgcn_s_setprio(1);
    if (LPRIO == 2) { if (w >= 6) __builtin_amdgcn_s_setprio(3); else if (w >= 4) __builtin_amdgcn_s_setprio(2); else if (w >= 2) __builtin_amdgcn_s_setprio(1); }

    u32x16 qf;
    const u32x4 doff = {addr[12], addr[13], addr[14], addr[15]};
    LAS char* mylist = smem + L_LIST + w * (2 * SUBB);
    int gcur = G1, pos = 0;
    auto batch = [&](auto first_) {
        constexpr bool first = decltype(first_)::value;
        auto entries = [&](int grp, unsigned (&e)[8]) {
#pragma unroll
            for (int m = 0; m < 8; ++m) e[m] = *(LAS unsigned*)(size_t)(lista + grp * 128 + m * 16);
        };
        unsigned cw = *(LAS unsigned*)(size_t)cnta, e0[8], e1[8];
        entries(0, e0);
        if constexpr (first) {
#pragma unroll
            for (int s = 0; s < 4; ++s) {
                const u32x4 v = *(LAS u32x4*)(size_t)(qa + s * 1024);
                qf[4 * s] = v.x; qf[4 * s + 1] = v.y; qf[4 * s + 2] = v.z; qf[4 * s + 3] = v.w;
            }
        }
        entries(1, e1);
        FENCE();
        int lo = __builtin_amdgcn_readlane(cw, 2 * w) >> 2, hi = __builtin_amdgcn_readlane(cw, 2 * w + 1) >> 2;
        if constexpr (first) {
            if (__builtin_amdgcn_readlane(cw, 16)) gcur = 0;
            if (__builtin_amdgcn_ballot_w64(cw > 4u * CAP2)) { gcur = -1; lo = 0; hi = 0; }
        }
        const int c = lo + hi, G = (c + 31) >> 5;
        const int nlast = G > 2 ? (((c - 1) & 31) >> 2) + 1 : 8;
        auto join = [&]() {
            for (int k = 0; k < G * 32 - lo; k += 64) {
                const unsigned e = *(LAS unsigned*)(mylist + SUBB + (l + k) * 4);
                if (l + k < G * 32 - lo) *(LAS unsigned*)(mylist + (lo + l + k) * 4) = l + k < hi ? e : PAD;
            }
        };
        const bool early = lo >= 64;
        if (__builtin_expect(!early, 0)) { join(); entries(0, e0); entries(1, e1); }
        if constexpr (first) asm volatile("s_waitcnt lgkmcnt(0)" : "+{v[160:175]}"(qf) :: "memory");
        if (G > 0) dma_group(ring, srd_x, e0, doff);
        if constexpr (first) {
            if (w < 4) {
                if (G > 1) dma_group(ring + 8192, srd_x, e1, doff);
                wv_resident();
                lds_arrive(smem + L_CNT + 92, l);
            } else {
                if (w >= 6) lds_await(smem + L_CNT + 92, 4);
                if (G > 1) dma_group(ring + 8192, srd_x, e1, doff);
            }
        } else if (G > 1) dma_group(ring + 8192, srd_x, e1, doff);
        f32x16 cc;
#pragma unroll
        for (int m = 0; m < 8; ++m) {
            cc[m] = (e0[m] & 0xFF) == keyn ? 0.f : __uint_as_float(inf);
            cc[8 + m] = (e1[m] & 0xFF) == keyn ? 0.f : __uint_as_float(inf);
        }
        if (early) join();
        STAMP(9);
        if (G > 0) gather_loop(acc, qf, addr, u32x4{lista, keyn, inf, 0u}, cc, srd_x, ring, G, nlast);
    };
    batch(std::true_type{});
    while (__builtin_expect(gcur < G1, 0)) {
        unsigned lim = G1 * 32;
        if (gcur >= 0) {
            const u64 m0 = __builtin_amdgcn_ballot_w64(l >= gcur && *(LAS unsigned*)(smem + L_HDR + l * 4) > 4u * CAP1);
            const u64 m1 = __builtin_amdgcn_ballot_w64(l + 64 >= gcur && *(LAS unsigned*)(smem + L_HDR + 256 + l * 4) > 4u * CAP1);
            if (!(m0 | m1)) break;
            gcur = m0 ? __builtin_ctzll(m0) : 64 + __builtin_ctzll(m1);
            unsigned v; const unsigned o = WS_NOVF + gcur * 4;
            asm volatile("buffer_load_dword %0, %1, %2, 0 offen sc1\n\ts_waitcnt vmcnt(0)" : "=&v"(v) : "v"(o), "s"(srd_ws) : "memory");
            lim = rfl(v);
        }
        const unsigned i = pos + t;
        const bool cand = t < CAP2 && i < lim;
        unsigned u;
        { const unsigned o = cand ? (gcur < 0 ? WS_INBOX + b * (G1 * 128) : WS_OVF + gcur * CH * 4) + i * 4 : OOB;
          asm volatile("buffer_load_dword %0, %1, %2, 0 offen sc1\n\ts_waitcnt vmcnt(0)" : "=&v"(u) : "v"(o), "s"(srd_ws) : "memory"); }
        WG_BARRIER();
        if (t < 16) *(LAS unsigned*)(smem + L_CNT + t * 4) = 0;
        WG_BARRIER();
        if (cand) {
            unsigned e; bool v;
            if (gcur < 0) { e = u ^ mykey; v = ((i & 31) - 2) * 4 < *(LAS unsigned*)(smem + L_HDR + (i >> 5) * 4); }
            else { e = (((u >> 16) + BIAS) << 8) | ((u >> 8) & 0xFF); v = (u & 0xFF) == (unsigned)b; }
            if (v) *(LAS unsigned*)(smem + L_LIST + (e & 7) * (2 * SUBB) + lds_add(smem + L_CNT + (e & 7) * 8, 4)) = e;
        }
        pos += CAP2;
        if ((unsigned)pos >= lim) { ++gcur; pos = 0; }
        WG_BARRIER();
        batch(std::false_type{});
    }
    STAMP(10);

    {
        bf16x8 wl[2][8], ag[4];
#pragma unroll
        for (int u = 0; u < 2; ++u)
#pragma unroll
            for (int a = 0; a < 8; ++a) {
                LAS char* pa = smem + L_WV1 + (cv ^ ((a >> 1) << 6) ^ ((a & 1) << 3)) + u * 8192;
                wl[u][a] = cat(tr_read(pa), tr_read(pa + 4096));
            }
#pragma unroll
        for (int u = 0; u < 4; ++u) {
            const u32x4 v = {pk2(acc[8 * u], acc[8 * u + 1]), pk2(acc[8 * u + 2], acc[8 * u + 3]),
                             pk2(acc[8 * u + 4], acc[8 * u + 5]), pk2(acc[8 * u + 6], acc[8 * u + 7])};
            ag[u] = __builtin_bit_cast(bf16x8, v);
        }
        FENCE();
        f32x4 oc[8];
#pragma unroll
        for (int a = 0; a < 8; ++a) oc[a] = mfma(__builtin_bit_cast(bf16x8, f32x4{wva[4 * a], wva[4 * a + 1], wva[4 * a + 2], wva[4 * a + 3]}), ag[0], f32x4{0.f, 0.f, 0.f, 0.f});
#pragma unroll
        for (int a = 0; a < 8; ++a) oc[a] = mfma(__builtin_bit_cast(bf16x8, f32x4{wvb[4 * a], wvb[4 * a + 1], wvb[4 * a + 2], wvb[4 * a + 3]}), ag[1], oc[a]);
#pragma unroll
        for (int a = 0; a < 8; ++a) oc[a] = mfma(wl[0][a], ag[2], oc[a]);
        FENCE();
        const unsigned ow = ring + n * 256 + ((g ^ n) << 4) + ((g & 1) << 3);
#pragma unroll
        for (int a = 0; a < 8; ++a) oc[a] = mfma(wl[1][a], ag[3], oc[a]);
#pragma unroll
        for (int a = 0; a < 8; ++a)
            *(LAS u32x2*)(size_t)(ow ^ ((a >> 1) << 6) ^ ((a & 1) << 3)) = u32x2{pk2(oc[a][0], oc[a][1]), pk2(oc[a][2], oc[a][3])};
        const auto rs_o = __builtin_amdgcn_make_buffer_rsrc((void*)out, 0, xbytes, 0x00020000);
#pragma unroll
        for (int m = 0; m < 4; ++m) {
            const u32x4 v = *(LAS u32x4*)(size_t)(ring + m * 1024 + l * 16);
            __builtin_amdgcn_raw_buffer_store_b128(v, rs_o, node_row(w, m) + ((n ^ g ^ (4 * m)) << 4), 0, OUT_AUX);
        }
    }
    if (w == 0) { const unsigned o = l == 0 ? WS_KEY + b * 4 : OOB; ST1(mykey, srd_ws, o, " sc1"); }
    STAMP(11);
}

extern "C" void kernel_launch(void* const* d_in, const int* in_sizes, int n_in,
                              void* d_out, int out_size, void* d_ws, size_t ws_size,
                              hipStream_t stream) {
    const int N = in_sizes[0] / 128, E = in_sizes[1];
    k_graph<<<NB, 512, 0, stream>>>((const unsigned short*)d_in[0], (const int*)d_in[1], (const int*)d_in[2],
                                    (const unsigned short*)d_in[3], (const unsigned short*)d_in[4],
                                    (unsigned*)((char*)d_ws + WS_BASE), (unsigned short*)d_out, N, E);
}
```

```cpp
#define POLL_T 340
#include <hip/hip_runtime.h>
#include <hip/hip_bf16.h>
#include <stdint.h>
#include <type_traits>
#define ORD 1
#define SKIP 0
#define STR2(x) #x
#define STR(x) STR2(x)

typedef __attribute__((ext_vector_type(8))) __bf16 bf16x8;
typedef __attribute__((ext_vector_type(4))) __bf16 bf16x4;
typedef __attribute__((ext_vector_type(2))) __bf16 bf16x2;
typedef __attribute__((ext_vector_type(4))) float f32x4;
typedef __attribute__((ext_vector_type(2))) float f32x2;
typedef __attribute__((ext_vector_type(4))) unsigned u32x4;
typedef __attribute__((ext_vector_type(2))) unsigned u32x2;
typedef __attribute__((ext_vector_type(32))) float f32x32;
typedef __attribute__((ext_vector_type(16))) float f32x16;
typedef __attribute__((ext_vector_type(16))) unsigned u32x16;
typedef __attribute__((ext_vector_type(12))) unsigned u32x12;
typedef unsigned long long u64;
#define LAS __attribute__((address_space(3)))

constexpr int NB = 256;
constexpr int G1 = 128;
constexpr int CH = 3136;
constexpr int CAP1 = 30;
constexpr int CAP2 = 192;
constexpr int SUBB = 784;
#define POLL_AT 3
#define OUT_AUX 16
#define PF_R 0
#define PF_D 0
#define PF_T 200
#define WMODE 2
#define LPRIO 3
#define PKADD 1
#define CODEPF 1
#define CODE_BYTES 21504
#define NP_SLEEP 0
#define W_SLEEP 3
#define PRIO 1
#define SPLIT 16384
constexpr unsigned KEY_STEP = 0x9E3779B1u;
constexpr unsigned WS_KEY = 0;
constexpr unsigned WS_NOVF = 2048;
constexpr unsigned WS_INBOX = 4096;
constexpr unsigned WS_OVF = WS_INBOX + NB * G1 * 128;
constexpr unsigned WS_BYTES = WS_OVF + G1 * CH * 4;
constexpr unsigned OOB = 0x80000000u;
constexpr unsigned BIAS = 32768 - SPLIT;
constexpr unsigned PAD = 0x00FFFFFFu;

constexpr int L_CNT = 0;
constexpr int L_HDR = 128;
constexpr int L_LIST = 768;
constexpr int L_RING = L_LIST + 16 * SUBB;
constexpr int L_IMG = L_RING;
constexpr int L_CNT1 = L_IMG + NB * 144;
constexpr int L_WQ = L_RING + 2 * 16384 + 8192;
constexpr int L_WV0 = L_RING + 6 * 16384 + 8192;
constexpr int L_WV1 = L_RING + 131072;
constexpr int L_DUMP = L_WV1 + 16384;
constexpr int L_TOTAL = L_DUMP + 2048;
static_assert(L_RING % 256 == 0 && L_TOTAL <= 163840, "LDS map");

#define STAMP_DECL
#define STAMP(k)
#define STAMP_FLUSH(base, k0, k1)

__device__ __forceinline__ u32x4 make_srd(const void* p, unsigned bytes, unsigned back = 0) {
    const u64 a = (u64)p - back;
    u32x4 s; s.x = (unsigned)a; s.y = (unsigned)(a >> 32) & 0xFFFFu; s.z = bytes + back; s.w = 0x00020000u; return s;
}
#define LD4(dst, srd, off, FL) asm volatile("buffer_load_dwordx4 %0, %1, %2, 0 offen" FL : "=v"(dst) : "v"(off), "s"(srd) : "memory")
#define LD4S(dst, srd, off, so, FL) asm volatile("buffer_load_dwordx4 %0, %1, %2, %3 offen" FL : "=v"(dst) : "v"(off), "s"(srd), "s"(so) : "memory")
#define LD1(dst, srd, off, FL) asm volatile("buffer_load_dword %0, %1, %2, 0 offen" FL : "=v"(dst) : "v"(off), "s"(srd) : "memory")
#define ST1(val, srd, off, FL) asm volatile("buffer_store_dword %0, %1, %2, 0 offen" FL :: "v"(val), "v"(off), "s"(srd) : "memory")
#define PIN(x) asm volatile("" : "+v"(x))
#define VMWAIT(n) asm volatile("s_waitcnt vmcnt(" #n ")" ::: "memory")
#define FENCE() __builtin_amdgcn_sched_barrier(0)
#define WG_BARRIER() do { asm volatile("s_waitcnt lgkmcnt(0)" ::: "memory"); __builtin_amdgcn_s_barrier(); asm volatile("" ::: "memory"); } while (0)

struct Srd4 { u32x4 s0, s1, s2, s3; };
__device__ __forceinline__ Srd4 make_srd4(const void* p, unsigned bytes, unsigned back = 0) {
    return Srd4{make_srd(p, bytes, back), make_srd(p, bytes, back + 1024), make_srd(p, bytes, back + 2048), make_srd(p, bytes, back + 3072)};
}

__device__ __forceinline__ void set_m0(unsigned v) { asm volatile("s_mov_b32 m0, %0" :: "s"(v) : "memory"); }
template <int R> __device__ __forceinline__ void dma_rows(const Srd4& d, unsigned e, unsigned off) {
    unsigned tmp, mask = 0x00FFFF00u;
    asm volatile("v_and_or_b32 %0, %1, %2, %3\n\tbuffer_load_dwordx4 %0, %4, 0 offen offset:%c5 lds"
                 : "=&v"(tmp) : "v"(e), "s"(mask), "v"(off), "s"(R == 0 ? d.s0 : R == 1 ? d.s1 : R == 2 ? d.s2 : d.s3), "i"(R * 1024) : "memory");
}
__device__ __forceinline__ void dma_half(unsigned ldsbase, const Srd4& d, const unsigned* e, const u32x4& off) {
    set_m0(ldsbase);
    dma_rows<0>(d, e[0], off.x); dma_rows<1>(d, e[1], off.y); dma_rows<2>(d, e[2], off.z); dma_rows<3>(d, e[3], off.w);
}
__device__ __forceinline__ void dma_group(unsigned ldsbase, const Srd4& d, const unsigned (&e)[8], const u32x4& off) {
    asm volatile("s_waitcnt lgkmcnt(0)" ::: "memory");
    dma_half(ldsbase, d, e, off); dma_half(ldsbase + 4096, d, e + 4, off);
}

__device__ __forceinline__ void gather_loop(f32x32& acc, const u32x16& qf, const u32x16& addr, u32x4 misc, f32x16 cc,
                                            const Srd4& d, unsigned ring, int G, int nlast) {
    f32x32 t0, t1; u32x12 t2; int tmp, pr; u64 sm; unsigned mask = 0x00FFFF00u;
    asm volatile(
        ".macro GWAIT k\n\ts_cmp_gt_i32 %[t], \\k\n\ts_cbranch_scc1 1f\n\ts_waitcnt vmcnt(\\k)\n\t.endm\n\t"
        ".macro GROWS so\n\t.irp s, 0, 1, 2, 3\n\t"
        "ds_read_b128 v[64+8*\\s:67+8*\\s], v[176+\\s] offset:\\so\n\t"
        "ds_read_b128 v[68+8*\\s:71+8*\\s], v[176+\\s] offset:\\so+4096\n\t.endr\n\t.endm\n\t"
        ".macro GTR c, so\n\t"
        "ds_read_b64_tr_b16 v[96+4*\\c:97+4*\\c], v[180+\\c] offset:\\so\n\t"
        "ds_read_b64_tr_b16 v[98+4*\\c:99+4*\\c], v[180+\\c] offset:\\so+4096\n\t.endm\n\t"
        ".macro GS s, c\n\t"
        "v_mfma_f32_16x16x32_bf16 v[\\c:\\c+3], v[64+8*\\s:67+8*\\s], v[160+4*\\s:163+4*\\s], v[\\c:\\c+3]\n\t"
        "v_mfma_f32_16x16x32_bf16 v[\\c+4:\\c+7], v[68+8*\\s:71+8*\\s], v[160+4*\\s:163+4*\\s], v[\\c+4:\\c+7]\n\t.endm\n\t"
        ".macro GLOAD m, r, srd\n\t"
        "v_and_or_b32 v195, v[212+\\m], %[mask], v[188+\\r]\n\t"
        "buffer_load_dwordx4 v195, \\srd, 0 offen offset:1024*\\r lds\n\t.endm\n\t"
        ".macro GPART m, r, srd\n\ts_cmp_le_i32 %[nl], \\m\n\ts_cbranch_scc1 4f\n\tGLOAD \\m, \\r, \\srd\n\t.endm\n\t"
        ".macro GSUM m\n\tv_mfma_f32_16x16x32_bf16 v[128+4*(\\m):131+4*(\\m)], v[96+4*(\\m):99+4*(\\m)], v[220:223], v[128+4*(\\m):131+4*(\\m)]\n\t.endm\n\t"
        ".macro GCMP m, d\n\tv_cmp_ne_u32_sdwa \\d, v[212+\\m], v193 src0_sel:BYTE_0 src1_sel:DWORD\n\t.endm\n\t"
        ".macro GPAIR m, c\n\t"
        "GSUM \\m\n\tGCMP \\m+1, %[sm]\n\tv_cndmask_b32_e32 v[\\c+\\m], 0, v194, vcc\n\t"
        "GSUM \\m+1\n\t.if \\m < 6\n\tGCMP \\m+2, vcc\n\t.else\n\ts_cmp_gt_i32 %[rem], 0\n\t.endif\n\t"
        "v_cndmask_b32_e64 v[\\c+\\m+1], 0, v194, %[sm]\n\t.endm\n\t"
        ".macro GSTEP so, c, eo\n\t"
        "GTR 0, \\so\n\t"
        "s_waitcnt lgkmcnt(2)\n\tGS 0, \\c\n\t"
        "GTR 1, \\so\n\tGTR 2, \\so\n\tGS 1, \\c\n\t"
        "GTR 3, \\so\n\tGTR 4, \\so\n\tGS 2, \\c\n\t"
        "GTR 5, \\so\n\tGTR 6, \\so\n\tGS 3, \\c\n\t"
        "GTR 7, \\so\n\t"
        "ds_read2_b32 v[212:213], v192 offset0:\\eo offset1:\\eo+4\n\t"
        "ds_read2_b32 v[214:215], v192 offset0:\\eo+8 offset1:\\eo+12\n\t"
        "ds_read2_b32 v[216:217], v192 offset0:\\eo+16 offset1:\\eo+20\n\t"
        "ds_read2_b32 v[218:219], v192 offset0:\\eo+24 offset1:\\eo+28\n\t"
        "s_add_u32 m0, %[ring], \\so\n\t"
        "s_cmp_gt_i32 %[rem], 3\n\t"
        ".irp k, 0, 1, 2, 3, 4, 5, 6, 7\n\tv_exp_f32_e32 v[\\c+\\k], v[\\c+\\k]\n\t.endr\n\t"
        "s_waitcnt lgkmcnt(0)\n\t"
        "s_cbranch_scc0 2f\n\t"
        "GLOAD 0, 0, %[s0]\n\tGLOAD 1, 1, %[s1]\n\tGLOAD 2, 2, %[s2]\n\tGLOAD 3, 3, %[s3]\n\t"
        "s_add_u32 m0, m0, 0x1000\n\t"
        "GLOAD 4, 0, %[s0]\n\tGLOAD 5, 1, %[s1]\n\tGLOAD 6, 2, %[s2]\n\tGLOAD 7, 3, %[s3]\n\t"
        "GSIG \\c\n\t"
        "s_waitcnt vmcnt(8)\n\t"
        "s_branch 1f\n\t"
        "2:\n\t"
        "s_mov_b32 %[t], 0\n\t"
        "s_cmp_eq_u32 %[rem], 3\n\ts_cbranch_scc0 4f\n\t"
        "s_mov_b32 %[t], %[nl]\n\t"
        "GLOAD 0, 0, %[s0]\n\tGPART 1, 1, %[s1]\n\tGPART 2, 2, %[s2]\n\tGPART 3, 3, %[s3]\n\t"
        "s_add_u32 m0, m0, 0x1000\n\t"
        "GPART 4, 0, %[s0]\n\tGPART 5, 1, %[s1]\n\tGPART 6, 2, %[s2]\n\tGPART 7, 3, %[s3]\n\t"
        "4:\n\t"
        "GSIG \\c\n\t"
        "s_waitcnt vmcnt(8)\n\t"
        "GWAIT 7\n\tGWAIT 6\n\tGWAIT 5\n\tGWAIT 4\n\tGWAIT 3\n\tGWAIT 2\n\tGWAIT 1\n\tGWAIT 0\n\t"
        "1:\n\t"
        "GROWS 8192-\\so\n\t"
        "GCMP 0, vcc\n\t"
        "s_add_i32 %[rem], %[rem], -1\n\t"
        "GPAIR 0, \\c\n\tGPAIR 2, \\c\n\tGPAIR 4, \\c\n\tGPAIR 6, \\c\n\t"
        ".endm\n\t"
        ".macro GSIG c\n\t"
        ".if " STR(PKADD) "\n\t.irp k, 0, 2, 4, 6\n\tv_pk_add_f32 v[\\c+\\k:\\c+\\k+1], v[\\c+\\k:\\c+\\k+1], 1.0 op_sel_hi:[1,0]\n\t.endr\n\t.else\n\t"
        ".irp k, 0, 1, 2, 3, 4, 5, 6, 7\n\tv_add_f32_e32 v[\\c+\\k], 1.0, v[\\c+\\k]\n\t.endr\n\t.endif\n\t"
        ".irp k, 0, 1, 2, 3, 4, 5, 6, 7\n\tv_rcp_f32_e32 v[\\c+\\k], v[\\c+\\k]\n\t.endr\n\t"
        ".irp k, 0, 1, 2, 3\n\tv_cvt_pk_bf16_f32 v[220+\\k], v[\\c+2*\\k], v[\\c+2*\\k+1]\n\t.endr\n\t.endm\n\t"
        "s_waitcnt vmcnt(8)\n\t"
        "s_cmp_gt_i32 %[rem], 1\n\ts_cbranch_scc1 1f\n\ts_waitcnt vmcnt(0)\n\t1:\n\t"
        "GROWS 0\n\t"
        ".if " STR(LPRIO) " >= 3\n\ts_mov_b32 %[pr], 0\n\ts_setprio 2\n\t.endif\n\t"
        "0:\n\t"
        ".if " STR(LPRIO) " == 3\n\t"
        "s_cmp_eq_u32 %[pr], 1\n\ts_cbranch_scc0 5f\n\ts_setprio 1\n\t5:\n\t"
        "s_cmp_eq_u32 %[pr], 2\n\ts_cbranch_scc0 5f\n\ts_setprio 0\n\t5:\n\t"
        "s_add_u32 %[pr], %[pr], 1\n\t.endif\n\t"
        "GSTEP 0, 196, 64\n\t"
        "s_cbranch_scc0 9f\n\t"
        "GSTEP 8192, 204, 96\n\t"
        "v_add_u32_e32 v192, 0x100, v192\n\t"
        "s_cbranch_scc1 0b\n\t"
        "9:\n\t"
        "s_waitcnt lgkmcnt(0)\n\ts_nop 6\n\t"
        ".purgem GWAIT\n\t.purgem GROWS\n\t.purgem GTR\n\t.purgem GS\n\t.purgem GLOAD\n\t.purgem GPART\n\t.purgem GSUM\n\t.purgem GCMP\n\t.purgem GPAIR\n\t.purgem GSTEP\n\t.purgem GSIG"
        : "+{v[128:159]}"(acc), "+{v[192:195]}"(misc), "+{v[196:211]}"(cc), "=&{v[64:95]}"(t0), "=&{v[96:127]}"(t1), "=&{v[212:223]}"(t2),
          [rem] "+s"(G), [t] "=&s"(tmp), [pr] "=&s"(pr), [sm] "=&s"(sm)
        : "{v[160:175]}"(qf), "{v[176:191]}"(addr), [s0] "s"(d.s0), [s1] "s"(d.s1), [s2] "s"(d.s2), [s3] "s"(d.s3),
          [mask] "s"(mask), [ring] "s"(ring), [nl] "s"(nlast)
        : "vcc", "scc", "memory");
}

__device__ __forceinline__ void inbox_request(u32x16& raw, const u32x4& srd, unsigned ib) {
    unsigned o1 = 0x1000, o2 = 0x2000, o3 = 0x3000;
    asm volatile("buffer_load_dwordx4 v[64:67], %1, %2, 0 offen sc1\n\tbuffer_load_dwordx4 v[68:71], %1, %2, %3 offen sc1\n\t"
                 "buffer_load_dwordx4 v[72:75], %1, %2, %4 offen sc1\n\tbuffer_load_dwordx4 v[76:79], %1, %2, %5 offen sc1"
                 : "=&{v[64:79]}"(raw) : "v"(ib), "s"(srd), "s"(o1), "s"(o2), "s"(o3) : "memory");
}
__device__ __forceinline__ void inbox_stage(u32x16 raw, const u32x4& srd, unsigned ib, unsigned key, int i4, unsigned hdra) {
    u32x16 t0; u32x4 t1; u64 m0, m1, m2, m3; unsigned todo, fresh, spin, o1, o2, o3;
    u64 pc0 = 0x0101010101010101ull;
    asm volatile(
        ".macro IVAL j, so\n\t"
        "s_bitcmp1_b32 %[todo], \\j\n\ts_cbranch_scc0 2f\n\t"
        ".irp k, 0, 1, 2, 3\n\tv_xor_b32_e32 v[64+4*\\j+\\k], %[key], v[64+4*\\j+\\k]\n\t.endr\n\t"
        "v_mov_b32_dpp v[80+\\j], v[64+4*\\j] quad_perm:[0,0,0,0] row_mask:0xf bank_mask:0xf\n\t"
        "v_add_u32_e32 v96, v[64+4*\\j], v[65+4*\\j]\n\t"
        "v_sub_u32_e32 v97, 0, v[65+4*\\j]\n\t"
        "v_mov_b32_dpp v[80+\\j], v[80+\\j] row_shr:4 row_mask:0xf bank_mask:0xa\n\t"
        "v_cndmask_b32_e64 v96, v96, v97, %[pc0]\n\t"
        "v_cmp_gt_i32_e32 vcc, v[80+\\j], v92\n\t"
        "v_add3_u32 v96, v96, v[66+4*\\j], v[67+4*\\j]\n\t"
        "v_lshrrev_b32_e32 v97, 14, v[80+\\j]\n\t"
        "v_cndmask_b32_e32 v96, 0, v96, vcc\n\t"
        "s_nop 1\n\tv_add_u32_dpp v96, v96, v96 quad_perm:[1,0,3,2] row_mask:0xf bank_mask:0xf\n\t"
        "s_nop 1\n\tv_add_u32_dpp v96, v96, v96 quad_perm:[2,3,0,1] row_mask:0xf bank_mask:0xf\n\t"
        "s_nop 1\n\tv_add_u32_dpp v96, v96, v96 row_half_mirror row_mask:0xf bank_mask:0xf\n\t"
        "v_or_b32_e32 v96, v96, v97\n\t"
        "v_cmp_ne_u32_e32 vcc, 0, v96\n\t"
        "s_cbranch_vccz 1f\n\t"
        "buffer_load_dwordx4 v[64+4*\\j:67+4*\\j], %[ib], %[srd], \\so offen sc1\n\t"
        "s_branch 2f\n\t"
        "1:\n\ts_bitset1_b32 %[fresh], \\j\n\t"
        "2:\n\t.endm\n\t"
        ".macro IRANK j\n\t"
        "s_bitcmp1_b32 %[fresh], \\j\n\ts_cbranch_scc0 2f\n\t"
        "v_cmp_lt_u32_e64 %[m0], v92, v[80+\\j]\n\tv_cmp_lt_u32_e64 %[m1], v93, v[80+\\j]\n\t"
        "v_cmp_lt_u32_e64 %[m2], v94, v[80+\\j]\n\tv_cmp_lt_u32_e64 %[m3], v95, v[80+\\j]\n\t"
        ".irp k, 0, 1, 2, 3\n\tv_lshrrev_b32_e32 v[84+\\k], 21, v[64+4*\\j+\\k]\n\t.endr\n\t"
        ".irp k, 0, 1, 2, 3\n\tv_and_b32_e32 v[84+\\k], 60, v[84+\\k]\n\t.endr\n\t"
        "v_cmp_lt_u32_e32 vcc, %c[cap], v[80+\\j]\n\t"
        "s_mov_b64 exec, %[m0]\n\tds_add_rtn_u32 v88, v84, v98 offset:%c[cnt]\n\t"
        "s_mov_b64 exec, %[m1]\n\tds_add_rtn_u32 v89, v85, v98 offset:%c[cnt]\n\t"
        "s_mov_b64 exec, %[m2]\n\tds_add_rtn_u32 v90, v86, v98 offset:%c[cnt]\n\t"
        "s_mov_b64 exec, %[m3]\n\tds_add_rtn_u32 v91, v87, v98 offset:%c[cnt]\n\t"
        "s_mov_b64 exec, %[pc0]\n\tds_write_b32 %[hdra], v[80+\\j] offset:128*\\j\n\t"
        "s_mov_b64 exec, -1\n\t"
        "s_cbranch_vccz 1f\n\t"
        "v_mov_b32_e32 v96, 0\n\tds_write_b32 v96, v98 offset:%c[cnt]+64\n\ts_waitcnt lgkmcnt(0)\n\t"
        "1:\n\ts_waitcnt lgkmcnt(1)\n\t"
        ".irp k, 0, 1, 2, 3\n\tv_min_u32_e32 v[88+\\k], %c[sub]-4, v[88+\\k]\n\t.endr\n\t"
        ".irp k, 0, 1, 2, 3\n\tv_mad_u32_u24 v[88+\\k], v[84+\\k], %[sub4], v[88+\\k]\n\t.endr\n\t"
        "s_mov_b64 exec, %[m0]\n\tds_write_b32 v88, v[64+4*\\j] offset:%c[list]\n\t"
        "s_mov_b64 exec, %[m1]\n\tds_write_b32 v89, v[65+4*\\j] offset:%c[list]\n\t"
        "s_mov_b64 exec, %[m2]\n\tds_write_b32 v90, v[66+4*\\j] offset:%c[list]\n\t"
        "s_mov_b64 exec, %[m3]\n\tds_write_b32 v91, v[67+4*\\j] offset:%c[list]\n\t"
        "s_mov_b64 exec, -1\n\t"
        "2:\n\t.endm\n\t"
        "s_movk_i32 %[o1], 0x1000\n\ts_movk_i32 %[o2], 0x2000\n\ts_movk_i32 %[o3], 0x3000\n\t"
        "v_mov_b32_e32 v92, %[i4]\n\tv_add_u32_e32 v93, 4, %[i4]\n\tv_add_u32_e32 v94, 8, %[i4]\n\tv_add_u32_e32 v95, 12, %[i4]\n\t"
        "v_mov_b32_e32 v98, 4\n\t"
        "s_mov_b32 %[todo], 15\n\ts_mov_b32 %[spin], 0x400000\n\t"
        "0:\n\t"
        "s_mov_b32 %[fresh], 0\n\t"
        "s_waitcnt vmcnt(3)\n\ts_cmp_eq_u32 %[todo], 15\n\ts_cbranch_scc1 3f\n\ts_waitcnt vmcnt(0)\n\t3:\n\t"
        "IVAL 0, 0\n\tIRANK 0\n\t"
        "s_waitcnt vmcnt(2)\n\tIVAL 1, %[o1]\n\tIRANK 1\n\t"
        "s_waitcnt vmcnt(1)\n\tIVAL 2, %[o2]\n\tIRANK 2\n\t"
        "s_cmp_eq_u32 %[todo], 15\n\ts_cbranch_scc0 3f\n\ts_waitcnt vmcnt(0)\n\t3:\n\tIVAL 3, %[o3]\n\tIRANK 3\n\t"
        "s_andn2_b32 %[todo], %[todo], %[fresh]\n\t"
        "s_cbranch_scc0 9f\n\t"
        "s_sub_u32 %[spin], %[spin], 1\n\t"
        "s_cbranch_scc0 0b\n\t"
        "s_waitcnt vmcnt(0)\n\t"
        "9:\n\t"
        ".purgem IVAL\n\t.purgem IRANK"
        : "+{v[64:79]}"(raw), "=&{v[80:95]}"(t0), "=&{v[96:99]}"(t1), [m0] "=&s"(m0), [m1] "=&s"(m1), [m2] "=&s"(m2), [m3] "=&s"(m3),
          [todo] "=&s"(todo), [fresh] "=&s"(fresh), [spin] "=&s"(spin), [o1] "=&s"(o1), [o2] "=&s"(o2), [o3] "=&s"(o3)
        : [srd] "s"(srd), [ib] "v"(ib), [key] "s"(key), [i4] "v"(i4), [hdra] "v"(hdra), [pc0] "s"(pc0), [sub4] "s"(SUBB / 4),
          [cnt] "i"(L_CNT), [list] "i"(L_LIST), [sub] "i"(SUBB), [cap] "i"(4 * CAP1)
        : "vcc", "scc", "memory");
}

template <int CTRL, int BANK> __device__ __forceinline__ unsigned dpp(unsigned old, unsigned v) { return __builtin_amdgcn_update_dpp(old, v, CTRL, 0xF, BANK, true); }
__device__ __forceinline__ unsigned bcast8(unsigned v) { v = dpp<0x00, 0xF>(0u, v); return __builtin_amdgcn_update_dpp(v, v, 0x114, 0xF, 0xA, false); }
__device__ __forceinline__ unsigned sum8(unsigned v) { v += dpp<0xB1, 0xF>(0u, v); v += dpp<0x4E, 0xF>(0u, v); return v + dpp<0x141, 0xF>(0u, v); }
__device__ __forceinline__ bf16x4 tr_read(LAS char* p) { return __builtin_amdgcn_ds_read_tr16_b64_v4bf16((LAS bf16x4*)p); }
__device__ __forceinline__ bf16x8 cat(bf16x4 a, bf16x4 b) { return __builtin_shufflevector(a, b, 0, 1, 2, 3, 4, 5, 6, 7); }
__device__ __forceinline__ unsigned pk2(float a, float b) { f32x2 v = {a, b}; bf16x2 r = __builtin_convertvector(v, bf16x2); return __builtin_bit_cast(unsigned, r); }
__device__ __forceinline__ f32x4 mfma(bf16x8 a, bf16x8 b, f32x4 c) { return __builtin_amdgcn_mfma_f32_16x16x32_bf16(a, b, c, 0, 0, 0); }
__device__ __forceinline__ unsigned lds_add(LAS char* p, unsigned v) { return __hip_atomic_fetch_add((LAS unsigned*)p, v, __ATOMIC_RELAXED, __HIP_MEMORY_SCOPE_WORKGROUP); }
__device__ __forceinline__ LAS unsigned* ldsp(unsigned a) { return (LAS unsigned*)(size_t)a; }
__device__ __forceinline__ unsigned byte0_x4(unsigned v) { unsigned r; asm("v_lshlrev_b32_sdwa %0, 2, %1 dst_sel:DWORD dst_unused:UNUSED_PAD src0_sel:DWORD src1_sel:BYTE_0" : "=v"(r) : "v"(v)); return r; }
__device__ __forceinline__ unsigned mad24(unsigned a, unsigned b, unsigned c) { unsigned r; asm("v_mad_u32_u24 %0, %1, %2, %3" : "=v"(r) : "v"(a), "s"(b), "v"(c)); return r; }
__device__ __forceinline__ void lds_arrive(LAS char* p, int lane) {
    asm volatile("s_waitcnt lgkmcnt(0)" ::: "memory");
    if (lane == 0) __hip_atomic_fetch_add((LAS unsigned*)p, 1u, __ATOMIC_RELAXED, __HIP_MEMORY_SCOPE_WORKGROUP);
}
__device__ __forceinline__ void lds_await(LAS char* p, unsigned n) {
    while (__builtin_amdgcn_readfirstlane(*(volatile LAS unsigned*)p) < n) __builtin_amdgcn_s_sleep(1);
    asm volatile("" ::: "memory");
}
__device__ __forceinline__ unsigned rfl(unsigned v) { return __builtin_amdgcn_readfirstlane(v); }

__global__ __launch_bounds__(512, 2) void k_graph(const unsigned short* __restrict__ x, const int* __restrict__ src, const int* __restrict__ dst,
                                                  const unsigned short* __restrict__ wq, const unsigned short* __restrict__ wv,
                                                  unsigned* __restrict__ ws, unsigned short* __restrict__ out, int N, int E) {
    __shared__ __attribute__((aligned(16))) char smem_[L_TOTAL];
    LAS char* smem = (LAS char*)smem_;
    const int t = threadIdx.x, l = t & 63, b = blockIdx.x;
    const int w = rfl(t >> 6);
    const int g = l >> 4, n = l & 15, q = (l >> 2) & 3, p = l & 3, pc = t & 7;
    STAMP_DECL;
    STAMP(0);
    const u64 t_start = __builtin_amdgcn_s_memrealtime();
    asm volatile("" :: "s"(src), "s"(dst), "s"(out), "s"(wq), "s"(wv), "s"(x), "s"(ws), "s"(N), "s"(E));
    const unsigned lds0 = (unsigned)(size_t)smem;
    const unsigned xbytes = (unsigned)N * 256u;
    const u32x4 srd_ws = make_srd(ws, WS_BYTES);
    const unsigned ring = lds0 + L_RING + w * 16384;
    const bool isprod = ((b >> 3) & 1) == 0;
    const int pg = ((b & 7) << 4) | (b >> 4);
    const int jn = 8 * n + w;
    const u32x4 srd_x0 = make_srd(x, xbytes);

    unsigned mykey = 0;
    auto node_row = [&](int tile, int m) { const int nd = b + 256 * (8 * (4 * m + g) + tile); return nd < N ? (unsigned)nd << 8 : OOB; };
    auto zero_staging = [&]() {
        const u32x4 z = {0u, 0u, 0u, 0u};
#pragma unroll
        for (int k = 0; k < 5; ++k) *(LAS u32x4*)(smem + L_IMG + k * 8192 + t * 16) = z;
    };

    u32x16 addr;
    f32x32 acc, wva, wvb;
    unsigned cv, lista, cnta, qa, keyn, inf;
    Srd4 srd_x;
    auto loop_constants = [&]() {
        srd_x = make_srd4(x, xbytes, BIAS * 256);
        lista = lds0 + L_LIST + w * (2 * SUBB) + g * 4; cnta = lds0 + L_CNT + (l & 31) * 4; qa = ring + l * 16; keyn = jn; inf = 0x7F800000u;
        asm volatile("" : "+s"(srd_x.s0), "+s"(srd_x.s1), "+s"(srd_x.s2), "+s"(srd_x.s3));
        asm volatile("" : "+v"(lista), "+v"(cnta), "+v"(qa), "+v"(keyn), "+v"(inf));
        const unsigned o0 = (n ^ (((g & 1) << 3) | (g >> 1))) << 4;
        const unsigned ra = (4 * (n & 3) + (n >> 2)) * 256 + (((4 * g) ^ (((n & 7) << 1) | (n >> 3))) << 4);
        const int i = 4 * g + q, r8 = i & 7;
        const unsigned ta = (4 * q + g) * 256 + 8 * (p & 1) + (((p >> 1) ^ (((i & 7) << 1) | (i >> 3))) << 4);
        cv = i * 256 + (((p ^ ((r8 & 1) | ((r8 & 6) << 1))) << 4) | ((p & 1) << 3));
#pragma unroll
        for (int s = 0; s < 4; ++s) { addr[s] = ring + (ra ^ (s << 4)); addr[12 + s] = o0 ^ (s << 5); }
#pragma unroll
        for (int c = 0; c < 8; ++c) addr[4 + c] = ring + (ta ^ (c << 5));
        asm volatile("" : "+{v[176:191]}"(addr), "+v"(cv));
    };
    auto clear_sums = [&]() { acc = 0.f; asm volatile("" : "+{v[128:159]}"(acc)); };
    auto wv_resident = [&]() {
#pragma unroll
        for (int a = 0; a < 8; ++a) {
            LAS char* pa = smem + L_WV0 + (cv ^ ((a >> 1) << 6) ^ ((a & 1) << 3));
            const f32x4 f0 = __builtin_bit_cast(f32x4, cat(tr_read(pa), tr_read(pa + 4096)));
            const f32x4 f1 = __builtin_bit_cast(f32x4, cat(tr_read(pa + 16384), tr_read(pa + 16384 + 4096)));
#pragma unroll
            for (int k = 0; k < 4; ++k) { wva[4 * a + k] = f0[k]; wvb[4 * a + k] = f1[k]; }
        }
        asm volatile("" : "+{v[224:255]}"(wva), "+{v[32:63]}"(wvb));
    };

    u32x16 ed, es;
    const unsigned e0 = pg * CH + 4 * t, e1 = pg * CH + 3072 + t;
    {
        const u32x4 srd_d = make_srd(dst, (unsigned)E * 4u), srd_s = make_srd(src, (unsigned)E * 4u);
        const unsigned o = e0 * 4, o1 = w == 0 ? e1 * 4 : OOB, ok = WS_KEY + t * 4, om = WS_KEY + b * 4;
        const unsigned oc = (t - 256) * 128, dump = lds0 + L_DUMP + w * 256;
        unsigned k4 = 4096, k8 = 8192; int role = (w < 4 ? 0 : 2) + (isprod ? 0 : 1);
        u32x4 srd_c;
        asm volatile("s_cmp_ge_u32 %12, 2\n\ts_cbranch_scc1 2f\n\t"
                     "s_cmp_eq_u32 %12, 1\n\ts_cbranch_scc1 3f\n\t"
                     "buffer_load_dwordx4 v[64:67], %3, %7, 0 offen\n\tbuffer_load_dwordx4 v[68:71], %3, %7, %10 offen\n\t"
                     "buffer_load_dwordx4 v[72:75], %3, %7, %11 offen\n\tbuffer_load_dword v76, %4, %7, 0 offen\n\t"
                     "buffer_load_dwordx4 v[80:83], %3, %8, 0 offen\n\tbuffer_load_dwordx4 v[84:87], %3, %8, %10 offen\n\t"
                     "buffer_load_dwordx4 v[88:91], %3, %8, %11 offen\n\tbuffer_load_dword v92, %4, %8, 0 offen\n\t"
                     "buffer_load_dword v93, %5, %9, 0 offen sc1\n\t"
                     "buffer_load_dword v94, %6, %9, 0 offen sc1\n\t"
                     "s_branch 3f\n\t"
                     "2:\n\t.if " STR(CODEPF) "\n\ts_getpc_b64 s[96:97]\n\ts_and_b32 s97, s97, 0xffff\n\ts_mov_b32 s98, " STR(CODE_BYTES) "\n\ts_mov_b32 s99, 0x20000\n\t"
                     "s_mov_b32 m0, %14\n\ts_nop 0\n\tbuffer_load_dword %13, s[96:99], 0 offen lds\n\t.endif\n\t"
                     "3:"
                     : "=&{v[64:79]}"(ed), "=&{v[80:95]}"(es), "=&{s[96:99]}"(srd_c)
                     : "v"(o), "v"(o1), "v"(ok), "v"(om), "s"(srd_d), "s"(srd_s), "s"(srd_ws), "s"(k4), "s"(k8), "s"(role), "v"(oc), "s"(dump) : "memory", "scc");
    }
    if (isprod) zero_staging();
    if (t < 8) *(LAS u32x4*)(smem + L_CNT + t * 16) = u32x4{0u, 0u, 0u, 0u};
    WG_BARRIER();

    if (__builtin_expect(w < 4, 1)) {
        if (__builtin_expect(isprod, 1)) {
            if (PRIO) __builtin_amdgcn_s_setprio(2);
            unsigned ob = WS_INBOX + ((t >> 3) * G1 + pg) * 128 + pc * 16, ima = lds0 + L_IMG + (t >> 3) * 144 + pc * 16;
            PIN(ob); PIN(ima);
            STAMP(1);
            unsigned b4[13], rk[13];
            const bool v1 = w == 0 && e1 < (unsigned)E;
            asm volatile("s_waitcnt vmcnt(6)" : "+{v[64:79]}"(ed) :: "memory");
            const u32x4 d4[3] = {{ed[0], ed[1], ed[2], ed[3]}, {ed[4], ed[5], ed[6], ed[7]}, {ed[8], ed[9], ed[10], ed[11]}};
            const unsigned d1 = ed[12];
            STAMP(2);
#pragma unroll
            for (int j = 0; j < 3; ++j)
                if (e0 + 1024 * j < (unsigned)E) {
#pragma unroll
                    for (int k = 0; k < 4; ++k) { b4[4 * j + k] = byte0_x4(d4[j][k]); rk[4 * j + k] = lds_add(smem + L_CNT1 + b4[4 * j + k], 4); }
                }
            if (v1) { b4[12] = byte0_x4(d1); rk[12] = lds_add(smem + L_CNT1 + b4[12], 4); }
            asm volatile("s_waitcnt vmcnt(0)" : "+{v[80:95]}"(es) :: "memory");
            const u32x4 s4[3] = {{es[0], es[1], es[2], es[3]}, {es[4], es[5], es[6], es[7]}, {es[8], es[9], es[10], es[11]}};
            const unsigned s1 = es[12], kb = es[13];
            mykey = es[14];
            auto put = [&](unsigned d, unsigned sv, unsigned bb, unsigned r) {
                *(LAS unsigned*)(smem + L_IMG + 8 + mad24(bb, 36, min(r, 4u * CAP1))) = __builtin_amdgcn_perm(sv + BIAS, d, 0x01050401u);
            };
            auto spill = [&](unsigned d, unsigned sv, unsigned r) {
                if (r >= 4u * CAP1) {
                    const unsigned o = WS_OVF + (pg * CH + lds_add(smem + L_CNT1 + 2048, 1)) * 4, v = (sv << 16) | d;
                    ST1(v, srd_ws, o, " sc1");
                }
            };
#pragma unroll
            for (int j = 0; j < 3; ++j)
                if (e0 + 1024 * j < (unsigned)E) {
#pragma unroll
                    for (int k = 0; k < 4; ++k) put(d4[j][k], s4[j][k], b4[4 * j + k], rk[4 * j + k]);
                    if (__builtin_expect(max(max(rk[4 * j], rk[4 * j + 1]), max(rk[4 * j + 2], rk[4 * j + 3])) >= 4u * CAP1, 0)) {
#pragma unroll
                        for (int k = 0; k < 4; ++k) spill(d4[j][k], s4[j][k], rk[4 * j + k]);
                    }
                }
            if (v1) { put(d1, s1, b4[12], rk[12]); spill(d1, s1, rk[12]); }
            *(LAS unsigned*)(smem + L_CNT1 + 1024 + t * 4) = kb + KEY_STEP;
            WG_BARRIER();
            STAMP(3);
            unsigned c4[8], kk[8], cs[8]; u32x4 v[8];
            const unsigned nov = *(LAS unsigned*)(smem + L_CNT1 + 2048);
#pragma unroll
            for (int it = 0; it < 8; ++it) {
                c4[it] = *(LAS unsigned*)(smem + L_CNT1 + (t >> 3) * 4 + it * 128);
                kk[it] = *(LAS unsigned*)(smem + L_CNT1 + 1024 + (t >> 3) * 4 + it * 128);
                v[it] = *(LAS u32x4*)(size_t)(ima + it * (32 * 144));
            }
#pragma unroll
            for (int it = 0; it < 8; ++it) cs[it] = sum8(v[it].x + v[it].y + v[it].z + v[it].w);
            lds_arrive(smem + L_CNT + 84, l);
            const unsigned no = rfl(nov);
            if (__builtin_expect(no != 0, 0)) {
                const unsigned o = t == 0 ? WS_NOVF + pg * 4 : OOB;
                ST1(no, srd_ws, o, " sc1");
                VMWAIT(0);
                WG_BARRIER();
            }
#pragma unroll
            for (int it = 0; it < 8; ++it) {
                v[it].x = pc == 0 ? c4[it] : v[it].x; v[it].y = pc == 0 ? cs[it] : v[it].y;
                v[it] ^= kk[it];
                const unsigned o = (WMODE == 2 || (int)c4[it] > 16 * pc - 8) ? ob : OOB, so = it * 32 * G1 * 128;
                asm volatile("buffer_store_dwordx4 %0, %1, %2, %3 offen sc1\n\ts_nop 1" :: "v"(v[it]), "v"(o), "s"(srd_ws), "s"(so) : "memory");
            }
            STAMP(4);
        } else {
            { const unsigned om = WS_KEY + b * 4; asm volatile("buffer_load_dword %0, %1, %2, 0 offen sc1\n\ts_waitcnt vmcnt(0)" : "=&v"(mykey) : "v"(om), "s"(srd_ws) : "memory"); }
            if (NP_SLEEP) __builtin_amdgcn_s_sleep(NP_SLEEP);
            const unsigned o = ((b >> 4) * 256 + t) * 128;
            set_m0(lds0 + L_DUMP + w * 256);
            STAMP(1);
            for (int r = 0; r < PF_R; ++r) {
                asm volatile("s_waitcnt vmcnt(%c0)" :: "i"(PF_D) : "memory");
                if (__builtin_amdgcn_s_memrealtime() > t_start + PF_T) break;
                const unsigned so = r << 19;
                asm volatile("buffer_load_dword %1, %0, %2 offen lds" :: "s"(srd_x0), "v"(o), "s"(so) : "memory");
            }
            if (PRIO) __builtin_amdgcn_s_setprio(2);
            STAMP(2); STAMP(3); STAMP(4);
        }
        STAMP(5);
        mykey = rfl(mykey) + KEY_STEP;
        loop_constants();
        clear_sums();
        unsigned ib = WS_INBOX + b * (G1 * 128) + t * 16;
        PIN(ib);
        while (__builtin_amdgcn_s_memrealtime() < t_start + POLL_T) __builtin_amdgcn_s_sleep(1);
        STAMP(6);
        u32x16 raw;
        inbox_request(raw, srd_ws, ib);
        inbox_stage(raw, srd_ws, ib, mykey, 16 * pc - 8, lds0 + L_HDR + (t >> 3) * 4);
        if (PRIO) __builtin_amdgcn_s_setprio(0);
        STAMP(7);
    } else {
        const int j = w - 4;
        STAMP(1);
        u32x4 wr[8], xr[8];
        {
            const u32x4 srd_q = make_srd(wq, 32768);
            const unsigned o = j * 8192 + l * 16, k4 = 4096;
            LD4(wr[0], srd_q, o, ""); LD4(wr[1], srd_q, o, " offset:1024"); LD4(wr[2], srd_q, o, " offset:2048"); LD4(wr[3], srd_q, o, " offset:3072");
            LD4S(wr[4], srd_q, o, k4, ""); LD4S(wr[5], srd_q, o, k4, " offset:1024"); LD4S(wr[6], srd_q, o, k4, " offset:2048"); LD4S(wr[7], srd_q, o, k4, " offset:3072");
#pragma unroll
            for (int m = 0; m < 8; ++m) { const unsigned ox = node_row(m < 4 ? j : w, m & 3) + n * 16; LD4(xr[m], srd_x0, ox, ""); }
        }
        unsigned aqa[8], xfa[4];
        unsigned xa = ring + g * 256 + ((n ^ g) << 4), wa = lds0 + L_WQ + j * 16384 + g * 256 + ((n ^ g) << 4) + ((n & 4) << 1);
        {
            const unsigned aq = lds0 + L_WQ + (8 * g + q) * 256 + ((((4 * p) ^ (q | ((g & 1) << 2))) << 4) | ((p & 1) << 3));
#pragma unroll
            for (int s = 0; s < 4; ++s) { xfa[s] = ring + n * 256 + (((4 * s + g) ^ n) << 4); PIN(xfa[s]); }
#pragma unroll
            for (int c = 0; c < 8; ++c) { aqa[c] = aq ^ ((c >> 1) << 4) ^ ((c & 1) << 3); PIN(aqa[c]); }
            PIN(xa); PIN(wa);
        }
        loop_constants();
        STAMP(2);
        if (isprod) {
            WG_BARRIER();
            if (__builtin_expect(rfl(*(LAS unsigned*)(smem + L_CNT1 + 2048)) != 0, 0)) WG_BARRIER();
            if (W_SLEEP) __builtin_amdgcn_s_sleep(W_SLEEP);
        }
        STAMP(3);
        asm volatile("s_waitcnt vmcnt(8)" : "+v"(wr[0]), "+v"(wr[1]), "+v"(wr[2]), "+v"(wr[3]), "+v"(wr[4]), "+v"(wr[5]), "+v"(wr[6]), "+v"(wr[7]) :: "memory");
#pragma unroll
        for (int m = 0; m < 8; ++m) {
            const unsigned a = (wa ^ ((m & 2) << 5)) + m * 1024;
            *(LAS u32x2*)(size_t)a = u32x2{wr[m].x, wr[m].y};
            *(LAS u32x2*)(size_t)(a ^ 8) = u32x2{wr[m].z, wr[m].w};
        }
        asm volatile("s_waitcnt vmcnt(0)" : "+v"(xr[0]), "+v"(xr[1]), "+v"(xr[2]), "+v"(xr[3]), "+v"(xr[4]), "+v"(xr[5]), "+v"(xr[6]), "+v"(xr[7]) :: "memory");
#pragma unroll
        for (int m = 0; m < 8; ++m) *(LAS u32x4*)(size_t)((xa ^ ((m & 3) << 6)) + m * 1024) = xr[m];
        STAMP(4);
        lds_arrive(smem + L_CNT + 80, l);
        lds_await(smem + L_CNT + 80, 4);
        STAMP(5);
        bf16x8 xo[2][4], fr[2][8];
        f32x4 qc[2][8];
        auto wq_frags = [&](int u, bf16x8 (&f)[8]) {
#pragma unroll
            for (int a = 0; a < 8; ++a) {
                LAS char* pa = (LAS char*)(size_t)aqa[a] + u * 16384;
                f[a] = cat(tr_read(pa), tr_read(pa + 1024));
            }
        };
#pragma unroll
        for (int u = 0; u < 4; ++u) { xo[0][u] = *(LAS bf16x8*)(size_t)xfa[u]; xo[1][u] = *(LAS bf16x8*)(size_t)(xfa[u] + 4096); }
        wq_frags(0, fr[0]);
        {
            const Srd4 srd_v = make_srd4(wv, 32768);
            const unsigned r0 = (j * 32 + g) << 8, o0 = (n ^ ((g & 1) | ((g & 2) << 1))) << 4;
            const unsigned e[8] = {r0, r0 + 1024, r0 + 2048, r0 + 3072, r0 + 4096, r0 + 5120, r0 + 6144, r0 + 7168};
            const unsigned base = lds0 + (j < 2 ? L_WV0 + j * 16384 : L_WV1 + (j - 2) * 8192);
            const u32x4 off = {o0, o0 ^ 128, o0, o0 ^ 128};
            dma_half(base, srd_v, e, off); dma_half(base + 4096, srd_v, e + 4, off);
        }
#pragma unroll
        for (int u = 0; u < 4; ++u) {
            if (u < 3) wq_frags(u + 1, fr[(u + 1) & 1]);
            FENCE();
#pragma unroll
            for (int a = 0; a < 8; ++a)
#pragma unroll
                for (int tt = 0; tt < 2; ++tt) qc[tt][a] = mfma(fr[u & 1][a], xo[tt][u], u ? qc[tt][a] : f32x4{0.f, 0.f, 0.f, 0.f});
            FENCE();
            if (u == 1 && j < 2) { VMWAIT(0); lds_arrive(smem + L_CNT + 88, l); }
        }
        STAMP(6);
        if (isprod) lds_await(smem + L_CNT + 84, 4);
#pragma unroll
        for (int tt = 0; tt < 2; ++tt)
#pragma unroll
            for (int s = 0; s < 4; ++s) {
                const float k = -0.08838834764831845f * 1.4426950408889634f;
                const f32x4 c0 = qc[tt][2 * s] * k, c1 = qc[tt][2 * s + 1] * k;
                *(LAS u32x4*)(size_t)(ring - (tt ? 0 : 65536) + s * 1024 + l * 16) = u32x4{pk2(c0[0], c0[1]), pk2(c0[2], c0[3]), pk2(c1[0], c1[1]), pk2(c1[2], c1[3])};
            }
        clear_sums();
        lds_await(smem + L_CNT + 88, 2);
        wv_resident();
        VMWAIT(0);
        STAMP(7);
    }
    WG_BARRIER();
    STAMP(8);
    if (LPRIO >= 3) __builtin_amdgcn_s_setprio(3);
    if (LPRIO == 1 && w >= 4) __builtin_amdgcn_s_setprio(1);
    if (LPRIO == 2) { if (w >= 6) __builtin_amdgcn_s_setprio(3); else if (w >= 4) __builtin_amdgcn_s_setprio(2); else if (w >= 2) __builtin_amdgcn_s_setprio(1); }

    u32x16 qf;
    const u32x4 doff = {addr[12], addr[13], addr[14], addr[15]};
    LAS char* mylist = smem + L_LIST + w * (2 * SUBB);
    int gcur = G1, pos = 0;
    auto batch = [&](auto first_) {
        constexpr bool first = decltype(first_)::value;
        auto entries = [&](int grp, unsigned (&e)[8]) {
#pragma unroll
            for (int m = 0; m < 8; ++m) e[m] = *(LAS unsigned*)(size_t)(lista + grp * 128 + m * 16);
        };
        unsigned cw = *(LAS unsigned*)(size_t)cnta, e0[8], e1[8];
        entries(0, e0);
        if constexpr (first) {
#pragma unroll
            for (int s = 0; s < 4; ++s) {
                const u32x4 v = *(LAS u32x4*)(size_t)(qa + s * 1024);
                qf[4 * s] = v.x; qf[4 * s + 1] = v.y; qf[4 * s + 2] = v.z; qf[4 * s + 3] = v.w;
            }
        }
        entries(1, e1);
        FENCE();
        int lo = __builtin_amdgcn_readlane(cw, 2 * w) >> 2, hi = __builtin_amdgcn_readlane(cw, 2 * w + 1) >> 2;
        if constexpr (first) {
            if (__builtin_amdgcn_readlane(cw, 16)) gcur = 0;
            if (__builtin_amdgcn_ballot_w64(cw > 4u * CAP2)) { gcur = -1; lo = 0; hi = 0; }
        }
        const int c = lo + hi, G = (c + 31) >> 5;
        const int nlast = G > 2 ? (((c - 1) & 31) >> 2) + 1 : 8;
        auto join = [&]() {
            for (int k = 0; k < G * 32 - lo; k += 64) {
                const unsigned e = *(LAS unsigned*)(mylist + SUBB + (l + k) * 4);
                if (l + k < G * 32 - lo) *(LAS unsigned*)(mylist + (lo + l + k) * 4) = l + k < hi ? e : PAD;
            }
        };
        const bool early = lo >= 64;
        if (__builtin_expect(!early, 0)) { join(); entries(0, e0); entries(1, e1); }
        if constexpr (first) asm volatile("s_waitcnt lgkmcnt(0)" : "+{v[160:175]}"(qf) :: "memory");
        if (G > 0) dma_group(ring, srd_x, e0, doff);
        if constexpr (first) {
            if (w < 4) {
                if (G > 1) dma_group(ring + 8192, srd_x, e1, doff);
                wv_resident();
                lds_arrive(smem + L_CNT + 92, l);
            } else {
                if (w >= 6) lds_await(smem + L_CNT + 92, 4);
                if (G > 1) dma_group(ring + 8192, srd_x, e1, doff);
            }
        } else if (G > 1) dma_group(ring + 8192, srd_x, e1, doff);
        f32x16 cc;
#pragma unroll
        for (int m = 0; m < 8; ++m) {
            cc[m] = (e0[m] & 0xFF) == keyn ? 0.f : __uint_as_float(inf);
            cc[8 + m] = (e1[m] & 0xFF) == keyn ? 0.f : __uint_as_float(inf);
        }
        if (early) join();
        STAMP(9);
        if (G > 0) gather_loop(acc, qf, addr, u32x4{lista, keyn, inf, 0u}, cc, srd_x, ring, G, nlast);
    };
    batch(std::true_type{});
    while (__builtin_expect(gcur < G1, 0)) {
        unsigned lim = G1 * 32;
        if (gcur >= 0) {
            const u64 m0 = __builtin_amdgcn_ballot_w64(l >= gcur && *(LAS unsigned*)(smem + L_HDR + l * 4) > 4u * CAP1);
            const u64 m1 = __builtin_amdgcn_ballot_w64(l + 64 >= gcur && *(LAS unsigned*)(smem + L_HDR + 256 + l * 4) > 4u * CAP1);
            if (!(m0 | m1)) break;
            gcur = m0 ? __builtin_ctzll(m0) : 64 + __builtin_ctzll(m1);
            unsigned v; const unsigned o = WS_NOVF + gcur * 4;
            asm volatile("buffer_load_dword %0, %1, %2, 0 offen sc1\n\ts_waitcnt vmcnt(0)" : "=&v"(v) : "v"(o), "s"(srd_ws) : "memory");
            lim = rfl(v);
        }
        const unsigned i = pos + t;
        const bool cand = t < CAP2 && i < lim;
        unsigned u;
        { const unsigned o = cand ? (gcur < 0 ? WS_INBOX + b * (G1 * 128) : WS_OVF + gcur * CH * 4) + i * 4 : OOB;
          asm volatile("buffer_load_dword %0, %1, %2, 0 offen sc1\n\ts_waitcnt vmcnt(0)" : "=&v"(u) : "v"(o), "s"(srd_ws) : "memory"); }
        WG_BARRIER();
        if (t < 16) *(LAS unsigned*)(smem + L_CNT + t * 4) = 0;
        WG_BARRIER();
        if (cand) {
            unsigned e; bool v;
            if (gcur < 0) { e = u ^ mykey; v = ((i & 31) - 2) * 4 < *(LAS unsigned*)(smem + L_HDR + (i >> 5) * 4); }
            else { e = (((u >> 16) + BIAS) << 8) | ((u >> 8) & 0xFF); v = (u & 0xFF) == (unsigned)b; }
            if (v) *(LAS unsigned*)(smem + L_LIST + (e & 7) * (2 * SUBB) + lds_add(smem + L_CNT + (e & 7) * 8, 4)) = e;
        }
        pos += CAP2;
        if ((unsigned)pos >= lim) { ++gcur; pos = 0; }
        WG_BARRIER();
        batch(std::false_type{});
    }
    STAMP(10);

    {
        bf16x8 wl[2][8], ag[4];
#pragma unroll
        for (int u = 0; u < 2; ++u)
#pragma unroll
            for (int a = 0; a < 8; ++a) {
                LAS char* pa = smem + L_WV1 + (cv ^ ((a >> 1) << 6) ^ ((a & 1) << 3)) + u * 8192;
                wl[u][a] = cat(tr_read(pa), tr_read(pa + 4096));
            }
#pragma unroll
        for (int u = 0; u < 4; ++u) {
            const u32x4 v = {pk2(acc[8 * u], acc[8 * u + 1]), pk2(acc[8 * u + 2], acc[8 * u + 3]),
                             pk2(acc[8 * u + 4], acc[8 * u + 5]), pk2(acc[8 * u + 6], acc[8 * u + 7])};
            ag[u] = __builtin_bit_cast(bf16x8, v);
        }
        FENCE();
        f32x4 oc[8];
#pragma unroll
        for (int a = 0; a < 8; ++a) oc[a] = mfma(__builtin_bit_cast(bf16x8, f32x4{wva[4 * a], wva[4 * a + 1], wva[4 * a + 2], wva[4 * a + 3]}), ag[0], f32x4{0.f, 0.f, 0.f, 0.f});
#pragma unroll
        for (int a = 0; a < 8; ++a) oc[a] = mfma(__builtin_bit_cast(bf16x8, f32x4{wvb[4 * a], wvb[4 * a + 1], wvb[4 * a + 2], wvb[4 * a + 3]}), ag[1], oc[a]);
#pragma unroll
        for (int a = 0; a < 8; ++a) oc[a] = mfma(wl[0][a], ag[2], oc[a]);
        FENCE();
        const unsigned ow = ring + n * 256 + ((g ^ n) << 4) + ((g & 1) << 3);
#pragma unroll
        for (int a = 0; a < 8; ++a) oc[a] = mfma(wl[1][a], ag[3], oc[a]);
#pragma unroll
        for (int a = 0; a < 8; ++a)
            *(LAS u32x2*)(size_t)(ow ^ ((a >> 1) << 6) ^ ((a & 1) << 3)) = u32x2{pk2(oc[a][0], oc[a][1]), pk2(oc[a][2], oc[a][3])};
        const auto rs_o = __builtin_amdgcn_make_buffer_rsrc((void*)out, 0, xbytes, 0x00020000);
#pragma unroll
        for (int m = 0; m < 4; ++m) {
            const u32x4 v = *(LAS u32x4*)(size_t)(ring + m * 1024 + l * 16);
            __builtin_amdgcn_raw_buffer_store_b128(v, rs_o, node_row(w, m) + ((n ^ g ^ (4 * m)) << 4), 0, OUT_AUX);
        }
    }
    if (w == 0) { const unsigned o = l == 0 ? WS_KEY + b * 4 : OOB; ST1(mykey, srd_ws, o, " sc1"); }
    STAMP(11);
}

extern "C" void kernel_launch(void* const* d_in, const int* in_sizes, int n_in,
                              void* d_out, int out_size, void* d_ws, size_t ws_size,
                              hipStream_t stream) {
    const int N = in_sizes[0] / 128, E = in_sizes[1];
    k_graph<<<NB, 512, 0, stream>>>((const unsigned short*)d_in[0], (const int*)d_in[1], (const int*)d_in[2],
                                    (const unsigned short*)d_in[3], (const unsigned short*)d_in[4],
                                    (unsigned*)d_ws, (unsigned short*)d_out, N, E);
}
```

```cpp
#define POLL_T 330
#include <hip/hip_runtime.h>
#include <hip/hip_bf16.h>
#include <stdint.h>
#include <type_traits>
#define ORD 1
#define SKIP 0
#define STR2(x) #x
#define STR(x) STR2(x)

typedef __attribute__((ext_vector_type(8))) __bf16 bf16x8;
typedef __attribute__((ext_vector_type(4))) __bf16 bf16x4;
typedef __attribute__((ext_vector_type(2))) __bf16 bf16x2;
typedef __attribute__((ext_vector_type(4))) float f32x4;
typedef __attribute__((ext_vector_type(2))) float f32x2;
typedef __attribute__((ext_vector_type(4))) unsigned u32x4;
typedef __attribute__((ext_vector_type(2))) unsigned u32x2;
typedef __attribute__((ext_vector_type(32))) float f32x32;
typedef __attribute__((ext_vector_type(16))) float f32x16;
typedef __attribute__((ext_vector_type(16))) unsigned u32x16;
typedef __attribute__((ext_vector_type(12))) unsigned u32x12;
typedef __attribute__((ext_vector_type(8))) unsigned u32x8;
typedef unsigned long long u64;
#define LAS __attribute__((address_space(3)))

constexpr int NB = 256;
constexpr int G1 = 128;
constexpr int CH = 3136;
constexpr int CAP1 = 30;
constexpr int CAP2 = 192;
constexpr int SUBB = 784;
#define POLL_AT 3
#define OUT_AUX 16
#define PF_R 0
#define PF_D 0
#define PF_T 200
#define LPRIO 3
#define PKADD 1
#define CODEPF 1
#define CODE_BYTES 21504
#define PARTNER 1
#define XSYNC 1
#define WVLATE 1
#define NP_SLEEP 0
#define W_SLEEP 3
#define PRIO 1
#define SPLIT 16384
constexpr unsigned KEY_STEP = 0x9E3779B1u;
constexpr unsigned WS_KEY = 0;
constexpr unsigned WS_NOVF = 2048;
constexpr unsigned WS_INBOX = 4096;
constexpr unsigned WS_OVF = WS_INBOX + NB * G1 * 128;
constexpr unsigned WS_BYTES = WS_OVF + G1 * CH * 4;
constexpr unsigned OOB = 0x80000000u;
constexpr unsigned BIAS = 32768 - SPLIT;
constexpr unsigned PAD = 0x00FFFFFFu;

constexpr int L_CNT = 0;
constexpr int L_HDR = 128;
constexpr int L_LIST = 768;
constexpr int L_RING = L_LIST + 16 * SUBB;
constexpr int L_IMG = L_RING;
constexpr int L_CNT1 = L_IMG + NB * 144;
constexpr int L_WQ = L_RING + 2 * 16384 + 8192;
constexpr int L_WV0 = L_RING + 6 * 16384 + 8192;
constexpr int L_WV1 = L_RING + 131072;
constexpr int L_DUMP = L_WV1 + 16384;
constexpr int L_TOTAL = L_DUMP + 2048;
static_assert(L_RING % 256 == 0 && L_TOTAL <= 163840, "LDS map");

#define STAMP_DECL
#define STAMP(k)
#define STAMP_FLUSH(base, k0, k1)

__device__ __forceinline__ u32x4 make_srd(const void* p, unsigned bytes, unsigned back = 0) {
    const u64 a = (u64)p - back;
    u32x4 s; s.x = (unsigned)a; s.y = (unsigned)(a >> 32); s.z = bytes + back; s.w = 0x00020000u; return s;
}
#define LD4(dst, srd, off, FL) asm volatile("buffer_load_dwordx4 %0, %1, %2, 0 offen" FL : "=v"(dst) : "v"(off), "s"(srd) : "memory")
#define LD4S(dst, srd, off, so, FL) asm volatile("buffer_load_dwordx4 %0, %1, %2, %3 offen" FL : "=v"(dst) : "v"(off), "s"(srd), "s"(so) : "memory")
#define LD1(dst, srd, off, FL) asm volatile("buffer_load_dword %0, %1, %2, 0 offen" FL : "=v"(dst) : "v"(off), "s"(srd) : "memory")
#define ST1(val, srd, off, FL) asm volatile("buffer_store_dword %0, %1, %2, 0 offen" FL :: "v"(val), "v"(off), "s"(srd) : "memory")
#define PIN(x) asm volatile("" : "+v"(x))
#define VMWAIT(n) asm volatile("s_waitcnt vmcnt(" #n ")" ::: "memory")
#define FENCE() __builtin_amdgcn_sched_barrier(0)
#define WG_BARRIER() do { asm volatile("s_waitcnt lgkmcnt(0)" ::: "memory"); __builtin_amdgcn_s_barrier(); asm volatile("" ::: "memory"); } while (0)

struct Srd4 { u32x4 s0, s1, s2, s3; };
__device__ __forceinline__ Srd4 make_srd4(const void* p, unsigned bytes, unsigned back = 0) {
    return Srd4{make_srd(p, bytes, back), make_srd(p, bytes, back + 1024), make_srd(p, bytes, back + 2048), make_srd(p, bytes, back + 3072)};
}

__device__ __forceinline__ void set_m0(unsigned v) { asm volatile("s_mov_b32 m0, %0" :: "s"(v) : "memory"); }
template <int R> __device__ __forceinline__ void dma_rows(const Srd4& d, unsigned e, unsigned off) {
    unsigned tmp, mask = 0x00FFFF00u;
    asm volatile("v_and_or_b32 %0, %1, %2, %3\n\tbuffer_load_dwordx4 %0, %4, 0 offen offset:%c5 lds"
                 : "=&v"(tmp) : "v"(e), "s"(mask), "v"(off), "s"(R == 0 ? d.s0 : R == 1 ? d.s1 : R == 2 ? d.s2 : d.s3), "i"(R * 1024) : "memory");
}
__device__ __forceinline__ void dma_half(unsigned ldsbase, const Srd4& d, const unsigned* e, const u32x4& off) {
    set_m0(ldsbase);
    dma_rows<0>(d, e[0], off.x); dma_rows<1>(d, e[1], off.y); dma_rows<2>(d, e[2], off.z); dma_rows<3>(d, e[3], off.w);
}
__device__ __forceinline__ void dma_group(unsigned ldsbase, const Srd4& d, const unsigned (&e)[8], const u32x4& off) {
    asm volatile("s_waitcnt lgkmcnt(0)" ::: "memory");
    dma_half(ldsbase, d, e, off); dma_half(ldsbase + 4096, d, e + 4, off);
}

__device__ __forceinline__ void gather_loop(f32x32& acc, const u32x16& qf, const u32x16& addr, u32x4 misc, f32x16 cc,
                                            const Srd4& d, unsigned ring, int G, int nlast) {
    f32x32 t0, t1; u32x12 t2; int tmp, pr; u64 sm; unsigned mask = 0x00FFFF00u;
    asm volatile(
        ".macro GWAIT k\n\ts_cmp_gt_i32 %[t], \\k\n\ts_cbranch_scc1 1f\n\ts_waitcnt vmcnt(\\k)\n\t.endm\n\t"
        ".macro GROWS so\n\t.irp s, 0, 1, 2, 3\n\t"
        "ds_read_b128 v[64+8*\\s:67+8*\\s], v[176+\\s] offset:\\so\n\t"
        "ds_read_b128 v[68+8*\\s:71+8*\\s], v[176+\\s] offset:\\so+4096\n\t.endr\n\t.endm\n\t"
        ".macro GTR c, so\n\t"
        "ds_read_b64_tr_b16 v[96+4*\\c:97+4*\\c], v[180+\\c] offset:\\so\n\t"
        "ds_read_b64_tr_b16 v[98+4*\\c:99+4*\\c], v[180+\\c] offset:\\so+4096\n\t.endm\n\t"
        ".macro GS s, c\n\t"
        "v_mfma_f32_16x16x32_bf16 v[\\c:\\c+3], v[64+8*\\s:67+8*\\s], v[160+4*\\s:163+4*\\s], v[\\c:\\c+3]\n\t"
        "v_mfma_f32_16x16x32_bf16 v[\\c+4:\\c+7], v[68+8*\\s:71+8*\\s], v[160+4*\\s:163+4*\\s], v[\\c+4:\\c+7]\n\t.endm\n\t"
        ".macro GLOAD m, r, srd\n\t"
        "v_and_or_b32 v195, v[212+\\m], %[mask], v[188+\\r]\n\t"
        "buffer_load_dwordx4 v195, \\srd, 0 offen offset:1024*\\r lds\n\t.endm\n\t"
        ".macro GPART m, r, srd\n\ts_cmp_le_i32 %[nl], \\m\n\ts_cbranch_scc1 4f\n\tGLOAD \\m, \\r, \\srd\n\t.endm\n\t"
        ".macro GSUM m\n\tv_mfma_f32_16x16x32_bf16 v[128+4*(\\m):131+4*(\\m)], v[96+4*(\\m):99+4*(\\m)], v[220:223], v[128+4*(\\m):131+4*(\\m)]\n\t.endm\n\t"
        ".macro GCMP m, d\n\tv_cmp_ne_u32_sdwa \\d, v[212+\\m], v193 src0_sel:BYTE_0 src1_sel:DWORD\n\t.endm\n\t"
        ".macro GPAIR m, c\n\t"
        "GSUM \\m\n\tGCMP \\m+1, %[sm]\n\tv_cndmask_b32_e32 v[\\c+\\m], 0, v194, vcc\n\t"
        "GSUM \\m+1\n\t.if \\m < 6\n\tGCMP \\m+2, vcc\n\t.else\n\ts_cmp_gt_i32 %[rem], 0\n\t.endif\n\t"
        "v_cndmask_b32_e64 v[\\c+\\m+1], 0, v194, %[sm]\n\t.endm\n\t"
        ".macro GSTEP so, c, eo\n\t"
        "GTR 0, \\so\n\t"
        "s_waitcnt lgkmcnt(2)\n\tGS 0, \\c\n\t"
        "GTR 1, \\so\n\tGTR 2, \\so\n\tGS 1, \\c\n\t"
        "GTR 3, \\so\n\tGTR 4, \\so\n\tGS 2, \\c\n\t"
        "GTR 5, \\so\n\tGTR 6, \\so\n\tGS 3, \\c\n\t"
        "GTR 7, \\so\n\t"
        "ds_read2_b32 v[212:213], v192 offset0:\\eo offset1:\\eo+4\n\t"
        "ds_read2_b32 v[214:215], v192 offset0:\\eo+8 offset1:\\eo+12\n\t"
        "ds_read2_b32 v[216:217], v192 offset0:\\eo+16 offset1:\\eo+20\n\t"
        "ds_read2_b32 v[218:219], v192 offset0:\\eo+24 offset1:\\eo+28\n\t"
        "s_add_u32 m0, %[ring], \\so\n\t"
        "s_cmp_gt_i32 %[rem], 3\n\t"
        ".irp k, 0, 1, 2, 3, 4, 5, 6, 7\n\tv_exp_f32_e32 v[\\c+\\k], v[\\c+\\k]\n\t.endr\n\t"
        "s_waitcnt lgkmcnt(0)\n\t"
        "s_cbranch_scc0 2f\n\t"
        "GLOAD 0, 0, %[s0]\n\tGLOAD 1, 1, %[s1]\n\tGLOAD 2, 2, %[s2]\n\tGLOAD 3, 3, %[s3]\n\t"
        "s_add_u32 m0, m0, 0x1000\n\t"
        "GLOAD 4, 0, %[s0]\n\tGLOAD 5, 1, %[s1]\n\tGLOAD 6, 2, %[s2]\n\tGLOAD 7, 3, %[s3]\n\t"
        "GSIG \\c\n\t"
        "s_waitcnt vmcnt(8)\n\t"
        "s_branch 1f\n\t"
        "2:\n\t"
        "s_mov_b32 %[t], 0\n\t"
        "s_cmp_eq_u32 %[rem], 3\n\ts_cbranch_scc0 4f\n\t"
        "s_mov_b32 %[t], %[nl]\n\t"
        "GLOAD 0, 0, %[s0]\n\tGPART 1, 1, %[s1]\n\tGPART 2, 2, %[s2]\n\tGPART 3, 3, %[s3]\n\t"
        "s_add_u32 m0, m0, 0x1000\n\t"
        "GPART 4, 0, %[s0]\n\tGPART 5, 1, %[s1]\n\tGPART 6, 2, %[s2]\n\tGPART 7, 3, %[s3]\n\t"
        "4:\n\t"
        "GSIG \\c\n\t"
        "s_waitcnt vmcnt(8)\n\t"
        "GWAIT 7\n\tGWAIT 6\n\tGWAIT 5\n\tGWAIT 4\n\tGWAIT 3\n\tGWAIT 2\n\tGWAIT 1\n\tGWAIT 0\n\t"
        "1:\n\t"
        "GROWS 8192-\\so\n\t"
        "GCMP 0, vcc\n\t"
        "s_add_i32 %[rem], %[rem], -1\n\t"
        "GPAIR 0, \\c\n\tGPAIR 2, \\c\n\tGPAIR 4, \\c\n\tGPAIR 6, \\c\n\t"
        ".endm\n\t"
        ".macro GSIG c\n\t"
        ".if " STR(PKADD) "\n\t.irp k, 0, 2, 4, 6\n\tv_pk_add_f32 v[\\c+\\k:\\c+\\k+1], v[\\c+\\k:\\c+\\k+1], 1.0 op_sel_hi:[1,0]\n\t.endr\n\t.else\n\t"
        ".irp k, 0, 1, 2, 3, 4, 5, 6, 7\n\tv_add_f32_e32 v[\\c+\\k], 1.0, v[\\c+\\k]\n\t.endr\n\t.endif\n\t"
        ".irp k, 0, 1, 2, 3, 4, 5, 6, 7\n\tv_rcp_f32_e32 v[\\c+\\k], v[\\c+\\k]\n\t.endr\n\t"
        ".irp k, 0, 1, 2, 3\n\tv_cvt_pk_bf16_f32 v[220+\\k], v[\\c+2*\\k], v[\\c+2*\\k+1]\n\t.endr\n\t.endm\n\t"
        "s_waitcnt vmcnt(8)\n\t"
        "s_cmp_gt_i32 %[rem], 1\n\ts_cbranch_scc1 1f\n\ts_waitcnt vmcnt(0)\n\t1:\n\t"
        "GROWS 0\n\t"
        ".if " STR(LPRIO) " >= 3\n\ts_mov_b32 %[pr], 0\n\ts_setprio 2\n\t.endif\n\t"
        "0:\n\t"
        ".if " STR(LPRIO) " == 3\n\t"
        "s_cmp_eq_u32 %[pr], 1\n\ts_cbranch_scc0 5f\n\ts_setprio 1\n\t5:\n\t"
        "s_cmp_eq_u32 %[pr], 2\n\ts_cbranch_scc0 5f\n\ts_setprio 0\n\t5:\n\t"
        "s_add_u32 %[pr], %[pr], 1\n\t.endif\n\t"
        "GSTEP 0, 196, 64\n\t"
        "s_cbranch_scc0 9f\n\t"
        "GSTEP 8192, 204, 96\n\t"
        "v_add_u32_e32 v192, 0x100, v192\n\t"
        "s_cbranch_scc1 0b\n\t"
        "9:\n\t"
        "s_waitcnt lgkmcnt(0)\n\ts_nop 6\n\t"
        ".purgem GWAIT\n\t.purgem GROWS\n\t.purgem GTR\n\t.purgem GS\n\t.purgem GLOAD\n\t.purgem GPART\n\t.purgem GSUM\n\t.purgem GCMP\n\t.purgem GPAIR\n\t.purgem GSTEP\n\t.purgem GSIG"
        : "+{v[128:159]}"(acc), "+{v[192:195]}"(misc), "+{v[196:211]}"(cc), "=&{v[64:95]}"(t0), "=&{v[96:127]}"(t1), "=&{v[212:223]}"(t2),
          [rem] "+s"(G), [t] "=&s"(tmp), [pr] "=&s"(pr), [sm] "=&s"(sm)
        : "{v[160:175]}"(qf), "{v[176:191]}"(addr), [s0] "s"(d.s0), [s1] "s"(d.s1), [s2] "s"(d.s2), [s3] "s"(d.s3),
          [mask] "s"(mask), [ring] "s"(ring), [nl] "s"(nlast)
        : "vcc", "scc", "memory");
}

__device__ __forceinline__ u64 rank_edges(u32x16& ed, u32x16& es, u32x16& rk, u64 m0, u64 m1, u64 m2, u64 m3, unsigned keya) {
    u32x16 b4; u64 over; unsigned sel = 0x01050401u, c36 = 36;
    asm volatile(
        ".macro RADD k\n\tv_lshlrev_b32_sdwa v[96+\\k], 2, v[64+\\k] dst_sel:DWORD dst_unused:UNUSED_PAD src0_sel:DWORD src1_sel:BYTE_0\n\t"
        "ds_add_rtn_u32 v[112+\\k], v[96+\\k], v111 offset:%c[cnt]\n\t.endm\n\t"
        ".macro RENT k\n\tv_add_u32_e32 v[80+\\k], %c[bias], v[80+\\k]\n\tv_perm_b32 v[80+\\k], v[80+\\k], v[64+\\k], %[sel]\n\t.endm\n\t"
        ".macro RPUT k\n\tv_min_u32_e32 v110, %c[cap], v[112+\\k]\n\tv_mad_u32_u24 v110, v[96+\\k], %[c36], v110\n\t"
        "ds_write_b32 v110, v[80+\\k] offset:%c[img]\n\t.endm\n\t"
        "v_mov_b32_e32 v111, 4\n\t"
        ".irp k, 0, 2, 4, 6, 8, 10, 12\n\tv_mov_b64_e32 v[112+\\k:113+\\k], 0\n\t.endr\n\t"
        "s_mov_b64 exec, %[m0]\n\ts_waitcnt vmcnt(9)\n\tRADD 0\n\tRADD 1\n\tRADD 2\n\tRADD 3\n\t"
        "s_mov_b64 exec, %[m1]\n\ts_waitcnt vmcnt(8)\n\tRADD 4\n\tRADD 5\n\tRADD 6\n\tRADD 7\n\t"
        "s_mov_b64 exec, %[m2]\n\ts_waitcnt vmcnt(7)\n\tRADD 8\n\tRADD 9\n\tRADD 10\n\tRADD 11\n\t"
        "s_mov_b64 exec, %[m3]\n\ts_waitcnt vmcnt(6)\n\tRADD 12\n\t"
        "s_mov_b64 exec, %[m0]\n\ts_waitcnt vmcnt(5)\n\tRENT 0\n\tRENT 1\n\tRENT 2\n\tRENT 3\n\t"
        "s_waitcnt lgkmcnt(9)\n\tRPUT 0\n\tRPUT 1\n\tRPUT 2\n\tRPUT 3\n\t"
        "s_mov_b64 exec, %[m1]\n\ts_waitcnt vmcnt(4)\n\tRENT 4\n\tRENT 5\n\tRENT 6\n\tRENT 7\n\t"
        "s_waitcnt lgkmcnt(9)\n\tRPUT 4\n\tRPUT 5\n\tRPUT 6\n\tRPUT 7\n\t"
        "s_mov_b64 exec, %[m2]\n\ts_waitcnt vmcnt(3)\n\tRENT 8\n\tRENT 9\n\tRENT 10\n\tRENT 11\n\t"
        "s_waitcnt lgkmcnt(9)\n\tRPUT 8\n\tRPUT 9\n\tRPUT 10\n\tRPUT 11\n\t"
        "s_mov_b64 exec, %[m3]\n\ts_waitcnt vmcnt(0)\n\tRENT 12\n\t"
        "s_waitcnt lgkmcnt(12)\n\tRPUT 12\n\t"
        "s_mov_b64 exec, -1\n\t"
        "v_add_u32_e32 v93, %c[step], v93\n\t"
        "v_max3_u32 v110, v112, v113, v114\n\tv_max3_u32 v109, v115, v116, v117\n\t"
        "ds_write_b32 %[keya], v93 offset:%c[cnt]+1024\n\t"
        "v_max3_u32 v110, v110, v118, v119\n\tv_max3_u32 v109, v109, v120, v121\n\t"
        "v_max3_u32 v110, v110, v122, v123\n\tv_max3_u32 v110, v110, v109, v124\n\t"
        "v_cmp_le_u32_e32 vcc, %c[cap], v110\n\ts_mov_b64 %[over], vcc\n\t"
        ".purgem RADD\n\t.purgem RENT\n\t.purgem RPUT"
        : "+{v[64:79]}"(ed), "+{v[80:95]}"(es), "=&{v[96:111]}"(b4), "=&{v[112:127]}"(rk), [over] "=&s"(over)
        : [m0] "s"(m0), [m1] "s"(m1), [m2] "s"(m2), [m3] "s"(m3), [keya] "v"(keya), [sel] "s"(sel), [c36] "s"(c36),
          [cnt] "i"(L_CNT1), [img] "i"(L_IMG + 8), [bias] "i"(BIAS), [cap] "i"(4 * CAP1), [step] "i"(KEY_STEP)
        : "vcc", "memory");
    return over;
}
__device__ __forceinline__ unsigned read_image(f32x32& img, u32x16& meta, u32x16& sums, unsigned ima, unsigned cna) {
    unsigned no;
    asm volatile(
        "v_mov_b32_e32 v121, 0\n\tds_read_b32 v120, v121 offset:%c[cnt]+2048\n\t"
        ".irp k, 0, 1, 2, 3, 4, 5, 6, 7\n\tds_read_b128 v[64+4*\\k:67+4*\\k], %[ima] offset:4608*\\k\n\t.endr\n\t"
        ".irp k, 0, 1, 2, 3\n\tds_read2_b32 v[96+2*\\k:97+2*\\k], %[cna] offset0:64*\\k offset1:64*\\k+32\n\t.endr\n\t"
        "s_waitcnt lgkmcnt(11)\n\tv_readfirstlane_b32 %[no], v120\n\t"
        "v_add_u32_e32 v121, 0x400, %[cna]\n\t"
        ".irp k, 0, 1, 2, 3\n\tds_read2_b32 v[104+2*\\k:105+2*\\k], v121 offset0:64*\\k offset1:64*\\k+32\n\t.endr\n\t"
        "s_waitcnt lgkmcnt(12)\n\t"
        ".irp k, 0, 1, 2, 3\n\tv_add3_u32 v[112+\\k], v[64+4*\\k], v[65+4*\\k], v[66+4*\\k]\n\tv_add_u32_e32 v[112+\\k], v[112+\\k], v[67+4*\\k]\n\t.endr\n\t"
        "s_waitcnt lgkmcnt(8)\n\t"
        ".irp k, 4, 5, 6, 7\n\tv_add3_u32 v[112+\\k], v[64+4*\\k], v[65+4*\\k], v[66+4*\\k]\n\tv_add_u32_e32 v[112+\\k], v[112+\\k], v[67+4*\\k]\n\t.endr\n\t"
        ".irp k, 0, 1, 2, 3, 4, 5, 6, 7\n\tv_add_u32_dpp v[112+\\k], v[112+\\k], v[112+\\k] quad_perm:[1,0,3,2] row_mask:0xf bank_mask:0xf\n\t.endr\n\t"
        ".irp k, 0, 1, 2, 3, 4, 5, 6, 7\n\tv_add_u32_dpp v[112+\\k], v[112+\\k], v[112+\\k] quad_perm:[2,3,0,1] row_mask:0xf bank_mask:0xf\n\t.endr\n\t"
        ".irp k, 0, 1, 2, 3, 4, 5, 6, 7\n\tv_add_u32_dpp v[112+\\k], v[112+\\k], v[112+\\k] row_half_mirror row_mask:0xf bank_mask:0xf\n\t.endr\n\t"
        "s_waitcnt lgkmcnt(0)"
        : "=&{v[64:95]}"(img), "=&{v[96:111]}"(meta), "=&{v[112:127]}"(sums), [no] "=&s"(no)
        : [ima] "v"(ima), [cna] "v"(cna), [cnt] "i"(L_CNT1) : "memory");
    return no;
}
__device__ __forceinline__ void publish_image(f32x32 img, u32x16 meta, u32x16 sums, const u32x4& srd, unsigned ob) {
    u64 pc0 = 0x0101010101010101ull; unsigned so;
    asm volatile(
        ".macro PUB k\n\ts_mov_b64 exec, %[pc0]\n\tv_mov_b32_e32 v[64+4*\\k], v[96+\\k]\n\tv_mov_b32_e32 v[65+4*\\k], v[112+\\k]\n\t"
        "s_mov_b64 exec, -1\n\tv_xor_b32_e32 v[64+4*\\k], v[64+4*\\k], v[104+\\k]\n\t"
        "buffer_store_dwordx4 v[64+4*\\k:67+4*\\k], %[ob], %[srd], %[so] offen sc1\n\ts_add_u32 %[so], %[so], %c[step]\n\t.endm\n\t"
        "s_mov_b32 %[so], 0\n\t"
        ".irp k, 0, 1, 2, 3, 4, 5, 6, 7\n\tPUB \\k\n\t.endr\n\t.purgem PUB"
        : "+{v[64:95]}"(img), [so] "=&s"(so)
        : "{v[96:111]}"(meta), "{v[112:127]}"(sums), [srd] "s"(srd), [ob] "v"(ob), [pc0] "s"(pc0), [step] "i"(32 * G1 * 128) : "scc", "memory");
}

__device__ __forceinline__ void inbox_request(u32x16& raw, const u32x4& srd, unsigned ib) {
    unsigned o1 = 0x1000, o2 = 0x2000, o3 = 0x3000;
    asm volatile("buffer_load_dwordx4 v[64:67], %1, %2, 0 offen sc1\n\tbuffer_load_dwordx4 v[68:71], %1, %2, %3 offen sc1\n\t"
                 "buffer_load_dwordx4 v[72:75], %1, %2, %4 offen sc1\n\tbuffer_load_dwordx4 v[76:79], %1, %2, %5 offen sc1"
                 : "=&{v[64:79]}"(raw) : "v"(ib), "s"(srd), "s"(o1), "s"(o2), "s"(o3) : "memory");
}
__device__ __forceinline__ unsigned inbox_stage(u32x16 raw, const u32x4& srd, unsigned ib, unsigned key, int i4, unsigned hdra) {
    u32x16 t0; u32x4 t1; u64 m0, m1, m2, m3; unsigned todo, fresh, spin, o1, o2, o3, over;
    u64 pc0 = 0x0101010101010101ull;
    asm volatile(
        ".macro IVAL j, so\n\t"
        "s_bitcmp1_b32 %[todo], \\j\n\ts_cbranch_scc0 2f\n\t"
        "v_xor_b32_e32 v[64+4*\\j], %[key], v[64+4*\\j]\n\t"
        "v_sub_u32_e32 v97, 0, v[65+4*\\j]\n\t"
        "v_add_u32_e32 v96, v[64+4*\\j], v[65+4*\\j]\n\t"
        "v_mov_b32_dpp v[80+\\j], v[64+4*\\j] quad_perm:[0,0,0,0] row_mask:0xf bank_mask:0xf\n\t"
        "v_cndmask_b32_e64 v96, v96, v97, %[pc0]\n\t"
        "s_nop 0\n\t"
        "v_mov_b32_dpp v[80+\\j], v[80+\\j] row_shr:4 row_mask:0xf bank_mask:0xa\n\t"
        "v_cmp_gt_i32_e32 vcc, v[80+\\j], v92\n\t"
        "v_add3_u32 v96, v96, v[66+4*\\j], v[67+4*\\j]\n\t"
        "v_lshrrev_b32_e32 v97, 14, v[80+\\j]\n\t"
        "v_cndmask_b32_e32 v96, 0, v96, vcc\n\t"
        "s_nop 1\n\tv_add_u32_dpp v96, v96, v96 quad_perm:[1,0,3,2] row_mask:0xf bank_mask:0xf\n\t"
        "s_nop 1\n\tv_add_u32_dpp v96, v96, v96 quad_perm:[2,3,0,1] row_mask:0xf bank_mask:0xf\n\t"
        "s_nop 1\n\tv_add_u32_dpp v96, v96, v96 row_half_mirror row_mask:0xf bank_mask:0xf\n\t"
        "v_or_b32_e32 v96, v96, v97\n\t"
        "v_cmp_ne_u32_e32 vcc, 0, v96\n\t"
        "s_cbranch_vccz 1f\n\t"
        "buffer_load_dwordx4 v[64+4*\\j:67+4*\\j], %[ib], %[srd], \\so offen sc1\n\t"
        "s_branch 2f\n\t"
        "1:\n\ts_bitset1_b32 %[fresh], \\j\n\t"
        "2:\n\t.endm\n\t"
        ".macro IRANK j\n\t"
        "s_bitcmp1_b32 %[fresh], \\j\n\ts_cbranch_scc0 2f\n\t"
        "v_cmp_lt_u32_e64 %[m0], v92, v[80+\\j]\n\tv_cmp_lt_u32_e64 %[m1], v93, v[80+\\j]\n\t"
        "v_cmp_lt_u32_e64 %[m2], v94, v[80+\\j]\n\tv_cmp_lt_u32_e64 %[m3], v95, v[80+\\j]\n\t"
        ".irp k, 0, 1, 2, 3\n\tv_lshrrev_b32_e32 v[84+\\k], 21, v[64+4*\\j+\\k]\n\t.endr\n\t"
        ".irp k, 0, 1, 2, 3\n\tv_and_b32_e32 v[84+\\k], 60, v[84+\\k]\n\t.endr\n\t"
        "v_cmp_lt_u32_e32 vcc, %c[cap], v[80+\\j]\n\t"
        "s_mov_b64 exec, %[m0]\n\tds_add_rtn_u32 v88, v84, v98 offset:%c[cnt]\n\t"
        "s_mov_b64 exec, %[m1]\n\tds_add_rtn_u32 v89, v85, v98 offset:%c[cnt]\n\t"
        "s_mov_b64 exec, %[m2]\n\tds_add_rtn_u32 v90, v86, v98 offset:%c[cnt]\n\t"
        "s_mov_b64 exec, %[m3]\n\tds_add_rtn_u32 v91, v87, v98 offset:%c[cnt]\n\t"
        "s_mov_b64 exec, %[pc0]\n\tds_write_b32 %[hdra], v[80+\\j] offset:128*\\j\n\t"
        "s_mov_b64 exec, -1\n\t"
        "s_cbranch_vccz 1f\n\t"
        "s_mov_b32 %[over], 1\n\t"
        "1:\n\ts_waitcnt lgkmcnt(1)\n\t"
        ".irp k, 0, 1, 2, 3\n\tv_min_u32_e32 v[88+\\k], %c[sub]-4, v[88+\\k]\n\t.endr\n\t"
        ".irp k, 0, 1, 2, 3\n\tv_mad_u32_u24 v[88+\\k], v[84+\\k], %[sub4], v[88+\\k]\n\t.endr\n\t"
        "s_mov_b64 exec, %[m0]\n\tds_write_b32 v88, v[64+4*\\j] offset:%c[list]\n\t"
        "s_mov_b64 exec, %[m1]\n\tds_write_b32 v89, v[65+4*\\j] offset:%c[list]\n\t"
        "s_mov_b64 exec, %[m2]\n\tds_write_b32 v90, v[66+4*\\j] offset:%c[list]\n\t"
        "s_mov_b64 exec, %[m3]\n\tds_write_b32 v91, v[67+4*\\j] offset:%c[list]\n\t"
        "s_mov_b64 exec, -1\n\t"
        "2:\n\t.endm\n\t"
        "s_movk_i32 %[o1], 0x1000\n\ts_movk_i32 %[o2], 0x2000\n\ts_movk_i32 %[o3], 0x3000\n\t"
        "v_mov_b32_e32 v92, %[i4]\n\tv_add_u32_e32 v93, 4, %[i4]\n\tv_add_u32_e32 v94, 8, %[i4]\n\tv_add_u32_e32 v95, 12, %[i4]\n\t"
        "v_mov_b32_e32 v98, 4\n\t"
        "s_mov_b32 %[todo], 15\n\ts_mov_b32 %[spin], 0x400000\n\ts_mov_b32 %[over], 0\n\t"
        "0:\n\t"
        "s_mov_b32 %[fresh], 0\n\t"
        "s_waitcnt vmcnt(3)\n\ts_cmp_eq_u32 %[todo], 15\n\ts_cbranch_scc1 3f\n\ts_waitcnt vmcnt(0)\n\t3:\n\t"
        "IVAL 0, 0\n\tIRANK 0\n\t"
        "s_waitcnt vmcnt(2)\n\tIVAL 1, %[o1]\n\tIRANK 1\n\t"
        "s_waitcnt vmcnt(1)\n\tIVAL 2, %[o2]\n\tIRANK 2\n\t"
        "s_cmp_eq_u32 %[todo], 15\n\ts_cbranch_scc0 3f\n\ts_waitcnt vmcnt(0)\n\t3:\n\tIVAL 3, %[o3]\n\tIRANK 3\n\t"
        "s_andn2_b32 %[todo], %[todo], %[fresh]\n\t"
        "s_cbranch_scc0 9f\n\t"
        "s_sub_u32 %[spin], %[spin], 1\n\t"
        "s_cbranch_scc0 0b\n\t"
        "s_waitcnt vmcnt(0)\n\t"
        "9:\n\t"
        ".purgem IVAL\n\t.purgem IRANK"
        : "+{v[64:79]}"(raw), "=&{v[80:95]}"(t0), "=&{v[96:99]}"(t1), [m0] "=&s"(m0), [m1] "=&s"(m1), [m2] "=&s"(m2), [m3] "=&s"(m3),
          [todo] "=&s"(todo), [fresh] "=&s"(fresh), [spin] "=&s"(spin), [o1] "=&s"(o1), [o2] "=&s"(o2), [o3] "=&s"(o3), [over] "=&s"(over)
        : [srd] "s"(srd), [ib] "v"(ib), [key] "s"(key), [i4] "v"(i4), [hdra] "v"(hdra), [pc0] "s"(pc0), [sub4] "s"(SUBB / 4),
          [cnt] "i"(L_CNT), [list] "i"(L_LIST), [sub] "i"(SUBB), [cap] "i"(4 * CAP1)
        : "vcc", "scc", "memory");
    return over;
}

template <int CTRL, int BANK> __device__ __forceinline__ unsigned dpp(unsigned old, unsigned v) { return __builtin_amdgcn_update_dpp(old, v, CTRL, 0xF, BANK, true); }
__device__ __forceinline__ unsigned bcast8(unsigned v) { v = dpp<0x00, 0xF>(0u, v); return __builtin_amdgcn_update_dpp(v, v, 0x114, 0xF, 0xA, false); }
__device__ __forceinline__ unsigned sum8(unsigned v) { v += dpp<0xB1, 0xF>(0u, v); v += dpp<0x4E, 0xF>(0u, v); return v + dpp<0x141, 0xF>(0u, v); }
__device__ __forceinline__ bf16x4 tr_read(LAS char* p) { return __builtin_amdgcn_ds_read_tr16_b64_v4bf16((LAS bf16x4*)p); }
__device__ __forceinline__ bf16x8 cat(bf16x4 a, bf16x4 b) { return __builtin_shufflevector(a, b, 0, 1, 2, 3, 4, 5, 6, 7); }
__device__ __forceinline__ unsigned pk2(float a, float b) { f32x2 v = {a, b}; bf16x2 r = __builtin_convertvector(v, bf16x2); return __builtin_bit_cast(unsigned, r); }
__device__ __forceinline__ f32x4 mfma(bf16x8 a, bf16x8 b, f32x4 c) { return __builtin_amdgcn_mfma_f32_16x16x32_bf16(a, b, c, 0, 0, 0); }
__device__ __forceinline__ unsigned lds_add(LAS char* p, unsigned v) { return __hip_atomic_fetch_add((LAS unsigned*)p, v, __ATOMIC_RELAXED, __HIP_MEMORY_SCOPE_WORKGROUP); }
__device__ __forceinline__ LAS unsigned* ldsp(unsigned a) { return (LAS unsigned*)(size_t)a; }
__device__ __forceinline__ unsigned byte0_x4(unsigned v) { unsigned r; asm("v_lshlrev_b32_sdwa %0, 2, %1 dst_sel:DWORD dst_unused:UNUSED_PAD src0_sel:DWORD src1_sel:BYTE_0" : "=v"(r) : "v"(v)); return r; }
__device__ __forceinline__ unsigned mad24(unsigned a, unsigned b, unsigned c) { unsigned r; asm("v_mad_u32_u24 %0, %1, %2, %3" : "=v"(r) : "v"(a), "s"(b), "v"(c)); return r; }
__device__ __forceinline__ void lds_arrive(LAS char* p, int lane) {
    asm volatile("s_waitcnt lgkmcnt(0)" ::: "memory");
    if (lane == 0) __hip_atomic_fetch_add((LAS unsigned*)p, 1u, __ATOMIC_RELAXED, __HIP_MEMORY_SCOPE_WORKGROUP);
}
__device__ __forceinline__ void lds_await(LAS char* p, unsigned n) {
    while (__builtin_amdgcn_readfirstlane(*(volatile LAS unsigned*)p) < n) __builtin_amdgcn_s_sleep(1);
    asm volatile("" ::: "memory");
}
__device__ __forceinline__ unsigned rfl(unsigned v) { return __builtin_amdgcn_readfirstlane(v); }

__global__ __launch_bounds__(512, 2) void k_graph(const unsigned short* x_, const int* src_, const int* dst_, const unsigned short* wq_, const unsigned short* wv_,
                                                  unsigned* ws_, unsigned short* out_, int N_, int E_) {
    u32x4 srd_d, srd_s, srd_ws, srd_x0, pw, po;
    u64 t_start;
    asm volatile("s_load_dwordx2 s[40:41], %[ka], 0x10\n\ts_load_dwordx2 s[44:45], %[ka], 0x8\n\ts_load_dwordx4 s[60:63], %[ka], 0x30\n\t"
                 "s_load_dwordx2 s[48:49], %[ka], 0x28\n\ts_load_dwordx2 s[52:53], %[ka], 0x0\n\ts_load_dwordx4 s[56:59], %[ka], 0x18\n\t"
                 "s_memrealtime s[64:65]\n\t"
                 "s_mov_b32 s43, 0x20000\n\ts_mov_b32 s47, 0x20000\n\ts_mov_b32 s51, 0x20000\n\ts_mov_b32 s55, 0x20000\n\ts_mov_b32 s50, %c[wsb]"
                 : "=&{s[40:43]}"(srd_d), "=&{s[44:47]}"(srd_s), "=&{s[48:51]}"(srd_ws), "=&{s[52:55]}"(srd_x0), "=&{s[56:59]}"(pw), "=&{s[60:63]}"(po), "=&{s[64:65]}"(t_start)
                 : [ka] "s"(__builtin_amdgcn_kernarg_segment_ptr()), [wsb] "i"(WS_BYTES));
    FENCE();
    __shared__ __attribute__((aligned(16))) char smem_[L_TOTAL];
    LAS char* smem = (LAS char*)smem_;
    const int t = threadIdx.x, l = t & 63, b = blockIdx.x;
    const int w = rfl(t >> 6);
    const int g = l >> 4, n = l & 15, q = (l >> 2) & 3, p = l & 3, pc = t & 7;
    const unsigned lds0 = (unsigned)(size_t)smem;
    const bool isprod = ((b >> 3) & 1) == 0;
    const int pg = ((b & 7) << 4) | (b >> 4);
    const unsigned e0 = pg * CH + 4 * t, e1 = pg * CH + 3072 + t;

    u32x16 ed, es;
    {
        const unsigned o = e0 * 4, o1 = w == 0 ? e1 * 4 : OOB, ok = WS_KEY + t * 4, om = WS_KEY + b * 4;
        const unsigned oc = w < 4 ? (t < 196 ? ((b - 8) << 8) + ((t >> 1) << 16) + ((t & 1) << 7) : OOB) : (t - 256) * 128, dump = lds0 + L_DUMP + w * 256;
        int role = (w < 4 ? 0 : 2) + (isprod ? 0 : 1);
        unsigned k4 = 4096, k8 = 8192;
        asm volatile("s_cmp_eq_u32 %[role], 0\n\ts_cbranch_scc1 1f\n\t"
                     "s_mov_b32 m0, %[dump]\n\ts_cmp_eq_u32 %[role], 1\n\ts_cbranch_scc1 2f\n\t"
                     "s_getpc_b64 s[68:69]\n\ts_mov_b32 s70, " STR(CODE_BYTES) "\n\ts_mov_b32 s71, 0x20000\n\t"
                     ".if " STR(CODEPF) "\n\tbuffer_load_dword %[oc], s[68:71], 0 offen lds\n\t.endif\n\t"
                     "s_waitcnt lgkmcnt(0)\n\ts_branch 3f\n\t"
                     "2:\n\ts_waitcnt lgkmcnt(0)\n\ts_lshl_b32 s54, s62, 8\n\t"
                     ".if " STR(PARTNER) "\n\tbuffer_load_dword %[oc], s[52:55], 0 offen lds\n\t.endif\n\ts_branch 3f\n\t"
                     "1:\n\ts_waitcnt lgkmcnt(0)\n\ts_lshl_b32 s42, s63, 2\n\ts_lshl_b32 s46, s63, 2\n\t"
                     "buffer_load_dwordx4 v[64:67], %[o], s[40:43], 0 offen\n\tbuffer_load_dwordx4 v[68:71], %[o], s[40:43], %[k4] offen\n\t"
                     "buffer_load_dwordx4 v[72:75], %[o], s[40:43], %[k8] offen\n\tbuffer_load_dword v76, %[o1], s[40:43], 0 offen\n\t"
                     "buffer_load_dwordx4 v[80:83], %[o], s[44:47], 0 offen\n\tbuffer_load_dwordx4 v[84:87], %[o], s[44:47], %[k4] offen\n\t"
                     "buffer_load_dwordx4 v[88:91], %[o], s[44:47], %[k8] offen\n\tbuffer_load_dword v92, %[o1], s[44:47], 0 offen\n\t"
                     "buffer_load_dword v93, %[ok], s[48:51], 0 offen sc1\n\tbuffer_load_dword v94, %[om], s[48:51], 0 offen sc1\n\t"
                     "3:\n\ts_lshl_b32 s54, s62, 8"
                     : "=&{v[64:79]}"(ed), "=&{v[80:95]}"(es)
                     : [o] "v"(o), [o1] "v"(o1), [ok] "v"(ok), [om] "v"(om), [oc] "v"(oc), [role] "s"(role), [dump] "s"(dump), [k4] "s"(k4), [k8] "s"(k8),
                       "{s[40:43]}"(srd_d), "{s[44:47]}"(srd_s), "{s[48:51]}"(srd_ws), "{s[52:55]}"(srd_x0), "{s[56:59]}"(pw), "{s[60:63]}"(po), "{s[64:65]}"(t_start)
                     : "memory", "scc", "s68", "s69", "s70", "s71");
        asm volatile("" : "+{s[48:51]}"(srd_ws), "+{s[52:55]}"(srd_x0), "+{s[56:59]}"(pw), "+{s[60:63]}"(po), "+{s[64:65]}"(t_start));
    }
    const void* const x = (const void*)((u64)srd_x0.x | ((u64)srd_x0.y << 32));
    const void* const wq = (const void*)((u64)pw.x | ((u64)pw.y << 32));
    const void* const wv = (const void*)((u64)pw.z | ((u64)pw.w << 32));
    void* const out = (void*)((u64)po.x | ((u64)po.y << 32));
    const int N = po.z, E = po.w;
    const unsigned xbytes = srd_x0.z;
    STAMP_DECL;
    const unsigned ring = lds0 + L_RING + w * 16384;
    const int jn = 8 * n + w;

    unsigned mykey = 0;
    auto node_row = [&](int tile, int m) { const int nd = b + 256 * (8 * (4 * m + g) + tile); return nd < N ? (unsigned)nd << 8 : OOB; };
    auto zero_staging = [&]() {
        const u32x4 z = {0u, 0u, 0u, 0u};
#pragma unroll
        for (int k = 0; k < 5; ++k) *(LAS u32x4*)(smem + L_IMG + k * 8192 + t * 16) = z;
    };

    u32x16 addr;
    f32x32 acc, wva, wvb;
    unsigned cv, lista, cnta, qa, keyn, inf;
    Srd4 srd_x;
    auto loop_constants = [&]() {
        srd_x = make_srd4(x, xbytes, BIAS * 256);
        lista = lds0 + L_LIST + w * (2 * SUBB) + g * 4; cnta = lds0 + L_CNT + (l & 31) * 4; qa = ring + l * 16; keyn = jn; inf = 0x7F800000u;
        asm volatile("" : "+s"(srd_x.s0), "+s"(srd_x.s1), "+s"(srd_x.s2), "+s"(srd_x.s3));
        asm volatile("" : "+v"(lista), "+v"(cnta), "+v"(qa), "+v"(keyn), "+v"(inf));
        const unsigned o0 = (n ^ (((g & 1) << 3) | (g >> 1))) << 4;
        const unsigned ra = (4 * (n & 3) + (n >> 2)) * 256 + (((4 * g) ^ (((n & 7) << 1) | (n >> 3))) << 4);
        const int i = 4 * g + q, r8 = i & 7;
        const unsigned ta = (4 * q + g) * 256 + 8 * (p & 1) + (((p >> 1) ^ (((i & 7) << 1) | (i >> 3))) << 4);
        cv = i * 256 + (((p ^ ((r8 & 1) | ((r8 & 6) << 1))) << 4) | ((p & 1) << 3));
#pragma unroll
        for (int s = 0; s < 4; ++s) { addr[s] = ring + (ra ^ (s << 4)); addr[12 + s] = o0 ^ (s << 5); }
#pragma unroll
        for (int c = 0; c < 8; ++c) addr[4 + c] = ring + (ta ^ (c << 5));
        asm volatile("" : "+{v[176:191]}"(addr), "+v"(cv));
    };
    auto clear_sums = [&]() { acc = 0.f; asm volatile("" : "+{v[128:159]}"(acc)); };
    auto wv_resident = [&]() {
#pragma unroll
        for (int a = 0; a < 8; ++a) {
            LAS char* pa = smem + L_WV0 + (cv ^ ((a >> 1) << 6) ^ ((a & 1) << 3));
            const f32x4 f0 = __builtin_bit_cast(f32x4, cat(tr_read(pa), tr_read(pa + 4096)));
            const f32x4 f1 = __builtin_bit_cast(f32x4, cat(tr_read(pa + 16384), tr_read(pa + 16384 + 4096)));
#pragma unroll
            for (int k = 0; k < 4; ++k) { wva[4 * a + k] = f0[k]; wvb[4 * a + k] = f1[k]; }
        }
        asm volatile("" : "+{v[224:255]}"(wva), "+{v[32:63]}"(wvb));
    };

    if (isprod) zero_staging();
    if (t < 8) *(LAS u32x4*)(smem + L_CNT + t * 16) = u32x4{0u, 0u, 0u, 0u};
    WG_BARRIER();

    if (__builtin_expect(w < 4, 1)) {
        if (__builtin_expect(isprod, 1)) {
            if (PRIO) __builtin_amdgcn_s_setprio(2);
            unsigned ob = WS_INBOX + ((t >> 3) * G1 + pg) * 128 + pc * 16, ima = lds0 + L_IMG + (t >> 3) * 144 + pc * 16;
            PIN(ob); PIN(ima);
            unsigned cna = lds0 + L_CNT1 + (t >> 3) * 4;
            PIN(cna);
            const bool v0 = e0 < (unsigned)E, v1 = e0 + 1024 < (unsigned)E, v2 = e0 + 2048 < (unsigned)E, v3 = w == 0 && e1 < (unsigned)E;
            STAMP(1);
            u32x16 rk;
            const u64 over = rank_edges(ed, es, rk, __builtin_amdgcn_ballot_w64(v0), __builtin_amdgcn_ballot_w64(v1), __builtin_amdgcn_ballot_w64(v2),
                                        __builtin_amdgcn_ballot_w64(v3), t * 4);
            mykey = es[14];
            if (__builtin_expect(over != 0, 0)) {
#pragma unroll
                for (int k = 0; k < 13; ++k)
                    if (rk[k] >= 4u * CAP1) {
                        const unsigned o = WS_OVF + (pg * CH + lds_add(smem + L_CNT1 + 2048, 1)) * 4;
                        const unsigned v = ((((es[k] >> 8) & 0xFFFF) - BIAS) << 16) | ed[k];
                        ST1(v, srd_ws, o, " sc1");
                    }
            }
            if (XSYNC) { lds_arrive(smem + L_CNT + 96, l); lds_await(smem + L_CNT + 96, 4); } else WG_BARRIER();
            STAMP(3);
            f32x32 img; u32x16 meta, sums;
            const unsigned no = read_image(img, meta, sums, ima, cna);
            lds_arrive(smem + L_CNT + 84, l);
            if (__builtin_expect(no != 0, 0)) {
                const unsigned o = t == 0 ? WS_NOVF + pg * 4 : OOB;
                ST1(no, srd_ws, o, " sc1");
                VMWAIT(0);
                if (XSYNC) { lds_arrive(smem + L_CNT + 100, l); lds_await(smem + L_CNT + 100, 4); } else WG_BARRIER();
            }
            publish_image(img, meta, sums, srd_ws, ob);
            STAMP(4);
        } else {
            { const unsigned om = WS_KEY + b * 4; asm volatile("buffer_load_dword %0, %1, %2, 0 offen sc1\n\ts_waitcnt vmcnt(0)" : "=&v"(mykey) : "v"(om), "s"(srd_ws) : "memory"); }
            if (NP_SLEEP) __builtin_amdgcn_s_sleep(NP_SLEEP);
            const unsigned o = ((b >> 4) * 256 + t) * 128;
            set_m0(lds0 + L_DUMP + w * 256);
            STAMP(1);
            for (int r = 0; r < PF_R; ++r) {
                asm volatile("s_waitcnt vmcnt(%c0)" :: "i"(PF_D) : "memory");
                if (__builtin_amdgcn_s_memrealtime() > t_start + PF_T) break;
                const unsigned so = r << 19;
                asm volatile("buffer_load_dword %1, %0, %2 offen lds" :: "s"(srd_x0), "v"(o), "s"(so) : "memory");
            }
            if (PRIO) __builtin_amdgcn_s_setprio(2);
            STAMP(2); STAMP(3); STAMP(4);
        }
        STAMP(5);
        mykey = rfl(mykey) + KEY_STEP;
        loop_constants();
        clear_sums();
        unsigned ib = WS_INBOX + b * (G1 * 128) + t * 16;
        PIN(ib);
        while (__builtin_amdgcn_s_memrealtime() < t_start + POLL_T) __builtin_amdgcn_s_sleep(1);
        STAMP(6);
        u32x16 raw;
        inbox_request(raw, srd_ws, ib);
        if (__builtin_expect(inbox_stage(raw, srd_ws, ib, mykey, 16 * pc - 8, lds0 + L_HDR + (t >> 3) * 4) != 0, 0)) {
            LAS unsigned* hp = (LAS unsigned*)(smem + L_HDR) + 32 * (l >> 3 & 3) + 8 * w + (l & 7);
            const unsigned hv = *hp;
            for (u64 m = __builtin_amdgcn_ballot_w64(l < 32 && hv > 4u * CAP1); m; m &= m - 1) {
                const int k = __builtin_ctzll(m), gp = 32 * (k >> 3) + 8 * w + (k & 7);
                unsigned u, no; const unsigned o = WS_OVF + (gp * CH + l) * 4, on = WS_NOVF + gp * 4;
                asm volatile("buffer_load_dword %0, %2, %4, 0 offen sc1\n\tbuffer_load_dword %1, %3, %4, 0 offen sc1\n\ts_waitcnt vmcnt(0)"
                             : "=&v"(u), "=&v"(no) : "v"(o), "v"(on), "s"(srd_ws) : "memory");
                if (rfl(no) <= 64) {
                    if (l < no && (u & 0xFF) == (unsigned)b) {
                        const unsigned e = __builtin_amdgcn_perm((u >> 16) + BIAS, u, 0x01050401u), key = (e >> 21) & 60;
                        *(LAS unsigned*)(smem + L_LIST + key * (SUBB / 4) + min(lds_add(smem + L_CNT + key, 4), SUBB - 4u)) = e;
                    }
                    if (l == k) *hp = hv | 0x80000000u;
                } else *(LAS unsigned*)(smem + L_CNT + 64) = 1;
            }
        }
        if (PRIO) __builtin_amdgcn_s_setprio(0);
        STAMP(7);
    } else {
        const int j = w - 4;
        STAMP(1);
        u32x4 wr[8], xr[8];
        {
            const u32x4 srd_q = make_srd(wq, 32768);
            const unsigned o = j * 8192 + l * 16, k4 = 4096;
            LD4(wr[0], srd_q, o, ""); LD4(wr[1], srd_q, o, " offset:1024"); LD4(wr[2], srd_q, o, " offset:2048"); LD4(wr[3], srd_q, o, " offset:3072");
            LD4S(wr[4], srd_q, o, k4, ""); LD4S(wr[5], srd_q, o, k4, " offset:1024"); LD4S(wr[6], srd_q, o, k4, " offset:2048"); LD4S(wr[7], srd_q, o, k4, " offset:3072");
#pragma unroll
            for (int m = 0; m < 8; ++m) { const unsigned ox = node_row(m < 4 ? j : w, m & 3) + n * 16; LD4(xr[m], srd_x0, ox, ""); }
        }
        unsigned aqa[8], xfa[4];
        unsigned xa = ring + g * 256 + ((n ^ g) << 4), wa = lds0 + L_WQ + j * 16384 + g * 256 + ((n ^ g) << 4) + ((n & 4) << 1);
        {
            const unsigned aq = lds0 + L_WQ + (8 * g + q) * 256 + ((((4 * p) ^ (q | ((g & 1) << 2))) << 4) | ((p & 1) << 3));
#pragma unroll
            for (int s = 0; s < 4; ++s) { xfa[s] = ring + n * 256 + (((4 * s + g) ^ n) << 4); PIN(xfa[s]); }
#pragma unroll
            for (int c = 0; c < 8; ++c) { aqa[c] = aq ^ ((c >> 1) << 4) ^ ((c & 1) << 3); PIN(aqa[c]); }
            PIN(xa); PIN(wa);
        }
        loop_constants();
        STAMP(2);
        if (isprod && !XSYNC) {
            WG_BARRIER();
            if (__builtin_expect(rfl(*(LAS unsigned*)(smem + L_CNT1 + 2048)) != 0, 0)) WG_BARRIER();
            if (W_SLEEP) __builtin_amdgcn_s_sleep(W_SLEEP);
        }
        STAMP(3);
        asm volatile("s_waitcnt vmcnt(%c8)" : "+v"(wr[0]), "+v"(wr[1]), "+v"(wr[2]), "+v"(wr[3]), "+v"(wr[4]), "+v"(wr[5]), "+v"(wr[6]), "+v"(wr[7]) : "i"(8) : "memory");
#pragma unroll
        for (int m = 0; m < 8; ++m) {
            const unsigned a = (wa ^ ((m & 2) << 5)) + m * 1024;
            *(LAS u32x2*)(size_t)a = u32x2{wr[m].x, wr[m].y};
            *(LAS u32x2*)(size_t)(a ^ 8) = u32x2{wr[m].z, wr[m].w};
        }
        asm volatile("s_waitcnt vmcnt(0)" : "+v"(xr[0]), "+v"(xr[1]), "+v"(xr[2]), "+v"(xr[3]), "+v"(xr[4]), "+v"(xr[5]), "+v"(xr[6]), "+v"(xr[7]) :: "memory");
#pragma unroll
        for (int m = 0; m < 8; ++m) *(LAS u32x4*)(size_t)((xa ^ ((m & 3) << 6)) + m * 1024) = xr[m];
        STAMP(4);
        lds_arrive(smem + L_CNT + 80, l);
        lds_await(smem + L_CNT + 80, 4);
        STAMP(5);
        bf16x8 xo[2][4], fr[2][8];
        f32x4 qc[2][8];
        auto wq_frags = [&](int u, bf16x8 (&f)[8]) {
#pragma unroll
            for (int a = 0; a < 8; ++a) {
                LAS char* pa = (LAS char*)(size_t)aqa[a] + u * 16384;
                f[a] = cat(tr_read(pa), tr_read(pa + 1024));
            }
        };
#pragma unroll
        for (int u = 0; u < 4; ++u) { xo[0][u] = *(LAS bf16x8*)(size_t)xfa[u]; xo[1][u] = *(LAS bf16x8*)(size_t)(xfa[u] + 4096); }
        wq_frags(0, fr[0]);
        {
            const Srd4 srd_v = make_srd4(wv, 32768);
            const unsigned r0 = (j * 32 + g) << 8, o0 = (n ^ ((g & 1) | ((g & 2) << 1))) << 4;
            const unsigned e[8] = {r0, r0 + 1024, r0 + 2048, r0 + 3072, r0 + 4096, r0 + 5120, r0 + 6144, r0 + 7168};
            const unsigned base = lds0 + (j < 2 ? L_WV0 + j * 16384 : L_WV1 + (j - 2) * 8192);
            const u32x4 off = {o0, o0 ^ 128, o0, o0 ^ 128};
            dma_half(base, srd_v, e, off); dma_half(base + 4096, srd_v, e + 4, off);
        }
#pragma unroll
        for (int u = 0; u < 4; ++u) {
            if (u < 3) wq_frags(u + 1, fr[(u + 1) & 1]);
            FENCE();
#pragma unroll
            for (int a = 0; a < 8; ++a)
#pragma unroll
                for (int tt = 0; tt < 2; ++tt) qc[tt][a] = mfma(fr[u & 1][a], xo[tt][u], u ? qc[tt][a] : f32x4{0.f, 0.f, 0.f, 0.f});
            FENCE();
            if (!WVLATE && u == 1 && j < 2) { VMWAIT(0); lds_arrive(smem + L_CNT + 88, l); }
        }
        STAMP(6);
        if (isprod) lds_await(smem + L_CNT + 84, 4);
#pragma unroll
        for (int tt = 0; tt < 2; ++tt)
#pragma unroll
            for (int s = 0; s < 4; ++s) {
                const float k = -0.08838834764831845f * 1.4426950408889634f;
                const f32x4 c0 = qc[tt][2 * s] * k, c1 = qc[tt][2 * s + 1] * k;
                *(LAS u32x4*)(size_t)(ring - (tt ? 0 : 65536) + s * 1024 + l * 16) = u32x4{pk2(c0[0], c0[1]), pk2(c0[2], c0[3]), pk2(c1[0], c1[1]), pk2(c1[2], c1[3])};
            }
        clear_sums();
        if (!WVLATE) { lds_await(smem + L_CNT + 88, 2); wv_resident(); }
        VMWAIT(0);
        STAMP(7);
    }
    WG_BARRIER();
    STAMP(8);
    if (LPRIO >= 3) __builtin_amdgcn_s_setprio(3);
    if (LPRIO == 1 && w >= 4) __builtin_amdgcn_s_setprio(1);
    if (LPRIO == 2) { if (w >= 6) __builtin_amdgcn_s_setprio(3); else if (w >= 4) __builtin_amdgcn_s_setprio(2); else if (w >= 2) __builtin_amdgcn_s_setprio(1); }

    u32x16 qf;
    const u32x4 doff = {addr[12], addr[13], addr[14], addr[15]};
    LAS char* mylist = smem + L_LIST + w * (2 * SUBB);
    int gcur = G1, pos = 0;
    unsigned hmask = 0xFFFFFFFFu;
    auto batch = [&](auto first_) {
        constexpr bool first = decltype(first_)::value;
        auto entries = [&](int grp, unsigned (&e)[8]) {
#pragma unroll
            for (int m = 0; m < 8; ++m) e[m] = *(LAS unsigned*)(size_t)(lista + grp * 128 + m * 16);
        };
        unsigned cw = *(LAS unsigned*)(size_t)(first ? cnta : lds0 + L_CNT + (__builtin_amdgcn_mbcnt_lo(~0u, 0u) & 31) * 4), e0[8], e1[8];
        entries(0, e0);
        if constexpr (first) {
#pragma unroll
            for (int s = 0; s < 4; ++s) {
                const u32x4 v = *(LAS u32x4*)(size_t)(qa + s * 1024);
                qf[4 * s] = v.x; qf[4 * s + 1] = v.y; qf[4 * s + 2] = v.z; qf[4 * s + 3] = v.w;
            }
        }
        entries(1, e1);
        FENCE();
        int lo = __builtin_amdgcn_readlane(cw, 2 * w) >> 2, hi = __builtin_amdgcn_readlane(cw, 2 * w + 1) >> 2;
        if constexpr (first) {
            if (__builtin_amdgcn_readlane(cw, 16)) gcur = 0;
            if (__builtin_amdgcn_ballot_w64(cw > 4u * CAP2)) { gcur = -1; lo = 0; hi = 0; hmask = 0x7FFFFFFFu; }
        }
        const int c = lo + hi, G = (c + 31) >> 5;
        const int nlast = G > 2 ? (((c - 1) & 31) >> 2) + 1 : 8;
        auto join = [&]() {
            for (int k = 0; k < G * 32 - lo; k += 64) {
                const unsigned e = *(LAS unsigned*)(mylist + SUBB + (l + k) * 4);
                if (l + k < G * 32 - lo) *(LAS unsigned*)(mylist + (lo + l + k) * 4) = l + k < hi ? e : PAD;
            }
        };
        const bool early = lo >= 64;
        if (__builtin_expect(!early, 0)) { join(); entries(0, e0); entries(1, e1); }
        if constexpr (first) asm volatile("s_waitcnt lgkmcnt(0)" : "+{v[160:175]}"(qf) :: "memory");
        if (G > 0) dma_group(ring, srd_x, e0, doff);
        if constexpr (first) {
            if (WVLATE) {
                if (G > 1 && w < 6) dma_group(ring + 8192, srd_x, e1, doff);
                wv_resident();
                lds_arrive(smem + L_CNT + 92, l);
                if (w >= 6) { lds_await(smem + L_CNT + 92, 8); if (G > 1) dma_group(ring + 8192, srd_x, e1, doff); }
            } else if (w < 4) {
                if (G > 1) dma_group(ring + 8192, srd_x, e1, doff);
                wv_resident();
                lds_arrive(smem + L_CNT + 92, l);
            } else {
                if (w >= 6) lds_await(smem + L_CNT + 92, 4);
                if (G > 1) dma_group(ring + 8192, srd_x, e1, doff);
            }
        } else if (G > 1) dma_group(ring + 8192, srd_x, e1, doff);
        f32x16 cc;
#pragma unroll
        for (int m = 0; m < 8; ++m) {
            cc[m] = (e0[m] & 0xFF) == keyn ? 0.f : __uint_as_float(inf);
            cc[8 + m] = (e1[m] & 0xFF) == keyn ? 0.f : __uint_as_float(inf);
        }
        if (early) join();
        STAMP(9);
        if (G > 0) gather_loop(acc, qf, addr, u32x4{lista, keyn, inf, 0u}, cc, srd_x, ring, G, nlast);
    };
    batch(std::true_type{});
    while (__builtin_expect(gcur < G1, 0)) {
        unsigned lim = G1 * 32;
        if (gcur >= 0) {
            const u64 m0 = __builtin_amdgcn_ballot_w64(l >= gcur && (int)(*(LAS unsigned*)(smem + L_HDR + l * 4) & hmask) > 4 * CAP1);
            const u64 m1 = __builtin_amdgcn_ballot_w64(l + 64 >= gcur && (int)(*(LAS unsigned*)(smem + L_HDR + 256 + l * 4) & hmask) > 4 * CAP1);
            if (!(m0 | m1)) break;
            gcur = m0 ? __builtin_ctzll(m0) : 64 + __builtin_ctzll(m1);
            unsigned v; const unsigned o = WS_NOVF + gcur * 4;
            asm volatile("buffer_load_dword %0, %1, %2, 0 offen sc1\n\ts_waitcnt vmcnt(0)" : "=&v"(v) : "v"(o), "s"(srd_ws) : "memory");
            lim = rfl(v);
        }
        const unsigned i = pos + t;
        const bool cand = t < CAP2 && i < lim;
        unsigned u;
        { const unsigned o = cand ? (gcur < 0 ? WS_INBOX + b * (G1 * 128) : WS_OVF + gcur * CH * 4) + i * 4 : OOB;
          asm volatile("buffer_load_dword %0, %1, %2, 0 offen sc1\n\ts_waitcnt vmcnt(0)" : "=&v"(u) : "v"(o), "s"(srd_ws) : "memory"); }
        WG_BARRIER();
        if (t < 16) *(LAS unsigned*)(smem + L_CNT + t * 4) = 0;
        WG_BARRIER();
        if (cand) {
            unsigned e; bool v;
            if (gcur < 0) { e = (i & 3) ? u : u ^ mykey; v = ((i & 31) - 2) * 4 < *(LAS unsigned*)(smem + L_HDR + (i >> 5) * 4); }
            else { e = (((u >> 16) + BIAS) << 8) | ((u >> 8) & 0xFF); v = (u & 0xFF) == (unsigned)b; }
            if (v) *(LAS unsigned*)(smem + L_LIST + (e & 7) * (2 * SUBB) + lds_add(smem + L_CNT + (e & 7) * 8, 4)) = e;
        }
        pos += CAP2;
        if ((unsigned)pos >= lim) { ++gcur; pos = 0; }
        WG_BARRIER();
        batch(std::false_type{});
    }
    STAMP(10);

    {
        bf16x8 wl[2][8], ag[4];
#pragma unroll
        for (int u = 0; u < 2; ++u)
#pragma unroll
            for (int a = 0; a < 8; ++a) {
                LAS char* pa = smem + L_WV1 + (cv ^ ((a >> 1) << 6) ^ ((a & 1) << 3)) + u * 8192;
                wl[u][a] = cat(tr_read(pa), tr_read(pa + 4096));
            }
#pragma unroll
        for (int u = 0; u < 4; ++u) {
            const u32x4 v = {pk2(acc[8 * u], acc[8 * u + 1]), pk2(acc[8 * u + 2], acc[8 * u + 3]),
                             pk2(acc[8 * u + 4], acc[8 * u + 5]), pk2(acc[8 * u + 6], acc[8 * u + 7])};
            ag[u] = __builtin_bit_cast(bf16x8, v);
        }
        FENCE();
        f32x4 oc[8];
#pragma unroll
        for (int a = 0; a < 8; ++a) oc[a] = mfma(__builtin_bit_cast(bf16x8, f32x4{wva[4 * a], wva[4 * a + 1], wva[4 * a + 2], wva[4 * a + 3]}), ag[0], f32x4{0.f, 0.f, 0.f, 0.f});
#pragma unroll
        for (int a = 0; a < 8; ++a) oc[a] = mfma(__builtin_bit_cast(bf16x8, f32x4{wvb[4 * a], wvb[4 * a + 1], wvb[4 * a + 2], wvb[4 * a + 3]}), ag[1], oc[a]);
#pragma unroll
        for (int a = 0; a < 8; ++a) oc[a] = mfma(wl[0][a], ag[2], oc[a]);
        FENCE();
        const unsigned ow = ring + n * 256 + ((g ^ n) << 4) + ((g & 1) << 3);
#pragma unroll
        for (int a = 0; a < 8; ++a) oc[a] = mfma(wl[1][a], ag[3], oc[a]);
#pragma unroll
        for (int a = 0; a < 8; ++a)
            *(LAS u32x2*)(size_t)(ow ^ ((a >> 1) << 6) ^ ((a & 1) << 3)) = u32x2{pk2(oc[a][0], oc[a][1]), pk2(oc[a][2], oc[a][3])};
        const auto rs_o = __builtin_amdgcn_make_buffer_rsrc(out, 0, xbytes, 0x00020000);
#pragma unroll
        for (int m = 0; m < 4; ++m) {
            const u32x4 v = *(LAS u32x4*)(size_t)(ring + m * 1024 + l * 16);
            __builtin_amdgcn_raw_buffer_store_b128(v, rs_o, node_row(w, m) + ((n ^ g ^ (4 * m)) << 4), 0, OUT_AUX);
        }
    }
    if (w == 0) { const unsigned o = l == 0 ? WS_KEY + b * 4 : OOB; ST1(mykey, srd_ws, o, " sc1"); }
    STAMP(11);
}

extern "C" void kernel_launch(void* const* d_in, const int* in_sizes, int n_in,
                              void* d_out, int out_size, void* d_ws, size_t ws_size,
                              hipStream_t stream) {
    const int N = in_sizes[0] / 128, E = in_sizes[1];
    k_graph<<<NB, 512, 0, stream>>>((const unsigned short*)d_in[0], (const int*)d_in[1], (const int*)d_in[2],
                                    (const unsigned short*)d_in[3], (const unsigned short*)d_in[4],
                                    (unsigned*)d_ws, (unsigned short*)d_out, N, E);
}
```

```cpp
#define POLL_T 340
#define SENT 0
#include <hip/hip_runtime.h>
#include <hip/hip_bf16.h>
#include <stdint.h>
#include <type_traits>
#define ORD 1
#define SKIP 0
#define STR2(x) #x
#define STR(x) STR2(x)

typedef __attribute__((ext_vector_type(8))) __bf16 bf16x8;
typedef __attribute__((ext_vector_type(4))) __bf16 bf16x4;
typedef __attribute__((ext_vector_type(2))) __bf16 bf16x2;
typedef __attribute__((ext_vector_type(4))) float f32x4;
typedef __attribute__((ext_vector_type(2))) float f32x2;
typedef __attribute__((ext_vector_type(4))) unsigned u32x4;
typedef __attribute__((ext_vector_type(2))) unsigned u32x2;
typedef __attribute__((ext_vector_type(32))) float f32x32;
typedef __attribute__((ext_vector_type(16))) float f32x16;
typedef __attribute__((ext_vector_type(16))) unsigned u32x16;
typedef __attribute__((ext_vector_type(12))) unsigned u32x12;
typedef __attribute__((ext_vector_type(8))) unsigned u32x8;
typedef unsigned long long u64;
#define LAS __attribute__((address_space(3)))

constexpr int NB = 256;
constexpr int G1 = 128;
constexpr int CH = 3136;
constexpr int CAP1 = 30;
constexpr int CAP2 = 192;
constexpr int SUBB = 784;
#define POLL_AT 3
#define OUT_AUX 16
#define PF_R 0
#define PF_D 0
#define PF_T 200
#define LPRIO 3
#define PKADD 1
#define CODEPF 1
#define CODE_BYTES 21504
#define PARTNER 1
#define XSYNC 1
#define WVLATE 1
#define SENT_SLEEP 2
#define NP_SLEEP 0
#define W_SLEEP 3
#define PRIO 1
#define SPLIT 16384
constexpr unsigned KEY_STEP = 0x9E3779B1u;
constexpr unsigned WS_KEY = 0;
constexpr unsigned WS_NOVF = 2048;
constexpr unsigned WS_INBOX = 4096;
constexpr unsigned WS_OVF = WS_INBOX + NB * G1 * 128;
constexpr unsigned WS_BYTES = WS_OVF + G1 * CH * 4;
constexpr unsigned OOB = 0x80000000u;
constexpr unsigned BIAS = 32768 - SPLIT;
constexpr unsigned PAD = 0x00FFFFFFu;

constexpr int L_CNT = 0;
constexpr int L_HDR = 128;
constexpr int L_LIST = 768;
constexpr int L_RING = L_LIST + 16 * SUBB;
constexpr int L_IMG = L_RING;
constexpr int L_CNT1 = L_IMG + NB * 144;
constexpr int L_WQ = L_RING + 2 * 16384 + 8192;
constexpr int L_WV0 = L_RING + 6 * 16384 + 8192;
constexpr int L_WV1 = L_RING + 131072;
constexpr int L_DUMP = L_WV1 + 16384;
constexpr int L_TOTAL = L_DUMP + 2048;
static_assert(L_RING % 256 == 0 && L_TOTAL <= 163840, "LDS map");

#define STAMP_DECL
#define STAMP(k)
#define STAMP_FLUSH(base, k0, k1)

__device__ __forceinline__ u32x4 make_srd(const void* p, unsigned bytes, unsigned back = 0) {
    const u64 a = (u64)p - back;
    u32x4 s; s.x = (unsigned)a; s.y = (unsigned)(a >> 32); s.z = bytes + back; s.w = 0x00020000u; return s;
}
#define LD4(dst, srd, off, FL) asm volatile("buffer_load_dwordx4 %0, %1, %2, 0 offen" FL : "=v"(dst) : "v"(off), "s"(srd) : "memory")
#define LD4S(dst, srd, off, so, FL) asm volatile("buffer_load_dwordx4 %0, %1, %2, %3 offen" FL : "=v"(dst) : "v"(off), "s"(srd), "s"(so) : "memory")
#define LD1(dst, srd, off, FL) asm volatile("buffer_load_dword %0, %1, %2, 0 offen" FL : "=v"(dst) : "v"(off), "s"(srd) : "memory")
#define ST1(val, srd, off, FL) asm volatile("buffer_store_dword %0, %1, %2, 0 offen" FL :: "v"(val), "v"(off), "s"(srd) : "memory")
#define PIN(x) asm volatile("" : "+v"(x))
#define VMWAIT(n) asm volatile("s_waitcnt vmcnt(" #n ")" ::: "memory")
#define FENCE() __builtin_amdgcn_sched_barrier(0)
#define WG_BARRIER() do { asm volatile("s_waitcnt lgkmcnt(0)" ::: "memory"); __builtin_amdgcn_s_barrier(); asm volatile("" ::: "memory"); } while (0)

struct Srd4 { u32x4 s0, s1, s2, s3; };
__device__ __forceinline__ Srd4 make_srd4(const void* p, unsigned bytes, unsigned back = 0) {
    return Srd4{make_srd(p, bytes, back), make_srd(p, bytes, back + 1024), make_srd(p, bytes, back + 2048), make_srd(p, bytes, back + 3072)};
}

__device__ __forceinline__ void set_m0(unsigned v) { asm volatile("s_mov_b32 m0, %0" :: "s"(v) : "memory"); }
template <int R> __device__ __forceinline__ void dma_rows(const Srd4& d, unsigned e, unsigned off) {
    unsigned tmp, mask = 0x00FFFF00u;
    asm volatile("v_and_or_b32 %0, %1, %2, %3\n\tbuffer_load_dwordx4 %0, %4, 0 offen offset:%c5 lds"
                 : "=&v"(tmp) : "v"(e), "s"(mask), "v"(off), "s"(R == 0 ? d.s0 : R == 1 ? d.s1 : R == 2 ? d.s2 : d.s3), "i"(R * 1024) : "memory");
}
__device__ __forceinline__ void dma_half(unsigned ldsbase, const Srd4& d, const unsigned* e, const u32x4& off) {
    set_m0(ldsbase);
    dma_rows<0>(d, e[0], off.x); dma_rows<1>(d, e[1], off.y); dma_rows<2>(d, e[2], off.z); dma_rows<3>(d, e[3], off.w);
}
__device__ __forceinline__ void dma_group(unsigned ldsbase, const Srd4& d, const unsigned (&e)[8], const u32x4& off) {
    asm volatile("s_waitcnt lgkmcnt(0)" ::: "memory");
    dma_half(ldsbase, d, e, off); dma_half(ldsbase + 4096, d, e + 4, off);
}

__device__ __forceinline__ void gather_loop(f32x32& acc, const u32x16& qf, const u32x16& addr, u32x4 misc, f32x16 cc,
                                            const Srd4& d, unsigned ring, int G, int nlast) {
    f32x32 t0, t1; u32x12 t2; int tmp, pr; u64 sm; unsigned mask = 0x00FFFF00u;
    asm volatile(
        ".macro GWAIT k\n\ts_cmp_gt_i32 %[t], \\k\n\ts_cbranch_scc1 1f\n\ts_waitcnt vmcnt(\\k)\n\t.endm\n\t"
        ".macro GROWS so\n\t.irp s, 0, 1, 2, 3\n\t"
        "ds_read_b128 v[64+8*\\s:67+8*\\s], v[176+\\s] offset:\\so\n\t"
        "ds_read_b128 v[68+8*\\s:71+8*\\s], v[176+\\s] offset:\\so+4096\n\t.endr\n\t.endm\n\t"
        ".macro GTR c, so\n\t"
        "ds_read_b64_tr_b16 v[96+4*\\c:97+4*\\c], v[180+\\c] offset:\\so\n\t"
        "ds_read_b64_tr_b16 v[98+4*\\c:99+4*\\c], v[180+\\c] offset:\\so+4096\n\t.endm\n\t"
        ".macro GS s, c\n\t"
        "v_mfma_f32_16x16x32_bf16 v[\\c:\\c+3], v[64+8*\\s:67+8*\\s], v[160+4*\\s:163+4*\\s], v[\\c:\\c+3]\n\t"
        "v_mfma_f32_16x16x32_bf16 v[\\c+4:\\c+7], v[68+8*\\s:71+8*\\s], v[160+4*\\s:163+4*\\s], v[\\c+4:\\c+7]\n\t.endm\n\t"
        ".macro GLOAD m, r, srd\n\t"
        "v_and_or_b32 v195, v[212+\\m], %[mask], v[188+\\r]\n\t"
        "buffer_load_dwordx4 v195, \\srd, 0 offen offset:1024*\\r lds\n\t.endm\n\t"
        ".macro GPART m, r, srd\n\ts_cmp_le_i32 %[nl], \\m\n\ts_cbranch_scc1 4f\n\tGLOAD \\m, \\r, \\srd\n\t.endm\n\t"
        ".macro GSUM m\n\tv_mfma_f32_16x16x32_bf16 v[128+4*(\\m):131+4*(\\m)], v[96+4*(\\m):99+4*(\\m)], v[220:223], v[128+4*(\\m):131+4*(\\m)]\n\t.endm\n\t"
        ".macro GCMP m, d\n\tv_cmp_ne_u32_sdwa \\d, v[212+\\m], v193 src0_sel:BYTE_0 src1_sel:DWORD\n\t.endm\n\t"
        ".macro GPAIR m, c\n\t"
        "GSUM \\m\n\tGCMP \\m+1, %[sm]\n\tv_cndmask_b32_e32 v[\\c+\\m], 0, v194, vcc\n\t"
        "GSUM \\m+1\n\t.if \\m < 6\n\tGCMP \\m+2, vcc\n\t.else\n\ts_cmp_gt_i32 %[rem], 0\n\t.endif\n\t"
        "v_cndmask_b32_e64 v[\\c+\\m+1], 0, v194, %[sm]\n\t.endm\n\t"
        ".macro GSTEP so, c, eo\n\t"
        "GTR 0, \\so\n\t"
        "s_waitcnt lgkmcnt(2)\n\tGS 0, \\c\n\t"
        "GTR 1, \\so\n\tGTR 2, \\so\n\tGS 1, \\c\n\t"
        "GTR 3, \\so\n\tGTR 4, \\so\n\tGS 2, \\c\n\t"
        "GTR 5, \\so\n\tGTR 6, \\so\n\tGS 3, \\c\n\t"
        "GTR 7, \\so\n\t"
        "ds_read2_b32 v[212:213], v192 offset0:\\eo offset1:\\eo+4\n\t"
        "ds_read2_b32 v[214:215], v192 offset0:\\eo+8 offset1:\\eo+12\n\t"
        "ds_read2_b32 v[216:217], v192 offset0:\\eo+16 offset1:\\eo+20\n\t"
        "ds_read2_b32 v[218:219], v192 offset0:\\eo+24 offset1:\\eo+28\n\t"
        "s_add_u32 m0, %[ring], \\so\n\t"
        "s_cmp_gt_i32 %[rem], 3\n\t"
        ".irp k, 0, 1, 2, 3, 4, 5, 6, 7\n\tv_exp_f32_e32 v[\\c+\\k], v[\\c+\\k]\n\t.endr\n\t"
        "s_waitcnt lgkmcnt(0)\n\t"
        "s_cbranch_scc0 2f\n\t"
        "GLOAD 0, 0, %[s0]\n\tGLOAD 1, 1, %[s1]\n\tGLOAD 2, 2, %[s2]\n\tGLOAD 3, 3, %[s3]\n\t"
        "s_add_u32 m0, m0, 0x1000\n\t"
        "GLOAD 4, 0, %[s0]\n\tGLOAD 5, 1, %[s1]\n\tGLOAD 6, 2, %[s2]\n\tGLOAD 7, 3, %[s3]\n\t"
        "GSIG \\c\n\t"
        "s_waitcnt vmcnt(8)\n\t"
        "s_branch 1f\n\t"
        "2:\n\t"
        "s_mov_b32 %[t], 0\n\t"
        "s_cmp_eq_u32 %[rem], 3\n\ts_cbranch_scc0 4f\n\t"
        "s_mov_b32 %[t], %[nl]\n\t"
        "GLOAD 0, 0, %[s0]\n\tGPART 1, 1, %[s1]\n\tGPART 2, 2, %[s2]\n\tGPART 3, 3, %[s3]\n\t"
        "s_add_u32 m0, m0, 0x1000\n\t"
        "GPART 4, 0, %[s0]\n\tGPART 5, 1, %[s1]\n\tGPART 6, 2, %[s2]\n\tGPART 7, 3, %[s3]\n\t"
        "4:\n\t"
        "GSIG \\c\n\t"
        "s_waitcnt vmcnt(8)\n\t"
        "GWAIT 7\n\tGWAIT 6\n\tGWAIT 5\n\tGWAIT 4\n\tGWAIT 3\n\tGWAIT 2\n\tGWAIT 1\n\tGWAIT 0\n\t"
        "1:\n\t"
        "GROWS 8192-\\so\n\t"
        "GCMP 0, vcc\n\t"
        "s_add_i32 %[rem], %[rem], -1\n\t"
        "GPAIR 0, \\c\n\tGPAIR 2, \\c\n\tGPAIR 4, \\c\n\tGPAIR 6, \\c\n\t"
        ".endm\n\t"
        ".macro GSIG c\n\t"
        ".if " STR(PKADD) "\n\t.irp k, 0, 2, 4, 6\n\tv_pk_add_f32 v[\\c+\\k:\\c+\\k+1], v[\\c+\\k:\\c+\\k+1], 1.0 op_sel_hi:[1,0]\n\t.endr\n\t.else\n\t"
        ".irp k, 0, 1, 2, 3, 4, 5, 6, 7\n\tv_add_f32_e32 v[\\c+\\k], 1.0, v[\\c+\\k]\n\t.endr\n\t.endif\n\t"
        ".irp k, 0, 1, 2, 3, 4, 5, 6, 7\n\tv_rcp_f32_e32 v[\\c+\\k], v[\\c+\\k]\n\t.endr\n\t"
        ".irp k, 0, 1, 2, 3\n\tv_cvt_pk_bf16_f32 v[220+\\k], v[\\c+2*\\k], v[\\c+2*\\k+1]\n\t.endr\n\t.endm\n\t"
        "s_waitcnt vmcnt(8)\n\t"
        "s_cmp_gt_i32 %[rem], 1\n\ts_cbranch_scc1 1f\n\ts_waitcnt vmcnt(0)\n\t1:\n\t"
        "GROWS 0\n\t"
        ".if " STR(LPRIO) " >= 3\n\ts_mov_b32 %[pr], 0\n\ts_setprio 2\n\t.endif\n\t"
        "0:\n\t"
        ".if " STR(LPRIO) " == 3\n\t"
        "s_cmp_eq_u32 %[pr], 1\n\ts_cbranch_scc0 5f\n\ts_setprio 1\n\t5:\n\t"
        "s_cmp_eq_u32 %[pr], 2\n\ts_cbranch_scc0 5f\n\ts_setprio 0\n\t5:\n\t"
        "s_add_u32 %[pr], %[pr], 1\n\t.endif\n\t"
        "GSTEP 0, 196, 64\n\t"
        "s_cbranch_scc0 9f\n\t"
        "GSTEP 8192, 204, 96\n\t"
        "v_add_u32_e32 v192, 0x100, v192\n\t"
        "s_cbranch_scc1 0b\n\t"
        "9:\n\t"
        "s_waitcnt lgkmcnt(0)\n\ts_nop 6\n\t"
        ".purgem GWAIT\n\t.purgem GROWS\n\t.purgem GTR\n\t.purgem GS\n\t.purgem GLOAD\n\t.purgem GPART\n\t.purgem GSUM\n\t.purgem GCMP\n\t.purgem GPAIR\n\t.purgem GSTEP\n\t.purgem GSIG"
        : "+{v[128:159]}"(acc), "+{v[192:195]}"(misc), "+{v[196:211]}"(cc), "=&{v[64:95]}"(t0), "=&{v[96:127]}"(t1), "=&{v[212:223]}"(t2),
          [rem] "+s"(G), [t] "=&s"(tmp), [pr] "=&s"(pr), [sm] "=&s"(sm)
        : "{v[160:175]}"(qf), "{v[176:191]}"(addr), [s0] "s"(d.s0), [s1] "s"(d.s1), [s2] "s"(d.s2), [s3] "s"(d.s3),
          [mask] "s"(mask), [ring] "s"(ring), [nl] "s"(nlast)
        : "vcc", "scc", "memory");
}

__device__ __forceinline__ u64 rank_edges(u32x16& ed, u32x16& es, u32x16& rk, u64 m0, u64 m1, u64 m2, u64 m3, unsigned keya) {
    u32x16 b4; u64 over; unsigned sel = 0x01050401u, c36 = 36;
    asm volatile(
        ".macro RADD k\n\tv_lshlrev_b32_sdwa v[96+\\k], 2, v[64+\\k] dst_sel:DWORD dst_unused:UNUSED_PAD src0_sel:DWORD src1_sel:BYTE_0\n\t"
        "ds_add_rtn_u32 v[112+\\k], v[96+\\k], v111 offset:%c[cnt]\n\t.endm\n\t"
        ".macro RENT k\n\tv_add_u32_e32 v[80+\\k], %c[bias], v[80+\\k]\n\tv_perm_b32 v[80+\\k], v[80+\\k], v[64+\\k], %[sel]\n\t.endm\n\t"
        ".macro RPUT k\n\tv_min_u32_e32 v110, %c[cap], v[112+\\k]\n\tv_mad_u32_u24 v110, v[96+\\k], %[c36], v110\n\t"
        "ds_write_b32 v110, v[80+\\k] offset:%c[img]\n\t.endm\n\t"
        "v_mov_b32_e32 v111, 4\n\t"
        ".irp k, 0, 2, 4, 6, 8, 10, 12\n\tv_mov_b64_e32 v[112+\\k:113+\\k], 0\n\t.endr\n\t"
        "s_mov_b64 exec, %[m0]\n\ts_waitcnt vmcnt(9)\n\tRADD 0\n\tRADD 1\n\tRADD 2\n\tRADD 3\n\t"
        "s_mov_b64 exec, %[m1]\n\ts_waitcnt vmcnt(8)\n\tRADD 4\n\tRADD 5\n\tRADD 6\n\tRADD 7\n\t"
        "s_mov_b64 exec, %[m2]\n\ts_waitcnt vmcnt(7)\n\tRADD 8\n\tRADD 9\n\tRADD 10\n\tRADD 11\n\t"
        "s_mov_b64 exec, %[m3]\n\ts_waitcnt vmcnt(6)\n\tRADD 12\n\t"
        "s_mov_b64 exec, %[m0]\n\ts_waitcnt vmcnt(5)\n\tRENT 0\n\tRENT 1\n\tRENT 2\n\tRENT 3\n\t"
        "s_waitcnt lgkmcnt(9)\n\tRPUT 0\n\tRPUT 1\n\tRPUT 2\n\tRPUT 3\n\t"
        "s_mov_b64 exec, %[m1]\n\ts_waitcnt vmcnt(4)\n\tRENT 4\n\tRENT 5\n\tRENT 6\n\tRENT 7\n\t"
        "s_waitcnt lgkmcnt(9)\n\tRPUT 4\n\tRPUT 5\n\tRPUT 6\n\tRPUT 7\n\t"
        "s_mov_b64 exec, %[m2]\n\ts_waitcnt vmcnt(3)\n\tRENT 8\n\tRENT 9\n\tRENT 10\n\tRENT 11\n\t"
        "s_waitcnt lgkmcnt(9)\n\tRPUT 8\n\tRPUT 9\n\tRPUT 10\n\tRPUT 11\n\t"
        "s_mov_b64 exec, %[m3]\n\ts_waitcnt vmcnt(0)\n\tRENT 12\n\t"
        "s_waitcnt lgkmcnt(12)\n\tRPUT 12\n\t"
        "s_mov_b64 exec, -1\n\t"
        "v_add_u32_e32 v93, %c[step], v93\n\t"
        "v_max3_u32 v110, v112, v113, v114\n\tv_max3_u32 v109, v115, v116, v117\n\t"
        "ds_write_b32 %[keya], v93 offset:%c[cnt]+1024\n\t"
        "v_max3_u32 v110, v110, v118, v119\n\tv_max3_u32 v109, v109, v120, v121\n\t"
        "v_max3_u32 v110, v110, v122, v123\n\tv_max3_u32 v110, v110, v109, v124\n\t"
        "v_cmp_le_u32_e32 vcc, %c[cap], v110\n\ts_mov_b64 %[over], vcc\n\t"
        ".purgem RADD\n\t.purgem RENT\n\t.purgem RPUT"
        : "+{v[64:79]}"(ed), "+{v[80:95]}"(es), "=&{v[96:111]}"(b4), "=&{v[112:127]}"(rk), [over] "=&s"(over)
        : [m0] "s"(m0), [m1] "s"(m1), [m2] "s"(m2), [m3] "s"(m3), [keya] "v"(keya), [sel] "s"(sel), [c36] "s"(c36),
          [cnt] "i"(L_CNT1), [img] "i"(L_IMG + 8), [bias] "i"(BIAS), [cap] "i"(4 * CAP1), [step] "i"(KEY_STEP)
        : "vcc", "memory");
    return over;
}
__device__ __forceinline__ unsigned read_image(f32x32& img, u32x16& meta, u32x16& sums, unsigned ima, unsigned cna) {
    unsigned no;
    asm volatile(
        "v_mov_b32_e32 v121, 0\n\tds_read_b32 v120, v121 offset:%c[cnt]+2048\n\t"
        ".irp k, 0, 1, 2, 3, 4, 5, 6, 7\n\tds_read_b128 v[64+4*\\k:67+4*\\k], %[ima] offset:4608*\\k\n\t.endr\n\t"
        ".irp k, 0, 1, 2, 3\n\tds_read2_b32 v[96+2*\\k:97+2*\\k], %[cna] offset0:64*\\k offset1:64*\\k+32\n\t.endr\n\t"
        "s_waitcnt lgkmcnt(11)\n\tv_readfirstlane_b32 %[no], v120\n\t"
        "v_add_u32_e32 v121, 0x400, %[cna]\n\t"
        ".irp k, 0, 1, 2, 3\n\tds_read2_b32 v[104+2*\\k:105+2*\\k], v121 offset0:64*\\k offset1:64*\\k+32\n\t.endr\n\t"
        "s_waitcnt lgkmcnt(12)\n\t"
        ".irp k, 0, 1, 2, 3\n\tv_add3_u32 v[112+\\k], v[64+4*\\k], v[65+4*\\k], v[66+4*\\k]\n\tv_add_u32_e32 v[112+\\k], v[112+\\k], v[67+4*\\k]\n\t.endr\n\t"
        "s_waitcnt lgkmcnt(8)\n\t"
        ".irp k, 4, 5, 6, 7\n\tv_add3_u32 v[112+\\k], v[64+4*\\k], v[65+4*\\k], v[66+4*\\k]\n\tv_add_u32_e32 v[112+\\k], v[112+\\k], v[67+4*\\k]\n\t.endr\n\t"
        ".irp k, 0, 1, 2, 3, 4, 5, 6, 7\n\tv_add_u32_dpp v[112+\\k], v[112+\\k], v[112+\\k] quad_perm:[1,0,3,2] row_mask:0xf bank_mask:0xf\n\t.endr\n\t"
        ".irp k, 0, 1, 2, 3, 4, 5, 6, 7\n\tv_add_u32_dpp v[112+\\k], v[112+\\k], v[112+\\k] quad_perm:[2,3,0,1] row_mask:0xf bank_mask:0xf\n\t.endr\n\t"
        ".irp k, 0, 1, 2, 3, 4, 5, 6, 7\n\tv_add_u32_dpp v[112+\\k], v[112+\\k], v[112+\\k] row_half_mirror row_mask:0xf bank_mask:0xf\n\t.endr\n\t"
        "s_waitcnt lgkmcnt(0)"
        : "=&{v[64:95]}"(img), "=&{v[96:111]}"(meta), "=&{v[112:127]}"(sums), [no] "=&s"(no)
        : [ima] "v"(ima), [cna] "v"(cna), [cnt] "i"(L_CNT1) : "memory");
    return no;
}
__device__ __forceinline__ void publish_image(f32x32 img, u32x16 meta, u32x16 sums, const u32x4& srd, unsigned ob) {
    u64 pc0 = 0x0101010101010101ull; unsigned so;
    asm volatile(
        ".macro PUB k\n\ts_mov_b64 exec, %[pc0]\n\tv_mov_b32_e32 v[64+4*\\k], v[96+\\k]\n\tv_mov_b32_e32 v[65+4*\\k], v[112+\\k]\n\t"
        "s_mov_b64 exec, -1\n\tv_xor_b32_e32 v[64+4*\\k], v[64+4*\\k], v[104+\\k]\n\t"
        "buffer_store_dwordx4 v[64+4*\\k:67+4*\\k], %[ob], %[srd], %[so] offen sc1\n\ts_add_u32 %[so], %[so], %c[step]\n\t.endm\n\t"
        "s_mov_b32 %[so], 0\n\t"
        ".irp k, 0, 1, 2, 3, 4, 5, 6, 7\n\tPUB \\k\n\t.endr\n\t.purgem PUB"
        : "+{v[64:95]}"(img), [so] "=&s"(so)
        : "{v[96:111]}"(meta), "{v[112:127]}"(sums), [srd] "s"(srd), [ob] "v"(ob), [pc0] "s"(pc0), [step] "i"(32 * G1 * 128) : "scc", "memory");
}

__device__ __forceinline__ void inbox_request(u32x16& raw, const u32x4& srd, unsigned ib, unsigned hb, unsigned key) {
    unsigned o1 = 0x1000, o2 = 0x2000, o3 = 0x3000, tries;
    asm volatile(".if " STR(SENT) "\n\ts_movk_i32 %1, 0x100\n\ts_mov_b64 exec, 15\n\t"
                 "0:\n\tbuffer_load_dword v64, %7, %3, 0 offen sc1\n\ts_waitcnt vmcnt(0)\n\t"
                 "v_xor_b32_e32 v64, %8, v64\n\tv_cmp_lt_u32_e32 vcc, 0x3fff, v64\n\t"
                 "s_cbranch_vccz 1f\n\ts_sleep " STR(SENT_SLEEP) "\n\ts_sub_u32 %1, %1, 1\n\ts_cbranch_scc0 0b\n\t"
                 "1:\n\ts_mov_b64 exec, -1\n\t.endif\n\t"
                 "buffer_load_dwordx4 v[64:67], %2, %3, 0 offen sc1\n\tbuffer_load_dwordx4 v[68:71], %2, %3, %4 offen sc1\n\t"
                 "buffer_load_dwordx4 v[72:75], %2, %3, %5 offen sc1\n\tbuffer_load_dwordx4 v[76:79], %2, %3, %6 offen sc1"
                 : "=&{v[64:79]}"(raw), "=&s"(tries) : "v"(ib), "s"(srd), "s"(o1), "s"(o2), "s"(o3), "v"(hb), "s"(key) : "memory", "vcc", "scc");
}
__device__ __forceinline__ unsigned inbox_stage(u32x16 raw, const u32x4& srd, unsigned ib, unsigned key, int i4, unsigned hdra) {
    u32x16 t0; u32x4 t1; u64 m0, m1, m2, m3; unsigned todo, fresh, spin, o1, o2, o3, over;
    u64 pc0 = 0x0101010101010101ull;
    asm volatile(
        ".macro IVAL j, so\n\t"
        "s_bitcmp1_b32 %[todo], \\j\n\ts_cbranch_scc0 2f\n\t"
        "v_xor_b32_e32 v[64+4*\\j], %[key], v[64+4*\\j]\n\t"
        "v_sub_u32_e32 v97, 0, v[65+4*\\j]\n\t"
        "v_add_u32_e32 v96, v[64+4*\\j], v[65+4*\\j]\n\t"
        "v_mov_b32_dpp v[80+\\j], v[64+4*\\j] quad_perm:[0,0,0,0] row_mask:0xf bank_mask:0xf\n\t"
        "v_cndmask_b32_e64 v96, v96, v97, %[pc0]\n\t"
        "s_nop 0\n\t"
        "v_mov_b32_dpp v[80+\\j], v[80+\\j] row_shr:4 row_mask:0xf bank_mask:0xa\n\t"
        "v_cmp_gt_i32_e32 vcc, v[80+\\j], v92\n\t"
        "v_add3_u32 v96, v96, v[66+4*\\j], v[67+4*\\j]\n\t"
        "v_lshrrev_b32_e32 v97, 14, v[80+\\j]\n\t"
        "v_cndmask_b32_e32 v96, 0, v96, vcc\n\t"
        "s_nop 1\n\tv_add_u32_dpp v96, v96, v96 quad_perm:[1,0,3,2] row_mask:0xf bank_mask:0xf\n\t"
        "s_nop 1\n\tv_add_u32_dpp v96, v96, v96 quad_perm:[2,3,0,1] row_mask:0xf bank_mask:0xf\n\t"
        "s_nop 1\n\tv_add_u32_dpp v96, v96, v96 row_half_mirror row_mask:0xf bank_mask:0xf\n\t"
        "v_or_b32_e32 v96, v96, v97\n\t"
        "v_cmp_ne_u32_e32 vcc, 0, v96\n\t"
        "s_cbranch_vccz 1f\n\t"
        "buffer_load_dwordx4 v[64+4*\\j:67+4*\\j], %[ib], %[srd], \\so offen sc1\n\t"
        "s_branch 2f\n\t"
        "1:\n\ts_bitset1_b32 %[fresh], \\j\n\t"
        "2:\n\t.endm\n\t"
        ".macro IRANK j\n\t"
        "s_bitcmp1_b32 %[fresh], \\j\n\ts_cbranch_scc0 2f\n\t"
        "v_cmp_lt_u32_e64 %[m0], v92, v[80+\\j]\n\tv_cmp_lt_u32_e64 %[m1], v93, v[80+\\j]\n\t"
        "v_cmp_lt_u32_e64 %[m2], v94, v[80+\\j]\n\tv_cmp_lt_u32_e64 %[m3], v95, v[80+\\j]\n\t"
        ".irp k, 0, 1, 2, 3\n\tv_lshrrev_b32_e32 v[84+\\k], 21, v[64+4*\\j+\\k]\n\t.endr\n\t"
        ".irp k, 0, 1, 2, 3\n\tv_and_b32_e32 v[84+\\k], 60, v[84+\\k]\n\t.endr\n\t"
        "v_cmp_lt_u32_e32 vcc, %c[cap], v[80+\\j]\n\t"
        "s_mov_b64 exec, %[m0]\n\tds_add_rtn_u32 v88, v84, v98 offset:%c[cnt]\n\t"
        "s_mov_b64 exec, %[m1]\n\tds_add_rtn_u32 v89, v85, v98 offset:%c[cnt]\n\t"
        "s_mov_b64 exec, %[m2]\n\tds_add_rtn_u32 v90, v86, v98 offset:%c[cnt]\n\t"
        "s_mov_b64 exec, %[m3]\n\tds_add_rtn_u32 v91, v87, v98 offset:%c[cnt]\n\t"
        "s_mov_b64 exec, %[pc0]\n\tds_write_b32 %[hdra], v[80+\\j] offset:128*\\j\n\t"
        "s_mov_b64 exec, -1\n\t"
        "s_cbranch_vccz 1f\n\t"
        "s_mov_b32 %[over], 1\n\t"
        "1:\n\ts_waitcnt lgkmcnt(1)\n\t"
        ".irp k, 0, 1, 2, 3\n\tv_min_u32_e32 v[88+\\k], %c[sub]-4, v[88+\\k]\n\t.endr\n\t"
        ".irp k, 0, 1, 2, 3\n\tv_mad_u32_u24 v[88+\\k], v[84+\\k], %[sub4], v[88+\\k]\n\t.endr\n\t"
        "s_mov_b64 exec, %[m0]\n\tds_write_b32 v88, v[64+4*\\j] offset:%c[list]\n\t"
        "s_mov_b64 exec, %[m1]\n\tds_write_b32 v89, v[65+4*\\j] offset:%c[list]\n\t"
        "s_mov_b64 exec, %[m2]\n\tds_write_b32 v90, v[66+4*\\j] offset:%c[list]\n\t"
        "s_mov_b64 exec, %[m3]\n\tds_write_b32 v91, v[67+4*\\j] offset:%c[list]\n\t"
        "s_mov_b64 exec, -1\n\t"
        "2:\n\t.endm\n\t"
        "s_movk_i32 %[o1], 0x1000\n\ts_movk_i32 %[o2], 0x2000\n\ts_movk_i32 %[o3], 0x3000\n\t"
        "v_mov_b32_e32 v92, %[i4]\n\tv_add_u32_e32 v93, 4, %[i4]\n\tv_add_u32_e32 v94, 8, %[i4]\n\tv_add_u32_e32 v95, 12, %[i4]\n\t"
        "v_mov_b32_e32 v98, 4\n\t"
        "s_mov_b32 %[todo], 15\n\ts_mov_b32 %[spin], 0x400000\n\ts_mov_b32 %[over], 0\n\t"
        "0:\n\t"
        "s_mov_b32 %[fresh], 0\n\t"
        "s_waitcnt vmcnt(3)\n\ts_cmp_eq_u32 %[todo], 15\n\ts_cbranch_scc1 3f\n\ts_waitcnt vmcnt(0)\n\t3:\n\t"
        "IVAL 0, 0\n\tIRANK 0\n\t"
        "s_waitcnt vmcnt(2)\n\tIVAL 1, %[o1]\n\tIRANK 1\n\t"
        "s_waitcnt vmcnt(1)\n\tIVAL 2, %[o2]\n\tIRANK 2\n\t"
        "s_cmp_eq_u32 %[todo], 15\n\ts_cbranch_scc0 3f\n\ts_waitcnt vmcnt(0)\n\t3:\n\tIVAL 3, %[o3]\n\tIRANK 3\n\t"
        "s_andn2_b32 %[todo], %[todo], %[fresh]\n\t"
        "s_cbranch_scc0 9f\n\t"
        "s_sub_u32 %[spin], %[spin], 1\n\t"
        "s_cbranch_scc0 0b\n\t"
        "s_waitcnt vmcnt(0)\n\t"
        "9:\n\t"
        ".purgem IVAL\n\t.purgem IRANK"
        : "+{v[64:79]}"(raw), "=&{v[80:95]}"(t0), "=&{v[96:99]}"(t1), [m0] "=&s"(m0), [m1] "=&s"(m1), [m2] "=&s"(m2), [m3] "=&s"(m3),
          [todo] "=&s"(todo), [fresh] "=&s"(fresh), [spin] "=&s"(spin), [o1] "=&s"(o1), [o2] "=&s"(o2), [o3] "=&s"(o3), [over] "=&s"(over)
        : [srd] "s"(srd), [ib] "v"(ib), [key] "s"(key), [i4] "v"(i4), [hdra] "v"(hdra), [pc0] "s"(pc0), [sub4] "s"(SUBB / 4),
          [cnt] "i"(L_CNT), [list] "i"(L_LIST), [sub] "i"(SUBB), [cap] "i"(4 * CAP1)
        : "vcc", "scc", "memory");
    return over;
}

template <int CTRL, int BANK> __device__ __forceinline__ unsigned dpp(unsigned old, unsigned v) { return __builtin_amdgcn_update_dpp(old, v, CTRL, 0xF, BANK, true); }
__device__ __forceinline__ unsigned bcast8(unsigned v) { v = dpp<0x00, 0xF>(0u, v); return __builtin_amdgcn_update_dpp(v, v, 0x114, 0xF, 0xA, false); }
__device__ __forceinline__ unsigned sum8(unsigned v) { v += dpp<0xB1, 0xF>(0u, v); v += dpp<0x4E, 0xF>(0u, v); return v + dpp<0x141, 0xF>(0u, v); }
__device__ __forceinline__ bf16x4 tr_read(LAS char* p) { return __builtin_amdgcn_ds_read_tr16_b64_v4bf16((LAS bf16x4*)p); }
__device__ __forceinline__ bf16x8 cat(bf16x4 a, bf16x4 b) { return __builtin_shufflevector(a, b, 0, 1, 2, 3, 4, 5, 6, 7); }
__device__ __forceinline__ unsigned pk2(float a, float b) { f32x2 v = {a, b}; bf16x2 r = __builtin_convertvector(v, bf16x2); return __builtin_bit_cast(unsigned, r); }
__device__ __forceinline__ f32x4 mfma(bf16x8 a, bf16x8 b, f32x4 c) { return __builtin_amdgcn_mfma_f32_16x16x32_bf16(a, b, c, 0, 0, 0); }
__device__ __forceinline__ unsigned lds_add(LAS char* p, unsigned v) { return __hip_atomic_fetch_add((LAS unsigned*)p, v, __ATOMIC_RELAXED, __HIP_MEMORY_SCOPE_WORKGROUP); }
__device__ __forceinline__ LAS unsigned* ldsp(unsigned a) { return (LAS unsigned*)(size_t)a; }
__device__ __forceinline__ unsigned byte0_x4(unsigned v) { unsigned r; asm("v_lshlrev_b32_sdwa %0, 2, %1 dst_sel:DWORD dst_unused:UNUSED_PAD src0_sel:DWORD src1_sel:BYTE_0" : "=v"(r) : "v"(v)); return r; }
__device__ __forceinline__ unsigned mad24(unsigned a, unsigned b, unsigned c) { unsigned r; asm("v_mad_u32_u24 %0, %1, %2, %3" : "=v"(r) : "v"(a), "s"(b), "v"(c)); return r; }
__device__ __forceinline__ void lds_arrive(LAS char* p, int lane) {
    asm volatile("s_waitcnt lgkmcnt(0)" ::: "memory");
    if (lane == 0) __hip_atomic_fetch_add((LAS unsigned*)p, 1u, __ATOMIC_RELAXED, __HIP_MEMORY_SCOPE_WORKGROUP);
}
__device__ __forceinline__ void lds_await(LAS char* p, unsigned n) {
    while (__builtin_amdgcn_readfirstlane(*(volatile LAS unsigned*)p) < n) __builtin_amdgcn_s_sleep(1);
    asm volatile("" ::: "memory");
}
__device__ __forceinline__ unsigned rfl(unsigned v) { return __builtin_amdgcn_readfirstlane(v); }

__global__ __launch_bounds__(512, 2) void k_graph(const unsigned short* x_, const int* src_, const int* dst_, const unsigned short* wq_, const unsigned short* wv_,
                                                  unsigned* ws_, unsigned short* out_, int N_, int E_) {
    u32x4 srd_d, srd_s, srd_ws, srd_x0, pw, po;
    u64 t_start;
    asm volatile("s_load_dwordx2 s[40:41], %[ka], 0x10\n\ts_load_dwordx2 s[44:45], %[ka], 0x8\n\ts_load_dwordx4 s[60:63], %[ka], 0x30\n\t"
                 "s_load_dwordx2 s[48:49], %[ka], 0x28\n\ts_load_dwordx2 s[52:53], %[ka], 0x0\n\ts_load_dwordx4 s[56:59], %[ka], 0x18\n\t"
                 "s_memrealtime s[64:65]\n\t"
                 "s_mov_b32 s43, 0x20000\n\ts_mov_b32 s47, 0x20000\n\ts_mov_b32 s51, 0x20000\n\ts_mov_b32 s55, 0x20000\n\ts_mov_b32 s50, %c[wsb]"
                 : "=&{s[40:43]}"(srd_d), "=&{s[44:47]}"(srd_s), "=&{s[48:51]}"(srd_ws), "=&{s[52:55]}"(srd_x0), "=&{s[56:59]}"(pw), "=&{s[60:63]}"(po), "=&{s[64:65]}"(t_start)
                 : [ka] "s"(__builtin_amdgcn_kernarg_segment_ptr()), [wsb] "i"(WS_BYTES));
    FENCE();
    __shared__ __attribute__((aligned(16))) char smem_[L_TOTAL];
    LAS char* smem = (LAS char*)smem_;
    const int t = threadIdx.x, l = t & 63, b = blockIdx.x;
    const int w = rfl(t >> 6);
    const int g = l >> 4, n = l & 15, q = (l >> 2) & 3, p = l & 3, pc = t & 7;
    const unsigned lds0 = (unsigned)(size_t)smem;
    const bool isprod = ((b >> 3) & 1) == 0;
    const int pg = ((b & 7) << 4) | (b >> 4);
    const unsigned e0 = pg * CH + 4 * t, e1 = pg * CH + 3072 + t;

    u32x16 ed, es;
    {
        const unsigned o = e0 * 4, o1 = w == 0 ? e1 * 4 : OOB, ok = WS_KEY + t * 4, om = WS_KEY + b * 4;
        const unsigned oc = w < 4 ? (t < 196 ? ((b - 8) << 8) + ((t >> 1) << 16) + ((t & 1) << 7) : OOB) : (t - 256) * 128, dump = lds0 + L_DUMP + w * 256;
        int role = (w < 4 ? 0 : 2) + (isprod ? 0 : 1);
        unsigned k4 = 4096, k8 = 8192;
        asm volatile("s_cmp_eq_u32 %[role], 0\n\ts_cbranch_scc1 1f\n\t"
                     "s_mov_b32 m0, %[dump]\n\ts_cmp_eq_u32 %[role], 1\n\ts_cbranch_scc1 2f\n\t"
                     "s_getpc_b64 s[68:69]\n\ts_mov_b32 s70, " STR(CODE_BYTES) "\n\ts_mov_b32 s71, 0x20000\n\t"
                     ".if " STR(CODEPF) "\n\tbuffer_load_dword %[oc], s[68:71], 0 offen lds\n\t.endif\n\t"
                     "s_waitcnt lgkmcnt(0)\n\ts_branch 3f\n\t"
                     "2:\n\ts_waitcnt lgkmcnt(0)\n\ts_lshl_b32 s54, s62, 8\n\t"
                     ".if " STR(PARTNER) "\n\tbuffer_load_dword %[oc], s[52:55], 0 offen lds\n\t.endif\n\ts_branch 3f\n\t"
                     "1:\n\ts_waitcnt lgkmcnt(0)\n\ts_lshl_b32 s42, s63, 2\n\ts_lshl_b32 s46, s63, 2\n\t"
                     "buffer_load_dwordx4 v[64:67], %[o], s[40:43], 0 offen\n\tbuffer_load_dwordx4 v[68:71], %[o], s[40:43], %[k4] offen\n\t"
                     "buffer_load_dwordx4 v[72:75], %[o], s[40:43], %[k8] offen\n\tbuffer_load_dword v76, %[o1], s[40:43], 0 offen\n\t"
                     "buffer_load_dwordx4 v[80:83], %[o], s[44:47], 0 offen\n\tbuffer_load_dwordx4 v[84:87], %[o], s[44:47], %[k4] offen\n\t"
                     "buffer_load_dwordx4 v[88:91], %[o], s[44:47], %[k8] offen\n\tbuffer_load_dword v92, %[o1], s[44:47], 0 offen\n\t"
                     "buffer_load_dword v93, %[ok], s[48:51], 0 offen sc1\n\tbuffer_load_dword v94, %[om], s[48:51], 0 offen sc1\n\t"
                     "3:\n\ts_lshl_b32 s54, s62, 8"
                     : "=&{v[64:79]}"(ed), "=&{v[80:95]}"(es)
                     : [o] "v"(o), [o1] "v"(o1), [ok] "v"(ok), [om] "v"(om), [oc] "v"(oc), [role] "s"(role), [dump] "s"(dump), [k4] "s"(k4), [k8] "s"(k8),
                       "{s[40:43]}"(srd_d), "{s[44:47]}"(srd_s), "{s[48:51]}"(srd_ws), "{s[52:55]}"(srd_x0), "{s[56:59]}"(pw), "{s[60:63]}"(po), "{s[64:65]}"(t_start)
                     : "memory", "scc", "s68", "s69", "s70", "s71");
        asm volatile("" : "+{s[48:51]}"(srd_ws), "+{s[52:55]}"(srd_x0), "+{s[56:59]}"(pw), "+{s[60:63]}"(po), "+{s[64:65]}"(t_start));
    }
    const void* const x = (const void*)((u64)srd_x0.x | ((u64)srd_x0.y << 32));
    const void* const wq = (const void*)((u64)pw.x | ((u64)pw.y << 32));
    const void* const wv = (const void*)((u64)pw.z | ((u64)pw.w << 32));
    void* const out = (void*)((u64)po.x | ((u64)po.y << 32));
    const int N = po.z, E = po.w;
    const unsigned xbytes = srd_x0.z;
    STAMP_DECL;
    const unsigned ring = lds0 + L_RING + w * 16384;
    const int jn = 8 * n + w;

    unsigned mykey = 0;
    auto node_row = [&](int tile, int m) { const int nd = b + 256 * (8 * (4 * m + g) + tile); return nd < N ? (unsigned)nd << 8 : OOB; };
    auto zero_staging = [&]() {
        const u32x4 z = {0u, 0u, 0u, 0u};
#pragma unroll
        for (int k = 0; k < 5; ++k) *(LAS u32x4*)(smem + L_IMG + k * 8192 + t * 16) = z;
    };

    u32x16 addr;
    f32x32 acc, wva, wvb;
    unsigned cv, lista, cnta, qa, keyn, inf;
    Srd4 srd_x;
    auto loop_constants = [&]() {
        srd_x = make_srd4(x, xbytes, BIAS * 256);
        lista = lds0 + L_LIST + w * (2 * SUBB) + g * 4; cnta = lds0 + L_CNT + (l & 31) * 4; qa = ring + l * 16; keyn = jn; inf = 0x7F800000u;
        asm volatile("" : "+s"(srd_x.s0), "+s"(srd_x.s1), "+s"(srd_x.s2), "+s"(srd_x.s3));
        asm volatile("" : "+v"(lista), "+v"(cnta), "+v"(qa), "+v"(keyn), "+v"(inf));
        const unsigned o0 = (n ^ (((g & 1) << 3) | (g >> 1))) << 4;
        const unsigned ra = (4 * (n & 3) + (n >> 2)) * 256 + (((4 * g) ^ (((n & 7) << 1) | (n >> 3))) << 4);
        const int i = 4 * g + q, r8 = i & 7;
        const unsigned ta = (4 * q + g) * 256 + 8 * (p & 1) + (((p >> 1) ^ (((i & 7) << 1) | (i >> 3))) << 4);
        cv = i * 256 + (((p ^ ((r8 & 1) | ((r8 & 6) << 1))) << 4) | ((p & 1) << 3));
#pragma unroll
        for (int s = 0; s < 4; ++s) { addr[s] = ring + (ra ^ (s << 4)); addr[12 + s] = o0 ^ (s << 5); }
#pragma unroll
        for (int c = 0; c < 8; ++c) addr[4 + c] = ring + (ta ^ (c << 5));
        asm volatile("" : "+{v[176:191]}"(addr), "+v"(cv));
    };
    auto clear_sums = [&]() { acc = 0.f; asm volatile("" : "+{v[128:159]}"(acc)); };
    auto wv_resident = [&]() {
#pragma unroll
        for (int a = 0; a < 8; ++a) {
            LAS char* pa = smem + L_WV0 + (cv ^ ((a >> 1) << 6) ^ ((a & 1) << 3));
            const f32x4 f0 = __builtin_bit_cast(f32x4, cat(tr_read(pa), tr_read(pa + 4096)));
            const f32x4 f1 = __builtin_bit_cast(f32x4, cat(tr_read(pa + 16384), tr_read(pa + 16384 + 4096)));
#pragma unroll
            for (int k = 0; k < 4; ++k) { wva[4 * a + k] = f0[k]; wvb[4 * a + k] = f1[k]; }
        }
        asm volatile("" : "+{v[224:255]}"(wva), "+{v[32:63]}"(wvb));
    };

    if (isprod) zero_staging();
    if (t < 8) *(LAS u32x4*)(smem + L_CNT + t * 16) = u32x4{0u, 0u, 0u, 0u};
    WG_BARRIER();

    if (__builtin_expect(w < 4, 1)) {
        if (__builtin_expect(isprod, 1)) {
            if (PRIO) __builtin_amdgcn_s_setprio(2);
            unsigned ob = WS_INBOX + ((t >> 3) * G1 + pg) * 128 + pc * 16, ima = lds0 + L_IMG + (t >> 3) * 144 + pc * 16;
            PIN(ob); PIN(ima);
            unsigned cna = lds0 + L_CNT1 + (t >> 3) * 4;
            PIN(cna);
            const bool v0 = e0 < (unsigned)E, v1 = e0 + 1024 < (unsigned)E, v2 = e0 + 2048 < (unsigned)E, v3 = w == 0 && e1 < (unsigned)E;
            STAMP(1);
            u32x16 rk;
            const u64 over = rank_edges(ed, es, rk, __builtin_amdgcn_ballot_w64(v0), __builtin_amdgcn_ballot_w64(v1), __builtin_amdgcn_ballot_w64(v2),
                                        __builtin_amdgcn_ballot_w64(v3), t * 4);
            mykey = es[14];
            if (__builtin_expect(over != 0, 0)) {
#pragma unroll
                for (int k = 0; k < 13; ++k)
                    if (rk[k] >= 4u * CAP1) {
                        const unsigned o = WS_OVF + (pg * CH + lds_add(smem + L_CNT1 + 2048, 1)) * 4;
                        const unsigned v = ((((es[k] >> 8) & 0xFFFF) - BIAS) << 16) | ed[k];
                        ST1(v, srd_ws, o, " sc1");
                    }
            }
            if (XSYNC) { lds_arrive(smem + L_CNT + 96, l); lds_await(smem + L_CNT + 96, 4); } else WG_BARRIER();
            STAMP(3);
            f32x32 img; u32x16 meta, sums;
            const unsigned no = read_image(img, meta, sums, ima, cna);
            lds_arrive(smem + L_CNT + 84, l);
            if (__builtin_expect(no != 0, 0)) {
                const unsigned o = t == 0 ? WS_NOVF + pg * 4 : OOB;
                ST1(no, srd_ws, o, " sc1");
                VMWAIT(0);
                if (XSYNC) { lds_arrive(smem + L_CNT + 100, l); lds_await(smem + L_CNT + 100, 4); } else WG_BARRIER();
            }
            publish_image(img, meta, sums, srd_ws, ob);
            STAMP(4);
        } else {
            { const unsigned om = WS_KEY + b * 4; asm volatile("buffer_load_dword %0, %1, %2, 0 offen sc1\n\ts_waitcnt vmcnt(0)" : "=&v"(mykey) : "v"(om), "s"(srd_ws) : "memory"); }
            if (NP_SLEEP) __builtin_amdgcn_s_sleep(NP_SLEEP);
            const unsigned o = ((b >> 4) * 256 + t) * 128;
            set_m0(lds0 + L_DUMP + w * 256);
            STAMP(1);
            for (int r = 0; r < PF_R; ++r) {
                asm volatile("s_waitcnt vmcnt(%c0)" :: "i"(PF_D) : "memory");
                if (__builtin_amdgcn_s_memrealtime() > t_start + PF_T) break;
                const unsigned so = r << 19;
                asm volatile("buffer_load_dword %1, %0, %2 offen lds" :: "s"(srd_x0), "v"(o), "s"(so) : "memory");
            }
            if (PRIO) __builtin_amdgcn_s_setprio(2);
            STAMP(2); STAMP(3); STAMP(4);
        }
        STAMP(5);
        mykey = rfl(mykey) + KEY_STEP;
        loop_constants();
        clear_sums();
        unsigned ib = WS_INBOX + b * (G1 * 128) + t * 16, hb = WS_INBOX + b * (G1 * 128) + (8 * w + 7 + 32 * (l & 3)) * 128;
        PIN(ib); PIN(hb);
        while (__builtin_amdgcn_s_memrealtime() < t_start + POLL_T) __builtin_amdgcn_s_sleep(1);
        STAMP(6);
        u32x16 raw;
        inbox_request(raw, srd_ws, ib, hb, mykey);
        if (__builtin_expect(inbox_stage(raw, srd_ws, ib, mykey, 16 * pc - 8, lds0 + L_HDR + (t >> 3) * 4) != 0, 0)) {
            LAS unsigned* hp = (LAS unsigned*)(smem + L_HDR) + 32 * (l >> 3 & 3) + 8 * w + (l & 7);
            const unsigned hv = *hp;
            for (u64 m = __builtin_amdgcn_ballot_w64(l < 32 && hv > 4u * CAP1); m; m &= m - 1) {
                const int k = __builtin_ctzll(m), gp = 32 * (k >> 3) + 8 * w + (k & 7);
                unsigned u, no; const unsigned o = WS_OVF + (gp * CH + l) * 4, on = WS_NOVF + gp * 4;
                asm volatile("buffer_load_dword %0, %2, %4, 0 offen sc1\n\tbuffer_load_dword %1, %3, %4, 0 offen sc1\n\ts_waitcnt vmcnt(0)"
                             : "=&v"(u), "=&v"(no) : "v"(o), "v"(on), "s"(srd_ws) : "memory");
                if (rfl(no) <= 64) {
                    if (l < no && (u & 0xFF) == (unsigned)b) {
                        const unsigned e = __builtin_amdgcn_perm((u >> 16) + BIAS, u, 0x01050401u), key = (e >> 21) & 60;
                        *(LAS unsigned*)(smem + L_LIST + key * (SUBB / 4) + min(lds_add(smem + L_CNT + key, 4), SUBB - 4u)) = e;
                    }
                    if (l == k) *hp = hv | 0x80000000u;
                } else *(LAS unsigned*)(smem + L_CNT + 64) = 1;
            }
        }
        if (PRIO) __builtin_amdgcn_s_setprio(0);
        STAMP(7);
    } else {
        const int j = w - 4;
        STAMP(1);
        u32x4 wr[8], xr[8];
        {
            const u32x4 srd_q = make_srd(wq, 32768);
            const unsigned o = j * 8192 + l * 16, k4 = 4096;
            LD4(wr[0], srd_q, o, ""); LD4(wr[1], srd_q, o, " offset:1024"); LD4(wr[2], srd_q, o, " offset:2048"); LD4(wr[3], srd_q, o, " offset:3072");
            LD4S(wr[4], srd_q, o, k4, ""); LD4S(wr[5], srd_q, o, k4, " offset:1024"); LD4S(wr[6], srd_q, o, k4, " offset:2048"); LD4S(wr[7], srd_q, o, k4, " offset:3072");
#pragma unroll
            for (int m = 0; m < 8; ++m) { const unsigned ox = node_row(m < 4 ? j : w, m & 3) + n * 16; LD4(xr[m], srd_x0, ox, ""); }
        }
        unsigned aqa[8], xfa[4];
        unsigned xa = ring + g * 256 + ((n ^ g) << 4), wa = lds0 + L_WQ + j * 16384 + g * 256 + ((n ^ g) << 4) + ((n & 4) << 1);
        {
            const unsigned aq = lds0 + L_WQ + (8 * g + q) * 256 + ((((4 * p) ^ (q | ((g & 1) << 2))) << 4) | ((p & 1) << 3));
#pragma unroll
            for (int s = 0; s < 4; ++s) { xfa[s] = ring + n * 256 + (((4 * s + g) ^ n) << 4); PIN(xfa[s]); }
#pragma unroll
            for (int c = 0; c < 8; ++c) { aqa[c] = aq ^ ((c >> 1) << 4) ^ ((c & 1) << 3); PIN(aqa[c]); }
            PIN(xa); PIN(wa);
        }
        loop_constants();
        STAMP(2);
        if (isprod && !XSYNC) {
            WG_BARRIER();
            if (__builtin_expect(rfl(*(LAS unsigned*)(smem + L_CNT1 + 2048)) != 0, 0)) WG_BARRIER();
            if (W_SLEEP) __builtin_amdgcn_s_sleep(W_SLEEP);
        }
        STAMP(3);
        asm volatile("s_waitcnt vmcnt(%c8)" : "+v"(wr[0]), "+v"(wr[1]), "+v"(wr[2]), "+v"(wr[3]), "+v"(wr[4]), "+v"(wr[5]), "+v"(wr[6]), "+v"(wr[7]) : "i"(8) : "memory");
#pragma unroll
        for (int m = 0; m < 8; ++m) {
            const unsigned a = (wa ^ ((m & 2) << 5)) + m * 1024;
            *(LAS u32x2*)(size_t)a = u32x2{wr[m].x, wr[m].y};
            *(LAS u32x2*)(size_t)(a ^ 8) = u32x2{wr[m].z, wr[m].w};
        }
        asm volatile("s_waitcnt vmcnt(0)" : "+v"(xr[0]), "+v"(xr[1]), "+v"(xr[2]), "+v"(xr[3]), "+v"(xr[4]), "+v"(xr[5]), "+v"(xr[6]), "+v"(xr[7]) :: "memory");
#pragma unroll
        for (int m = 0; m < 8; ++m) *(LAS u32x4*)(size_t)((xa ^ ((m & 3) << 6)) + m * 1024) = xr[m];
        STAMP(4);
        lds_arrive(smem + L_CNT + 80, l);
        lds_await(smem + L_CNT + 80, 4);
        STAMP(5);
        bf16x8 xo[2][4], fr[2][8];
        f32x4 qc[2][8];
        auto wq_frags = [&](int u, bf16x8 (&f)[8]) {
#pragma unroll
            for (int a = 0; a < 8; ++a) {
                LAS char* pa = (LAS char*)(size_t)aqa[a] + u * 16384;
                f[a] = cat(tr_read(pa), tr_read(pa + 1024));
            }
        };
#pragma unroll
        for (int u = 0; u < 4; ++u) { xo[0][u] = *(LAS bf16x8*)(size_t)xfa[u]; xo[1][u] = *(LAS bf16x8*)(size_t)(xfa[u] + 4096); }
        wq_frags(0, fr[0]);
        {
            const Srd4 srd_v = make_srd4(wv, 32768);
            const unsigned r0 = (j * 32 + g) << 8, o0 = (n ^ ((g & 1) | ((g & 2) << 1))) << 4;
            const unsigned e[8] = {r0, r0 + 1024, r0 + 2048, r0 + 3072, r0 + 4096, r0 + 5120, r0 + 6144, r0 + 7168};
            const unsigned base = lds0 + (j < 2 ? L_WV0 + j * 16384 : L_WV1 + (j - 2) * 8192);
            const u32x4 off = {o0, o0 ^ 128, o0, o0 ^ 128};
            dma_half(base, srd_v, e, off); dma_half(base + 4096, srd_v, e + 4, off);
        }
#pragma unroll
        for (int u = 0; u < 4; ++u) {
            if (u < 3) wq_frags(u + 1, fr[(u + 1) & 1]);
            FENCE();
#pragma unroll
            for (int a = 0; a < 8; ++a)
#pragma unroll
                for (int tt = 0; tt < 2; ++tt) qc[tt][a] = mfma(fr[u & 1][a], xo[tt][u], u ? qc[tt][a] : f32x4{0.f, 0.f, 0.f, 0.f});
            FENCE();
            if (!WVLATE && u == 1 && j < 2) { VMWAIT(0); lds_arrive(smem + L_CNT + 88, l); }
        }
        STAMP(6);
        if (isprod) lds_await(smem + L_CNT + 84, 4);
#pragma unroll
        for (int tt = 0; tt < 2; ++tt)
#pragma unroll
            for (int s = 0; s < 4; ++s) {
                const float k = -0.08838834764831845f * 1.4426950408889634f;
                const f32x4 c0 = qc[tt][2 * s] * k, c1 = qc[tt][2 * s + 1] * k;
                *(LAS u32x4*)(size_t)(ring - (tt ? 0 : 65536) + s * 1024 + l * 16) = u32x4{pk2(c0[0], c0[1]), pk2(c0[2], c0[3]), pk2(c1[0], c1[1]), pk2(c1[2], c1[3])};
            }
        clear_sums();
        if (!WVLATE) { lds_await(smem + L_CNT + 88, 2); wv_resident(); }
        VMWAIT(0);
        STAMP(7);
    }
    WG_BARRIER();
    STAMP(8);
    if (LPRIO >= 3) __builtin_amdgcn_s_setprio(3);
    if (LPRIO == 1 && w >= 4) __builtin_amdgcn_s_setprio(1);
    if (LPRIO == 2) { if (w >= 6) __builtin_amdgcn_s_setprio(3); else if (w >= 4) __builtin_amdgcn_s_setprio(2); else if (w >= 2) __builtin_amdgcn_s_setprio(1); }

    u32x16 qf;
    const u32x4 doff = {addr[12], addr[13], addr[14], addr[15]};
    LAS char* mylist = smem + L_LIST + w * (2 * SUBB);
    int gcur = G1, pos = 0;
    unsigned hmask = 0xFFFFFFFFu;
    auto batch = [&](auto first_) {
        constexpr bool first = decltype(first_)::value;
        auto entries = [&](int grp, unsigned (&e)[8]) {
#pragma unroll
            for (int m = 0; m < 8; ++m) e[m] = *(LAS unsigned*)(size_t)(lista + grp * 128 + m * 16);
        };
        unsigned cw = *(LAS unsigned*)(size_t)(first ? cnta : lds0 + L_CNT + (__builtin_amdgcn_mbcnt_lo(~0u, 0u) & 31) * 4), e0[8], e1[8];
        entries(0, e0);
        if constexpr (first) {
#pragma unroll
            for (int s = 0; s < 4; ++s) {
                const u32x4 v = *(LAS u32x4*)(size_t)(qa + s * 1024);
                qf[4 * s] = v.x; qf[4 * s + 1] = v.y; qf[4 * s + 2] = v.z; qf[4 * s + 3] = v.w;
            }
        }
        entries(1, e1);
        FENCE();
        int lo = __builtin_amdgcn_readlane(cw, 2 * w) >> 2, hi = __builtin_amdgcn_readlane(cw, 2 * w + 1) >> 2;
        if constexpr (first) {
            if (__builtin_amdgcn_readlane(cw, 16)) gcur = 0;
            if (__builtin_amdgcn_ballot_w64(cw > 4u * CAP2)) { gcur = -1; lo = 0; hi = 0; hmask = 0x7FFFFFFFu; }
        }
        const int c = lo + hi, G = (c + 31) >> 5;
        const int nlast = G > 2 ? (((c - 1) & 31) >> 2) + 1 : 8;
        auto join = [&]() {
            for (int k = 0; k < G * 32 - lo; k += 64) {
                const unsigned e = *(LAS unsigned*)(mylist + SUBB + (l + k) * 4);
                if (l + k < G * 32 - lo) *(LAS unsigned*)(mylist + (lo + l + k) * 4) = l + k < hi ? e : PAD;
            }
        };
        const bool early = lo >= 64;
        if (__builtin_expect(!early, 0)) { join(); entries(0, e0); entries(1, e1); }
        if constexpr (first) asm volatile("s_waitcnt lgkmcnt(0)" : "+{v[160:175]}"(qf) :: "memory");
        if (G > 0) dma_group(ring, srd_x, e0, doff);
        if constexpr (first) {
            if (WVLATE) {
                if (G > 1 && w < 6) dma_group(ring + 8192, srd_x, e1, doff);
                wv_resident();
                lds_arrive(smem + L_CNT + 92, l);
                if (w >= 6) { lds_await(smem + L_CNT + 92, 8); if (G > 1) dma_group(ring + 8192, srd_x, e1, doff); }
            } else if (w < 4) {
                if (G > 1) dma_group(ring + 8192, srd_x, e1, doff);
                wv_resident();
                lds_arrive(smem + L_CNT + 92, l);
            } else {
                if (w >= 6) lds_await(smem + L_CNT + 92, 4);
                if (G > 1) dma_group(ring + 8192, srd_x, e1, doff);
            }
        } else if (G > 1) dma_group(ring + 8192, srd_x, e1, doff);
        f32x16 cc;
#pragma unroll
        for (int m = 0; m < 8; ++m) {
            cc[m] = (e0[m] & 0xFF) == keyn ? 0.f : __uint_as_float(inf);
            cc[8 + m] = (e1[m] & 0xFF) == keyn ? 0.f : __uint_as_float(inf);
        }
        if (early) join();
        STAMP(9);
        if (G > 0) gather_loop(acc, qf, addr, u32x4{lista, keyn, inf, 0u}, cc, srd_x, ring, G, nlast);
    };
    batch(std::true_type{});
    while (__builtin_expect(gcur < G1, 0)) {
        unsigned lim = G1 * 32;
        if (gcur >= 0) {
            const u64 m0 = __builtin_amdgcn_ballot_w64(l >= gcur && (int)(*(LAS unsigned*)(smem + L_HDR + l * 4) & hmask) > 4 * CAP1);
            const u64 m1 = __builtin_amdgcn_ballot_w64(l + 64 >= gcur && (int)(*(LAS unsigned*)(smem + L_HDR + 256 + l * 4) & hmask) > 4 * CAP1);
            if (!(m0 | m1)) break;
            gcur = m0 ? __builtin_ctzll(m0) : 64 + __builtin_ctzll(m1);
            unsigned v; const unsigned o = WS_NOVF + gcur * 4;
            asm volatile("buffer_load_dword %0, %1, %2, 0 offen sc1\n\ts_waitcnt vmcnt(0)" : "=&v"(v) : "v"(o), "s"(srd_ws) : "memory");
            lim = rfl(v);
        }
        const unsigned i = pos + t;
        const bool cand = t < CAP2 && i < lim;
        unsigned u;
        { const unsigned o = cand ? (gcur < 0 ? WS_INBOX + b * (G1 * 128) : WS_OVF + gcur * CH * 4) + i * 4 : OOB;
          asm volatile("buffer_load_dword %0, %1, %2, 0 offen sc1\n\ts_waitcnt vmcnt(0)" : "=&v"(u) : "v"(o), "s"(srd_ws) : "memory"); }
        WG_BARRIER();
        if (t < 16) *(LAS unsigned*)(smem + L_CNT + t * 4) = 0;
        WG_BARRIER();
        if (cand) {
            unsigned e; bool v;
            if (gcur < 0) { e = (i & 3) ? u : u ^ mykey; v = ((i & 31) - 2) * 4 < *(LAS unsigned*)(smem + L_HDR + (i >> 5) * 4); }
            else { e = (((u >> 16) + BIAS) << 8) | ((u >> 8) & 0xFF); v = (u & 0xFF) == (unsigned)b; }
            if (v) *(LAS unsigned*)(smem + L_LIST + (e & 7) * (2 * SUBB) + lds_add(smem + L_CNT + (e & 7) * 8, 4)) = e;
        }
        pos += CAP2;
        if ((unsigned)pos >= lim) { ++gcur; pos = 0; }
        WG_BARRIER();
        batch(std::false_type{});
    }
    STAMP(10);

    {
        bf16x8 wl[2][8], ag[4];
#pragma unroll
        for (int u = 0; u < 2; ++u)
#pragma unroll
            for (int a = 0; a < 8; ++a) {
                LAS char* pa = smem + L_WV1 + (cv ^ ((a >> 1) << 6) ^ ((a & 1) << 3)) + u * 8192;
                wl[u][a] = cat(tr_read(pa), tr_read(pa + 4096));
            }
#pragma unroll
        for (int u = 0; u < 4; ++u) {
            const u32x4 v = {pk2(acc[8 * u], acc[8 * u + 1]), pk2(acc[8 * u + 2], acc[8 * u + 3]),
                             pk2(acc[8 * u + 4], acc[8 * u + 5]), pk2(acc[8 * u + 6], acc[8 * u + 7])};
            ag[u] = __builtin_bit_cast(bf16x8, v);
        }
        FENCE();
        f32x4 oc[8];
#pragma unroll
        for (int a = 0; a < 8; ++a) oc[a] = mfma(__builtin_bit_cast(bf16x8, f32x4{wva[4 * a], wva[4 * a + 1], wva[4 * a + 2], wva[4 * a + 3]}), ag[0], f32x4{0.f, 0.f, 0.f, 0.f});
#pragma unroll
        for (int a = 0; a < 8; ++a) oc[a] = mfma(__builtin_bit_cast(bf16x8, f32x4{wvb[4 * a], wvb[4 * a + 1], wvb[4 * a + 2], wvb[4 * a + 3]}), ag[1], oc[a]);
#pragma unroll
        for (int a = 0; a < 8; ++a) oc[a] = mfma(wl[0][a], ag[2], oc[a]);
        FENCE();
        const unsigned ow = ring + n * 256 + ((g ^ n) << 4) + ((g & 1) << 3);
#pragma unroll
        for (int a = 0; a < 8; ++a) oc[a] = mfma(wl[1][a], ag[3], oc[a]);
#pragma unroll
        for (int a = 0; a < 8; ++a)
            *(LAS u32x2*)(size_t)(ow ^ ((a >> 1) << 6) ^ ((a & 1) << 3)) = u32x2{pk2(oc[a][0], oc[a][1]), pk2(oc[a][2], oc[a][3])};
        const auto rs_o = __builtin_amdgcn_make_buffer_rsrc(out, 0, xbytes, 0x00020000);
#pragma unroll
        for (int m = 0; m < 4; ++m) {
            const u32x4 v = *(LAS u32x4*)(size_t)(ring + m * 1024 + l * 16);
            __builtin_amdgcn_raw_buffer_store_b128(v, rs_o, node_row(w, m) + ((n ^ g ^ (4 * m)) << 4), 0, OUT_AUX);
        }
    }
    if (w == 0) { const unsigned o = l == 0 ? WS_KEY + b * 4 : OOB; ST1(mykey, srd_ws, o, " sc1"); }
    STAMP(11);
}

extern "C" void kernel_launch(void* const* d_in, const int* in_sizes, int n_in,
                              void* d_out, int out_size, void* d_ws, size_t ws_size,
                              hipStream_t stream) {
    const int N = in_sizes[0] / 128, E = in_sizes[1];
    k_graph<<<NB, 512, 0, stream>>>((const unsigned short*)d_in[0], (const int*)d_in[1], (const int*)d_in[2],
                                    (const unsigned short*)d_in[3], (const unsigned short*)d_in[4],
                                    (unsigned*)d_ws, (unsigned short*)d_out, N, E);
}
```

```cpp
#define STAMPS
#define PROBE_A 0
#define PROBE_B 13
#define PROBE_M 0
#include <hip/hip_runtime.h>
#include <hip/hip_bf16.h>
#include <stdint.h>
#include <type_traits>
#define ORD 1
#define SKIP 0
#define STR2(x) #x
#define STR(x) STR2(x)

typedef __attribute__((ext_vector_type(8))) __bf16 bf16x8;
typedef __attribute__((ext_vector_type(4))) __bf16 bf16x4;
typedef __attribute__((ext_vector_type(2))) __bf16 bf16x2;
typedef __attribute__((ext_vector_type(4))) float f32x4;
typedef __attribute__((ext_vector_type(2))) float f32x2;
typedef __attribute__((ext_vector_type(4))) unsigned u32x4;
typedef __attribute__((ext_vector_type(2))) unsigned u32x2;
typedef __attribute__((ext_vector_type(32))) float f32x32;
typedef __attribute__((ext_vector_type(16))) float f32x16;
typedef __attribute__((ext_vector_type(16))) unsigned u32x16;
typedef __attribute__((ext_vector_type(12))) unsigned u32x12;
typedef __attribute__((ext_vector_type(8))) unsigned u32x8;
typedef unsigned long long u64;
#define LAS __attribute__((address_space(3)))

constexpr int NB = 256;
constexpr int G1 = 128;
constexpr int CH = 3136;
constexpr int CAP1 = 30;
constexpr int CAP2 = 192;
constexpr int SUBB = 784;
#define POLL_AT 3
#define OUT_AUX 16
#define PF_R 0
#define PF_D 0
#define POLL_T 340
#define PF_T 200
#define LPRIO 3
#define PKADD 1
#define CODEPF 1
#define CODE_BYTES 21504
#define PARTNER 1
#define XSYNC 1
#define WVLATE 1
#define NP_SLEEP 0
#define W_SLEEP 3
#define PRIO 1
#define SPLIT 16384
constexpr unsigned KEY_STEP = 0x9E3779B1u;
constexpr unsigned WS_KEY = 0;
constexpr unsigned WS_NOVF = 2048;
constexpr unsigned WS_INBOX = 4096;
constexpr unsigned WS_OVF = WS_INBOX + NB * G1 * 128;
constexpr unsigned WS_BYTES = WS_OVF + G1 * CH * 4;
constexpr unsigned OOB = 0x80000000u;
constexpr unsigned BIAS = 32768 - SPLIT;
constexpr unsigned PAD = 0x00FFFFFFu;

constexpr int L_CNT = 0;
constexpr int L_HDR = 128;
constexpr int L_LIST = 768;
constexpr int L_RING = L_LIST + 16 * SUBB;
constexpr int L_IMG = L_RING;
constexpr int L_CNT1 = L_IMG + NB * 144;
constexpr int L_WQ = L_RING + 2 * 16384 + 8192;
constexpr int L_WV0 = L_RING + 6 * 16384 + 8192;
constexpr int L_WV1 = L_RING + 131072;
constexpr int L_DUMP = L_WV1 + 16384;
constexpr int L_TOTAL = L_DUMP + 2048;
static_assert(L_RING % 256 == 0 && L_TOTAL <= 163840, "LDS map");

#define STAMP_N 24
#define STAMP_WGS 256
#define STAMP_OFF (200u << 20)
#define STAMP_ROLE 0
#define STAMP_VALID(w, k) ((k) < 14 && ((STAMP_MASK >> (k)) & 1) && ((k) == 0 || STAMP_ROLE == 2 || ((((w) >> 3) & 1) == STAMP_ROLE)))
#define STAMP_DECL unsigned long long st_[STAMP_N] = {}
#define STAMP_MASK 0xFFFFFF
#define STAMP(k) do { if (!((STAMP_MASK >> (k)) & 1)) break; __builtin_amdgcn_sched_barrier(0); asm volatile("s_memrealtime %0\n\ts_waitcnt lgkmcnt(0)" : "=s"(st_[k]) :: "memory"); __builtin_amdgcn_sched_barrier(0); } while (0)
#define STAMP_THREAD 0
#define STAMP_FLUSH(base, k0, k1) do { if (threadIdx.x == STAMP_THREAD) { _Pragma("unroll") for (int k_ = k0; k_ < k1; ++k_) ((unsigned long long*)((char*)(base) + STAMP_OFF))[(size_t)blockIdx.x * STAMP_N + k_] = st_[k_]; } } while (0)

__device__ __forceinline__ u32x4 make_srd(const void* p, unsigned bytes, unsigned back = 0) {
    const u64 a = (u64)p - back;
    u32x4 s; s.x = (unsigned)a; s.y = (unsigned)(a >> 32); s.z = bytes + back; s.w = 0x00020000u; return s;
}
#define LD4(dst, srd, off, FL) asm volatile("buffer_load_dwordx4 %0, %1, %2, 0 offen" FL : "=v"(dst) : "v"(off), "s"(srd) : "memory")
#define LD4S(dst, srd, off, so, FL) asm volatile("buffer_load_dwordx4 %0, %1, %2, %3 offen" FL : "=v"(dst) : "v"(off), "s"(srd), "s"(so) : "memory")
#define LD1(dst, srd, off, FL) asm volatile("buffer_load_dword %0, %1, %2, 0 offen" FL : "=v"(dst) : "v"(off), "s"(srd) : "memory")
#define ST1(val, srd, off, FL) asm volatile("buffer_store_dword %0, %1, %2, 0 offen" FL :: "v"(val), "v"(off), "s"(srd) : "memory")
#define PIN(x) asm volatile("" : "+v"(x))
#define VMWAIT(n) asm volatile("s_waitcnt vmcnt(" #n ")" ::: "memory")
#define FENCE() __builtin_amdgcn_sched_barrier(0)
#define WG_BARRIER() do { asm volatile("s_waitcnt lgkmcnt(0)" ::: "memory"); __builtin_amdgcn_s_barrier(); asm volatile("" ::: "memory"); } while (0)

struct Srd4 { u32x4 s0, s1, s2, s3; };
__device__ __forceinline__ Srd4 make_srd4(const void* p, unsigned bytes, unsigned back = 0) {
    return Srd4{make_srd(p, bytes, back), make_srd(p, bytes, back + 1024), make_srd(p, bytes, back + 2048), make_srd(p, bytes, back + 3072)};
}

__device__ __forceinline__ void set_m0(unsigned v) { asm volatile("s_mov_b32 m0, %0" :: "s"(v) : "memory"); }
template <int R> __device__ __forceinline__ void dma_rows(const Srd4& d, unsigned e, unsigned off) {
    unsigned tmp, mask = 0x00FFFF00u;
    asm volatile("v_and_or_b32 %0, %1, %2, %3\n\tbuffer_load_dwordx4 %0, %4, 0 offen offset:%c5 lds"
                 : "=&v"(tmp) : "v"(e), "s"(mask), "v"(off), "s"(R == 0 ? d.s0 : R == 1 ? d.s1 : R == 2 ? d.s2 : d.s3), "i"(R * 1024) : "memory");
}
__device__ __forceinline__ void dma_half(unsigned ldsbase, const Srd4& d, const unsigned* e, const u32x4& off) {
    set_m0(ldsbase);
    dma_rows<0>(d, e[0], off.x); dma_rows<1>(d, e[1], off.y); dma_rows<2>(d, e[2], off.z); dma_rows<3>(d, e[3], off.w);
}
__device__ __forceinline__ void dma_group(unsigned ldsbase, const Srd4& d, const unsigned (&e)[8], const u32x4& off) {
    asm volatile("s_waitcnt lgkmcnt(0)" ::: "memory");
    dma_half(ldsbase, d, e, off); dma_half(ldsbase + 4096, d, e + 4, off);
}

__device__ __forceinline__ void gather_loop(f32x32& acc, const u32x16& qf, const u32x16& addr, u32x4 misc, f32x16 cc,
                                            const Srd4& d, unsigned ring, int G, int nlast) {
    f32x32 t0, t1; u32x12 t2; int tmp, pr; u64 sm; unsigned mask = 0x00FFFF00u;
    asm volatile(
        ".macro GWAIT k\n\ts_cmp_gt_i32 %[t], \\k\n\ts_cbranch_scc1 1f\n\ts_waitcnt vmcnt(\\k)\n\t.endm\n\t"
        ".macro GROWS so\n\t.irp s, 0, 1, 2, 3\n\t"
        "ds_read_b128 v[64+8*\\s:67+8*\\s], v[176+\\s] offset:\\so\n\t"
        "ds_read_b128 v[68+8*\\s:71+8*\\s], v[176+\\s] offset:\\so+4096\n\t.endr\n\t.endm\n\t"
        ".macro GTR c, so\n\t"
        "ds_read_b64_tr_b16 v[96+4*\\c:97+4*\\c], v[180+\\c] offset:\\so\n\t"
        "ds_read_b64_tr_b16 v[98+4*\\c:99+4*\\c], v[180+\\c] offset:\\so+4096\n\t.endm\n\t"
        ".macro GS s, c\n\t"
        "v_mfma_f32_16x16x32_bf16 v[\\c:\\c+3], v[64+8*\\s:67+8*\\s], v[160+4*\\s:163+4*\\s], v[\\c:\\c+3]\n\t"
        "v_mfma_f32_16x16x32_bf16 v[\\c+4:\\c+7], v[68+8*\\s:71+8*\\s], v[160+4*\\s:163+4*\\s], v[\\c+4:\\c+7]\n\t.endm\n\t"
        ".macro GLOAD m, r, srd\n\t"
        "v_and_or_b32 v195, v[212+\\m], %[mask], v[188+\\r]\n\t"
        "buffer_load_dwordx4 v195, \\srd, 0 offen offset:1024*\\r lds\n\t.endm\n\t"
        ".macro GPART m, r, srd\n\ts_cmp_le_i32 %[nl], \\m\n\ts_cbranch_scc1 4f\n\tGLOAD \\m, \\r, \\srd\n\t.endm\n\t"
        ".macro GSUM m\n\tv_mfma_f32_16x16x32_bf16 v[128+4*(\\m):131+4*(\\m)], v[96+4*(\\m):99+4*(\\m)], v[220:223], v[128+4*(\\m):131+4*(\\m)]\n\t.endm\n\t"
        ".macro GCMP m, d\n\tv_cmp_ne_u32_sdwa \\d, v[212+\\m], v193 src0_sel:BYTE_0 src1_sel:DWORD\n\t.endm\n\t"
        ".macro GPAIR m, c\n\t"
        "GSUM \\m\n\tGCMP \\m+1, %[sm]\n\tv_cndmask_b32_e32 v[\\c+\\m], 0, v194, vcc\n\t"
        "GSUM \\m+1\n\t.if \\m < 6\n\tGCMP \\m+2, vcc\n\t.else\n\ts_cmp_gt_i32 %[rem], 0\n\t.endif\n\t"
        "v_cndmask_b32_e64 v[\\c+\\m+1], 0, v194, %[sm]\n\t.endm\n\t"
        ".macro GSTEP so, c, eo\n\t"
        "GTR 0, \\so\n\t"
        "s_waitcnt lgkmcnt(2)\n\tGS 0, \\c\n\t"
        "GTR 1, \\so\n\tGTR 2, \\so\n\tGS 1, \\c\n\t"
        "GTR 3, \\so\n\tGTR 4, \\so\n\tGS 2, \\c\n\t"
        "GTR 5, \\so\n\tGTR 6, \\so\n\tGS 3, \\c\n\t"
        "GTR 7, \\so\n\t"
        "ds_read2_b32 v[212:213], v192 offset0:\\eo offset1:\\eo+4\n\t"
        "ds_read2_b32 v[214:215], v192 offset0:\\eo+8 offset1:\\eo+12\n\t"
        "ds_read2_b32 v[216:217], v192 offset0:\\eo+16 offset1:\\eo+20\n\t"
        "ds_read2_b32 v[218:219], v192 offset0:\\eo+24 offset1:\\eo+28\n\t"
        "s_add_u32 m0, %[ring], \\so\n\t"
        "s_cmp_gt_i32 %[rem], 3\n\t"
        ".irp k, 0, 1, 2, 3, 4, 5, 6, 7\n\tv_exp_f32_e32 v[\\c+\\k], v[\\c+\\k]\n\t.endr\n\t"
        "s_waitcnt lgkmcnt(0)\n\t"
        "s_cbranch_scc0 2f\n\t"
        "GLOAD 0, 0, %[s0]\n\tGLOAD 1, 1, %[s1]\n\tGLOAD 2, 2, %[s2]\n\tGLOAD 3, 3, %[s3]\n\t"
        "s_add_u32 m0, m0, 0x1000\n\t"
        "GLOAD 4, 0, %[s0]\n\tGLOAD 5, 1, %[s1]\n\tGLOAD 6, 2, %[s2]\n\tGLOAD 7, 3, %[s3]\n\t"
        "GSIG \\c\n\t"
        "s_waitcnt vmcnt(8)\n\t"
        "s_branch 1f\n\t"
        "2:\n\t"
        "s_mov_b32 %[t], 0\n\t"
        "s_cmp_eq_u32 %[rem], 3\n\ts_cbranch_scc0 4f\n\t"
        "s_mov_b32 %[t], %[nl]\n\t"
        "GLOAD 0, 0, %[s0]\n\tGPART 1, 1, %[s1]\n\tGPART 2, 2, %[s2]\n\tGPART 3, 3, %[s3]\n\t"
        "s_add_u32 m0, m0, 0x1000\n\t"
        "GPART 4, 0, %[s0]\n\tGPART 5, 1, %[s1]\n\tGPART 6, 2, %[s2]\n\tGPART 7, 3, %[s3]\n\t"
        "4:\n\t"
        "GSIG \\c\n\t"
        "s_waitcnt vmcnt(8)\n\t"
        "GWAIT 7\n\tGWAIT 6\n\tGWAIT 5\n\tGWAIT 4\n\tGWAIT 3\n\tGWAIT 2\n\tGWAIT 1\n\tGWAIT 0\n\t"
        "1:\n\t"
        "GROWS 8192-\\so\n\t"
        "GCMP 0, vcc\n\t"
        "s_add_i32 %[rem], %[rem], -1\n\t"
        "GPAIR 0, \\c\n\tGPAIR 2, \\c\n\tGPAIR 4, \\c\n\tGPAIR 6, \\c\n\t"
        ".endm\n\t"
        ".macro GSIG c\n\t"
        ".if " STR(PKADD) "\n\t.irp k, 0, 2, 4, 6\n\tv_pk_add_f32 v[\\c+\\k:\\c+\\k+1], v[\\c+\\k:\\c+\\k+1], 1.0 op_sel_hi:[1,0]\n\t.endr\n\t.else\n\t"
        ".irp k, 0, 1, 2, 3, 4, 5, 6, 7\n\tv_add_f32_e32 v[\\c+\\k], 1.0, v[\\c+\\k]\n\t.endr\n\t.endif\n\t"
        ".irp k, 0, 1, 2, 3, 4, 5, 6, 7\n\tv_rcp_f32_e32 v[\\c+\\k], v[\\c+\\k]\n\t.endr\n\t"
        ".irp k, 0, 1, 2, 3\n\tv_cvt_pk_bf16_f32 v[220+\\k], v[\\c+2*\\k], v[\\c+2*\\k+1]\n\t.endr\n\t.endm\n\t"
        "s_waitcnt vmcnt(8)\n\t"
        "s_cmp_gt_i32 %[rem], 1\n\ts_cbranch_scc1 1f\n\ts_waitcnt vmcnt(0)\n\t1:\n\t"
        "GROWS 0\n\t"
        ".if " STR(LPRIO) " >= 3\n\ts_mov_b32 %[pr], 0\n\ts_setprio 2\n\t.endif\n\t"
        "0:\n\t"
        ".if " STR(LPRIO) " == 3\n\t"
        "s_cmp_eq_u32 %[pr], 1\n\ts_cbranch_scc0 5f\n\ts_setprio 1\n\t5:\n\t"
        "s_cmp_eq_u32 %[pr], 2\n\ts_cbranch_scc0 5f\n\ts_setprio 0\n\t5:\n\t"
        "s_add_u32 %[pr], %[pr], 1\n\t.endif\n\t"
        "GSTEP 0, 196, 64\n\t"
        "s_cbranch_scc0 9f\n\t"
        "GSTEP 8192, 204, 96\n\t"
        "v_add_u32_e32 v192, 0x100, v192\n\t"
        "s_cbranch_scc1 0b\n\t"
        "9:\n\t"
        "s_waitcnt lgkmcnt(0)\n\ts_nop 6\n\t"
        ".purgem GWAIT\n\t.purgem GROWS\n\t.purgem GTR\n\t.purgem GS\n\t.purgem GLOAD\n\t.purgem GPART\n\t.purgem GSUM\n\t.purgem GCMP\n\t.purgem GPAIR\n\t.purgem GSTEP\n\t.purgem GSIG"
        : "+{v[128:159]}"(acc), "+{v[192:195]}"(misc), "+{v[196:211]}"(cc), "=&{v[64:95]}"(t0), "=&{v[96:127]}"(t1), "=&{v[212:223]}"(t2),
          [rem] "+s"(G), [t] "=&s"(tmp), [pr] "=&s"(pr), [sm] "=&s"(sm)
        : "{v[160:175]}"(qf), "{v[176:191]}"(addr), [s0] "s"(d.s0), [s1] "s"(d.s1), [s2] "s"(d.s2), [s3] "s"(d.s3),
          [mask] "s"(mask), [ring] "s"(ring), [nl] "s"(nlast)
        : "vcc", "scc", "memory");
}

__device__ __forceinline__ u64 rank_edges(u32x16& ed, u32x16& es, u32x16& rk, u64 m0, u64 m1, u64 m2, u64 m3, unsigned keya) {
    u32x16 b4; u64 over; unsigned sel = 0x01050401u, c36 = 36;
    asm volatile(
        ".macro RADD k\n\tv_lshlrev_b32_sdwa v[96+\\k], 2, v[64+\\k] dst_sel:DWORD dst_unused:UNUSED_PAD src0_sel:DWORD src1_sel:BYTE_0\n\t"
        "ds_add_rtn_u32 v[112+\\k], v[96+\\k], v111 offset:%c[cnt]\n\t.endm\n\t"
        ".macro RENT k\n\tv_add_u32_e32 v[80+\\k], %c[bias], v[80+\\k]\n\tv_perm_b32 v[80+\\k], v[80+\\k], v[64+\\k], %[sel]\n\t.endm\n\t"
        ".macro RPUT k\n\tv_min_u32_e32 v110, %c[cap], v[112+\\k]\n\tv_mad_u32_u24 v110, v[96+\\k], %[c36], v110\n\t"
        "ds_write_b32 v110, v[80+\\k] offset:%c[img]\n\t.endm\n\t"
        "v_mov_b32_e32 v111, 4\n\t"
        ".irp k, 0, 2, 4, 6, 8, 10, 12\n\tv_mov_b64_e32 v[112+\\k:113+\\k], 0\n\t.endr\n\t"
        "s_mov_b64 exec, %[m0]\n\ts_waitcnt vmcnt(9)\n\tRADD 0\n\tRADD 1\n\tRADD 2\n\tRADD 3\n\t"
        "s_mov_b64 exec, %[m1]\n\ts_waitcnt vmcnt(8)\n\tRADD 4\n\tRADD 5\n\tRADD 6\n\tRADD 7\n\t"
        "s_mov_b64 exec, %[m2]\n\ts_waitcnt vmcnt(7)\n\tRADD 8\n\tRADD 9\n\tRADD 10\n\tRADD 11\n\t"
        "s_mov_b64 exec, %[m3]\n\ts_waitcnt vmcnt(6)\n\tRADD 12\n\t"
        "s_mov_b64 exec, %[m0]\n\ts_waitcnt vmcnt(5)\n\tRENT 0\n\tRENT 1\n\tRENT 2\n\tRENT 3\n\t"
        "s_waitcnt lgkmcnt(9)\n\tRPUT 0\n\tRPUT 1\n\tRPUT 2\n\tRPUT 3\n\t"
        "s_mov_b64 exec, %[m1]\n\ts_waitcnt vmcnt(4)\n\tRENT 4\n\tRENT 5\n\tRENT 6\n\tRENT 7\n\t"
        "s_waitcnt lgkmcnt(9)\n\tRPUT 4\n\tRPUT 5\n\tRPUT 6\n\tRPUT 7\n\t"
        "s_mov_b64 exec, %[m2]\n\ts_waitcnt vmcnt(3)\n\tRENT 8\n\tRENT 9\n\tRENT 10\n\tRENT 11\n\t"
        "s_waitcnt lgkmcnt(9)\n\tRPUT 8\n\tRPUT 9\n\tRPUT 10\n\tRPUT 11\n\t"
        "s_mov_b64 exec, %[m3]\n\ts_waitcnt vmcnt(0)\n\tRENT 12\n\t"
        "s_waitcnt lgkmcnt(12)\n\tRPUT 12\n\t"
        "s_mov_b64 exec, -1\n\t"
        "v_add_u32_e32 v93, %c[step], v93\n\t"
        "v_max3_u32 v110, v112, v113, v114\n\tv_max3_u32 v109, v115, v116, v117\n\t"
        "ds_write_b32 %[keya], v93 offset:%c[cnt]+1024\n\t"
        "v_max3_u32 v110, v110, v118, v119\n\tv_max3_u32 v109, v109, v120, v121\n\t"
        "v_max3_u32 v110, v110, v122, v123\n\tv_max3_u32 v110, v110, v109, v124\n\t"
        "v_cmp_le_u32_e32 vcc, %c[cap], v110\n\ts_mov_b64 %[over], vcc\n\t"
        ".purgem RADD\n\t.purgem RENT\n\t.purgem RPUT"
        : "+{v[64:79]}"(ed), "+{v[80:95]}"(es), "=&{v[96:111]}"(b4), "=&{v[112:127]}"(rk), [over] "=&s"(over)
        : [m0] "s"(m0), [m1] "s"(m1), [m2] "s"(m2), [m3] "s"(m3), [keya] "v"(keya), [sel] "s"(sel), [c36] "s"(c36),
          [cnt] "i"(L_CNT1), [img] "i"(L_IMG + 8), [bias] "i"(BIAS), [cap] "i"(4 * CAP1), [step] "i"(KEY_STEP)
        : "vcc", "memory");
    return over;
}
__device__ __forceinline__ unsigned read_image(f32x32& img, u32x16& meta, u32x16& sums, unsigned ima, unsigned cna) {
    unsigned no;
    asm volatile(
        "v_mov_b32_e32 v121, 0\n\tds_read_b32 v120, v121 offset:%c[cnt]+2048\n\t"
        ".irp k, 0, 1, 2, 3, 4, 5, 6, 7\n\tds_read_b128 v[64+4*\\k:67+4*\\k], %[ima] offset:4608*\\k\n\t.endr\n\t"
        ".irp k, 0, 1, 2, 3\n\tds_read2_b32 v[96+2*\\k:97+2*\\k], %[cna] offset0:64*\\k offset1:64*\\k+32\n\t.endr\n\t"
        "s_waitcnt lgkmcnt(11)\n\tv_readfirstlane_b32 %[no], v120\n\t"
        "v_add_u32_e32 v121, 0x400, %[cna]\n\t"
        ".irp k, 0, 1, 2, 3\n\tds_read2_b32 v[104+2*\\k:105+2*\\k], v121 offset0:64*\\k offset1:64*\\k+32\n\t.endr\n\t"
        "s_waitcnt lgkmcnt(12)\n\t"
        ".irp k, 0, 1, 2, 3\n\tv_add3_u32 v[112+\\k], v[64+4*\\k], v[65+4*\\k], v[66+4*\\k]\n\tv_add_u32_e32 v[112+\\k], v[112+\\k], v[67+4*\\k]\n\t.endr\n\t"
        "s_waitcnt lgkmcnt(8)\n\t"
        ".irp k, 4, 5, 6, 7\n\tv_add3_u32 v[112+\\k], v[64+4*\\k], v[65+4*\\k], v[66+4*\\k]\n\tv_add_u32_e32 v[112+\\k], v[112+\\k], v[67+4*\\k]\n\t.endr\n\t"
        ".irp k, 0, 1, 2, 3, 4, 5, 6, 7\n\tv_add_u32_dpp v[112+\\k], v[112+\\k], v[112+\\k] quad_perm:[1,0,3,2] row_mask:0xf bank_mask:0xf\n\t.endr\n\t"
        ".irp k, 0, 1, 2, 3, 4, 5, 6, 7\n\tv_add_u32_dpp v[112+\\k], v[112+\\k], v[112+\\k] quad_perm:[2,3,0,1] row_mask:0xf bank_mask:0xf\n\t.endr\n\t"
        ".irp k, 0, 1, 2, 3, 4, 5, 6, 7\n\tv_add_u32_dpp v[112+\\k], v[112+\\k], v[112+\\k] row_half_mirror row_mask:0xf bank_mask:0xf\n\t.endr\n\t"
        "s_waitcnt lgkmcnt(0)"
        : "=&{v[64:95]}"(img), "=&{v[96:111]}"(meta), "=&{v[112:127]}"(sums), [no] "=&s"(no)
        : [ima] "v"(ima), [cna] "v"(cna), [cnt] "i"(L_CNT1) : "memory");
    return no;
}
__device__ __forceinline__ void publish_image(f32x32 img, u32x16 meta, u32x16 sums, const u32x4& srd, unsigned ob) {
    u64 pc0 = 0x0101010101010101ull; unsigned so;
    asm volatile(
        ".macro PUB k\n\ts_mov_b64 exec, %[pc0]\n\tv_mov_b32_e32 v[64+4*\\k], v[96+\\k]\n\tv_mov_b32_e32 v[65+4*\\k], v[112+\\k]\n\t"
        "s_mov_b64 exec, -1\n\tv_xor_b32_e32 v[64+4*\\k], v[64+4*\\k], v[104+\\k]\n\t"
        "buffer_store_dwordx4 v[64+4*\\k:67+4*\\k], %[ob], %[srd], %[so] offen sc1\n\ts_add_u32 %[so], %[so], %c[step]\n\t.endm\n\t"
        "s_mov_b32 %[so], 0\n\t"
        ".irp k, 0, 1, 2, 3, 4, 5, 6, 7\n\tPUB \\k\n\t.endr\n\t.purgem PUB"
        : "+{v[64:95]}"(img), [so] "=&s"(so)
        : "{v[96:111]}"(meta), "{v[112:127]}"(sums), [srd] "s"(srd), [ob] "v"(ob), [pc0] "s"(pc0), [step] "i"(32 * G1 * 128) : "scc", "memory");
}

__device__ __forceinline__ void inbox_request(u32x16& raw, const u32x4& srd, unsigned ib) {
    unsigned o1 = 0x1000, o2 = 0x2000, o3 = 0x3000;
    asm volatile("buffer_load_dwordx4 v[64:67], %1, %2, 0 offen sc1\n\tbuffer_load_dwordx4 v[68:71], %1, %2, %3 offen sc1\n\t"
                 "buffer_load_dwordx4 v[72:75], %1, %2, %4 offen sc1\n\tbuffer_load_dwordx4 v[76:79], %1, %2, %5 offen sc1"
                 : "=&{v[64:79]}"(raw) : "v"(ib), "s"(srd), "s"(o1), "s"(o2), "s"(o3) : "memory");
}
__device__ __forceinline__ unsigned inbox_stage(u32x16 raw, const u32x4& srd, unsigned ib, unsigned key, int i4, unsigned hdra) {
    u32x16 t0; u32x4 t1; u64 m0, m1, m2, m3; unsigned todo, fresh, spin, o1, o2, o3, over;
    u64 pc0 = 0x0101010101010101ull;
    asm volatile(
        ".macro IVAL j, so\n\t"
        "s_bitcmp1_b32 %[todo], \\j\n\ts_cbranch_scc0 2f\n\t"
        "v_xor_b32_e32 v[64+4*\\j], %[key], v[64+4*\\j]\n\t"
        "v_sub_u32_e32 v97, 0, v[65+4*\\j]\n\t"
        "v_add_u32_e32 v96, v[64+4*\\j], v[65+4*\\j]\n\t"
        "v_mov_b32_dpp v[80+\\j], v[64+4*\\j] quad_perm:[0,0,0,0] row_mask:0xf bank_mask:0xf\n\t"
        "v_cndmask_b32_e64 v96, v96, v97, %[pc0]\n\t"
        "s_nop 0\n\t"
        "v_mov_b32_dpp v[80+\\j], v[80+\\j] row_shr:4 row_mask:0xf bank_mask:0xa\n\t"
        "v_cmp_gt_i32_e32 vcc, v[80+\\j], v92\n\t"
        "v_add3_u32 v96, v96, v[66+4*\\j], v[67+4*\\j]\n\t"
        "v_lshrrev_b32_e32 v97, 14, v[80+\\j]\n\t"
        "v_cndmask_b32_e32 v96, 0, v96, vcc\n\t"
        "s_nop 1\n\tv_add_u32_dpp v96, v96, v96 quad_perm:[1,0,3,2] row_mask:0xf bank_mask:0xf\n\t"
        "s_nop 1\n\tv_add_u32_dpp v96, v96, v96 quad_perm:[2,3,0,1] row_mask:0xf bank_mask:0xf\n\t"
        "s_nop 1\n\tv_add_u32_dpp v96, v96, v96 row_half_mirror row_mask:0xf bank_mask:0xf\n\t"
        "v_or_b32_e32 v96, v96, v97\n\t"
        "v_cmp_ne_u32_e32 vcc, 0, v96\n\t"
        "s_cbranch_vccz 1f\n\t"
        "buffer_load_dwordx4 v[64+4*\\j:67+4*\\j], %[ib], %[srd], \\so offen sc1\n\t"
        "s_branch 2f\n\t"
        "1:\n\ts_bitset1_b32 %[fresh], \\j\n\t"
        "2:\n\t.endm\n\t"
        ".macro IRANK j\n\t"
        "s_bitcmp1_b32 %[fresh], \\j\n\ts_cbranch_scc0 2f\n\t"
        "v_cmp_lt_u32_e64 %[m0], v92, v[80+\\j]\n\tv_cmp_lt_u32_e64 %[m1], v93, v[80+\\j]\n\t"
        "v_cmp_lt_u32_e64 %[m2], v94, v[80+\\j]\n\tv_cmp_lt_u32_e64 %[m3], v95, v[80+\\j]\n\t"
        ".irp k, 0, 1, 2, 3\n\tv_lshrrev_b32_e32 v[84+\\k], 21, v[64+4*\\j+\\k]\n\t.endr\n\t"
        ".irp k, 0, 1, 2, 3\n\tv_and_b32_e32 v[84+\\k], 60, v[84+\\k]\n\t.endr\n\t"
        "v_cmp_lt_u32_e32 vcc, %c[cap], v[80+\\j]\n\t"
        "s_mov_b64 exec, %[m0]\n\tds_add_rtn_u32 v88, v84, v98 offset:%c[cnt]\n\t"
        "s_mov_b64 exec, %[m1]\n\tds_add_rtn_u32 v89, v85, v98 offset:%c[cnt]\n\t"
        "s_mov_b64 exec, %[m2]\n\tds_add_rtn_u32 v90, v86, v98 offset:%c[cnt]\n\t"
        "s_mov_b64 exec, %[m3]\n\tds_add_rtn_u32 v91, v87, v98 offset:%c[cnt]\n\t"
        "s_mov_b64 exec, %[pc0]\n\tds_write_b32 %[hdra], v[80+\\j] offset:128*\\j\n\t"
        "s_mov_b64 exec, -1\n\t"
        "s_cbranch_vccz 1f\n\t"
        "s_mov_b32 %[over], 1\n\t"
        "1:\n\ts_waitcnt lgkmcnt(1)\n\t"
        ".irp k, 0, 1, 2, 3\n\tv_min_u32_e32 v[88+\\k], %c[sub]-4, v[88+\\k]\n\t.endr\n\t"
        ".irp k, 0, 1, 2, 3\n\tv_mad_u32_u24 v[88+\\k], v[84+\\k], %[sub4], v[88+\\k]\n\t.endr\n\t"
        "s_mov_b64 exec, %[m0]\n\tds_write_b32 v88, v[64+4*\\j] offset:%c[list]\n\t"
        "s_mov_b64 exec, %[m1]\n\tds_write_b32 v89, v[65+4*\\j] offset:%c[list]\n\t"
        "s_mov_b64 exec, %[m2]\n\tds_write_b32 v90, v[66+4*\\j] offset:%c[list]\n\t"
        "s_mov_b64 exec, %[m3]\n\tds_write_b32 v91, v[67+4*\\j] offset:%c[list]\n\t"
        "s_mov_b64 exec, -1\n\t"
        "2:\n\t.endm\n\t"
        "s_movk_i32 %[o1], 0x1000\n\ts_movk_i32 %[o2], 0x2000\n\ts_movk_i32 %[o3], 0x3000\n\t"
        "v_mov_b32_e32 v92, %[i4]\n\tv_add_u32_e32 v93, 4, %[i4]\n\tv_add_u32_e32 v94, 8, %[i4]\n\tv_add_u32_e32 v95, 12, %[i4]\n\t"
        "v_mov_b32_e32 v98, 4\n\t"
        "s_mov_b32 %[todo], 15\n\ts_mov_b32 %[spin], 0x400000\n\ts_mov_b32 %[over], 0\n\t"
        "0:\n\t"
        "s_mov_b32 %[fresh], 0\n\t"
        "s_waitcnt vmcnt(3)\n\ts_cmp_eq_u32 %[todo], 15\n\ts_cbranch_scc1 3f\n\ts_waitcnt vmcnt(0)\n\t3:\n\t"
        "IVAL 0, 0\n\tIRANK 0\n\t"
        "s_waitcnt vmcnt(2)\n\tIVAL 1, %[o1]\n\tIRANK 1\n\t"
        "s_waitcnt vmcnt(1)\n\tIVAL 2, %[o2]\n\tIRANK 2\n\t"
        "s_cmp_eq_u32 %[todo], 15\n\ts_cbranch_scc0 3f\n\ts_waitcnt vmcnt(0)\n\t3:\n\tIVAL 3, %[o3]\n\tIRANK 3\n\t"
        "s_andn2_b32 %[todo], %[todo], %[fresh]\n\t"
        "s_cbranch_scc0 9f\n\t"
        "s_sub_u32 %[spin], %[spin], 1\n\t"
        "s_cbranch_scc0 0b\n\t"
        "s_waitcnt vmcnt(0)\n\t"
        "9:\n\t"
        ".purgem IVAL\n\t.purgem IRANK"
        : "+{v[64:79]}"(raw), "=&{v[80:95]}"(t0), "=&{v[96:99]}"(t1), [m0] "=&s"(m0), [m1] "=&s"(m1), [m2] "=&s"(m2), [m3] "=&s"(m3),
          [todo] "=&s"(todo), [fresh] "=&s"(fresh), [spin] "=&s"(spin), [o1] "=&s"(o1), [o2] "=&s"(o2), [o3] "=&s"(o3), [over] "=&s"(over)
        : [srd] "s"(srd), [ib] "v"(ib), [key] "s"(key), [i4] "v"(i4), [hdra] "v"(hdra), [pc0] "s"(pc0), [sub4] "s"(SUBB / 4),
          [cnt] "i"(L_CNT), [list] "i"(L_LIST), [sub] "i"(SUBB), [cap] "i"(4 * CAP1)
        : "vcc", "scc", "memory");
    return over;
}

template <int CTRL, int BANK> __device__ __forceinline__ unsigned dpp(unsigned old, unsigned v) { return __builtin_amdgcn_update_dpp(old, v, CTRL, 0xF, BANK, true); }
__device__ __forceinline__ unsigned bcast8(unsigned v) { v = dpp<0x00, 0xF>(0u, v); return __builtin_amdgcn_update_dpp(v, v, 0x114, 0xF, 0xA, false); }
__device__ __forceinline__ unsigned sum8(unsigned v) { v += dpp<0xB1, 0xF>(0u, v); v += dpp<0x4E, 0xF>(0u, v); return v + dpp<0x141, 0xF>(0u, v); }
__device__ __forceinline__ bf16x4 tr_read(LAS char* p) { return __builtin_amdgcn_ds_read_tr16_b64_v4bf16((LAS bf16x4*)p); }
__device__ __forceinline__ bf16x8 cat(bf16x4 a, bf16x4 b) { return __builtin_shufflevector(a, b, 0, 1, 2, 3, 4, 5, 6, 7); }
__device__ __forceinline__ unsigned pk2(float a, float b) { f32x2 v = {a, b}; bf16x2 r = __builtin_convertvector(v, bf16x2); return __builtin_bit_cast(unsigned, r); }
__device__ __forceinline__ f32x4 mfma(bf16x8 a, bf16x8 b, f32x4 c) { return __builtin_amdgcn_mfma_f32_16x16x32_bf16(a, b, c, 0, 0, 0); }
__device__ __forceinline__ unsigned lds_add(LAS char* p, unsigned v) { return __hip_atomic_fetch_add((LAS unsigned*)p, v, __ATOMIC_RELAXED, __HIP_MEMORY_SCOPE_WORKGROUP); }
__device__ __forceinline__ LAS unsigned* ldsp(unsigned a) { return (LAS unsigned*)(size_t)a; }
__device__ __forceinline__ unsigned byte0_x4(unsigned v) { unsigned r; asm("v_lshlrev_b32_sdwa %0, 2, %1 dst_sel:DWORD dst_unused:UNUSED_PAD src0_sel:DWORD src1_sel:BYTE_0" : "=v"(r) : "v"(v)); return r; }
__device__ __forceinline__ unsigned mad24(unsigned a, unsigned b, unsigned c) { unsigned r; asm("v_mad_u32_u24 %0, %1, %2, %3" : "=v"(r) : "v"(a), "s"(b), "v"(c)); return r; }
__device__ __forceinline__ void lds_arrive(LAS char* p, int lane) {
    asm volatile("s_waitcnt lgkmcnt(0)" ::: "memory");
    if (lane == 0) __hip_atomic_fetch_add((LAS unsigned*)p, 1u, __ATOMIC_RELAXED, __HIP_MEMORY_SCOPE_WORKGROUP);
}
__device__ __forceinline__ void lds_await(LAS char* p, unsigned n) {
    while (__builtin_amdgcn_readfirstlane(*(volatile LAS unsigned*)p) < n) __builtin_amdgcn_s_sleep(1);
    asm volatile("" ::: "memory");
}
__device__ __forceinline__ unsigned rfl(unsigned v) { return __builtin_amdgcn_readfirstlane(v); }

__global__ __launch_bounds__(512, 2) void k_graph(const unsigned short* x_, const int* src_, const int* dst_, const unsigned short* wq_, const unsigned short* wv_,
                                                  unsigned* ws_, unsigned short* out_, int N_, int E_) {
    u32x4 srd_d, srd_s, srd_ws, srd_x0, pw, po;
    u64 t_start;
    asm volatile("s_load_dwordx2 s[40:41], %[ka], 0x10\n\ts_load_dwordx2 s[44:45], %[ka], 0x8\n\ts_load_dwordx4 s[60:63], %[ka], 0x30\n\t"
                 "s_load_dwordx2 s[48:49], %[ka], 0x28\n\ts_load_dwordx2 s[52:53], %[ka], 0x0\n\ts_load_dwordx4 s[56:59], %[ka], 0x18\n\t"
                 "s_memrealtime s[64:65]\n\t"
                 "s_mov_b32 s43, 0x20000\n\ts_mov_b32 s47, 0x20000\n\ts_mov_b32 s51, 0x20000\n\ts_mov_b32 s55, 0x20000\n\ts_mov_b32 s50, %c[wsb]"
                 : "=&{s[40:43]}"(srd_d), "=&{s[44:47]}"(srd_s), "=&{s[48:51]}"(srd_ws), "=&{s[52:55]}"(srd_x0), "=&{s[56:59]}"(pw), "=&{s[60:63]}"(po), "=&{s[64:65]}"(t_start)
                 : [ka] "s"(__builtin_amdgcn_kernarg_segment_ptr()), [wsb] "i"(WS_BYTES));
    FENCE();
    __shared__ __attribute__((aligned(16))) char smem_[L_TOTAL];
    LAS char* smem = (LAS char*)smem_;
    const int t = threadIdx.x, l = t & 63, b = blockIdx.x;
    const int w = rfl(t >> 6);
    const int g = l >> 4, n = l & 15, q = (l >> 2) & 3, p = l & 3, pc = t & 7;
    const unsigned lds0 = (unsigned)(size_t)smem;
    const bool isprod = ((b >> 3) & 1) == 0;
    const int pg = ((b & 7) << 4) | (b >> 4);
    const unsigned e0 = pg * CH + 4 * t, e1 = pg * CH + 3072 + t;

    u32x16 ed, es;
    {
        const unsigned o = e0 * 4, o1 = w == 0 ? e1 * 4 : OOB, ok = WS_KEY + t * 4, om = WS_KEY + b * 4;
        const unsigned oc = w < 4 ? (t < 196 ? ((b - 8) << 8) + ((t >> 1) << 16) + ((t & 1) << 7) : OOB) : (t - 256) * 128, dump = lds0 + L_DUMP + w * 256;
        int role = (w < 4 ? 0 : 2) + (isprod ? 0 : 1);
        unsigned k4 = 4096, k8 = 8192;
        asm volatile("s_cmp_eq_u32 %[role], 0\n\ts_cbranch_scc1 1f\n\t"
                     "s_mov_b32 m0, %[dump]\n\ts_cmp_eq_u32 %[role], 1\n\ts_cbranch_scc1 2f\n\t"
                     "s_getpc_b64 s[68:69]\n\ts_mov_b32 s70, " STR(CODE_BYTES) "\n\ts_mov_b32 s71, 0x20000\n\t"
                     ".if " STR(CODEPF) "\n\tbuffer_load_dword %[oc], s[68:71], 0 offen lds\n\t.endif\n\t"
                     "s_waitcnt lgkmcnt(0)\n\ts_branch 3f\n\t"
                     "2:\n\ts_waitcnt lgkmcnt(0)\n\ts_lshl_b32 s54, s62, 8\n\t"
                     ".if " STR(PARTNER) "\n\tbuffer_load_dword %[oc], s[52:55], 0 offen lds\n\t.endif\n\ts_branch 3f\n\t"
                     "1:\n\ts_waitcnt lgkmcnt(0)\n\ts_lshl_b32 s42, s63, 2\n\ts_lshl_b32 s46, s63, 2\n\t"
                     "buffer_load_dwordx4 v[64:67], %[o], s[40:43], 0 offen\n\tbuffer_load_dwordx4 v[68:71], %[o], s[40:43], %[k4] offen\n\t"
                     "buffer_load_dwordx4 v[72:75], %[o], s[40:43], %[k8] offen\n\tbuffer_load_dword v76, %[o1], s[40:43], 0 offen\n\t"
                     "buffer_load_dwordx4 v[80:83], %[o], s[44:47], 0 offen\n\tbuffer_load_dwordx4 v[84:87], %[o], s[44:47], %[k4] offen\n\t"
                     "buffer_load_dwordx4 v[88:91], %[o], s[44:47], %[k8] offen\n\tbuffer_load_dword v92, %[o1], s[44:47], 0 offen\n\t"
                     "buffer_load_dword v93, %[ok], s[48:51], 0 offen sc1\n\tbuffer_load_dword v94, %[om], s[48:51], 0 offen sc1\n\t"
                     "3:\n\ts_lshl_b32 s54, s62, 8"
                     : "=&{v[64:79]}"(ed), "=&{v[80:95]}"(es)
                     : [o] "v"(o), [o1] "v"(o1), [ok] "v"(ok), [om] "v"(om), [oc] "v"(oc), [role] "s"(role), [dump] "s"(dump), [k4] "s"(k4), [k8] "s"(k8),
                       "{s[40:43]}"(srd_d), "{s[44:47]}"(srd_s), "{s[48:51]}"(srd_ws), "{s[52:55]}"(srd_x0), "{s[56:59]}"(pw), "{s[60:63]}"(po), "{s[64:65]}"(t_start)
                     : "memory", "scc", "s68", "s69", "s70", "s71");
        asm volatile("" : "+{s[48:51]}"(srd_ws), "+{s[52:55]}"(srd_x0), "+{s[56:59]}"(pw), "+{s[60:63]}"(po), "+{s[64:65]}"(t_start));
    }
    const void* const x = (const void*)((u64)srd_x0.x | ((u64)srd_x0.y << 32));
    const void* const wq = (const void*)((u64)pw.x | ((u64)pw.y << 32));
    const void* const wv = (const void*)((u64)pw.z | ((u64)pw.w << 32));
    void* const out = (void*)((u64)po.x | ((u64)po.y << 32));
    const int N = po.z, E = po.w;
    const unsigned xbytes = srd_x0.z;
    unsigned* const ws = (unsigned*)((u64)srd_ws.x | ((u64)srd_ws.y << 32));
    STAMP_DECL;
    st_[0] = t_start;
    const unsigned ring = lds0 + L_RING + w * 16384;
    const int jn = 8 * n + w;

    unsigned mykey = 0;
    auto node_row = [&](int tile, int m) { const int nd = b + 256 * (8 * (4 * m + g) + tile); return nd < N ? (unsigned)nd << 8 : OOB; };
    auto zero_staging = [&]() {
        const u32x4 z = {0u, 0u, 0u, 0u};
#pragma unroll
        for (int k = 0; k < 5; ++k) *(LAS u32x4*)(smem + L_IMG + k * 8192 + t * 16) = z;
    };

    u32x16 addr;
    f32x32 acc, wva, wvb;
    unsigned cv, lista, cnta, qa, keyn, inf;
    Srd4 srd_x;
    auto loop_constants = [&]() {
        srd_x = make_srd4(x, xbytes, BIAS * 256);
        lista = lds0 + L_LIST + w * (2 * SUBB) + g * 4; cnta = lds0 + L_CNT + (l & 31) * 4; qa = ring + l * 16; keyn = jn; inf = 0x7F800000u;
        asm volatile("" : "+s"(srd_x.s0), "+s"(srd_x.s1), "+s"(srd_x.s2), "+s"(srd_x.s3));
        asm volatile("" : "+v"(lista), "+v"(cnta), "+v"(qa), "+v"(keyn), "+v"(inf));
        const unsigned o0 = (n ^ (((g & 1) << 3) | (g >> 1))) << 4;
        const unsigned ra = (4 * (n & 3) + (n >> 2)) * 256 + (((4 * g) ^ (((n & 7) << 1) | (n >> 3))) << 4);
        const int i = 4 * g + q, r8 = i & 7;
        const unsigned ta = (4 * q + g) * 256 + 8 * (p & 1) + (((p >> 1) ^ (((i & 7) << 1) | (i >> 3))) << 4);
        cv = i * 256 + (((p ^ ((r8 & 1) | ((r8 & 6) << 1))) << 4) | ((p & 1) << 3));
#pragma unroll
        for (int s = 0; s < 4; ++s) { addr[s] = ring + (ra ^ (s << 4)); addr[12 + s] = o0 ^ (s << 5); }
#pragma unroll
        for (int c = 0; c < 8; ++c) addr[4 + c] = ring + (ta ^ (c << 5));
        asm volatile("" : "+{v[176:191]}"(addr), "+v"(cv));
    };
    auto clear_sums = [&]() { acc = 0.f; asm volatile("" : "+{v[128:159]}"(acc)); };
    auto wv_resident = [&]() {
#pragma unroll
        for (int a = 0; a < 8; ++a) {
            LAS char* pa = smem + L_WV0 + (cv ^ ((a >> 1) << 6) ^ ((a & 1) << 3));
            const f32x4 f0 = __builtin_bit_cast(f32x4, cat(tr_read(pa), tr_read(pa + 4096)));
            const f32x4 f1 = __builtin_bit_cast(f32x4, cat(tr_read(pa + 16384), tr_read(pa + 16384 + 4096)));
#pragma unroll
            for (int k = 0; k < 4; ++k) { wva[4 * a + k] = f0[k]; wvb[4 * a + k] = f1[k]; }
        }
        asm volatile("" : "+{v[224:255]}"(wva), "+{v[32:63]}"(wvb));
    };

    if (isprod) zero_staging();
    if (t < 8) *(LAS u32x4*)(smem + L_CNT + t * 16) = u32x4{0u, 0u, 0u, 0u};
    WG_BARRIER();

    if (__builtin_expect(w < 4, 1)) {
        if (__builtin_expect(isprod, 1)) {
            if (PRIO) __builtin_amdgcn_s_setprio(2);
            unsigned ob = WS_INBOX + ((t >> 3) * G1 + pg) * 128 + pc * 16, ima = lds0 + L_IMG + (t >> 3) * 144 + pc * 16;
            PIN(ob); PIN(ima);
            unsigned cna = lds0 + L_CNT1 + (t >> 3) * 4;
            PIN(cna);
            const bool v0 = e0 < (unsigned)E, v1 = e0 + 1024 < (unsigned)E, v2 = e0 + 2048 < (unsigned)E, v3 = w == 0 && e1 < (unsigned)E;
            STAMP(1);
            u32x16 rk;
            const u64 over = rank_edges(ed, es, rk, __builtin_amdgcn_ballot_w64(v0), __builtin_amdgcn_ballot_w64(v1), __builtin_amdgcn_ballot_w64(v2),
                                        __builtin_amdgcn_ballot_w64(v3), t * 4);
            mykey = es[14];
            if (__builtin_expect(over != 0, 0)) {
#pragma unroll
                for (int k = 0; k < 13; ++k)
                    if (rk[k] >= 4u * CAP1) {
                        const unsigned o = WS_OVF + (pg * CH + lds_add(smem + L_CNT1 + 2048, 1)) * 4;
                        const unsigned v = ((((es[k] >> 8) & 0xFFFF) - BIAS) << 16) | ed[k];
                        ST1(v, srd_ws, o, " sc1");
                    }
            }
            if (XSYNC) { lds_arrive(smem + L_CNT + 96, l); lds_await(smem + L_CNT + 96, 4); } else WG_BARRIER();
            STAMP(3);
            f32x32 img; u32x16 meta, sums;
            const unsigned no = read_image(img, meta, sums, ima, cna);
            lds_arrive(smem + L_CNT + 84, l);
            if (__builtin_expect(no != 0, 0)) {
                const unsigned o = t == 0 ? WS_NOVF + pg * 4 : OOB;
                ST1(no, srd_ws, o, " sc1");
                VMWAIT(0);
                if (XSYNC) { lds_arrive(smem + L_CNT + 100, l); lds_await(smem + L_CNT + 100, 4); } else WG_BARRIER();
            }
            publish_image(img, meta, sums, srd_ws, ob);
            STAMP(4);
        } else {
            { const unsigned om = WS_KEY + b * 4; asm volatile("buffer_load_dword %0, %1, %2, 0 offen sc1\n\ts_waitcnt vmcnt(0)" : "=&v"(mykey) : "v"(om), "s"(srd_ws) : "memory"); }
            if (NP_SLEEP) __builtin_amdgcn_s_sleep(NP_SLEEP);
            const unsigned o = ((b >> 4) * 256 + t) * 128;
            set_m0(lds0 + L_DUMP + w * 256);
            STAMP(1);
            for (int r = 0; r < PF_R; ++r) {
                asm volatile("s_waitcnt vmcnt(%c0)" :: "i"(PF_D) : "memory");
                if (__builtin_amdgcn_s_memrealtime() > t_start + PF_T) break;
                const unsigned so = r << 19;
                asm volatile("buffer_load_dword %1, %0, %2 offen lds" :: "s"(srd_x0), "v"(o), "s"(so) : "memory");
            }
            if (PRIO) __builtin_amdgcn_s_setprio(2);
            STAMP(2); STAMP(3); STAMP(4);
        }
        STAMP(5);
        mykey = rfl(mykey) + KEY_STEP;
        loop_constants();
        clear_sums();
        unsigned ib = WS_INBOX + b * (G1 * 128) + t * 16;
        PIN(ib);
        while (__builtin_amdgcn_s_memrealtime() < t_start + POLL_T) __builtin_amdgcn_s_sleep(1);
        STAMP(6);
        u32x16 raw;
        inbox_request(raw, srd_ws, ib);
        if (__builtin_expect(inbox_stage(raw, srd_ws, ib, mykey, 16 * pc - 8, lds0 + L_HDR + (t >> 3) * 4) != 0, 0)) {
            LAS unsigned* hp = (LAS unsigned*)(smem + L_HDR) + 32 * (l >> 3 & 3) + 8 * w + (l & 7);
            const unsigned hv = *hp;
            for (u64 m = __builtin_amdgcn_ballot_w64(l < 32 && hv > 4u * CAP1); m; m &= m - 1) {
                const int k = __builtin_ctzll(m), gp = 32 * (k >> 3) + 8 * w + (k & 7);
                unsigned u, no; const unsigned o = WS_OVF + (gp * CH + l) * 4, on = WS_NOVF + gp * 4;
                asm volatile("buffer_load_dword %0, %2, %4, 0 offen sc1\n\tbuffer_load_dword %1, %3, %4, 0 offen sc1\n\ts_waitcnt vmcnt(0)"
                             : "=&v"(u), "=&v"(no) : "v"(o), "v"(on), "s"(srd_ws) : "memory");
                if (rfl(no) <= 64) {
                    if (l < no && (u & 0xFF) == (unsigned)b) {
                        const unsigned e = __builtin_amdgcn_perm((u >> 16) + BIAS, u, 0x01050401u), key = (e >> 21) & 60;
                        *(LAS unsigned*)(smem + L_LIST + key * (SUBB / 4) + min(lds_add(smem + L_CNT + key, 4), SUBB - 4u)) = e;
                    }
                    if (l == k) *hp = hv | 0x80000000u;
                } else *(LAS unsigned*)(smem + L_CNT + 64) = 1;
            }
        }
        if (PRIO) __builtin_amdgcn_s_setprio(0);
        STAMP(7);
    } else {
        const int j = w - 4;
        STAMP(1);
        u32x4 wr[8], xr[8];
        {
            const u32x4 srd_q = make_srd(wq, 32768);
            const unsigned o = j * 8192 + l * 16, k4 = 4096;
            LD4(wr[0], srd_q, o, ""); LD4(wr[1], srd_q, o, " offset:1024"); LD4(wr[2], srd_q, o, " offset:2048"); LD4(wr[3], srd_q, o, " offset:3072");
            LD4S(wr[4], srd_q, o, k4, ""); LD4S(wr[5], srd_q, o, k4, " offset:1024"); LD4S(wr[6], srd_q, o, k4, " offset:2048"); LD4S(wr[7], srd_q, o, k4, " offset:3072");
#pragma unroll
            for (int m = 0; m < 8; ++m) { const unsigned ox = node_row(m < 4 ? j : w, m & 3) + n * 16; LD4(xr[m], srd_x0, ox, ""); }
        }
        unsigned aqa[8], xfa[4];
        unsigned xa = ring + g * 256 + ((n ^ g) << 4), wa = lds0 + L_WQ + j * 16384 + g * 256 + ((n ^ g) << 4) + ((n & 4) << 1);
        {
            const unsigned aq = lds0 + L_WQ + (8 * g + q) * 256 + ((((4 * p) ^ (q | ((g & 1) << 2))) << 4) | ((p & 1) << 3));
#pragma unroll
            for (int s = 0; s < 4; ++s) { xfa[s] = ring + n * 256 + (((4 * s + g) ^ n) << 4); PIN(xfa[s]); }
#pragma unroll
            for (int c = 0; c < 8; ++c) { aqa[c] = aq ^ ((c >> 1) << 4) ^ ((c & 1) << 3); PIN(aqa[c]); }
            PIN(xa); PIN(wa);
        }
        loop_constants();
        STAMP(2);
        if (isprod && !XSYNC) {
            WG_BARRIER();
            if (__builtin_expect(rfl(*(LAS unsigned*)(smem + L_CNT1 + 2048)) != 0, 0)) WG_BARRIER();
            if (W_SLEEP) __builtin_amdgcn_s_sleep(W_SLEEP);
        }
        STAMP(3);
        asm volatile("s_waitcnt vmcnt(%c8)" : "+v"(wr[0]), "+v"(wr[1]), "+v"(wr[2]), "+v"(wr[3]), "+v"(wr[4]), "+v"(wr[5]), "+v"(wr[6]), "+v"(wr[7]) : "i"(8) : "memory");
#pragma unroll
        for (int m = 0; m < 8; ++m) {
            const unsigned a = (wa ^ ((m & 2) << 5)) + m * 1024;
            *(LAS u32x2*)(size_t)a = u32x2{wr[m].x, wr[m].y};
            *(LAS u32x2*)(size_t)(a ^ 8) = u32x2{wr[m].z, wr[m].w};
        }
        asm volatile("s_waitcnt vmcnt(0)" : "+v"(xr[0]), "+v"(xr[1]), "+v"(xr[2]), "+v"(xr[3]), "+v"(xr[4]), "+v"(xr[5]), "+v"(xr[6]), "+v"(xr[7]) :: "memory");
#pragma unroll
        for (int m = 0; m < 8; ++m) *(LAS u32x4*)(size_t)((xa ^ ((m & 3) << 6)) + m * 1024) = xr[m];
        STAMP(4);
        lds_arrive(smem + L_CNT + 80, l);
        lds_await(smem + L_CNT + 80, 4);
        STAMP(5);
        bf16x8 xo[2][4], fr[2][8];
        f32x4 qc[2][8];
        auto wq_frags = [&](int u, bf16x8 (&f)[8]) {
#pragma unroll
            for (int a = 0; a < 8; ++a) {
                LAS char* pa = (LAS char*)(size_t)aqa[a] + u * 16384;
                f[a] = cat(tr_read(pa), tr_read(pa + 1024));
            }
        };
#pragma unroll
        for (int u = 0; u < 4; ++u) { xo[0][u] = *(LAS bf16x8*)(size_t)xfa[u]; xo[1][u] = *(LAS bf16x8*)(size_t)(xfa[u] + 4096); }
        wq_frags(0, fr[0]);
        {
            const Srd4 srd_v = make_srd4(wv, 32768);
            const unsigned r0 = (j * 32 + g) << 8, o0 = (n ^ ((g & 1) | ((g & 2) << 1))) << 4;
            const unsigned e[8] = {r0, r0 + 1024, r0 + 2048, r0 + 3072, r0 + 4096, r0 + 5120, r0 + 6144, r0 + 7168};
            const unsigned base = lds0 + (j < 2 ? L_WV0 + j * 16384 : L_WV1 + (j - 2) * 8192);
            const u32x4 off = {o0, o0 ^ 128, o0, o0 ^ 128};
            dma_half(base, srd_v, e, off); dma_half(base + 4096, srd_v, e + 4, off);
        }
#pragma unroll
        for (int u = 0; u < 4; ++u) {
            if (u < 3) wq_frags(u + 1, fr[(u + 1) & 1]);
            FENCE();
#pragma unroll
            for (int a = 0; a < 8; ++a)
#pragma unroll
                for (int tt = 0; tt < 2; ++tt) qc[tt][a] = mfma(fr[u & 1][a], xo[tt][u], u ? qc[tt][a] : f32x4{0.f, 0.f, 0.f, 0.f});
            FENCE();
            if (!WVLATE && u == 1 && j < 2) { VMWAIT(0); lds_arrive(smem + L_CNT + 88, l); }
        }
        STAMP(6);
        if (isprod) lds_await(smem + L_CNT + 84, 4);
#pragma unroll
        for (int tt = 0; tt < 2; ++tt)
#pragma unroll
            for (int s = 0; s < 4; ++s) {
                const float k = -0.08838834764831845f * 1.4426950408889634f;
                const f32x4 c0 = qc[tt][2 * s] * k, c1 = qc[tt][2 * s + 1] * k;
                *(LAS u32x4*)(size_t)(ring - (tt ? 0 : 65536) + s * 1024 + l * 16) = u32x4{pk2(c0[0], c0[1]), pk2(c0[2], c0[3]), pk2(c1[0], c1[1]), pk2(c1[2], c1[3])};
            }
        clear_sums();
        if (!WVLATE) { lds_await(smem + L_CNT + 88, 2); wv_resident(); }
        VMWAIT(0);
        STAMP(7);
    }
    WG_BARRIER();
    STAMP(8);
    if (LPRIO >= 3) __builtin_amdgcn_s_setprio(3);
    if (LPRIO == 1 && w >= 4) __builtin_amdgcn_s_setprio(1);
    if (LPRIO == 2) { if (w >= 6) __builtin_amdgcn_s_setprio(3); else if (w >= 4) __builtin_amdgcn_s_setprio(2); else if (w >= 2) __builtin_amdgcn_s_setprio(1); }

    u32x16 qf;
    const u32x4 doff = {addr[12], addr[13], addr[14], addr[15]};
    LAS char* mylist = smem + L_LIST + w * (2 * SUBB);
    int gcur = G1, pos = 0;
    unsigned hmask = 0xFFFFFFFFu;
    auto batch = [&](auto first_) {
        constexpr bool first = decltype(first_)::value;
        auto entries = [&](int grp, unsigned (&e)[8]) {
#pragma unroll
            for (int m = 0; m < 8; ++m) e[m] = *(LAS unsigned*)(size_t)(lista + grp * 128 + m * 16);
        };
        unsigned cw = *(LAS unsigned*)(size_t)(first ? cnta : lds0 + L_CNT + (__builtin_amdgcn_mbcnt_lo(~0u, 0u) & 31) * 4), e0[8], e1[8];
        entries(0, e0);
        if constexpr (first) {
#pragma unroll
            for (int s = 0; s < 4; ++s) {
                const u32x4 v = *(LAS u32x4*)(size_t)(qa + s * 1024);
                qf[4 * s] = v.x; qf[4 * s + 1] = v.y; qf[4 * s + 2] = v.z; qf[4 * s + 3] = v.w;
            }
        }
        entries(1, e1);
        FENCE();
        int lo = __builtin_amdgcn_readlane(cw, 2 * w) >> 2, hi = __builtin_amdgcn_readlane(cw, 2 * w + 1) >> 2;
        if constexpr (first) {
            if (__builtin_amdgcn_readlane(cw, 16)) gcur = 0;
            if (__builtin_amdgcn_ballot_w64(cw > 4u * CAP2)) { gcur = -1; lo = 0; hi = 0; hmask = 0x7FFFFFFFu; }
        }
        const int c = lo + hi, G = (c + 31) >> 5;
        const int nlast = G > 2 ? (((c - 1) & 31) >> 2) + 1 : 8;
        auto join = [&]() {
            for (int k = 0; k < G * 32 - lo; k += 64) {
                const unsigned e = *(LAS unsigned*)(mylist + SUBB + (l + k) * 4);
                if (l + k < G * 32 - lo) *(LAS unsigned*)(mylist + (lo + l + k) * 4) = l + k < hi ? e : PAD;
            }
        };
        const bool early = lo >= 64;
        if (__builtin_expect(!early, 0)) { join(); entries(0, e0); entries(1, e1); }
        if constexpr (first) asm volatile("s_waitcnt lgkmcnt(0)" : "+{v[160:175]}"(qf) :: "memory");
        if (G > 0) dma_group(ring, srd_x, e0, doff);
        if constexpr (first) {
            if (WVLATE) {
                if (G > 1 && w < 6) dma_group(ring + 8192, srd_x, e1, doff);
                wv_resident();
                lds_arrive(smem + L_CNT + 92, l);
                if (w >= 6) { lds_await(smem + L_CNT + 92, 8); if (G > 1) dma_group(ring + 8192, srd_x, e1, doff); }
            } else if (w < 4) {
                if (G > 1) dma_group(ring + 8192, srd_x, e1, doff);
                wv_resident();
                lds_arrive(smem + L_CNT + 92, l);
            } else {
                if (w >= 6) lds_await(smem + L_CNT + 92, 4);
                if (G > 1) dma_group(ring + 8192, srd_x, e1, doff);
            }
        } else if (G > 1) dma_group(ring + 8192, srd_x, e1, doff);
        f32x16 cc;
#pragma unroll
        for (int m = 0; m < 8; ++m) {
            cc[m] = (e0[m] & 0xFF) == keyn ? 0.f : __uint_as_float(inf);
            cc[8 + m] = (e1[m] & 0xFF) == keyn ? 0.f : __uint_as_float(inf);
        }
        if (early) join();
        STAMP(9);
        if (G > 0) gather_loop(acc, qf, addr, u32x4{lista, keyn, inf, 0u}, cc, srd_x, ring, G, nlast);
    };
    batch(std::true_type{});
    while (__builtin_expect(gcur < G1, 0)) {
        unsigned lim = G1 * 32;
        if (gcur >= 0) {
            const u64 m0 = __builtin_amdgcn_ballot_w64(l >= gcur && (int)(*(LAS unsigned*)(smem + L_HDR + l * 4) & hmask) > 4 * CAP1);
            const u64 m1 = __builtin_amdgcn_ballot_w64(l + 64 >= gcur && (int)(*(LAS unsigned*)(smem + L_HDR + 256 + l * 4) & hmask) > 4 * CAP1);
            if (!(m0 | m1)) break;
            gcur = m0 ? __builtin_ctzll(m0) : 64 + __builtin_ctzll(m1);
            unsigned v; const unsigned o = WS_NOVF + gcur * 4;
            asm volatile("buffer_load_dword %0, %1, %2, 0 offen sc1\n\ts_waitcnt vmcnt(0)" : "=&v"(v) : "v"(o), "s"(srd_ws) : "memory");
            lim = rfl(v);
        }
        const unsigned i = pos + t;
        const bool cand = t < CAP2 && i < lim;
        unsigned u;
        { const unsigned o = cand ? (gcur < 0 ? WS_INBOX + b * (G1 * 128) : WS_OVF + gcur * CH * 4) + i * 4 : OOB;
          asm volatile("buffer_load_dword %0, %1, %2, 0 offen sc1\n\ts_waitcnt vmcnt(0)" : "=&v"(u) : "v"(o), "s"(srd_ws) : "memory"); }
        WG_BARRIER();
        if (t < 16) *(LAS unsigned*)(smem + L_CNT + t * 4) = 0;
        WG_BARRIER();
        if (cand) {
            unsigned e; bool v;
            if (gcur < 0) { e = (i & 3) ? u : u ^ mykey; v = ((i & 31) - 2) * 4 < *(LAS unsigned*)(smem + L_HDR + (i >> 5) * 4); }
            else { e = (((u >> 16) + BIAS) << 8) | ((u >> 8) & 0xFF); v = (u & 0xFF) == (unsigned)b; }
            if (v) *(LAS unsigned*)(smem + L_LIST + (e & 7) * (2 * SUBB) + lds_add(smem + L_CNT + (e & 7) * 8, 4)) = e;
        }
        pos += CAP2;
        if ((unsigned)pos >= lim) { ++gcur; pos = 0; }
        WG_BARRIER();
        batch(std::false_type{});
    }
    STAMP(10);

    {
        bf16x8 wl[2][8], ag[4];
#pragma unroll
        for (int u = 0; u < 2; ++u)
#pragma unroll
            for (int a = 0; a < 8; ++a) {
                LAS char* pa = smem + L_WV1 + (cv ^ ((a >> 1) << 6) ^ ((a & 1) << 3)) + u * 8192;
                wl[u][a] = cat(tr_read(pa), tr_read(pa + 4096));
            }
#pragma unroll
        for (int u = 0; u < 4; ++u) {
            const u32x4 v = {pk2(acc[8 * u], acc[8 * u + 1]), pk2(acc[8 * u + 2], acc[8 * u + 3]),
                             pk2(acc[8 * u + 4], acc[8 * u + 5]), pk2(acc[8 * u + 6], acc[8 * u + 7])};
            ag[u] = __builtin_bit_cast(bf16x8, v);
        }
        FENCE();
        f32x4 oc[8];
#pragma unroll
        for (int a = 0; a < 8; ++a) oc[a] = mfma(__builtin_bit_cast(bf16x8, f32x4{wva[4 * a], wva[4 * a + 1], wva[4 * a + 2], wva[4 * a + 3]}), ag[0], f32x4{0.f, 0.f, 0.f, 0.f});
#pragma unroll
        for (int a = 0; a < 8; ++a) oc[a] = mfma(__builtin_bit_cast(bf16x8, f32x4{wvb[4 * a], wvb[4 * a + 1], wvb[4 * a + 2], wvb[4 * a + 3]}), ag[1], oc[a]);
#pragma unroll
        for (int a = 0; a < 8; ++a) oc[a] = mfma(wl[0][a], ag[2], oc[a]);
        FENCE();
        const unsigned ow = ring + n * 256 + ((g ^ n) << 4) + ((g & 1) << 3);
#pragma unroll
        for (int a = 0; a < 8; ++a) oc[a] = mfma(wl[1][a], ag[3], oc[a]);
#pragma unroll
        for (int a = 0; a < 8; ++a)
            *(LAS u32x2*)(size_t)(ow ^ ((a >> 1) << 6) ^ ((a & 1) << 3)) = u32x2{pk2(oc[a][0], oc[a][1]), pk2(oc[a][2], oc[a][3])};
        const auto rs_o = __builtin_amdgcn_make_buffer_rsrc(out, 0, xbytes, 0x00020000);
#pragma unroll
        for (int m = 0; m < 4; ++m) {
            const u32x4 v = *(LAS u32x4*)(size_t)(ring + m * 1024 + l * 16);
            __builtin_amdgcn_raw_buffer_store_b128(v, rs_o, node_row(w, m) + ((n ^ g ^ (4 * m)) << 4), 0, OUT_AUX);
        }
    }
    if (w == 0) { const unsigned o = l == 0 ? WS_KEY + b * 4 : OOB; ST1(mykey, srd_ws, o, " sc1"); }
    STAMP(11);
    asm volatile("s_waitcnt vmcnt(0)" ::: "memory");
    STAMP(12);
    __syncthreads();
    STAMP(13);
    {
        const unsigned long long tgt = st_[13] + (unsigned long long)PROBE_M * (st_[PROBE_B] - st_[PROBE_A]);
        if (threadIdx.x < 64) { int guard = 0; while (wall_clock64() < tgt && ++guard < 200000) __builtin_amdgcn_s_sleep(2); }
    }
}

extern "C" void kernel_launch(void* const* d_in, const int* in_sizes, int n_in,
                              void* d_out, int out_size, void* d_ws, size_t ws_size,
                              hipStream_t stream) {
    const int N = in_sizes[0] / 128, E = in_sizes[1];
    k_graph<<<NB, 512, 0, stream>>>((const unsigned short*)d_in[0], (const int*)d_in[1], (const int*)d_in[2],
                                    (const unsigned short*)d_in[3], (const unsigned short*)d_in[4],
                                    (unsigned*)d_ws, (unsigned short*)d_out, N, E);
}
```

```cpp
#include <hip/hip_runtime.h>
#include <hip/hip_bf16.h>
#include <stdint.h>
#include <type_traits>
#define ORD 1
#define SKIP 0
#define STR2(x) #x
#define STR(x) STR2(x)

typedef __attribute__((ext_vector_type(8))) __bf16 bf16x8;
typedef __attribute__((ext_vector_type(4))) __bf16 bf16x4;
typedef __attribute__((ext_vector_type(2))) __bf16 bf16x2;
typedef __attribute__((ext_vector_type(4))) float f32x4;
typedef __attribute__((ext_vector_type(2))) float f32x2;
typedef __attribute__((ext_vector_type(4))) unsigned u32x4;
typedef __attribute__((ext_vector_type(2))) unsigned u32x2;
typedef __attribute__((ext_vector_type(32))) float f32x32;
typedef __attribute__((ext_vector_type(16))) float f32x16;
typedef __attribute__((ext_vector_type(16))) unsigned u32x16;
typedef __attribute__((ext_vector_type(12))) unsigned u32x12;
typedef __attribute__((ext_vector_type(8))) unsigned u32x8;
typedef unsigned long long u64;
#define LAS __attribute__((address_space(3)))

constexpr int NB = 256;
constexpr int G1 = 128;
constexpr int CH = 3136;
constexpr int CAP1 = 30;
constexpr int CAP2 = 192;
constexpr int SUBB = 784;
#define POLL_AT 3
#define OUT_AUX 16
#define PF_R 0
#define PF_D 0
#define POLL_T 340
#define PF_T 200
#define LPRIO 3
#define PKADD 1
#define CODEPF 1
#define CODE_BYTES 21504
#define PARTNER 1
#define XSYNC 1
#define WVLATE 1
#define PF_T0 100
#define W_SLEEP 3
#define PRIO 1
#define SPLIT 16384
constexpr unsigned KEY_STEP = 0x9E3779B1u;
constexpr unsigned WS_KEY = 0;
constexpr unsigned WS_NOVF = 2048;
constexpr unsigned WS_INBOX = 4096;
constexpr unsigned WS_OVF = WS_INBOX + NB * G1 * 128;
constexpr unsigned WS_BYTES = WS_OVF + G1 * CH * 4;
constexpr unsigned OOB = 0x80000000u;
constexpr unsigned BIAS = 32768 - SPLIT;
constexpr unsigned PAD = 0x00FFFFFFu;

constexpr int L_CNT = 0;
constexpr int L_HDR = 128;
constexpr int L_LIST = 768;
constexpr int L_RING = L_LIST + 16 * SUBB;
constexpr int L_IMG = L_RING;
constexpr int L_CNT1 = L_IMG + NB * 144;
constexpr int L_WQ = L_RING + 2 * 16384 + 8192;
constexpr int L_WV0 = L_RING + 6 * 16384 + 8192;
constexpr int L_WV1 = L_RING + 131072;
constexpr int L_DUMP = L_WV1 + 16384;
constexpr int L_TOTAL = L_DUMP + 2048;
static_assert(L_RING % 256 == 0 && L_TOTAL <= 163840, "LDS map");

#define STAMP_DECL
#define STAMP(k)
#define STAMP_FLUSH(base, k0, k1)

__device__ __forceinline__ u32x4 make_srd(const void* p, unsigned bytes, unsigned back = 0) {
    const u64 a = (u64)p - back;
    u32x4 s; s.x = (unsigned)a; s.y = (unsigned)(a >> 32); s.z = bytes + back; s.w = 0x00020000u; return s;
}
#define LD4(dst, srd, off, FL) asm volatile("buffer_load_dwordx4 %0, %1, %2, 0 offen" FL : "=v"(dst) : "v"(off), "s"(srd) : "memory")
#define LD4S(dst, srd, off, so, FL) asm volatile("buffer_load_dwordx4 %0, %1, %2, %3 offen" FL : "=v"(dst) : "v"(off), "s"(srd), "s"(so) : "memory")
#define LD1(dst, srd, off, FL) asm volatile("buffer_load_dword %0, %1, %2, 0 offen" FL : "=v"(dst) : "v"(off), "s"(srd) : "memory")
#define ST1(val, srd, off, FL) asm volatile("buffer_store_dword %0, %1, %2, 0 offen" FL :: "v"(val), "v"(off), "s"(srd) : "memory")
#define PIN(x) asm volatile("" : "+v"(x))
#define VMWAIT(n) asm volatile("s_waitcnt vmcnt(" #n ")" ::: "memory")
#define FENCE() __builtin_amdgcn_sched_barrier(0)
#define WG_BARRIER() do { asm volatile("s_waitcnt lgkmcnt(0)" ::: "memory"); __builtin_amdgcn_s_barrier(); asm volatile("" ::: "memory"); } while (0)

struct Srd4 { u32x4 s0, s1, s2, s3; };
__device__ __forceinline__ Srd4 make_srd4(const void* p, unsigned bytes, unsigned back = 0) {
    return Srd4{make_srd(p, bytes, back), make_srd(p, bytes, back + 1024), make_srd(p, bytes, back + 2048), make_srd(p, bytes, back + 3072)};
}

__device__ __forceinline__ void set_m0(unsigned v) { asm volatile("s_mov_b32 m0, %0" :: "s"(v) : "memory"); }
template <int R> __device__ __forceinline__ void dma_rows(const Srd4& d, unsigned e, unsigned off) {
    unsigned tmp, mask = 0x00FFFF00u;
    asm volatile("v_and_or_b32 %0, %1, %2, %3\n\tbuffer_load_dwordx4 %0, %4, 0 offen offset:%c5 lds"
                 : "=&v"(tmp) : "v"(e), "s"(mask), "v"(off), "s"(R == 0 ? d.s0 : R == 1 ? d.s1 : R == 2 ? d.s2 : d.s3), "i"(R * 1024) : "memory");
}
__device__ __forceinline__ void dma_half(unsigned ldsbase, const Srd4& d, const unsigned* e, const u32x4& off) {
    set_m0(ldsbase);
    dma_rows<0>(d, e[0], off.x); dma_rows<1>(d, e[1], off.y); dma_rows<2>(d, e[2], off.z); dma_rows<3>(d, e[3], off.w);
}
__device__ __forceinline__ void dma_group(unsigned ldsbase, const Srd4& d, const unsigned (&e)[8], const u32x4& off) {
    asm volatile("s_waitcnt lgkmcnt(0)" ::: "memory");
    dma_half(ldsbase, d, e, off); dma_half(ldsbase + 4096, d, e + 4, off);
}

__device__ __forceinline__ void gather_loop(f32x32& acc, const u32x16& qf, const u32x16& addr, u32x4 misc, f32x16 cc,
                                            const Srd4& d, unsigned ring, int G, int nlast) {
    f32x32 t0, t1; u32x12 t2; int tmp, pr; u64 sm; unsigned mask = 0x00FFFF00u;
    asm volatile(
        ".macro GWAIT k\n\ts_cmp_gt_i32 %[t], \\k\n\ts_cbranch_scc1 1f\n\ts_waitcnt vmcnt(\\k)\n\t.endm\n\t"
        ".macro GROWS so\n\t.irp s, 0, 1, 2, 3\n\t"
        "ds_read_b128 v[64+8*\\s:67+8*\\s], v[176+\\s] offset:\\so\n\t"
        "ds_read_b128 v[68+8*\\s:71+8*\\s], v[176+\\s] offset:\\so+4096\n\t.endr\n\t.endm\n\t"
        ".macro GTR c, so\n\t"
        "ds_read_b64_tr_b16 v[96+4*\\c:97+4*\\c], v[180+\\c] offset:\\so\n\t"
        "ds_read_b64_tr_b16 v[98+4*\\c:99+4*\\c], v[180+\\c] offset:\\so+4096\n\t.endm\n\t"
        ".macro GS s, c\n\t"
        "v_mfma_f32_16x16x32_bf16 v[\\c:\\c+3], v[64+8*\\s:67+8*\\s], v[160+4*\\s:163+4*\\s], v[\\c:\\c+3]\n\t"
        "v_mfma_f32_16x16x32_bf16 v[\\c+4:\\c+7], v[68+8*\\s:71+8*\\s], v[160+4*\\s:163+4*\\s], v[\\c+4:\\c+7]\n\t.endm\n\t"
        ".macro GLOAD m, r, srd\n\t"
        "v_and_or_b32 v195, v[212+\\m], %[mask], v[188+\\r]\n\t"
        "buffer_load_dwordx4 v195, \\srd, 0 offen offset:1024*\\r lds\n\t.endm\n\t"
        ".macro GPART m, r, srd\n\ts_cmp_le_i32 %[nl], \\m\n\ts_cbranch_scc1 4f\n\tGLOAD \\m, \\r, \\srd\n\t.endm\n\t"
        ".macro GSUM m\n\tv_mfma_f32_16x16x32_bf16 v[128+4*(\\m):131+4*(\\m)], v[96+4*(\\m):99+4*(\\m)], v[220:223], v[128+4*(\\m):131+4*(\\m)]\n\t.endm\n\t"
        ".macro GCMP m, d\n\tv_cmp_ne_u32_sdwa \\d, v[212+\\m], v193 src0_sel:BYTE_0 src1_sel:DWORD\n\t.endm\n\t"
        ".macro GPAIR m, c\n\t"
        "GSUM \\m\n\tGCMP \\m+1, %[sm]\n\tv_cndmask_b32_e32 v[\\c+\\m], 0, v194, vcc\n\t"
        "GSUM \\m+1\n\t.if \\m < 6\n\tGCMP \\m+2, vcc\n\t.else\n\ts_cmp_gt_i32 %[rem], 0\n\t.endif\n\t"
        "v_cndmask_b32_e64 v[\\c+\\m+1], 0, v194, %[sm]\n\t.endm\n\t"
        ".macro GSTEP so, c, eo\n\t"
        "GTR 0, \\so\n\t"
        "s_waitcnt lgkmcnt(2)\n\tGS 0, \\c\n\t"
        "GTR 1, \\so\n\tGTR 2, \\so\n\tGS 1, \\c\n\t"
        "GTR 3, \\so\n\tGTR 4, \\so\n\tGS 2, \\c\n\t"
        "GTR 5, \\so\n\tGTR 6, \\so\n\tGS 3, \\c\n\t"
        "GTR 7, \\so\n\t"
        "ds_read2_b32 v[212:213], v192 offset0:\\eo offset1:\\eo+4\n\t"
        "ds_read2_b32 v[214:215], v192 offset0:\\eo+8 offset1:\\eo+12\n\t"
        "ds_read2_b32 v[216:217], v192 offset0:\\eo+16 offset1:\\eo+20\n\t"
        "ds_read2_b32 v[218:219], v192 offset0:\\eo+24 offset1:\\eo+28\n\t"
        "s_add_u32 m0, %[ring], \\so\n\t"
        "s_cmp_gt_i32 %[rem], 3\n\t"
        ".irp k, 0, 1, 2, 3, 4, 5, 6, 7\n\tv_exp_f32_e32 v[\\c+\\k], v[\\c+\\k]\n\t.endr\n\t"
        "s_waitcnt lgkmcnt(0)\n\t"
        "s_cbranch_scc0 2f\n\t"
        "GLOAD 0, 0, %[s0]\n\tGLOAD 1, 1, %[s1]\n\tGLOAD 2, 2, %[s2]\n\tGLOAD 3, 3, %[s3]\n\t"
        "s_add_u32 m0, m0, 0x1000\n\t"
        "GLOAD 4, 0, %[s0]\n\tGLOAD 5, 1, %[s1]\n\tGLOAD 6, 2, %[s2]\n\tGLOAD 7, 3, %[s3]\n\t"
        "GSIG \\c\n\t"
        "s_waitcnt vmcnt(8)\n\t"
        "s_branch 1f\n\t"
        "2:\n\t"
        "s_mov_b32 %[t], 0\n\t"
        "s_cmp_eq_u32 %[rem], 3\n\ts_cbranch_scc0 4f\n\t"
        "s_mov_b32 %[t], %[nl]\n\t"
        "GLOAD 0, 0, %[s0]\n\tGPART 1, 1, %[s1]\n\tGPART 2, 2, %[s2]\n\tGPART 3, 3, %[s3]\n\t"
        "s_add_u32 m0, m0, 0x1000\n\t"
        "GPART 4, 0, %[s0]\n\tGPART 5, 1, %[s1]\n\tGPART 6, 2, %[s2]\n\tGPART 7, 3, %[s3]\n\t"
        "4:\n\t"
        "GSIG \\c\n\t"
        "s_waitcnt vmcnt(8)\n\t"
        "GWAIT 7\n\tGWAIT 6\n\tGWAIT 5\n\tGWAIT 4\n\tGWAIT 3\n\tGWAIT 2\n\tGWAIT 1\n\tGWAIT 0\n\t"
        "1:\n\t"
        "GROWS 8192-\\so\n\t"
        "GCMP 0, vcc\n\t"
        "s_add_i32 %[rem], %[rem], -1\n\t"
        "GPAIR 0, \\c\n\tGPAIR 2, \\c\n\tGPAIR 4, \\c\n\tGPAIR 6, \\c\n\t"
        ".endm\n\t"
        ".macro GSIG c\n\t"
        ".if " STR(PKADD) "\n\t.irp k, 0, 2, 4, 6\n\tv_pk_add_f32 v[\\c+\\k:\\c+\\k+1], v[\\c+\\k:\\c+\\k+1], 1.0 op_sel_hi:[1,0]\n\t.endr\n\t.else\n\t"
        ".irp k, 0, 1, 2, 3, 4, 5, 6, 7\n\tv_add_f32_e32 v[\\c+\\k], 1.0, v[\\c+\\k]\n\t.endr\n\t.endif\n\t"
        ".irp k, 0, 1, 2, 3, 4, 5, 6, 7\n\tv_rcp_f32_e32 v[\\c+\\k], v[\\c+\\k]\n\t.endr\n\t"
        ".irp k, 0, 1, 2, 3\n\tv_cvt_pk_bf16_f32 v[220+\\k], v[\\c+2*\\k], v[\\c+2*\\k+1]\n\t.endr\n\t.endm\n\t"
        "s_waitcnt vmcnt(8)\n\t"
        "s_cmp_gt_i32 %[rem], 1\n\ts_cbranch_scc1 1f\n\ts_waitcnt vmcnt(0)\n\t1:\n\t"
        "GROWS 0\n\t"
        ".if " STR(LPRIO) " >= 3\n\ts_mov_b32 %[pr], 0\n\ts_setprio 2\n\t.endif\n\t"
        "0:\n\t"
        ".if " STR(LPRIO) " == 3\n\t"
        "s_cmp_eq_u32 %[pr], 1\n\ts_cbranch_scc0 5f\n\ts_setprio 1\n\t5:\n\t"
        "s_cmp_eq_u32 %[pr], 2\n\ts_cbranch_scc0 5f\n\ts_setprio 0\n\t5:\n\t"
        "s_add_u32 %[pr], %[pr], 1\n\t.endif\n\t"
        "GSTEP 0, 196, 64\n\t"
        "s_cbranch_scc0 9f\n\t"
        "GSTEP 8192, 204, 96\n\t"
        "v_add_u32_e32 v192, 0x100, v192\n\t"
        "s_cbranch_scc1 0b\n\t"
        "9:\n\t"
        "s_waitcnt lgkmcnt(0)\n\ts_nop 6\n\t"
        ".purgem GWAIT\n\t.purgem GROWS\n\t.purgem GTR\n\t.purgem GS\n\t.purgem GLOAD\n\t.purgem GPART\n\t.purgem GSUM\n\t.purgem GCMP\n\t.purgem GPAIR\n\t.purgem GSTEP\n\t.purgem GSIG"
        : "+{v[128:159]}"(acc), "+{v[192:195]}"(misc), "+{v[196:211]}"(cc), "=&{v[64:95]}"(t0), "=&{v[96:127]}"(t1), "=&{v[212:223]}"(t2),
          [rem] "+s"(G), [t] "=&s"(tmp), [pr] "=&s"(pr), [sm] "=&s"(sm)
        : "{v[160:175]}"(qf), "{v[176:191]}"(addr), [s0] "s"(d.s0), [s1] "s"(d.s1), [s2] "s"(d.s2), [s3] "s"(d.s3),
          [mask] "s"(mask), [ring] "s"(ring), [nl] "s"(nlast)
        : "vcc", "scc", "memory");
}

__device__ __forceinline__ u64 rank_edges(u32x16& ed, u32x16& es, u32x16& rk, u64 m0, u64 m1, u64 m2, u64 m3, unsigned keya) {
    u32x16 b4; u64 over; unsigned sel = 0x01050401u, c36 = 36;
    asm volatile(
        ".macro RADD k\n\tv_lshlrev_b32_sdwa v[96+\\k], 2, v[64+\\k] dst_sel:DWORD dst_unused:UNUSED_PAD src0_sel:DWORD src1_sel:BYTE_0\n\t"
        "ds_add_rtn_u32 v[112+\\k], v[96+\\k], v111 offset:%c[cnt]\n\t.endm\n\t"
        ".macro RENT k\n\tv_add_u32_e32 v[80+\\k], %c[bias], v[80+\\k]\n\tv_perm_b32 v[80+\\k], v[80+\\k], v[64+\\k], %[sel]\n\t.endm\n\t"
        ".macro RPUT k\n\tv_min_u32_e32 v110, %c[cap], v[112+\\k]\n\tv_mad_u32_u24 v110, v[96+\\k], %[c36], v110\n\t"
        "ds_write_b32 v110, v[80+\\k] offset:%c[img]\n\t.endm\n\t"
        "v_mov_b32_e32 v111, 4\n\t"
        ".irp k, 0, 2, 4, 6, 8, 10, 12\n\tv_mov_b64_e32 v[112+\\k:113+\\k], 0\n\t.endr\n\t"
        "s_mov_b64 exec, %[m0]\n\ts_waitcnt vmcnt(9)\n\tRADD 0\n\tRADD 1\n\tRADD 2\n\tRADD 3\n\t"
        "s_mov_b64 exec, %[m1]\n\ts_waitcnt vmcnt(8)\n\tRADD 4\n\tRADD 5\n\tRADD 6\n\tRADD 7\n\t"
        "s_mov_b64 exec, %[m2]\n\ts_waitcnt vmcnt(7)\n\tRADD 8\n\tRADD 9\n\tRADD 10\n\tRADD 11\n\t"
        "s_mov_b64 exec, %[m3]\n\ts_waitcnt vmcnt(6)\n\tRADD 12\n\t"
        "s_mov_b64 exec, %[m0]\n\ts_waitcnt vmcnt(5)\n\tRENT 0\n\tRENT 1\n\tRENT 2\n\tRENT 3\n\t"
        "s_waitcnt lgkmcnt(9)\n\tRPUT 0\n\tRPUT 1\n\tRPUT 2\n\tRPUT 3\n\t"
        "s_mov_b64 exec, %[m1]\n\ts_waitcnt vmcnt(4)\n\tRENT 4\n\tRENT 5\n\tRENT 6\n\tRENT 7\n\t"
        "s_waitcnt lgkmcnt(9)\n\tRPUT 4\n\tRPUT 5\n\tRPUT 6\n\tRPUT 7\n\t"
        "s_mov_b64 exec, %[m2]\n\ts_waitcnt vmcnt(3)\n\tRENT 8\n\tRENT 9\n\tRENT 10\n\tRENT 11\n\t"
        "s_waitcnt lgkmcnt(9)\n\tRPUT 8\n\tRPUT 9\n\tRPUT 10\n\tRPUT 11\n\t"
        "s_mov_b64 exec, %[m3]\n\ts_waitcnt vmcnt(0)\n\tRENT 12\n\t"
        "s_waitcnt lgkmcnt(12)\n\tRPUT 12\n\t"
        "s_mov_b64 exec, -1\n\t"
        "v_add_u32_e32 v93, %c[step], v93\n\t"
        "v_max3_u32 v110, v112, v113, v114\n\tv_max3_u32 v109, v115, v116, v117\n\t"
        "ds_write_b32 %[keya], v93 offset:%c[cnt]+1024\n\t"
        "v_max3_u32 v110, v110, v118, v119\n\tv_max3_u32 v109, v109, v120, v121\n\t"
        "v_max3_u32 v110, v110, v122, v123\n\tv_max3_u32 v110, v110, v109, v124\n\t"
        "v_cmp_le_u32_e32 vcc, %c[cap], v110\n\ts_mov_b64 %[over], vcc\n\t"
        ".purgem RADD\n\t.purgem RENT\n\t.purgem RPUT"
        : "+{v[64:79]}"(ed), "+{v[80:95]}"(es), "=&{v[96:111]}"(b4), "=&{v[112:127]}"(rk), [over] "=&s"(over)
        : [m0] "s"(m0), [m1] "s"(m1), [m2] "s"(m2), [m3] "s"(m3), [keya] "v"(keya), [sel] "s"(sel), [c36] "s"(c36),
          [cnt] "i"(L_CNT1), [img] "i"(L_IMG + 8), [bias] "i"(BIAS), [cap] "i"(4 * CAP1), [step] "i"(KEY_STEP)
        : "vcc", "memory");
    return over;
}
__device__ __forceinline__ unsigned read_image(f32x32& img, u32x16& meta, u32x16& sums, unsigned ima, unsigned cna) {
    unsigned no;
    asm volatile(
        "v_mov_b32_e32 v121, 0\n\tds_read_b32 v120, v121 offset:%c[cnt]+2048\n\t"
        ".irp k, 0, 1, 2, 3, 4, 5, 6, 7\n\tds_read_b128 v[64+4*\\k:67+4*\\k], %[ima] offset:4608*\\k\n\t.endr\n\t"
        ".irp k, 0, 1, 2, 3\n\tds_read2_b32 v[96+2*\\k:97+2*\\k], %[cna] offset0:64*\\k offset1:64*\\k+32\n\t.endr\n\t"
        "s_waitcnt lgkmcnt(11)\n\tv_readfirstlane_b32 %[no], v120\n\t"
        "v_add_u32_e32 v121, 0x400, %[cna]\n\t"
        ".irp k, 0, 1, 2, 3\n\tds_read2_b32 v[104+2*\\k:105+2*\\k], v121 offset0:64*\\k offset1:64*\\k+32\n\t.endr\n\t"
        "s_waitcnt lgkmcnt(12)\n\t"
        ".irp k, 0, 1, 2, 3\n\tv_add3_u32 v[112+\\k], v[64+4*\\k], v[65+4*\\k], v[66+4*\\k]\n\tv_add_u32_e32 v[112+\\k], v[112+\\k], v[67+4*\\k]\n\t.endr\n\t"
        "s_waitcnt lgkmcnt(8)\n\t"
        ".irp k, 4, 5, 6, 7\n\tv_add3_u32 v[112+\\k], v[64+4*\\k], v[65+4*\\k], v[66+4*\\k]\n\tv_add_u32_e32 v[112+\\k], v[112+\\k], v[67+4*\\k]\n\t.endr\n\t"
        ".irp k, 0, 1, 2, 3, 4, 5, 6, 7\n\tv_add_u32_dpp v[112+\\k], v[112+\\k], v[112+\\k] quad_perm:[1,0,3,2] row_mask:0xf bank_mask:0xf\n\t.endr\n\t"
        ".irp k, 0, 1, 2, 3, 4, 5, 6, 7\n\tv_add_u32_dpp v[112+\\k], v[112+\\k], v[112+\\k] quad_perm:[2,3,0,1] row_mask:0xf bank_mask:0xf\n\t.endr\n\t"
        ".irp k, 0, 1, 2, 3, 4, 5, 6, 7\n\tv_add_u32_dpp v[112+\\k], v[112+\\k], v[112+\\k] row_half_mirror row_mask:0xf bank_mask:0xf\n\t.endr\n\t"
        "s_waitcnt lgkmcnt(0)"
        : "=&{v[64:95]}"(img), "=&{v[96:111]}"(meta), "=&{v[112:127]}"(sums), [no] "=&s"(no)
        : [ima] "v"(ima), [cna] "v"(cna), [cnt] "i"(L_CNT1) : "memory");
    return no;
}
__device__ __forceinline__ void publish_image(f32x32 img, u32x16 meta, u32x16 sums, const u32x4& srd, unsigned ob) {
    u64 pc0 = 0x0101010101010101ull; unsigned so;
    asm volatile(
        ".macro PUB k\n\ts_mov_b64 exec, %[pc0]\n\tv_mov_b32_e32 v[64+4*\\k], v[96+\\k]\n\tv_mov_b32_e32 v[65+4*\\k], v[112+\\k]\n\t"
        "s_mov_b64 exec, -1\n\tv_xor_b32_e32 v[64+4*\\k], v[64+4*\\k], v[104+\\k]\n\t"
        "buffer_store_dwordx4 v[64+4*\\k:67+4*\\k], %[ob], %[srd], %[so] offen sc1\n\ts_add_u32 %[so], %[so], %c[step]\n\t.endm\n\t"
        "s_mov_b32 %[so], 0\n\t"
        ".irp k, 0, 1, 2, 3, 4, 5, 6, 7\n\tPUB \\k\n\t.endr\n\t.purgem PUB"
        : "+{v[64:95]}"(img), [so] "=&s"(so)
        : "{v[96:111]}"(meta), "{v[112:127]}"(sums), [srd] "s"(srd), [ob] "v"(ob), [pc0] "s"(pc0), [step] "i"(32 * G1 * 128) : "scc", "memory");
}

__device__ __forceinline__ void inbox_request(u32x16& raw, const u32x4& srd, unsigned ib) {
    unsigned o1 = 0x1000, o2 = 0x2000, o3 = 0x3000;
    asm volatile("buffer_load_dwordx4 v[64:67], %1, %2, 0 offen sc1\n\tbuffer_load_dwordx4 v[68:71], %1, %2, %3 offen sc1\n\t"
                 "buffer_load_dwordx4 v[72:75], %1, %2, %4 offen sc1\n\tbuffer_load_dwordx4 v[76:79], %1, %2, %5 offen sc1"
                 : "=&{v[64:79]}"(raw) : "v"(ib), "s"(srd), "s"(o1), "s"(o2), "s"(o3) : "memory");
}
__device__ __forceinline__ unsigned inbox_stage(u32x16 raw, const u32x4& srd, unsigned ib, unsigned key, int i4, unsigned hdra) {
    u32x16 t0; u32x4 t1; u64 m0, m1, m2, m3; unsigned todo, fresh, spin, o1, o2, o3, over;
    u64 pc0 = 0x0101010101010101ull;
    asm volatile(
        ".macro IVAL j, so\n\t"
        "s_bitcmp1_b32 %[todo], \\j\n\ts_cbranch_scc0 2f\n\t"
        "v_xor_b32_e32 v[64+4*\\j], %[key], v[64+4*\\j]\n\t"
        "v_sub_u32_e32 v97, 0, v[65+4*\\j]\n\t"
        "v_add_u32_e32 v96, v[64+4*\\j], v[65+4*\\j]\n\t"
        "v_mov_b32_dpp v[80+\\j], v[64+4*\\j] quad_perm:[0,0,0,0] row_mask:0xf bank_mask:0xf\n\t"
        "v_cndmask_b32_e64 v96, v96, v97, %[pc0]\n\t"
        "s_nop 0\n\t"
        "v_mov_b32_dpp v[80+\\j], v[80+\\j] row_shr:4 row_mask:0xf bank_mask:0xa\n\t"
        "v_cmp_gt_i32_e32 vcc, v[80+\\j], v92\n\t"
        "v_add3_u32 v96, v96, v[66+4*\\j], v[67+4*\\j]\n\t"
        "v_lshrrev_b32_e32 v97, 14, v[80+\\j]\n\t"
        "v_cndmask_b32_e32 v96, 0, v96, vcc\n\t"
        "s_nop 1\n\tv_add_u32_dpp v96, v96, v96 quad_perm:[1,0,3,2] row_mask:0xf bank_mask:0xf\n\t"
        "s_nop 1\n\tv_add_u32_dpp v96, v96, v96 quad_perm:[2,3,0,1] row_mask:0xf bank_mask:0xf\n\t"
        "s_nop 1\n\tv_add_u32_dpp v96, v96, v96 row_half_mirror row_mask:0xf bank_mask:0xf\n\t"
        "v_or_b32_e32 v96, v96, v97\n\t"
        "v_cmp_ne_u32_e32 vcc, 0, v96\n\t"
        "s_cbranch_vccz 1f\n\t"
        "buffer_load_dwordx4 v[64+4*\\j:67+4*\\j], %[ib], %[srd], \\so offen sc1\n\t"
        "s_branch 2f\n\t"
        "1:\n\ts_bitset1_b32 %[fresh], \\j\n\t"
        "2:\n\t.endm\n\t"
        ".macro IRANK j\n\t"
        "s_bitcmp1_b32 %[fresh], \\j\n\ts_cbranch_scc0 2f\n\t"
        "v_cmp_lt_u32_e64 %[m0], v92, v[80+\\j]\n\tv_cmp_lt_u32_e64 %[m1], v93, v[80+\\j]\n\t"
        "v_cmp_lt_u32_e64 %[m2], v94, v[80+\\j]\n\tv_cmp_lt_u32_e64 %[m3], v95, v[80+\\j]\n\t"
        ".irp k, 0, 1, 2, 3\n\tv_lshrrev_b32_e32 v[84+\\k], 21, v[64+4*\\j+\\k]\n\t.endr\n\t"
        ".irp k, 0, 1, 2, 3\n\tv_and_b32_e32 v[84+\\k], 60, v[84+\\k]\n\t.endr\n\t"
        "v_cmp_lt_u32_e32 vcc, %c[cap], v[80+\\j]\n\t"
        "s_mov_b64 exec, %[m0]\n\tds_add_rtn_u32 v88, v84, v98 offset:%c[cnt]\n\t"
        "s_mov_b64 exec, %[m1]\n\tds_add_rtn_u32 v89, v85, v98 offset:%c[cnt]\n\t"
        "s_mov_b64 exec, %[m2]\n\tds_add_rtn_u32 v90, v86, v98 offset:%c[cnt]\n\t"
        "s_mov_b64 exec, %[m3]\n\tds_add_rtn_u32 v91, v87, v98 offset:%c[cnt]\n\t"
        "s_mov_b64 exec, %[pc0]\n\tds_write_b32 %[hdra], v[80+\\j] offset:128*\\j\n\t"
        "s_mov_b64 exec, -1\n\t"
        "s_cbranch_vccz 1f\n\t"
        "s_mov_b32 %[over], 1\n\t"
        "1:\n\ts_waitcnt lgkmcnt(1)\n\t"
        ".irp k, 0, 1, 2, 3\n\tv_min_u32_e32 v[88+\\k], %c[sub]-4, v[88+\\k]\n\t.endr\n\t"
        ".irp k, 0, 1, 2, 3\n\tv_mad_u32_u24 v[88+\\k], v[84+\\k], %[sub4], v[88+\\k]\n\t.endr\n\t"
        "s_mov_b64 exec, %[m0]\n\tds_write_b32 v88, v[64+4*\\j] offset:%c[list]\n\t"
        "s_mov_b64 exec, %[m1]\n\tds_write_b32 v89, v[65+4*\\j] offset:%c[list]\n\t"
        "s_mov_b64 exec, %[m2]\n\tds_write_b32 v90, v[66+4*\\j] offset:%c[list]\n\t"
        "s_mov_b64 exec, %[m3]\n\tds_write_b32 v91, v[67+4*\\j] offset:%c[list]\n\t"
        "s_mov_b64 exec, -1\n\t"
        "2:\n\t.endm\n\t"
        "s_movk_i32 %[o1], 0x1000\n\ts_movk_i32 %[o2], 0x2000\n\ts_movk_i32 %[o3], 0x3000\n\t"
        "v_mov_b32_e32 v92, %[i4]\n\tv_add_u32_e32 v93, 4, %[i4]\n\tv_add_u32_e32 v94, 8, %[i4]\n\tv_add_u32_e32 v95, 12, %[i4]\n\t"
        "v_mov_b32_e32 v98, 4\n\t"
        "s_mov_b32 %[todo], 15\n\ts_mov_b32 %[spin], 0x400000\n\ts_mov_b32 %[over], 0\n\t"
        "0:\n\t"
        "s_mov_b32 %[fresh], 0\n\t"
        "s_waitcnt vmcnt(3)\n\ts_cmp_eq_u32 %[todo], 15\n\ts_cbranch_scc1 3f\n\ts_waitcnt vmcnt(0)\n\t3:\n\t"
        "IVAL 0, 0\n\tIRANK 0\n\t"
        "s_waitcnt vmcnt(2)\n\tIVAL 1, %[o1]\n\tIRANK 1\n\t"
        "s_waitcnt vmcnt(1)\n\tIVAL 2, %[o2]\n\tIRANK 2\n\t"
        "s_cmp_eq_u32 %[todo], 15\n\ts_cbranch_scc0 3f\n\ts_waitcnt vmcnt(0)\n\t3:\n\tIVAL 3, %[o3]\n\tIRANK 3\n\t"
        "s_andn2_b32 %[todo], %[todo], %[fresh]\n\t"
        "s_cbranch_scc0 9f\n\t"
        "s_sub_u32 %[spin], %[spin], 1\n\t"
        "s_cbranch_scc0 0b\n\t"
        "s_waitcnt vmcnt(0)\n\t"
        "9:\n\t"
        ".purgem IVAL\n\t.purgem IRANK"
        : "+{v[64:79]}"(raw), "=&{v[80:95]}"(t0), "=&{v[96:99]}"(t1), [m0] "=&s"(m0), [m1] "=&s"(m1), [m2] "=&s"(m2), [m3] "=&s"(m3),
          [todo] "=&s"(todo), [fresh] "=&s"(fresh), [spin] "=&s"(spin), [o1] "=&s"(o1), [o2] "=&s"(o2), [o3] "=&s"(o3), [over] "=&s"(over)
        : [srd] "s"(srd), [ib] "v"(ib), [key] "s"(key), [i4] "v"(i4), [hdra] "v"(hdra), [pc0] "s"(pc0), [sub4] "s"(SUBB / 4),
          [cnt] "i"(L_CNT), [list] "i"(L_LIST), [sub] "i"(SUBB), [cap] "i"(4 * CAP1)
        : "vcc", "scc", "memory");
    return over;
}

template <int CTRL, int BANK> __device__ __forceinline__ unsigned dpp(unsigned old, unsigned v) { return __builtin_amdgcn_update_dpp(old, v, CTRL, 0xF, BANK, true); }
__device__ __forceinline__ unsigned bcast8(unsigned v) { v = dpp<0x00, 0xF>(0u, v); return __builtin_amdgcn_update_dpp(v, v, 0x114, 0xF, 0xA, false); }
__device__ __forceinline__ unsigned sum8(unsigned v) { v += dpp<0xB1, 0xF>(0u, v); v += dpp<0x4E, 0xF>(0u, v); return v + dpp<0x141, 0xF>(0u, v); }
__device__ __forceinline__ bf16x4 tr_read(LAS char* p) { return __builtin_amdgcn_ds_read_tr16_b64_v4bf16((LAS bf16x4*)p); }
__device__ __forceinline__ bf16x8 cat(bf16x4 a, bf16x4 b) { return __builtin_shufflevector(a, b, 0, 1, 2, 3, 4, 5, 6, 7); }
__device__ __forceinline__ unsigned pk2(float a, float b) { f32x2 v = {a, b}; bf16x2 r = __builtin_convertvector(v, bf16x2); return __builtin_bit_cast(unsigned, r); }
__device__ __forceinline__ f32x4 mfma(bf16x8 a, bf16x8 b, f32x4 c) { return __builtin_amdgcn_mfma_f32_16x16x32_bf16(a, b, c, 0, 0, 0); }
__device__ __forceinline__ unsigned lds_add(LAS char* p, unsigned v) { return __hip_atomic_fetch_add((LAS unsigned*)p, v, __ATOMIC_RELAXED, __HIP_MEMORY_SCOPE_WORKGROUP); }
__device__ __forceinline__ LAS unsigned* ldsp(unsigned a) { return (LAS unsigned*)(size_t)a; }
__device__ __forceinline__ unsigned byte0_x4(unsigned v) { unsigned r; asm("v_lshlrev_b32_sdwa %0, 2, %1 dst_sel:DWORD dst_unused:UNUSED_PAD src0_sel:DWORD src1_sel:BYTE_0" : "=v"(r) : "v"(v)); return r; }
__device__ __forceinline__ unsigned mad24(unsigned a, unsigned b, unsigned c) { unsigned r; asm("v_mad_u32_u24 %0, %1, %2, %3" : "=v"(r) : "v"(a), "s"(b), "v"(c)); return r; }
__device__ __forceinline__ void lds_arrive(LAS char* p, int lane) {
    asm volatile("s_waitcnt lgkmcnt(0)" ::: "memory");
    if (lane == 0) __hip_atomic_fetch_add((LAS unsigned*)p, 1u, __ATOMIC_RELAXED, __HIP_MEMORY_SCOPE_WORKGROUP);
}
__device__ __forceinline__ void lds_await(LAS char* p, unsigned n) {
    while (__builtin_amdgcn_readfirstlane(*(volatile LAS unsigned*)p) < n) __builtin_amdgcn_s_sleep(1);
    asm volatile("" ::: "memory");
}
__device__ __forceinline__ unsigned rfl(unsigned v) { return __builtin_amdgcn_readfirstlane(v); }

__global__ __launch_bounds__(512, 2) void k_graph(const unsigned short* x_, const int* src_, const int* dst_, const unsigned short* wq_, const unsigned short* wv_,
                                                  unsigned* ws_, unsigned short* out_, int N_, int E_) {
    u32x4 srd_d, srd_s, srd_ws, srd_x0, pw, po;
    u64 t_start;
    asm volatile("s_load_dwordx2 s[40:41], %[ka], 0x10\n\ts_load_dwordx2 s[44:45], %[ka], 0x8\n\ts_load_dwordx4 s[60:63], %[ka], 0x30\n\t"
                 "s_load_dwordx2 s[48:49], %[ka], 0x28\n\ts_load_dwordx2 s[52:53], %[ka], 0x0\n\ts_load_dwordx4 s[56:59], %[ka], 0x18\n\t"
                 "s_memrealtime s[64:65]\n\t"
                 "s_mov_b32 s43, 0x20000\n\ts_mov_b32 s47, 0x20000\n\ts_mov_b32 s51, 0x20000\n\ts_mov_b32 s55, 0x20000\n\ts_mov_b32 s50, %c[wsb]"
                 : "=&{s[40:43]}"(srd_d), "=&{s[44:47]}"(srd_s), "=&{s[48:51]}"(srd_ws), "=&{s[52:55]}"(srd_x0), "=&{s[56:59]}"(pw), "=&{s[60:63]}"(po), "=&{s[64:65]}"(t_start)
                 : [ka] "s"(__builtin_amdgcn_kernarg_segment_ptr()), [wsb] "i"(WS_BYTES));
    FENCE();
    __shared__ __attribute__((aligned(16))) char smem_[L_TOTAL];
    LAS char* smem = (LAS char*)smem_;
    const int t = threadIdx.x, l = t & 63, b = blockIdx.x;
    const int w = rfl(t >> 6);
    const int g = l >> 4, n = l & 15, q = (l >> 2) & 3, p = l & 3, pc = t & 7;
    const unsigned lds0 = (unsigned)(size_t)smem;
    const bool isprod = ((b >> 3) & 1) == 0;
    const int pg = ((b & 7) << 4) | (b >> 4);
    const unsigned e0 = pg * CH + 4 * t, e1 = pg * CH + 3072 + t;

    u32x16 ed, es;
    {
        const unsigned o = e0 * 4, o1 = w == 0 ? e1 * 4 : OOB, ok = WS_KEY + t * 4, om = WS_KEY + b * 4;
        const unsigned oc = w < 4 ? (t < 196 ? ((b - 8) << 8) + ((t >> 1) << 16) + ((t & 1) << 7) : OOB) : (t - 256) * 128, dump = lds0 + L_DUMP + w * 256;
        int role = (w < 4 ? 0 : 2) + (isprod ? 0 : 1);
        unsigned k4 = 4096, k8 = 8192;
        asm volatile("s_cmp_eq_u32 %[role], 0\n\ts_cbranch_scc1 1f\n\t"
                     "s_mov_b32 m0, %[dump]\n\ts_cmp_eq_u32 %[role], 1\n\ts_cbranch_scc1 2f\n\t"
                     "s_getpc_b64 s[68:69]\n\ts_mov_b32 s70, " STR(CODE_BYTES) "\n\ts_mov_b32 s71, 0x20000\n\t"
                     ".if " STR(CODEPF) "\n\tbuffer_load_dword %[oc], s[68:71], 0 offen lds\n\t.endif\n\t"
                     "s_waitcnt lgkmcnt(0)\n\ts_branch 3f\n\t"
                     "2:\n\ts_waitcnt lgkmcnt(0)\n\ts_lshl_b32 s54, s62, 8\n\t"
                     ".if " STR(PARTNER) "\n\tbuffer_load_dword %[oc], s[52:55], 0 offen lds\n\t.endif\n\ts_branch 3f\n\t"
                     "1:\n\ts_waitcnt lgkmcnt(0)\n\ts_lshl_b32 s42, s63, 2\n\ts_lshl_b32 s46, s63, 2\n\t"
                     "buffer_load_dwordx4 v[64:67], %[o], s[40:43], 0 offen\n\tbuffer_load_dwordx4 v[68:71], %[o], s[40:43], %[k4] offen\n\t"
                     "buffer_load_dwordx4 v[72:75], %[o], s[40:43], %[k8] offen\n\tbuffer_load_dword v76, %[o1], s[40:43], 0 offen\n\t"
                     "buffer_load_dwordx4 v[80:83], %[o], s[44:47], 0 offen\n\tbuffer_load_dwordx4 v[84:87], %[o], s[44:47], %[k4] offen\n\t"
                     "buffer_load_dwordx4 v[88:91], %[o], s[44:47], %[k8] offen\n\tbuffer_load_dword v92, %[o1], s[44:47], 0 offen\n\t"
                     "buffer_load_dword v93, %[ok], s[48:51], 0 offen sc1\n\tbuffer_load_dword v94, %[om], s[48:51], 0 offen sc1\n\t"
                     "3:\n\ts_lshl_b32 s54, s62, 8"
                     : "=&{v[64:79]}"(ed), "=&{v[80:95]}"(es)
                     : [o] "v"(o), [o1] "v"(o1), [ok] "v"(ok), [om] "v"(om), [oc] "v"(oc), [role] "s"(role), [dump] "s"(dump), [k4] "s"(k4), [k8] "s"(k8),
                       "{s[40:43]}"(srd_d), "{s[44:47]}"(srd_s), "{s[48:51]}"(srd_ws), "{s[52:55]}"(srd_x0), "{s[56:59]}"(pw), "{s[60:63]}"(po), "{s[64:65]}"(t_start)
                     : "memory", "scc", "s68", "s69", "s70", "s71");
        asm volatile("" : "+{s[48:51]}"(srd_ws), "+{s[52:55]}"(srd_x0), "+{s[56:59]}"(pw), "+{s[60:63]}"(po), "+{s[64:65]}"(t_start));
    }
    const void* const x = (const void*)((u64)srd_x0.x | ((u64)srd_x0.y << 32));
    const void* const wq = (const void*)((u64)pw.x | ((u64)pw.y << 32));
    const void* const wv = (const void*)((u64)pw.z | ((u64)pw.w << 32));
    void* const out = (void*)((u64)po.x | ((u64)po.y << 32));
    const int N = po.z, E = po.w;
    const unsigned xbytes = srd_x0.z;
    STAMP_DECL;
    const unsigned ring = lds0 + L_RING + w * 16384;
    const int jn = 8 * n + w;

    unsigned mykey = 0;
    auto node_row = [&](int tile, int m) { const int nd = b + 256 * (8 * (4 * m + g) + tile); return nd < N ? (unsigned)nd << 8 : OOB; };
    auto zero_staging = [&]() {
        const u32x4 z = {0u, 0u, 0u, 0u};
#pragma unroll
        for (int k = 0; k < 5; ++k) *(LAS u32x4*)(smem + L_IMG + k * 8192 + t * 16) = z;
    };

    u32x16 addr;
    f32x32 acc, wva, wvb;
    unsigned cv, lista, cnta, qa, keyn, inf;
    Srd4 srd_x;
    auto loop_constants = [&]() {
        srd_x = make_srd4(x, xbytes, BIAS * 256);
        lista = lds0 + L_LIST + w * (2 * SUBB) + g * 4; cnta = lds0 + L_CNT + (l & 31) * 4; qa = ring + l * 16; keyn = jn; inf = 0x7F800000u;
        asm volatile("" : "+s"(srd_x.s0), "+s"(srd_x.s1), "+s"(srd_x.s2), "+s"(srd_x.s3));
        asm volatile("" : "+v"(lista), "+v"(cnta), "+v"(qa), "+v"(keyn), "+v"(inf));
        const unsigned o0 = (n ^ (((g & 1) << 3) | (g >> 1))) << 4;
        const unsigned ra = (4 * (n & 3) + (n >> 2)) * 256 + (((4 * g) ^ (((n & 7) << 1) | (n >> 3))) << 4);
        const int i = 4 * g + q, r8 = i & 7;
        const unsigned ta = (4 * q + g) * 256 + 8 * (p & 1) + (((p >> 1) ^ (((i & 7) << 1) | (i >> 3))) << 4);
        cv = i * 256 + (((p ^ ((r8 & 1) | ((r8 & 6) << 1))) << 4) | ((p & 1) << 3));
#pragma unroll
        for (int s = 0; s < 4; ++s) { addr[s] = ring + (ra ^ (s << 4)); addr[12 + s] = o0 ^ (s << 5); }
#pragma unroll
        for (int c = 0; c < 8; ++c) addr[4 + c] = ring + (ta ^ (c << 5));
        asm volatile("" : "+{v[176:191]}"(addr), "+v"(cv));
    };
    auto clear_sums = [&]() { acc = 0.f; asm volatile("" : "+{v[128:159]}"(acc)); };
    auto wv_resident = [&]() {
#pragma unroll
        for (int a = 0; a < 8; ++a) {
            LAS char* pa = smem + L_WV0 + (cv ^ ((a >> 1) << 6) ^ ((a & 1) << 3));
            const f32x4 f0 = __builtin_bit_cast(f32x4, cat(tr_read(pa), tr_read(pa + 4096)));
            const f32x4 f1 = __builtin_bit_cast(f32x4, cat(tr_read(pa + 16384), tr_read(pa + 16384 + 4096)));
#pragma unroll
            for (int k = 0; k < 4; ++k) { wva[4 * a + k] = f0[k]; wvb[4 * a + k] = f1[k]; }
        }
        asm volatile("" : "+{v[224:255]}"(wva), "+{v[32:63]}"(wvb));
    };

    if (isprod) zero_staging();
    if (t < 8) *(LAS u32x4*)(smem + L_CNT + t * 16) = u32x4{0u, 0u, 0u, 0u};
    WG_BARRIER();

    if (__builtin_expect(w < 4, 1)) {
        if (__builtin_expect(isprod, 1)) {
            if (PRIO) __builtin_amdgcn_s_setprio(2);
            unsigned ob = WS_INBOX + ((t >> 3) * G1 + pg) * 128 + pc * 16, ima = lds0 + L_IMG + (t >> 3) * 144 + pc * 16;
            PIN(ob); PIN(ima);
            unsigned cna = lds0 + L_CNT1 + (t >> 3) * 4;
            PIN(cna);
            const bool v0 = e0 < (unsigned)E, v1 = e0 + 1024 < (unsigned)E, v2 = e0 + 2048 < (unsigned)E, v3 = w == 0 && e1 < (unsigned)E;
            STAMP(1);
            u32x16 rk;
            const u64 over = rank_edges(ed, es, rk, __builtin_amdgcn_ballot_w64(v0), __builtin_amdgcn_ballot_w64(v1), __builtin_amdgcn_ballot_w64(v2),
                                        __builtin_amdgcn_ballot_w64(v3), t * 4);
            mykey = es[14];
            if (__builtin_expect(over != 0, 0)) {
#pragma unroll
                for (int k = 0; k < 13; ++k)
                    if (rk[k] >= 4u * CAP1) {
                        const unsigned o = WS_OVF + (pg * CH + lds_add(smem + L_CNT1 + 2048, 1)) * 4;
                        const unsigned v = ((((es[k] >> 8) & 0xFFFF) - BIAS) << 16) | ed[k];
                        ST1(v, srd_ws, o, " sc1");
                    }
            }
            if (XSYNC) { lds_arrive(smem + L_CNT + 96, l); lds_await(smem + L_CNT + 96, 4); } else WG_BARRIER();
            STAMP(3);
            f32x32 img; u32x16 meta, sums;
            const unsigned no = read_image(img, meta, sums, ima, cna);
            lds_arrive(smem + L_CNT + 84, l);
            if (__builtin_expect(no != 0, 0)) {
                const unsigned o = t == 0 ? WS_NOVF + pg * 4 : OOB;
                ST1(no, srd_ws, o, " sc1");
                VMWAIT(0);
                if (XSYNC) { lds_arrive(smem + L_CNT + 100, l); lds_await(smem + L_CNT + 100, 4); } else WG_BARRIER();
            }
            publish_image(img, meta, sums, srd_ws, ob);
            STAMP(4);
        } else {
            const unsigned o = ((b >> 4) * 256 + t) * 128, om = WS_KEY + b * 4;
            STAMP(1);
            if (PF_R) while (__builtin_amdgcn_s_memrealtime() < t_start + PF_T0) __builtin_amdgcn_s_sleep(1);
            for (int r = 0; r < PF_R; ++r) {
                asm volatile("s_waitcnt vmcnt(%c0)" :: "i"(PF_D) : "memory");
                if (__builtin_amdgcn_s_memrealtime() > t_start + PF_T) break;
                const unsigned so = r << 19;
                asm volatile("buffer_load_dword %1, %0, %2 offen lds" :: "s"(srd_x0), "v"(o), "s"(so) : "memory");
            }
            asm volatile("buffer_load_dword %0, %1, %2, 0 offen sc1\n\ts_waitcnt vmcnt(0)" : "=&v"(mykey) : "v"(om), "s"(srd_ws) : "memory");
            if (PRIO) __builtin_amdgcn_s_setprio(2);
            STAMP(2); STAMP(3); STAMP(4);
        }
        STAMP(5);
        mykey = rfl(mykey) + KEY_STEP;
        loop_constants();
        clear_sums();
        unsigned ib = WS_INBOX + b * (G1 * 128) + t * 16;
        PIN(ib);
        while (__builtin_amdgcn_s_memrealtime() < t_start + POLL_T) __builtin_amdgcn_s_sleep(1);
        STAMP(6);
        u32x16 raw;
        inbox_request(raw, srd_ws, ib);
        if (__builtin_expect(inbox_stage(raw, srd_ws, ib, mykey, 16 * pc - 8, lds0 + L_HDR + (t >> 3) * 4) != 0, 0)) {
            LAS unsigned* hp = (LAS unsigned*)(smem + L_HDR) + 32 * (l >> 3 & 3) + 8 * w + (l & 7);
            const unsigned hv = *hp;
            for (u64 m = __builtin_amdgcn_ballot_w64(l < 32 && hv > 4u * CAP1); m; m &= m - 1) {
                const int k = __builtin_ctzll(m), gp = 32 * (k >> 3) + 8 * w + (k & 7);
                unsigned u, no; const unsigned o = WS_OVF + (gp * CH + l) * 4, on = WS_NOVF + gp * 4;
                asm volatile("buffer_load_dword %0, %2, %4, 0 offen sc1\n\tbuffer_load_dword %1, %3, %4, 0 offen sc1\n\ts_waitcnt vmcnt(0)"
                             : "=&v"(u), "=&v"(no) : "v"(o), "v"(on), "s"(srd_ws) : "memory");
                if (rfl(no) <= 64) {
                    if (l < no && (u & 0xFF) == (unsigned)b) {
                        const unsigned e = __builtin_amdgcn_perm((u >> 16) + BIAS, u, 0x01050401u), key = (e >> 21) & 60;
                        *(LAS unsigned*)(smem + L_LIST + key * (SUBB / 4) + min(lds_add(smem + L_CNT + key, 4), SUBB - 4u)) = e;
                    }
                    if (l == k) *hp = hv | 0x80000000u;
                } else *(LAS unsigned*)(smem + L_CNT + 64) = 1;
            }
        }
        if (PRIO) __builtin_amdgcn_s_setprio(0);
        STAMP(7);
    } else {
        const int j = w - 4;
        STAMP(1);
        u32x4 wr[8], xr[8];
        {
            const u32x4 srd_q = make_srd(wq, 32768);
            const unsigned o = j * 8192 + l * 16, k4 = 4096;
            LD4(wr[0], srd_q, o, ""); LD4(wr[1], srd_q, o, " offset:1024"); LD4(wr[2], srd_q, o, " offset:2048"); LD4(wr[3], srd_q, o, " offset:3072");
            LD4S(wr[4], srd_q, o, k4, ""); LD4S(wr[5], srd_q, o, k4, " offset:1024"); LD4S(wr[6], srd_q, o, k4, " offset:2048"); LD4S(wr[7], srd_q, o, k4, " offset:3072");
#pragma unroll
            for (int m = 0; m < 8; ++m) { const unsigned ox = node_row(m < 4 ? j : w, m & 3) + n * 16; LD4(xr[m], srd_x0, ox, ""); }
        }
        unsigned aqa[8], xfa[4];
        unsigned xa = ring + g * 256 + ((n ^ g) << 4), wa = lds0 + L_WQ + j * 16384 + g * 256 + ((n ^ g) << 4) + ((n & 4) << 1);
        {
            const unsigned aq = lds0 + L_WQ + (8 * g + q) * 256 + ((((4 * p) ^ (q | ((g & 1) << 2))) << 4) | ((p & 1) << 3));
#pragma unroll
            for (int s = 0; s < 4; ++s) { xfa[s] = ring + n * 256 + (((4 * s + g) ^ n) << 4); PIN(xfa[s]); }
#pragma unroll
            for (int c = 0; c < 8; ++c) { aqa[c] = aq ^ ((c >> 1) << 4) ^ ((c & 1) << 3); PIN(aqa[c]); }
            PIN(xa); PIN(wa);
        }
        loop_constants();
        STAMP(2);
        if (isprod && !XSYNC) {
            WG_BARRIER();
            if (__builtin_expect(rfl(*(LAS unsigned*)(smem + L_CNT1 + 2048)) != 0, 0)) WG_BARRIER();
            if (W_SLEEP) __builtin_amdgcn_s_sleep(W_SLEEP);
        }
        STAMP(3);
        asm volatile("s_waitcnt vmcnt(%c8)" : "+v"(wr[0]), "+v"(wr[1]), "+v"(wr[2]), "+v"(wr[3]), "+v"(wr[4]), "+v"(wr[5]), "+v"(wr[6]), "+v"(wr[7]) : "i"(8) : "memory");
#pragma unroll
        for (int m = 0; m < 8; ++m) {
            const unsigned a = (wa ^ ((m & 2) << 5)) + m * 1024;
            *(LAS u32x2*)(size_t)a = u32x2{wr[m].x, wr[m].y};
            *(LAS u32x2*)(size_t)(a ^ 8) = u32x2{wr[m].z, wr[m].w};
        }
        asm volatile("s_waitcnt vmcnt(0)" : "+v"(xr[0]), "+v"(xr[1]), "+v"(xr[2]), "+v"(xr[3]), "+v"(xr[4]), "+v"(xr[5]), "+v"(xr[6]), "+v"(xr[7]) :: "memory");
#pragma unroll
        for (int m = 0; m < 8; ++m) *(LAS u32x4*)(size_t)((xa ^ ((m & 3) << 6)) + m * 1024) = xr[m];
        STAMP(4);
        lds_arrive(smem + L_CNT + 80, l);
        lds_await(smem + L_CNT + 80, 4);
        STAMP(5);
        bf16x8 xo[2][4], fr[2][8];
        f32x4 qc[2][8];
        auto wq_frags = [&](int u, bf16x8 (&f)[8]) {
#pragma unroll
            for (int a = 0; a < 8; ++a) {
                LAS char* pa = (LAS char*)(size_t)aqa[a] + u * 16384;
                f[a] = cat(tr_read(pa), tr_read(pa + 1024));
            }
        };
#pragma unroll
        for (int u = 0; u < 4; ++u) { xo[0][u] = *(LAS bf16x8*)(size_t)xfa[u]; xo[1][u] = *(LAS bf16x8*)(size_t)(xfa[u] + 4096); }
        wq_frags(0, fr[0]);
        {
            const Srd4 srd_v = make_srd4(wv, 32768);
            const unsigned r0 = (j * 32 + g) << 8, o0 = (n ^ ((g & 1) | ((g & 2) << 1))) << 4;
            const unsigned e[8] = {r0, r0 + 1024, r0 + 2048, r0 + 3072, r0 + 4096, r0 + 5120, r0 + 6144, r0 + 7168};
            const unsigned base = lds0 + (j < 2 ? L_WV0 + j * 16384 : L_WV1 + (j - 2) * 8192);
            const u32x4 off = {o0, o0 ^ 128, o0, o0 ^ 128};
            dma_half(base, srd_v, e, off); dma_half(base + 4096, srd_v, e + 4, off);
        }
#pragma unroll
        for (int u = 0; u < 4; ++u) {
            if (u < 3) wq_frags(u + 1, fr[(u + 1) & 1]);
            FENCE();
#pragma unroll
            for (int a = 0; a < 8; ++a)
#pragma unroll
                for (int tt = 0; tt < 2; ++tt) qc[tt][a] = mfma(fr[u & 1][a], xo[tt][u], u ? qc[tt][a] : f32x4{0.f, 0.f, 0.f, 0.f});
            FENCE();
            if (!WVLATE && u == 1 && j < 2) { VMWAIT(0); lds_arrive(smem + L_CNT + 88, l); }
        }
        STAMP(6);
        if (isprod) lds_await(smem + L_CNT + 84, 4);
#pragma unroll
        for (int tt = 0; tt < 2; ++tt)
#pragma unroll
            for (int s = 0; s < 4; ++s) {
                const float k = -0.08838834764831845f * 1.4426950408889634f;
                const f32x4 c0 = qc[tt][2 * s] * k, c1 = qc[tt][2 * s + 1] * k;
                *(LAS u32x4*)(size_t)(ring - (tt ? 0 : 65536) + s * 1024 + l * 16) = u32x4{pk2(c0[0], c0[1]), pk2(c0[2], c0[3]), pk2(c1[0], c1[1]), pk2(c1[2], c1[3])};
            }
        clear_sums();
        if (!WVLATE) { lds_await(smem + L_CNT + 88, 2); wv_resident(); }
        VMWAIT(0);
        STAMP(7);
    }
    WG_BARRIER();
    STAMP(8);
    if (LPRIO >= 3) __builtin_amdgcn_s_setprio(3);
    if (LPRIO == 1 && w >= 4) __builtin_amdgcn_s_setprio(1);
    if (LPRIO == 2) { if (w >= 6) __builtin_amdgcn_s_setprio(3); else if (w >= 4) __builtin_amdgcn_s_setprio(2); else if (w >= 2) __builtin_amdgcn_s_setprio(1); }

    u32x16 qf;
    const u32x4 doff = {addr[12], addr[13], addr[14], addr[15]};
    LAS char* mylist = smem + L_LIST + w * (2 * SUBB);
    int gcur = G1, pos = 0;
    unsigned hmask = 0xFFFFFFFFu;
    auto batch = [&](auto first_) {
        constexpr bool first = decltype(first_)::value;
        auto entries = [&](int grp, unsigned (&e)[8]) {
#pragma unroll
            for (int m = 0; m < 8; ++m) e[m] = *(LAS unsigned*)(size_t)(lista + grp * 128 + m * 16);
        };
        unsigned cw = *(LAS unsigned*)(size_t)(first ? cnta : lds0 + L_CNT + (__builtin_amdgcn_mbcnt_lo(~0u, 0u) & 31) * 4), e0[8], e1[8];
        entries(0, e0);
        if constexpr (first) {
#pragma unroll
            for (int s = 0; s < 4; ++s) {
                const u32x4 v = *(LAS u32x4*)(size_t)(qa + s * 1024);
                qf[4 * s] = v.x; qf[4 * s + 1] = v.y; qf[4 * s + 2] = v.z; qf[4 * s + 3] = v.w;
            }
        }
        entries(1, e1);
        FENCE();
        int lo = __builtin_amdgcn_readlane(cw, 2 * w) >> 2, hi = __builtin_amdgcn_readlane(cw, 2 * w + 1) >> 2;
        if constexpr (first) {
            if (__builtin_amdgcn_readlane(cw, 16)) gcur = 0;
            if (__builtin_amdgcn_ballot_w64(cw > 4u * CAP2)) { gcur = -1; lo = 0; hi = 0; hmask = 0x7FFFFFFFu; }
        }
        const int c = lo + hi, G = (c + 31) >> 5;
        const int nlast = G > 2 ? (((c - 1) & 31) >> 2) + 1 : 8;
        auto join = [&]() {
            for (int k = 0; k < G * 32 - lo; k += 64) {
                const unsigned e = *(LAS unsigned*)(mylist + SUBB + (l + k) * 4);
                if (l + k < G * 32 - lo) *(LAS unsigned*)(mylist + (lo + l + k) * 4) = l + k < hi ? e : PAD;
            }
        };
        const bool early = lo >= 64;
        if (__builtin_expect(!early, 0)) { join(); entries(0, e0); entries(1, e1); }
        if constexpr (first) asm volatile("s_waitcnt lgkmcnt(0)" : "+{v[160:175]}"(qf) :: "memory");
        if (G > 0) dma_group(ring, srd_x, e0, doff);
        if constexpr (first) {
            if (WVLATE) {
                if (G > 1 && w < 6) dma_group(ring + 8192, srd_x, e1, doff);
                wv_resident();
                lds_arrive(smem + L_CNT + 92, l);
                if (w >= 6) { lds_await(smem + L_CNT + 92, 8); if (G > 1) dma_group(ring + 8192, srd_x, e1, doff); }
            } else if (w < 4) {
                if (G > 1) dma_group(ring + 8192, srd_x, e1, doff);
                wv_resident();
                lds_arrive(smem + L_CNT + 92, l);
            } else {
                if (w >= 6) lds_await(smem + L_CNT + 92, 4);
                if (G > 1) dma_group(ring + 8192, srd_x, e1, doff);
            }
        } else if (G > 1) dma_group(ring + 8192, srd_x, e1, doff);
        f32x16 cc;
#pragma unroll
        for (int m = 0; m < 8; ++m) {
            cc[m] = (e0[m] & 0xFF) == keyn ? 0.f : __uint_as_float(inf);
            cc[8 + m] = (e1[m] & 0xFF) == keyn ? 0.f : __uint_as_float(inf);
        }
        if (early) join();
        STAMP(9);
        if (G > 0) gather_loop(acc, qf, addr, u32x4{lista, keyn, inf, 0u}, cc, srd_x, ring, G, nlast);
    };
    batch(std::true_type{});
    while (__builtin_expect(gcur < G1, 0)) {
        unsigned lim = G1 * 32;
        if (gcur >= 0) {
            const u64 m0 = __builtin_amdgcn_ballot_w64(l >= gcur && (int)(*(LAS unsigned*)(smem + L_HDR + l * 4) & hmask) > 4 * CAP1);
            const u64 m1 = __builtin_amdgcn_ballot_w64(l + 64 >= gcur && (int)(*(LAS unsigned*)(smem + L_HDR + 256 + l * 4) & hmask) > 4 * CAP1);
            if (!(m0 | m1)) break;
            gcur = m0 ? __builtin_ctzll(m0) : 64 + __builtin_ctzll(m1);
            unsigned v; const unsigned o = WS_NOVF + gcur * 4;
            asm volatile("buffer_load_dword %0, %1, %2, 0 offen sc1\n\ts_waitcnt vmcnt(0)" : "=&v"(v) : "v"(o), "s"(srd_ws) : "memory");
            lim = rfl(v);
        }
        const unsigned i = pos + t;
        const bool cand = t < CAP2 && i < lim;
        unsigned u;
        { const unsigned o = cand ? (gcur < 0 ? WS_INBOX + b * (G1 * 128) : WS_OVF + gcur * CH * 4) + i * 4 : OOB;
          asm volatile("buffer_load_dword %0, %1, %2, 0 offen sc1\n\ts_waitcnt vmcnt(0)" : "=&v"(u) : "v"(o), "s"(srd_ws) : "memory"); }
        WG_BARRIER();
        if (t < 16) *(LAS unsigned*)(smem + L_CNT + t * 4) = 0;
        WG_BARRIER();
        if (cand) {
            unsigned e; bool v;
            if (gcur < 0) { e = (i & 3) ? u : u ^ mykey; v = ((i & 31) - 2) * 4 < *(LAS unsigned*)(smem + L_HDR + (i >> 5) * 4); }
            else { e = (((u >> 16) + BIAS) << 8) | ((u >> 8) & 0xFF); v = (u & 0xFF) == (unsigned)b; }
            if (v) *(LAS unsigned*)(smem + L_LIST + (e & 7) * (2 * SUBB) + lds_add(smem + L_CNT + (e & 7) * 8, 4)) = e;
        }
        pos += CAP2;
        if ((unsigned)pos >= lim) { ++gcur; pos = 0; }
        WG_BARRIER();
        batch(std::false_type{});
    }
    STAMP(10);

    {
        bf16x8 wl[2][8], ag[4];
#pragma unroll
        for (int u = 0; u < 2; ++u)
#pragma unroll
            for (int a = 0; a < 8; ++a) {
                LAS char* pa = smem + L_WV1 + (cv ^ ((a >> 1) << 6) ^ ((a & 1) << 3)) + u * 8192;
                wl[u][a] = cat(tr_read(pa), tr_read(pa + 4096));
            }
#pragma unroll
        for (int u = 0; u < 4; ++u) {
            const u32x4 v = {pk2(acc[8 * u], acc[8 * u + 1]), pk2(acc[8 * u + 2], acc[8 * u + 3]),
                             pk2(acc[8 * u + 4], acc[8 * u + 5]), pk2(acc[8 * u + 6], acc[8 * u + 7])};
            ag[u] = __builtin_bit_cast(bf16x8, v);
        }
        FENCE();
        f32x4 oc[8];
#pragma unroll
        for (int a = 0; a < 8; ++a) oc[a] = mfma(__builtin_bit_cast(bf16x8, f32x4{wva[4 * a], wva[4 * a + 1], wva[4 * a + 2], wva[4 * a + 3]}), ag[0], f32x4{0.f, 0.f, 0.f, 0.f});
#pragma unroll
        for (int a = 0; a < 8; ++a) oc[a] = mfma(__builtin_bit_cast(bf16x8, f32x4{wvb[4 * a], wvb[4 * a + 1], wvb[4 * a + 2], wvb[4 * a + 3]}), ag[1], oc[a]);
#pragma unroll
        for (int a = 0; a < 8; ++a) oc[a] = mfma(wl[0][a], ag[2], oc[a]);
        FENCE();
        const unsigned ow = ring + n * 256 + ((g ^ n) << 4) + ((g & 1) << 3);
#pragma unroll
        for (int a = 0; a < 8; ++a) oc[a] = mfma(wl[1][a], ag[3], oc[a]);
#pragma unroll
        for (int a = 0; a < 8; ++a)
            *(LAS u32x2*)(size_t)(ow ^ ((a >> 1) << 6) ^ ((a & 1) << 3)) = u32x2{pk2(oc[a][0], oc[a][1]), pk2(oc[a][2], oc[a][3])};
        const auto rs_o = __builtin_amdgcn_make_buffer_rsrc(out, 0, xbytes, 0x00020000);
#pragma unroll
        for (int m = 0; m < 4; ++m) {
            const u32x4 v = *(LAS u32x4*)(size_t)(ring + m * 1024 + l * 16);
            __builtin_amdgcn_raw_buffer_store_b128(v, rs_o, node_row(w, m) + ((n ^ g ^ (4 * m)) << 4), 0, OUT_AUX);
        }
    }
    if (w == 0) { const unsigned o = l == 0 ? WS_KEY + b * 4 : OOB; ST1(mykey, srd_ws, o, " sc1"); }
    STAMP(11);
}

extern "C" void kernel_launch(void* const* d_in, const int* in_sizes, int n_in,
                              void* d_out, int out_size, void* d_ws, size_t ws_size,
                              hipStream_t stream) {
    const int N = in_sizes[0] / 128, E = in_sizes[1];
    k_graph<<<NB, 512, 0, stream>>>((const unsigned short*)d_in[0], (const int*)d_in[1], (const int*)d_in[2],
                                    (const unsigned short*)d_in[3], (const unsigned short*)d_in[4],
                                    (unsigned*)d_ws, (unsigned short*)d_out, N, E);
}
```

```cpp
#define POLL_D 80
#include <hip/hip_runtime.h>
#include <hip/hip_bf16.h>
#include <stdint.h>
#include <type_traits>
#define ORD 1
#define SKIP 0
#define STR2(x) #x
#define STR(x) STR2(x)

typedef __attribute__((ext_vector_type(8))) __bf16 bf16x8;
typedef __attribute__((ext_vector_type(4))) __bf16 bf16x4;
typedef __attribute__((ext_vector_type(2))) __bf16 bf16x2;
typedef __attribute__((ext_vector_type(4))) float f32x4;
typedef __attribute__((ext_vector_type(2))) float f32x2;
typedef __attribute__((ext_vector_type(4))) unsigned u32x4;
typedef __attribute__((ext_vector_type(2))) unsigned u32x2;
typedef __attribute__((ext_vector_type(32))) float f32x32;
typedef __attribute__((ext_vector_type(16))) float f32x16;
typedef __attribute__((ext_vector_type(16))) unsigned u32x16;
typedef __attribute__((ext_vector_type(12))) unsigned u32x12;
typedef __attribute__((ext_vector_type(8))) unsigned u32x8;
typedef unsigned long long u64;
#define LAS __attribute__((address_space(3)))

constexpr int NB = 256;
constexpr int G1 = 128;
constexpr int CH = 3136;
constexpr int CAP1 = 30;
constexpr int CAP2 = 192;
constexpr int SUBB = 784;
#define POLL_AT 3
#define OUT_AUX 16
#define POLL_T 340
#define LPRIO 3
#define PKADD 1
#define CODEPF 1
#define CODE_BYTES 21504
#define PARTNER 1
#define XSYNC 1
#define WVLATE 1
#define FASTIN 1
#define W_SLEEP 3
#define PRIO 1
#define SPLIT 16384
constexpr unsigned KEY_STEP = 0x9E3779B1u;
constexpr unsigned WS_KEY = 0;
constexpr unsigned WS_NOVF = 2048;
constexpr unsigned WS_INBOX = 4096;
constexpr unsigned WS_OVF = WS_INBOX + NB * G1 * 128;
constexpr unsigned WS_BYTES = WS_OVF + G1 * CH * 4;
constexpr unsigned OOB = 0x80000000u;
constexpr unsigned BIAS = 32768 - SPLIT;
constexpr unsigned PAD = 0x00FFFFFFu;

constexpr int L_CNT = 0;
constexpr int L_HDR = 128;
constexpr int L_LIST = 768;
constexpr int L_RING = L_LIST + 16 * SUBB;
constexpr int L_IMG = L_RING;
constexpr int L_CNT1 = L_IMG + NB * 144;
constexpr int L_WQ = L_RING + 2 * 16384 + 8192;
constexpr int L_WV0 = L_RING + 6 * 16384 + 8192;
constexpr int L_WV1 = L_RING + 131072;
constexpr int L_DUMP = L_WV1 + 16384;
constexpr int L_TOTAL = L_DUMP + 2048;
static_assert(L_RING % 256 == 0 && L_TOTAL <= 163840, "LDS map");

#define STAMP_DECL
#define STAMP(k)
#define STAMP_FLUSH(base, k0, k1)

__device__ __forceinline__ u32x4 make_srd(const void* p, unsigned bytes, unsigned back = 0) {
    const u64 a = (u64)p - back;
    u32x4 s; s.x = (unsigned)a; s.y = (unsigned)(a >> 32); s.z = bytes + back; s.w = 0x00020000u; return s;
}
#define LD4(dst, srd, off, FL) asm volatile("buffer_load_dwordx4 %0, %1, %2, 0 offen" FL : "=v"(dst) : "v"(off), "s"(srd) : "memory")
#define LD4S(dst, srd, off, so, FL) asm volatile("buffer_load_dwordx4 %0, %1, %2, %3 offen" FL : "=v"(dst) : "v"(off), "s"(srd), "s"(so) : "memory")
#define LD1(dst, srd, off, FL) asm volatile("buffer_load_dword %0, %1, %2, 0 offen" FL : "=v"(dst) : "v"(off), "s"(srd) : "memory")
#define ST1(val, srd, off, FL) asm volatile("buffer_store_dword %0, %1, %2, 0 offen" FL :: "v"(val), "v"(off), "s"(srd) : "memory")
#define PIN(x) asm volatile("" : "+v"(x))
#define VMWAIT(n) asm volatile("s_waitcnt vmcnt(" #n ")" ::: "memory")
#define FENCE() __builtin_amdgcn_sched_barrier(0)
#define WG_BARRIER() do { asm volatile("s_waitcnt lgkmcnt(0)" ::: "memory"); __builtin_amdgcn_s_barrier(); asm volatile("" ::: "memory"); } while (0)

struct Srd4 { u32x4 s0, s1, s2, s3; };
__device__ __forceinline__ Srd4 make_srd4(const void* p, unsigned bytes, unsigned back = 0) {
    return Srd4{make_srd(p, bytes, back), make_srd(p, bytes, back + 1024), make_srd(p, bytes, back + 2048), make_srd(p, bytes, back + 3072)};
}

__device__ __forceinline__ void set_m0(unsigned v) { asm volatile("s_mov_b32 m0, %0" :: "s"(v) : "memory"); }
template <int R> __device__ __forceinline__ void dma_rows(const Srd4& d, unsigned e, unsigned off) {
    unsigned tmp, mask = 0x00FFFF00u;
    asm volatile("v_and_or_b32 %0, %1, %2, %3\n\tbuffer_load_dwordx4 %0, %4, 0 offen offset:%c5 lds"
                 : "=&v"(tmp) : "v"(e), "s"(mask), "v"(off), "s"(R == 0 ? d.s0 : R == 1 ? d.s1 : R == 2 ? d.s2 : d.s3), "i"(R * 1024) : "memory");
}
__device__ __forceinline__ void dma_half(unsigned ldsbase, const Srd4& d, const unsigned* e, const u32x4& off) {
    set_m0(ldsbase);
    dma_rows<0>(d, e[0], off.x); dma_rows<1>(d, e[1], off.y); dma_rows<2>(d, e[2], off.z); dma_rows<3>(d, e[3], off.w);
}
__device__ __forceinline__ void dma_group(unsigned ldsbase, const Srd4& d, const unsigned (&e)[8], const u32x4& off) {
    asm volatile("s_waitcnt lgkmcnt(0)" ::: "memory");
    dma_half(ldsbase, d, e, off); dma_half(ldsbase + 4096, d, e + 4, off);
}

__device__ __forceinline__ void gather_loop(f32x32& acc, const u32x16& qf, const u32x16& addr, u32x4 misc, f32x16 cc,
                                            const Srd4& d, unsigned ring, int G, int nlast) {
    f32x32 t0, t1; u32x12 t2; int tmp, pr; u64 sm; unsigned mask = 0x00FFFF00u;
    asm volatile(
        ".macro GWAIT k\n\ts_cmp_gt_i32 %[t], \\k\n\ts_cbranch_scc1 1f\n\ts_waitcnt vmcnt(\\k)\n\t.endm\n\t"
        ".macro GROWS so\n\t.irp s, 0, 1, 2, 3\n\t"
        "ds_read_b128 v[64+8*\\s:67+8*\\s], v[176+\\s] offset:\\so\n\t"
        "ds_read_b128 v[68+8*\\s:71+8*\\s], v[176+\\s] offset:\\so+4096\n\t.endr\n\t.endm\n\t"
        ".macro GTR c, so\n\t"
        "ds_read_b64_tr_b16 v[96+4*\\c:97+4*\\c], v[180+\\c] offset:\\so\n\t"
        "ds_read_b64_tr_b16 v[98+4*\\c:99+4*\\c], v[180+\\c] offset:\\so+4096\n\t.endm\n\t"
        ".macro GS s, c\n\t"
        "v_mfma_f32_16x16x32_bf16 v[\\c:\\c+3], v[64+8*\\s:67+8*\\s], v[160+4*\\s:163+4*\\s], v[\\c:\\c+3]\n\t"
        "v_mfma_f32_16x16x32_bf16 v[\\c+4:\\c+7], v[68+8*\\s:71+8*\\s], v[160+4*\\s:163+4*\\s], v[\\c+4:\\c+7]\n\t.endm\n\t"
        ".macro GLOAD m, r, srd\n\t"
        "v_and_or_b32 v195, v[212+\\m], %[mask], v[188+\\r]\n\t"
        "buffer_load_dwordx4 v195, \\srd, 0 offen offset:1024*\\r lds\n\t.endm\n\t"
        ".macro GPART m, r, srd\n\ts_cmp_le_i32 %[nl], \\m\n\ts_cbranch_scc1 4f\n\tGLOAD \\m, \\r, \\srd\n\t.endm\n\t"
        ".macro GSUM m\n\tv_mfma_f32_16x16x32_bf16 v[128+4*(\\m):131+4*(\\m)], v[96+4*(\\m):99+4*(\\m)], v[220:223], v[128+4*(\\m):131+4*(\\m)]\n\t.endm\n\t"
        ".macro GCMP m, d\n\tv_cmp_ne_u32_sdwa \\d, v[212+\\m], v193 src0_sel:BYTE_0 src1_sel:DWORD\n\t.endm\n\t"
        ".macro GPAIR m, c\n\t"
        "GSUM \\m\n\tGCMP \\m+1, %[sm]\n\tv_cndmask_b32_e32 v[\\c+\\m], 0, v194, vcc\n\t"
        "GSUM \\m+1\n\t.if \\m < 6\n\tGCMP \\m+2, vcc\n\t.else\n\ts_cmp_gt_i32 %[rem], 0\n\t.endif\n\t"
        "v_cndmask_b32_e64 v[\\c+\\m+1], 0, v194, %[sm]\n\t.endm\n\t"
        ".macro GSTEP so, c, eo\n\t"
        "GTR 0, \\so\n\t"
        "s_waitcnt lgkmcnt(2)\n\tGS 0, \\c\n\t"
        "GTR 1, \\so\n\tGTR 2, \\so\n\tGS 1, \\c\n\t"
        "GTR 3, \\so\n\tGTR 4, \\so\n\tGS 2, \\c\n\t"
        "GTR 5, \\so\n\tGTR 6, \\so\n\tGS 3, \\c\n\t"
        "GTR 7, \\so\n\t"
        "ds_read2_b32 v[212:213], v192 offset0:\\eo offset1:\\eo+4\n\t"
        "ds_read2_b32 v[214:215], v192 offset0:\\eo+8 offset1:\\eo+12\n\t"
        "ds_read2_b32 v[216:217], v192 offset0:\\eo+16 offset1:\\eo+20\n\t"
        "ds_read2_b32 v[218:219], v192 offset0:\\eo+24 offset1:\\eo+28\n\t"
        "s_add_u32 m0, %[ring], \\so\n\t"
        "s_cmp_gt_i32 %[rem], 3\n\t"
        ".irp k, 0, 1, 2, 3, 4, 5, 6, 7\n\tv_exp_f32_e32 v[\\c+\\k], v[\\c+\\k]\n\t.endr\n\t"
        "s_waitcnt lgkmcnt(0)\n\t"
        "s_cbranch_scc0 2f\n\t"
        "GLOAD 0, 0, %[s0]\n\tGLOAD 1, 1, %[s1]\n\tGLOAD 2, 2, %[s2]\n\tGLOAD 3, 3, %[s3]\n\t"
        "s_add_u32 m0, m0, 0x1000\n\t"
        "GLOAD 4, 0, %[s0]\n\tGLOAD 5, 1, %[s1]\n\tGLOAD 6, 2, %[s2]\n\tGLOAD 7, 3, %[s3]\n\t"
        "GSIG \\c\n\t"
        "s_waitcnt vmcnt(8)\n\t"
        "s_branch 1f\n\t"
        "2:\n\t"
        "s_mov_b32 %[t], 0\n\t"
        "s_cmp_eq_u32 %[rem], 3\n\ts_cbranch_scc0 4f\n\t"
        "s_mov_b32 %[t], %[nl]\n\t"
        "GLOAD 0, 0, %[s0]\n\tGPART 1, 1, %[s1]\n\tGPART 2, 2, %[s2]\n\tGPART 3, 3, %[s3]\n\t"
        "s_add_u32 m0, m0, 0x1000\n\t"
        "GPART 4, 0, %[s0]\n\tGPART 5, 1, %[s1]\n\tGPART 6, 2, %[s2]\n\tGPART 7, 3, %[s3]\n\t"
        "4:\n\t"
        "GSIG \\c\n\t"
        "s_waitcnt vmcnt(8)\n\t"
        "GWAIT 7\n\tGWAIT 6\n\tGWAIT 5\n\tGWAIT 4\n\tGWAIT 3\n\tGWAIT 2\n\tGWAIT 1\n\tGWAIT 0\n\t"
        "1:\n\t"
        "GROWS 8192-\\so\n\t"
        "GCMP 0, vcc\n\t"
        "s_add_i32 %[rem], %[rem], -1\n\t"
        "GPAIR 0, \\c\n\tGPAIR 2, \\c\n\tGPAIR 4, \\c\n\tGPAIR 6, \\c\n\t"
        ".endm\n\t"
        ".macro GSIG c\n\t"
        ".if " STR(PKADD) "\n\t.irp k, 0, 2, 4, 6\n\tv_pk_add_f32 v[\\c+\\k:\\c+\\k+1], v[\\c+\\k:\\c+\\k+1], 1.0 op_sel_hi:[1,0]\n\t.endr\n\t.else\n\t"
        ".irp k, 0, 1, 2, 3, 4, 5, 6, 7\n\tv_add_f32_e32 v[\\c+\\k], 1.0, v[\\c+\\k]\n\t.endr\n\t.endif\n\t"
        ".irp k, 0, 1, 2, 3, 4, 5, 6, 7\n\tv_rcp_f32_e32 v[\\c+\\k], v[\\c+\\k]\n\t.endr\n\t"
        ".irp k, 0, 1, 2, 3\n\tv_cvt_pk_bf16_f32 v[220+\\k], v[\\c+2*\\k], v[\\c+2*\\k+1]\n\t.endr\n\t.endm\n\t"
        "s_waitcnt vmcnt(8)\n\t"
        "s_cmp_gt_i32 %[rem], 1\n\ts_cbranch_scc1 1f\n\ts_waitcnt vmcnt(0)\n\t1:\n\t"
        "GROWS 0\n\t"
        ".if " STR(LPRIO) " >= 3\n\ts_mov_b32 %[pr], 0\n\ts_setprio 2\n\t.endif\n\t"
        "0:\n\t"
        ".if " STR(LPRIO) " == 3\n\t"
        "s_cmp_eq_u32 %[pr], 1\n\ts_cbranch_scc0 5f\n\ts_setprio 1\n\t5:\n\t"
        "s_cmp_eq_u32 %[pr], 2\n\ts_cbranch_scc0 5f\n\ts_setprio 0\n\t5:\n\t"
        "s_add_u32 %[pr], %[pr], 1\n\t.endif\n\t"
        "GSTEP 0, 196, 64\n\t"
        "s_cbranch_scc0 9f\n\t"
        "GSTEP 8192, 204, 96\n\t"
        "v_add_u32_e32 v192, 0x100, v192\n\t"
        "s_cbranch_scc1 0b\n\t"
        "9:\n\t"
        "s_waitcnt lgkmcnt(0)\n\ts_nop 6\n\t"
        ".purgem GWAIT\n\t.purgem GROWS\n\t.purgem GTR\n\t.purgem GS\n\t.purgem GLOAD\n\t.purgem GPART\n\t.purgem GSUM\n\t.purgem GCMP\n\t.purgem GPAIR\n\t.purgem GSTEP\n\t.purgem GSIG"
        : "+{v[128:159]}"(acc), "+{v[192:195]}"(misc), "+{v[196:211]}"(cc), "=&{v[64:95]}"(t0), "=&{v[96:127]}"(t1), "=&{v[212:223]}"(t2),
          [rem] "+s"(G), [t] "=&s"(tmp), [pr] "=&s"(pr), [sm] "=&s"(sm)
        : "{v[160:175]}"(qf), "{v[176:191]}"(addr), [s0] "s"(d.s0), [s1] "s"(d.s1), [s2] "s"(d.s2), [s3] "s"(d.s3),
          [mask] "s"(mask), [ring] "s"(ring), [nl] "s"(nlast)
        : "vcc", "scc", "memory");
}

__device__ __forceinline__ u64 rank_edges(u32x16& ed, u32x16& es, u32x16& rk, u64 m0, u64 m1, u64 m2, u64 m3, unsigned keya) {
    u32x16 b4; u64 over; unsigned sel = 0x01050401u, c36 = 36;
    asm volatile(
        ".macro RADD k\n\tv_lshlrev_b32_sdwa v[96+\\k], 2, v[64+\\k] dst_sel:DWORD dst_unused:UNUSED_PAD src0_sel:DWORD src1_sel:BYTE_0\n\t"
        "ds_add_rtn_u32 v[112+\\k], v[96+\\k], v111 offset:%c[cnt]\n\t.endm\n\t"
        ".macro RENT k\n\tv_add_u32_e32 v[80+\\k], %c[bias], v[80+\\k]\n\tv_perm_b32 v[80+\\k], v[80+\\k], v[64+\\k], %[sel]\n\t.endm\n\t"
        ".macro RPUT k\n\tv_min_u32_e32 v110, %c[cap], v[112+\\k]\n\tv_mad_u32_u24 v110, v[96+\\k], %[c36], v110\n\t"
        "ds_write_b32 v110, v[80+\\k] offset:%c[img]\n\t.endm\n\t"
        "v_mov_b32_e32 v111, 4\n\t"
        ".irp k, 0, 2, 4, 6, 8, 10, 12\n\tv_mov_b64_e32 v[112+\\k:113+\\k], 0\n\t.endr\n\t"
        "s_mov_b64 exec, %[m0]\n\ts_waitcnt vmcnt(9)\n\tRADD 0\n\tRADD 1\n\tRADD 2\n\tRADD 3\n\t"
        "s_mov_b64 exec, %[m1]\n\ts_waitcnt vmcnt(8)\n\tRADD 4\n\tRADD 5\n\tRADD 6\n\tRADD 7\n\t"
        "s_mov_b64 exec, %[m2]\n\ts_waitcnt vmcnt(7)\n\tRADD 8\n\tRADD 9\n\tRADD 10\n\tRADD 11\n\t"
        "s_mov_b64 exec, %[m3]\n\ts_waitcnt vmcnt(6)\n\tRADD 12\n\t"
        "s_mov_b64 exec, %[m0]\n\ts_waitcnt vmcnt(5)\n\tRENT 0\n\tRENT 1\n\tRENT 2\n\tRENT 3\n\t"
        "s_waitcnt lgkmcnt(9)\n\tRPUT 0\n\tRPUT 1\n\tRPUT 2\n\tRPUT 3\n\t"
        "s_mov_b64 exec, %[m1]\n\ts_waitcnt vmcnt(4)\n\tRENT 4\n\tRENT 5\n\tRENT 6\n\tRENT 7\n\t"
        "s_waitcnt lgkmcnt(9)\n\tRPUT 4\n\tRPUT 5\n\tRPUT 6\n\tRPUT 7\n\t"
        "s_mov_b64 exec, %[m2]\n\ts_waitcnt vmcnt(3)\n\tRENT 8\n\tRENT 9\n\tRENT 10\n\tRENT 11\n\t"
        "s_waitcnt lgkmcnt(9)\n\tRPUT 8\n\tRPUT 9\n\tRPUT 10\n\tRPUT 11\n\t"
        "s_mov_b64 exec, %[m3]\n\ts_waitcnt vmcnt(0)\n\tRENT 12\n\t"
        "s_waitcnt lgkmcnt(12)\n\tRPUT 12\n\t"
        "s_mov_b64 exec, -1\n\t"
        "v_add_u32_e32 v93, %c[step], v93\n\t"
        "v_max3_u32 v110, v112, v113, v114\n\tv_max3_u32 v109, v115, v116, v117\n\t"
        "ds_write_b32 %[keya], v93 offset:%c[cnt]+1024\n\t"
        "v_max3_u32 v110, v110, v118, v119\n\tv_max3_u32 v109, v109, v120, v121\n\t"
        "v_max3_u32 v110, v110, v122, v123\n\tv_max3_u32 v110, v110, v109, v124\n\t"
        "v_cmp_le_u32_e32 vcc, %c[cap], v110\n\ts_mov_b64 %[over], vcc\n\t"
        ".purgem RADD\n\t.purgem RENT\n\t.purgem RPUT"
        : "+{v[64:79]}"(ed), "+{v[80:95]}"(es), "=&{v[96:111]}"(b4), "=&{v[112:127]}"(rk), [over] "=&s"(over)
        : [m0] "s"(m0), [m1] "s"(m1), [m2] "s"(m2), [m3] "s"(m3), [keya] "v"(keya), [sel] "s"(sel), [c36] "s"(c36),
          [cnt] "i"(L_CNT1), [img] "i"(L_IMG + 8), [bias] "i"(BIAS), [cap] "i"(4 * CAP1), [step] "i"(KEY_STEP)
        : "vcc", "memory");
    return over;
}
__device__ __forceinline__ unsigned read_image(f32x32& img, u32x16& meta, u32x16& sums, unsigned ima, unsigned cna) {
    unsigned no;
    asm volatile(
        "v_mov_b32_e32 v121, 0\n\tds_read_b32 v120, v121 offset:%c[cnt]+2048\n\t"
        ".irp k, 0, 1, 2, 3, 4, 5, 6, 7\n\tds_read_b128 v[64+4*\\k:67+4*\\k], %[ima] offset:4608*\\k\n\t.endr\n\t"
        ".irp k, 0, 1, 2, 3\n\tds_read2_b32 v[96+2*\\k:97+2*\\k], %[cna] offset0:64*\\k offset1:64*\\k+32\n\t.endr\n\t"
        "s_waitcnt lgkmcnt(11)\n\tv_readfirstlane_b32 %[no], v120\n\t"
        "v_add_u32_e32 v121, 0x400, %[cna]\n\t"
        ".irp k, 0, 1, 2, 3\n\tds_read2_b32 v[104+2*\\k:105+2*\\k], v121 offset0:64*\\k offset1:64*\\k+32\n\t.endr\n\t"
        "s_waitcnt lgkmcnt(12)\n\t"
        ".irp k, 0, 1, 2, 3\n\tv_add3_u32 v[112+\\k], v[64+4*\\k], v[65+4*\\k], v[66+4*\\k]\n\tv_add_u32_e32 v[112+\\k], v[112+\\k], v[67+4*\\k]\n\t.endr\n\t"
        "s_waitcnt lgkmcnt(8)\n\t"
        ".irp k, 4, 5, 6, 7\n\tv_add3_u32 v[112+\\k], v[64+4*\\k], v[65+4*\\k], v[66+4*\\k]\n\tv_add_u32_e32 v[112+\\k], v[112+\\k], v[67+4*\\k]\n\t.endr\n\t"
        ".irp k, 0, 1, 2, 3, 4, 5, 6, 7\n\tv_add_u32_dpp v[112+\\k], v[112+\\k], v[112+\\k] quad_perm:[1,0,3,2] row_mask:0xf bank_mask:0xf\n\t.endr\n\t"
        ".irp k, 0, 1, 2, 3, 4, 5, 6, 7\n\tv_add_u32_dpp v[112+\\k], v[112+\\k], v[112+\\k] quad_perm:[2,3,0,1] row_mask:0xf bank_mask:0xf\n\t.endr\n\t"
        ".irp k, 0, 1, 2, 3, 4, 5, 6, 7\n\tv_add_u32_dpp v[112+\\k], v[112+\\k], v[112+\\k] row_half_mirror row_mask:0xf bank_mask:0xf\n\t.endr\n\t"
        "s_waitcnt lgkmcnt(0)"
        : "=&{v[64:95]}"(img), "=&{v[96:111]}"(meta), "=&{v[112:127]}"(sums), [no] "=&s"(no)
        : [ima] "v"(ima), [cna] "v"(cna), [cnt] "i"(L_CNT1) : "memory");
    return no;
}
__device__ __forceinline__ void publish_image(f32x32 img, u32x16 meta, u32x16 sums, const u32x4& srd, unsigned ob) {
    u64 pc0 = 0x0101010101010101ull; unsigned so;
    asm volatile(
        ".macro PUB k\n\ts_mov_b64 exec, %[pc0]\n\tv_mov_b32_e32 v[64+4*\\k], v[96+\\k]\n\tv_mov_b32_e32 v[65+4*\\k], v[112+\\k]\n\t"
        "s_mov_b64 exec, -1\n\tv_xor_b32_e32 v[64+4*\\k], v[64+4*\\k], v[104+\\k]\n\t"
        "buffer_store_dwordx4 v[64+4*\\k:67+4*\\k], %[ob], %[srd], %[so] offen sc1\n\ts_add_u32 %[so], %[so], %c[step]\n\t.endm\n\t"
        "s_mov_b32 %[so], 0\n\t"
        ".irp k, 0, 1, 2, 3, 4, 5, 6, 7\n\tPUB \\k\n\t.endr\n\t.purgem PUB"
        : "+{v[64:95]}"(img), [so] "=&s"(so)
        : "{v[96:111]}"(meta), "{v[112:127]}"(sums), [srd] "s"(srd), [ob] "v"(ob), [pc0] "s"(pc0), [step] "i"(32 * G1 * 128) : "scc", "memory");
}

__device__ __forceinline__ void inbox_request(u32x16& raw, const u32x4& srd, unsigned ib) {
    unsigned o1 = 0x1000, o2 = 0x2000, o3 = 0x3000;
    asm volatile("buffer_load_dwordx4 v[64:67], %1, %2, 0 offen sc1\n\tbuffer_load_dwordx4 v[68:71], %1, %2, %3 offen sc1\n\t"
                 "buffer_load_dwordx4 v[72:75], %1, %2, %4 offen sc1\n\tbuffer_load_dwordx4 v[76:79], %1, %2, %5 offen sc1"
                 : "=&{v[64:79]}"(raw) : "v"(ib), "s"(srd), "s"(o1), "s"(o2), "s"(o3) : "memory");
}
__device__ __forceinline__ unsigned inbox_stage(u32x16 raw, const u32x4& srd, unsigned ib, unsigned key, int i4, unsigned hdra) {
    u32x16 t0, t2; u32x4 t1; u64 m0, m1, m2, m3; unsigned todo, fresh, spin, o1, o2, o3, over;
    u64 pc0 = 0x0101010101010101ull;
    asm volatile(
        ".macro IVAL j, so\n\t"
        "s_bitcmp1_b32 %[todo], \\j\n\ts_cbranch_scc0 2f\n\t"
        "v_xor_b32_e32 v[64+4*\\j], %[key], v[64+4*\\j]\n\t"
        "v_sub_u32_e32 v97, 0, v[65+4*\\j]\n\t"
        "v_add_u32_e32 v96, v[64+4*\\j], v[65+4*\\j]\n\t"
        "v_mov_b32_dpp v[80+\\j], v[64+4*\\j] quad_perm:[0,0,0,0] row_mask:0xf bank_mask:0xf\n\t"
        "v_cndmask_b32_e64 v96, v96, v97, %[pc0]\n\t"
        "s_nop 0\n\t"
        "v_mov_b32_dpp v[80+\\j], v[80+\\j] row_shr:4 row_mask:0xf bank_mask:0xa\n\t"
        "v_cmp_gt_i32_e32 vcc, v[80+\\j], v92\n\t"
        "v_add3_u32 v96, v96, v[66+4*\\j], v[67+4*\\j]\n\t"
        "v_lshrrev_b32_e32 v97, 14, v[80+\\j]\n\t"
        "v_cndmask_b32_e32 v96, 0, v96, vcc\n\t"
        "s_nop 1\n\tv_add_u32_dpp v96, v96, v96 quad_perm:[1,0,3,2] row_mask:0xf bank_mask:0xf\n\t"
        "s_nop 1\n\tv_add_u32_dpp v96, v96, v96 quad_perm:[2,3,0,1] row_mask:0xf bank_mask:0xf\n\t"
        "s_nop 1\n\tv_add_u32_dpp v96, v96, v96 row_half_mirror row_mask:0xf bank_mask:0xf\n\t"
        "v_or_b32_e32 v96, v96, v97\n\t"
        "v_cmp_ne_u32_e32 vcc, 0, v96\n\t"
        "s_cbranch_vccz 1f\n\t"
        "buffer_load_dwordx4 v[64+4*\\j:67+4*\\j], %[ib], %[srd], \\so offen sc1\n\t"
        "s_branch 2f\n\t"
        "1:\n\ts_bitset1_b32 %[fresh], \\j\n\t"
        "2:\n\t.endm\n\t"
        ".macro IRANK j\n\t"
        "s_bitcmp1_b32 %[fresh], \\j\n\ts_cbranch_scc0 2f\n\t"
        "v_cmp_lt_u32_e64 %[m0], v92, v[80+\\j]\n\tv_cmp_lt_u32_e64 %[m1], v93, v[80+\\j]\n\t"
        "v_cmp_lt_u32_e64 %[m2], v94, v[80+\\j]\n\tv_cmp_lt_u32_e64 %[m3], v95, v[80+\\j]\n\t"
        ".irp k, 0, 1, 2, 3\n\tv_lshrrev_b32_e32 v[84+\\k], 21, v[64+4*\\j+\\k]\n\t.endr\n\t"
        ".irp k, 0, 1, 2, 3\n\tv_and_b32_e32 v[84+\\k], 60, v[84+\\k]\n\t.endr\n\t"
        "v_cmp_lt_u32_e32 vcc, %c[cap], v[80+\\j]\n\t"
        "s_mov_b64 exec, %[m0]\n\tds_add_rtn_u32 v88, v84, v98 offset:%c[cnt]\n\t"
        "s_mov_b64 exec, %[m1]\n\tds_add_rtn_u32 v89, v85, v98 offset:%c[cnt]\n\t"
        "s_mov_b64 exec, %[m2]\n\tds_add_rtn_u32 v90, v86, v98 offset:%c[cnt]\n\t"
        "s_mov_b64 exec, %[m3]\n\tds_add_rtn_u32 v91, v87, v98 offset:%c[cnt]\n\t"
        "s_mov_b64 exec, %[pc0]\n\tds_write_b32 %[hdra], v[80+\\j] offset:128*\\j\n\t"
        "s_mov_b64 exec, -1\n\t"
        "s_cbranch_vccz 1f\n\t"
        "s_mov_b32 %[over], 1\n\t"
        "1:\n\ts_waitcnt lgkmcnt(1)\n\t"
        ".irp k, 0, 1, 2, 3\n\tv_min_u32_e32 v[88+\\k], %c[sub]-4, v[88+\\k]\n\t.endr\n\t"
        ".irp k, 0, 1, 2, 3\n\tv_mad_u32_u24 v[88+\\k], v[84+\\k], %[sub4], v[88+\\k]\n\t.endr\n\t"
        "s_mov_b64 exec, %[m0]\n\tds_write_b32 v88, v[64+4*\\j] offset:%c[list]\n\t"
        "s_mov_b64 exec, %[m1]\n\tds_write_b32 v89, v[65+4*\\j] offset:%c[list]\n\t"
        "s_mov_b64 exec, %[m2]\n\tds_write_b32 v90, v[66+4*\\j] offset:%c[list]\n\t"
        "s_mov_b64 exec, %[m3]\n\tds_write_b32 v91, v[67+4*\\j] offset:%c[list]\n\t"
        "s_mov_b64 exec, -1\n\t"
        "2:\n\t.endm\n\t"
        ".macro FADD j, kb, rb\n\t"
        ".irp k, 0, 1, 2, 3\n\tv_lshrrev_b32_e32 v[\\kb+\\k], 21, v[64+4*\\j+\\k]\n\t.endr\n\t"
        ".irp k, 0, 1, 2, 3\n\tv_and_b32_e32 v[\\kb+\\k], 60, v[\\kb+\\k]\n\t.endr\n\t"
        "v_cmpx_lt_u32_e64 %[m0], v94, v[80+\\j]\n\tds_add_rtn_u32 v[\\rb+2], v[\\kb+2], v98 offset:%c[cnt]\n\t"
        "v_cmpx_lt_u32_e64 %[m0], v95, v[80+\\j]\n\tds_add_rtn_u32 v[\\rb+3], v[\\kb+3], v98 offset:%c[cnt]\n\t"
        "s_mov_b64 exec, -1\n\t"
        "v_cmpx_lt_u32_e64 %[m0], v92, v[80+\\j]\n\tds_add_rtn_u32 v[\\rb], v[\\kb], v98 offset:%c[cnt]\n\t"
        "v_cmpx_lt_u32_e64 %[m0], v93, v[80+\\j]\n\tds_add_rtn_u32 v[\\rb+1], v[\\kb+1], v98 offset:%c[cnt]\n\t"
        "s_mov_b64 exec, -1\n\t.endm\n\t"
        ".macro FPUT j, kb, rb, n\n\t"
        "s_waitcnt lgkmcnt(\\n)\n\t"
        ".irp k, 0, 1, 2, 3\n\tv_min_u32_e32 v[\\rb+\\k], %c[sub]-4, v[\\rb+\\k]\n\t.endr\n\t"
        ".irp k, 0, 1, 2, 3\n\tv_mad_u32_u24 v[\\rb+\\k], v[\\kb+\\k], %[sub4], v[\\rb+\\k]\n\t.endr\n\t"
        "v_cmpx_lt_u32_e64 %[m0], v94, v[80+\\j]\n\tds_write_b32 v[\\rb+2], v[66+4*\\j] offset:%c[list]\n\t"
        "v_cmpx_lt_u32_e64 %[m0], v95, v[80+\\j]\n\tds_write_b32 v[\\rb+3], v[67+4*\\j] offset:%c[list]\n\t"
        "s_mov_b64 exec, -1\n\t"
        "v_cmpx_lt_u32_e64 %[m0], v92, v[80+\\j]\n\tds_write_b32 v[\\rb], v[64+4*\\j] offset:%c[list]\n\t"
        "v_cmpx_lt_u32_e64 %[m0], v93, v[80+\\j]\n\tds_write_b32 v[\\rb+1], v[65+4*\\j] offset:%c[list]\n\t"
        "s_mov_b64 exec, -1\n\t.endm\n\t"
        "s_movk_i32 %[o1], 0x1000\n\ts_movk_i32 %[o2], 0x2000\n\ts_movk_i32 %[o3], 0x3000\n\t"
        "v_mov_b32_e32 v92, %[i4]\n\tv_add_u32_e32 v93, 4, %[i4]\n\tv_add_u32_e32 v94, 8, %[i4]\n\tv_add_u32_e32 v95, 12, %[i4]\n\t"
        "v_mov_b32_e32 v98, 4\n\t"
        "s_mov_b32 %[todo], 15\n\ts_mov_b32 %[spin], 0x400000\n\ts_mov_b32 %[over], 0\n\t"
        ".if " STR(FASTIN) "\n\t"
        "s_waitcnt vmcnt(0)\n\t"
        ".irp j, 0, 1, 2, 3\n\tv_xor_b32_e32 v[64+4*\\j], %[key], v[64+4*\\j]\n\t.endr\n\t"
        ".irp j, 0, 1, 2, 3\n\tv_mov_b32_dpp v[80+\\j], v[64+4*\\j] quad_perm:[0,0,0,0] row_mask:0xf bank_mask:0xf\n\t.endr\n\t"
        ".irp j, 0, 1, 2, 3\n\tv_add_u32_e32 v[84+\\j], v[64+4*\\j], v[65+4*\\j]\n\t.endr\n\t"
        ".irp j, 0, 1, 2, 3\n\tv_mov_b32_dpp v[80+\\j], v[80+\\j] row_shr:4 row_mask:0xf bank_mask:0xa\n\t.endr\n\t"
        "s_mov_b64 exec, %[pc0]\n\t"
        ".irp j, 0, 1, 2, 3\n\tv_sub_u32_e32 v[84+\\j], 0, v[65+4*\\j]\n\t.endr\n\t"
        "s_mov_b64 exec, -1\n\t"
        "v_cmp_gt_i32_e64 %[m0], v80, v92\n\tv_cmp_gt_i32_e64 %[m1], v81, v92\n\tv_cmp_gt_i32_e64 %[m2], v82, v92\n\tv_cmp_gt_i32_e64 %[m3], v83, v92\n\t"
        ".irp j, 0, 1, 2, 3\n\tv_add3_u32 v[84+\\j], v[84+\\j], v[66+4*\\j], v[67+4*\\j]\n\t.endr\n\t"
        "v_cndmask_b32_e64 v84, 0, v84, %[m0]\n\tv_cndmask_b32_e64 v85, 0, v85, %[m1]\n\tv_cndmask_b32_e64 v86, 0, v86, %[m2]\n\tv_cndmask_b32_e64 v87, 0, v87, %[m3]\n\t"
        ".irp j, 0, 1, 2, 3\n\tv_add_u32_dpp v[84+\\j], v[84+\\j], v[84+\\j] quad_perm:[1,0,3,2] row_mask:0xf bank_mask:0xf\n\t.endr\n\t"
        ".irp j, 0, 1, 2, 3\n\tv_add_u32_dpp v[84+\\j], v[84+\\j], v[84+\\j] quad_perm:[2,3,0,1] row_mask:0xf bank_mask:0xf\n\t.endr\n\t"
        ".irp j, 0, 1, 2, 3\n\tv_add_u32_dpp v[84+\\j], v[84+\\j], v[84+\\j] row_half_mirror row_mask:0xf bank_mask:0xf\n\t.endr\n\t"
        "v_or3_b32 v96, v80, v81, v82\n\tv_max3_u32 v97, v80, v81, v82\n\tv_or_b32_e32 v96, v96, v83\n\tv_max_u32_e32 v97, v97, v83\n\t"
        "v_lshrrev_b32_e32 v96, 14, v96\n\tv_or3_b32 v96, v96, v84, v85\n\tv_or3_b32 v96, v96, v86, v87\n\t"
        "v_cmp_ne_u32_e32 vcc, 0, v96\n\t"
        "s_cbranch_vccnz 7f\n\t"
        "v_cmp_lt_u32_e32 vcc, %c[cap], v97\n\t"
        "FADD 0, 100, 104\n\tFADD 1, 108, 112\n\t"
        "s_cbranch_vccz 6f\n\ts_mov_b32 %[over], 1\n\t6:\n\t"
        "FPUT 0, 100, 104, 4\n\tFADD 2, 100, 104\n\tFPUT 1, 108, 112, 8\n\tFADD 3, 108, 112\n\tFPUT 2, 100, 104, 8\n\tFPUT 3, 108, 112, 4\n\t"
        "s_mov_b64 exec, %[pc0]\n\t"
        ".irp j, 0, 1, 2, 3\n\tds_write_b32 %[hdra], v[80+\\j] offset:128*\\j\n\t.endr\n\t"
        "s_mov_b64 exec, -1\n\t"
        "s_branch 9f\n\t"
        "7:\n\t.irp j, 0, 1, 2, 3\n\tv_xor_b32_e32 v[64+4*\\j], %[key], v[64+4*\\j]\n\t.endr\n\t"
        ".endif\n\t"
        "0:\n\t"
        "s_mov_b32 %[fresh], 0\n\t"
        "s_waitcnt vmcnt(0)\n\t"
        "IVAL 0, 0\n\tIVAL 1, %[o1]\n\tIVAL 2, %[o2]\n\tIVAL 3, %[o3]\n\t"
        "IRANK 0\n\tIRANK 1\n\tIRANK 2\n\tIRANK 3\n\t"
        "s_andn2_b32 %[todo], %[todo], %[fresh]\n\t"
        "s_cbranch_scc0 9f\n\t"
        "s_sub_u32 %[spin], %[spin], 1\n\t"
        "s_cbranch_scc0 0b\n\t"
        "s_waitcnt vmcnt(0)\n\t"
        "9:\n\t"
        ".purgem IVAL\n\t.purgem IRANK\n\t.purgem FADD\n\t.purgem FPUT"
        : "+{v[64:79]}"(raw), "=&{v[80:95]}"(t0), "=&{v[96:99]}"(t1), "=&{v[100:115]}"(t2), [m0] "=&s"(m0), [m1] "=&s"(m1), [m2] "=&s"(m2), [m3] "=&s"(m3),
          [todo] "=&s"(todo), [fresh] "=&s"(fresh), [spin] "=&s"(spin), [o1] "=&s"(o1), [o2] "=&s"(o2), [o3] "=&s"(o3), [over] "=&s"(over)
        : [srd] "s"(srd), [ib] "v"(ib), [key] "s"(key), [i4] "v"(i4), [hdra] "v"(hdra), [pc0] "s"(pc0), [sub4] "s"(SUBB / 4),
          [cnt] "i"(L_CNT), [list] "i"(L_LIST), [sub] "i"(SUBB), [cap] "i"(4 * CAP1)
        : "vcc", "scc", "memory");
    return over;
}

template <int CTRL, int BANK> __device__ __forceinline__ unsigned dpp(unsigned old, unsigned v) { return __builtin_amdgcn_update_dpp(old, v, CTRL, 0xF, BANK, true); }
__device__ __forceinline__ unsigned bcast8(unsigned v) { v = dpp<0x00, 0xF>(0u, v); return __builtin_amdgcn_update_dpp(v, v, 0x114, 0xF, 0xA, false); }
__device__ __forceinline__ unsigned sum8(unsigned v) { v += dpp<0xB1, 0xF>(0u, v); v += dpp<0x4E, 0xF>(0u, v); return v + dpp<0x141, 0xF>(0u, v); }
__device__ __forceinline__ bf16x4 tr_read(LAS char* p) { return __builtin_amdgcn_ds_read_tr16_b64_v4bf16((LAS bf16x4*)p); }
__device__ __forceinline__ bf16x8 cat(bf16x4 a, bf16x4 b) { return __builtin_shufflevector(a, b, 0, 1, 2, 3, 4, 5, 6, 7); }
__device__ __forceinline__ unsigned pk2(float a, float b) { f32x2 v = {a, b}; bf16x2 r = __builtin_convertvector(v, bf16x2); return __builtin_bit_cast(unsigned, r); }
__device__ __forceinline__ f32x4 mfma(bf16x8 a, bf16x8 b, f32x4 c) { return __builtin_amdgcn_mfma_f32_16x16x32_bf16(a, b, c, 0, 0, 0); }
__device__ __forceinline__ unsigned lds_add(LAS char* p, unsigned v) { return __hip_atomic_fetch_add((LAS unsigned*)p, v, __ATOMIC_RELAXED, __HIP_MEMORY_SCOPE_WORKGROUP); }
__device__ __forceinline__ LAS unsigned* ldsp(unsigned a) { return (LAS unsigned*)(size_t)a; }
__device__ __forceinline__ unsigned byte0_x4(unsigned v) { unsigned r; asm("v_lshlrev_b32_sdwa %0, 2, %1 dst_sel:DWORD dst_unused:UNUSED_PAD src0_sel:DWORD src1_sel:BYTE_0" : "=v"(r) : "v"(v)); return r; }
__device__ __forceinline__ unsigned mad24(unsigned a, unsigned b, unsigned c) { unsigned r; asm("v_mad_u32_u24 %0, %1, %2, %3" : "=v"(r) : "v"(a), "s"(b), "v"(c)); return r; }
__device__ __forceinline__ void lds_arrive(LAS char* p, int lane) {
    asm volatile("s_waitcnt lgkmcnt(0)" ::: "memory");
    if (lane == 0) __hip_atomic_fetch_add((LAS unsigned*)p, 1u, __ATOMIC_RELAXED, __HIP_MEMORY_SCOPE_WORKGROUP);
}
__device__ __forceinline__ void lds_await(LAS char* p, unsigned n) {
    while (__builtin_amdgcn_readfirstlane(*(volatile LAS unsigned*)p) < n) __builtin_amdgcn_s_sleep(1);
    asm volatile("" ::: "memory");
}
__device__ __forceinline__ unsigned rfl(unsigned v) { return __builtin_amdgcn_readfirstlane(v); }

__global__ __launch_bounds__(512, 2) void k_graph(const unsigned short* x_, const int* src_, const int* dst_, const unsigned short* wq_, const unsigned short* wv_,
                                                  unsigned* ws_, unsigned short* out_, int N_, int E_) {
    u32x4 srd_d, srd_s, srd_ws, srd_x0, pw, po;
    u64 t_start;
    asm volatile("s_load_dwordx2 s[40:41], %[ka], 0x10\n\ts_load_dwordx2 s[44:45], %[ka], 0x8\n\ts_load_dwordx4 s[60:63], %[ka], 0x30\n\t"
                 "s_load_dwordx2 s[48:49], %[ka], 0x28\n\ts_load_dwordx2 s[52:53], %[ka], 0x0\n\ts_load_dwordx4 s[56:59], %[ka], 0x18\n\t"
                 "s_memrealtime s[64:65]\n\t"
                 "s_mov_b32 s43, 0x20000\n\ts_mov_b32 s47, 0x20000\n\ts_mov_b32 s51, 0x20000\n\ts_mov_b32 s55, 0x20000\n\ts_mov_b32 s50, %c[wsb]"
                 : "=&{s[40:43]}"(srd_d), "=&{s[44:47]}"(srd_s), "=&{s[48:51]}"(srd_ws), "=&{s[52:55]}"(srd_x0), "=&{s[56:59]}"(pw), "=&{s[60:63]}"(po), "=&{s[64:65]}"(t_start)
                 : [ka] "s"(__builtin_amdgcn_kernarg_segment_ptr()), [wsb] "i"(WS_BYTES));
    FENCE();
    __shared__ __attribute__((aligned(16))) char smem_[L_TOTAL];
    LAS char* smem = (LAS char*)smem_;
    const int t = threadIdx.x, l = t & 63, b = blockIdx.x;
    const int w = rfl(t >> 6);
    const int g = l >> 4, n = l & 15, q = (l >> 2) & 3, p = l & 3, pc = t & 7;
    const unsigned lds0 = (unsigned)(size_t)smem;
    const bool isprod = ((b >> 3) & 1) == 0;
    const int pg = ((b & 7) << 4) | (b >> 4);
    const unsigned e0 = pg * CH + 4 * t, e1 = pg * CH + 3072 + t;

    u32x16 ed, es;
    {
        const unsigned o = e0 * 4, o1 = w == 0 ? e1 * 4 : OOB, ok = WS_KEY + t * 4, om = WS_KEY + b * 4;
        const unsigned oc = w < 4 ? (t < 196 ? ((b - 8) << 8) + ((t >> 1) << 16) + ((t & 1) << 7) : OOB) : (t - 256) * 128, dump = lds0 + L_DUMP + w * 256;
        int role = (w < 4 ? 0 : 2) + (isprod ? 0 : 1);
        unsigned k4 = 4096, k8 = 8192;
        asm volatile("s_cmp_eq_u32 %[role], 0\n\ts_cbranch_scc1 1f\n\t"
                     "s_mov_b32 m0, %[dump]\n\ts_cmp_eq_u32 %[role], 1\n\ts_cbranch_scc1 2f\n\t"
                     "s_getpc_b64 s[68:69]\n\ts_mov_b32 s70, " STR(CODE_BYTES) "\n\ts_mov_b32 s71, 0x20000\n\t"
                     ".if " STR(CODEPF) "\n\tbuffer_load_dword %[oc], s[68:71], 0 offen lds\n\t.endif\n\t"
                     "s_waitcnt lgkmcnt(0)\n\ts_branch 3f\n\t"
                     "2:\n\ts_waitcnt lgkmcnt(0)\n\ts_lshl_b32 s54, s62, 8\n\t"
                     ".if " STR(PARTNER) "\n\tbuffer_load_dword %[oc], s[52:55], 0 offen lds\n\t.endif\n\ts_branch 3f\n\t"
                     "1:\n\ts_waitcnt lgkmcnt(0)\n\ts_lshl_b32 s42, s63, 2\n\ts_lshl_b32 s46, s63, 2\n\t"
                     "buffer_load_dwordx4 v[64:67], %[o], s[40:43], 0 offen\n\tbuffer_load_dwordx4 v[68:71], %[o], s[40:43], %[k4] offen\n\t"
                     "buffer_load_dwordx4 v[72:75], %[o], s[40:43], %[k8] offen\n\tbuffer_load_dword v76, %[o1], s[40:43], 0 offen\n\t"
                     "buffer_load_dwordx4 v[80:83], %[o], s[44:47], 0 offen\n\tbuffer_load_dwordx4 v[84:87], %[o], s[44:47], %[k4] offen\n\t"
                     "buffer_load_dwordx4 v[88:91], %[o], s[44:47], %[k8] offen\n\tbuffer_load_dword v92, %[o1], s[44:47], 0 offen\n\t"
                     "buffer_load_dword v93, %[ok], s[48:51], 0 offen sc1\n\tbuffer_load_dword v94, %[om], s[48:51], 0 offen sc1\n\t"
                     "3:\n\ts_lshl_b32 s54, s62, 8"
                     : "=&{v[64:79]}"(ed), "=&{v[80:95]}"(es)
                     : [o] "v"(o), [o1] "v"(o1), [ok] "v"(ok), [om] "v"(om), [oc] "v"(oc), [role] "s"(role), [dump] "s"(dump), [k4] "s"(k4), [k8] "s"(k8),
                       "{s[40:43]}"(srd_d), "{s[44:47]}"(srd_s), "{s[48:51]}"(srd_ws), "{s[52:55]}"(srd_x0), "{s[56:59]}"(pw), "{s[60:63]}"(po), "{s[64:65]}"(t_start)
                     : "memory", "scc", "s68", "s69", "s70", "s71");
        asm volatile("" : "+{s[48:51]}"(srd_ws), "+{s[52:55]}"(srd_x0), "+{s[56:59]}"(pw), "+{s[60:63]}"(po), "+{s[64:65]}"(t_start));
    }
    const void* const x = (const void*)((u64)srd_x0.x | ((u64)srd_x0.y << 32));
    const void* const wq = (const void*)((u64)pw.x | ((u64)pw.y << 32));
    const void* const wv = (const void*)((u64)pw.z | ((u64)pw.w << 32));
    void* const out = (void*)((u64)po.x | ((u64)po.y << 32));
    const int N = po.z, E = po.w;
    const unsigned xbytes = srd_x0.z;
    STAMP_DECL;
    const unsigned ring = lds0 + L_RING + w * 16384;
    const int jn = 8 * n + w;

    unsigned mykey = 0;
    auto node_row = [&](int tile, int m) { const int nd = b + 256 * (8 * (4 * m + g) + tile); return nd < N ? (unsigned)nd << 8 : OOB; };
    auto zero_staging = [&]() {
        const u32x4 z = {0u, 0u, 0u, 0u};
#pragma unroll
        for (int k = 0; k < 5; ++k) *(LAS u32x4*)(smem + L_IMG + k * 8192 + t * 16) = z;
    };

    u32x16 addr;
    f32x32 acc, wva, wvb;
    unsigned cv, lista, cnta, qa, keyn, inf;
    Srd4 srd_x;
    auto loop_constants = [&]() {
        srd_x = make_srd4(x, xbytes, BIAS * 256);
        lista = lds0 + L_LIST + w * (2 * SUBB) + g * 4; cnta = lds0 + L_CNT + (l & 31) * 4; qa = ring + l * 16; keyn = jn; inf = 0x7F800000u;
        asm volatile("" : "+s"(srd_x.s0), "+s"(srd_x.s1), "+s"(srd_x.s2), "+s"(srd_x.s3));
        asm volatile("" : "+v"(lista), "+v"(cnta), "+v"(qa), "+v"(keyn), "+v"(inf));
        const unsigned o0 = (n ^ (((g & 1) << 3) | (g >> 1))) << 4;
        const unsigned ra = (4 * (n & 3) + (n >> 2)) * 256 + (((4 * g) ^ (((n & 7) << 1) | (n >> 3))) << 4);
        const int i = 4 * g + q, r8 = i & 7;
        const unsigned ta = (4 * q + g) * 256 + 8 * (p & 1) + (((p >> 1) ^ (((i & 7) << 1) | (i >> 3))) << 4);
        cv = i * 256 + (((p ^ ((r8 & 1) | ((r8 & 6) << 1))) << 4) | ((p & 1) << 3));
#pragma unroll
        for (int s = 0; s < 4; ++s) { addr[s] = ring + (ra ^ (s << 4)); addr[12 + s] = o0 ^ (s << 5); }
#pragma unroll
        for (int c = 0; c < 8; ++c) addr[4 + c] = ring + (ta ^ (c << 5));
        asm volatile("" : "+{v[176:191]}"(addr), "+v"(cv));
    };
    auto clear_sums = [&]() { acc = 0.f; asm volatile("" : "+{v[128:159]}"(acc)); };
    auto wv_resident = [&]() {
#pragma unroll
        for (int a = 0; a < 8; ++a) {
            LAS char* pa = smem + L_WV0 + (cv ^ ((a >> 1) << 6) ^ ((a & 1) << 3));
            const f32x4 f0 = __builtin_bit_cast(f32x4, cat(tr_read(pa), tr_read(pa + 4096)));
            const f32x4 f1 = __builtin_bit_cast(f32x4, cat(tr_read(pa + 16384), tr_read(pa + 16384 + 4096)));
#pragma unroll
            for (int k = 0; k < 4; ++k) { wva[4 * a + k] = f0[k]; wvb[4 * a + k] = f1[k]; }
        }
        asm volatile("" : "+{v[224:255]}"(wva), "+{v[32:63]}"(wvb));
    };

    if (isprod) zero_staging();
    if (t < 8) *(LAS u32x4*)(smem + L_CNT + t * 16) = u32x4{0u, 0u, 0u, 0u};
    WG_BARRIER();

    if (__builtin_expect(w < 4, 1)) {
        if (__builtin_expect(isprod, 1)) {
            if (PRIO) __builtin_amdgcn_s_setprio(2);
            unsigned ob = WS_INBOX + ((t >> 3) * G1 + pg) * 128 + pc * 16, ima = lds0 + L_IMG + (t >> 3) * 144 + pc * 16;
            PIN(ob); PIN(ima);
            unsigned cna = lds0 + L_CNT1 + (t >> 3) * 4;
            PIN(cna);
            const bool v0 = e0 < (unsigned)E, v1 = e0 + 1024 < (unsigned)E, v2 = e0 + 2048 < (unsigned)E, v3 = w == 0 && e1 < (unsigned)E;
            STAMP(1);
            u32x16 rk;
            const u64 over = rank_edges(ed, es, rk, __builtin_amdgcn_ballot_w64(v0), __builtin_amdgcn_ballot_w64(v1), __builtin_amdgcn_ballot_w64(v2),
                                        __builtin_amdgcn_ballot_w64(v3), t * 4);
            mykey = es[14];
            if (__builtin_expect(over != 0, 0)) {
#pragma unroll
                for (int k = 0; k < 13; ++k)
                    if (rk[k] >= 4u * CAP1) {
                        const unsigned o = WS_OVF + (pg * CH + lds_add(smem + L_CNT1 + 2048, 1)) * 4;
                        const unsigned v = ((((es[k] >> 8) & 0xFFFF) - BIAS) << 16) | ed[k];
                        ST1(v, srd_ws, o, " sc1");
                    }
            }
            if (XSYNC) { lds_arrive(smem + L_CNT + 96, l); lds_await(smem + L_CNT + 96, 4); } else WG_BARRIER();
            STAMP(3);
            f32x32 img; u32x16 meta, sums;
            const unsigned no = read_image(img, meta, sums, ima, cna);
            lds_arrive(smem + L_CNT + 84, l);
            if (__builtin_expect(no != 0, 0)) {
                const unsigned o = t == 0 ? WS_NOVF + pg * 4 : OOB;
                ST1(no, srd_ws, o, " sc1");
                VMWAIT(0);
                if (XSYNC) { lds_arrive(smem + L_CNT + 100, l); lds_await(smem + L_CNT + 100, 4); } else WG_BARRIER();
            }
            publish_image(img, meta, sums, srd_ws, ob);
            STAMP(4);
        } else {
            { const unsigned om = WS_KEY + b * 4; asm volatile("buffer_load_dword %0, %1, %2, 0 offen sc1\n\ts_waitcnt vmcnt(0)" : "=&v"(mykey) : "v"(om), "s"(srd_ws) : "memory"); }
            STAMP(1);
            if (PRIO) __builtin_amdgcn_s_setprio(2);
            STAMP(2); STAMP(3); STAMP(4);
        }
        STAMP(5);
        mykey = rfl(mykey) + KEY_STEP;
        loop_constants();
        clear_sums();
        unsigned ib = WS_INBOX + b * (G1 * 128) + t * 16;
        PIN(ib);
        for (u64 t_go = t_start + POLL_T;;) {
            const unsigned staged = rfl(*(volatile LAS unsigned*)(smem + L_CNT + 80));
            const u64 now = __builtin_amdgcn_s_memrealtime();
            if (staged >= 4 && now + POLL_D < t_go) { t_go = now + POLL_D; while (__builtin_amdgcn_s_memrealtime() < t_go) __builtin_amdgcn_s_sleep(1); break; }
            if (now >= t_go) break;
            __builtin_amdgcn_s_sleep(1);
        }
        STAMP(6);
        u32x16 raw;
        inbox_request(raw, srd_ws, ib);
        if (__builtin_expect(inbox_stage(raw, srd_ws, ib, mykey, 16 * pc - 8, lds0 + L_HDR + (t >> 3) * 4) != 0, 0)) {
            LAS unsigned* hp = (LAS unsigned*)(smem + L_HDR) + 32 * (l >> 3 & 3) + 8 * w + (l & 7);
            const unsigned hv = *hp;
            for (u64 m = __builtin_amdgcn_ballot_w64(l < 32 && hv > 4u * CAP1); m; m &= m - 1) {
                const int k = __builtin_ctzll(m), gp = 32 * (k >> 3) + 8 * w + (k & 7);
                unsigned u, no; const unsigned o = WS_OVF + (gp * CH + l) * 4, on = WS_NOVF + gp * 4;
                asm volatile("buffer_load_dword %0, %2, %4, 0 offen sc1\n\tbuffer_load_dword %1, %3, %4, 0 offen sc1\n\ts_waitcnt vmcnt(0)"
                             : "=&v"(u), "=&v"(no) : "v"(o), "v"(on), "s"(srd_ws) : "memory");
                if (rfl(no) <= 64) {
                    if (l < no && (u & 0xFF) == (unsigned)b) {
                        const unsigned e = __builtin_amdgcn_perm((u >> 16) + BIAS, u, 0x01050401u), key = (e >> 21) & 60;
                        *(LAS unsigned*)(smem + L_LIST + key * (SUBB / 4) + min(lds_add(smem + L_CNT + key, 4), SUBB - 4u)) = e;
                    }
                    if (l == k) *hp = hv | 0x80000000u;
                } else *(LAS unsigned*)(smem + L_CNT + 64) = 1;
            }
        }
        if (PRIO) __builtin_amdgcn_s_setprio(0);
        STAMP(7);
    } else {
        const int j = w - 4;
        STAMP(1);
        u32x4 wr[8], xr[8];
        {
            const u32x4 srd_q = make_srd(wq, 32768);
            const unsigned o = j * 8192 + l * 16, k4 = 4096;
            LD4(wr[0], srd_q, o, ""); LD4(wr[1], srd_q, o, " offset:1024"); LD4(wr[2], srd_q, o, " offset:2048"); LD4(wr[3], srd_q, o, " offset:3072");
            LD4S(wr[4], srd_q, o, k4, ""); LD4S(wr[5], srd_q, o, k4, " offset:1024"); LD4S(wr[6], srd_q, o, k4, " offset:2048"); LD4S(wr[7], srd_q, o, k4, " offset:3072");
#pragma unroll
            for (int m = 0; m < 8; ++m) { const unsigned ox = node_row(m < 4 ? j : w, m & 3) + n * 16; LD4(xr[m], srd_x0, ox, ""); }
        }
        unsigned aqa[8], xfa[4];
        unsigned xa = ring + g * 256 + ((n ^ g) << 4), wa = lds0 + L_WQ + j * 16384 + g * 256 + ((n ^ g) << 4) + ((n & 4) << 1);
        {
            const unsigned aq = lds0 + L_WQ + (8 * g + q) * 256 + ((((4 * p) ^ (q | ((g & 1) << 2))) << 4) | ((p & 1) << 3));
#pragma unroll
            for (int s = 0; s < 4; ++s) { xfa[s] = ring + n * 256 + (((4 * s + g) ^ n) << 4); PIN(xfa[s]); }
#pragma unroll
            for (int c = 0; c < 8; ++c) { aqa[c] = aq ^ ((c >> 1) << 4) ^ ((c & 1) << 3); PIN(aqa[c]); }
            PIN(xa); PIN(wa);
        }
        loop_constants();
        STAMP(2);
        if (isprod && !XSYNC) {
            WG_BARRIER();
            if (__builtin_expect(rfl(*(LAS unsigned*)(smem + L_CNT1 + 2048)) != 0, 0)) WG_BARRIER();
            if (W_SLEEP) __builtin_amdgcn_s_sleep(W_SLEEP);
        }
        STAMP(3);
        asm volatile("s_waitcnt vmcnt(%c8)" : "+v"(wr[0]), "+v"(wr[1]), "+v"(wr[2]), "+v"(wr[3]), "+v"(wr[4]), "+v"(wr[5]), "+v"(wr[6]), "+v"(wr[7]) : "i"(8) : "memory");
#pragma unroll
        for (int m = 0; m < 8; ++m) {
            const unsigned a = (wa ^ ((m & 2) << 5)) + m * 1024;
            *(LAS u32x2*)(size_t)a = u32x2{wr[m].x, wr[m].y};
            *(LAS u32x2*)(size_t)(a ^ 8) = u32x2{wr[m].z, wr[m].w};
        }
        asm volatile("s_waitcnt vmcnt(0)" : "+v"(xr[0]), "+v"(xr[1]), "+v"(xr[2]), "+v"(xr[3]), "+v"(xr[4]), "+v"(xr[5]), "+v"(xr[6]), "+v"(xr[7]) :: "memory");
#pragma unroll
        for (int m = 0; m < 8; ++m) *(LAS u32x4*)(size_t)((xa ^ ((m & 3) << 6)) + m * 1024) = xr[m];
        STAMP(4);
        lds_arrive(smem + L_CNT + 80, l);
        lds_await(smem + L_CNT + 80, 4);
        STAMP(5);
        bf16x8 xo[2][4], fr[2][8];
        f32x4 qc[2][8];
        auto wq_frags = [&](int u, bf16x8 (&f)[8]) {
#pragma unroll
            for (int a = 0; a < 8; ++a) {
                LAS char* pa = (LAS char*)(size_t)aqa[a] + u * 16384;
                f[a] = cat(tr_read(pa), tr_read(pa + 1024));
            }
        };
#pragma unroll
        for (int u = 0; u < 4; ++u) { xo[0][u] = *(LAS bf16x8*)(size_t)xfa[u]; xo[1][u] = *(LAS bf16x8*)(size_t)(xfa[u] + 4096); }
        wq_frags(0, fr[0]);
        {
            const Srd4 srd_v = make_srd4(wv, 32768);
            const unsigned r0 = (j * 32 + g) << 8, o0 = (n ^ ((g & 1) | ((g & 2) << 1))) << 4;
            const unsigned e[8] = {r0, r0 + 1024, r0 + 2048, r0 + 3072, r0 + 4096, r0 + 5120, r0 + 6144, r0 + 7168};
            const unsigned base = lds0 + (j < 2 ? L_WV0 + j * 16384 : L_WV1 + (j - 2) * 8192);
            const u32x4 off = {o0, o0 ^ 128, o0, o0 ^ 128};
            dma_half(base, srd_v, e, off); dma_half(base + 4096, srd_v, e + 4, off);
        }
#pragma unroll
        for (int u = 0; u < 4; ++u) {
            if (u < 3) wq_frags(u + 1, fr[(u + 1) & 1]);
            FENCE();
#pragma unroll
            for (int a = 0; a < 8; ++a)
#pragma unroll
                for (int tt = 0; tt < 2; ++tt) qc[tt][a] = mfma(fr[u & 1][a], xo[tt][u], u ? qc[tt][a] : f32x4{0.f, 0.f, 0.f, 0.f});
            FENCE();
            if (!WVLATE && u == 1 && j < 2) { VMWAIT(0); lds_arrive(smem + L_CNT + 88, l); }
        }
        STAMP(6);
        if (isprod) lds_await(smem + L_CNT + 84, 4);
#pragma unroll
        for (int tt = 0; tt < 2; ++tt)
#pragma unroll
            for (int s = 0; s < 4; ++s) {
                const float k = -0.08838834764831845f * 1.4426950408889634f;
                const f32x4 c0 = qc[tt][2 * s] * k, c1 = qc[tt][2 * s + 1] * k;
                *(LAS u32x4*)(size_t)(ring - (tt ? 0 : 65536) + s * 1024 + l * 16) = u32x4{pk2(c0[0], c0[1]), pk2(c0[2], c0[3]), pk2(c1[0], c1[1]), pk2(c1[2], c1[3])};
            }
        clear_sums();
        if (!WVLATE) { lds_await(smem + L_CNT + 88, 2); wv_resident(); }
        VMWAIT(0);
        STAMP(7);
    }
    WG_BARRIER();
    STAMP(8);
    if (LPRIO >= 3) __builtin_amdgcn_s_setprio(3);
    if (LPRIO == 1 && w >= 4) __builtin_amdgcn_s_setprio(1);
    if (LPRIO == 2) { if (w >= 6) __builtin_amdgcn_s_setprio(3); else if (w >= 4) __builtin_amdgcn_s_setprio(2); else if (w >= 2) __builtin_amdgcn_s_setprio(1); }

    u32x16 qf;
    const u32x4 doff = {addr[12], addr[13], addr[14], addr[15]};
    LAS char* mylist = smem + L_LIST + w * (2 * SUBB);
    int gcur = G1, pos = 0;
    unsigned hmask = 0xFFFFFFFFu;
    auto batch = [&](auto first_) {
        constexpr bool first = decltype(first_)::value;
        auto entries = [&](int grp, unsigned (&e)[8]) {
#pragma unroll
            for (int m = 0; m < 8; ++m) e[m] = *(LAS unsigned*)(size_t)(lista + grp * 128 + m * 16);
        };
        unsigned cw = *(LAS unsigned*)(size_t)(first ? cnta : lds0 + L_CNT + (__builtin_amdgcn_mbcnt_lo(~0u, 0u) & 31) * 4), e0[8], e1[8];
        entries(0, e0);
        if constexpr (first) {
#pragma unroll
            for (int s = 0; s < 4; ++s) {
                const u32x4 v = *(LAS u32x4*)(size_t)(qa + s * 1024);
                qf[4 * s] = v.x; qf[4 * s + 1] = v.y; qf[4 * s + 2] = v.z; qf[4 * s + 3] = v.w;
            }
        }
        entries(1, e1);
        FENCE();
        int lo = __builtin_amdgcn_readlane(cw, 2 * w) >> 2, hi = __builtin_amdgcn_readlane(cw, 2 * w + 1) >> 2;
        if constexpr (first) {
            if (__builtin_amdgcn_readlane(cw, 16)) gcur = 0;
            if (__builtin_amdgcn_ballot_w64(cw > 4u * CAP2)) { gcur = -1; lo = 0; hi = 0; hmask = 0x7FFFFFFFu; }
        }
        const int c = lo + hi, G = (c + 31) >> 5;
        const int nlast = G > 2 ? (((c - 1) & 31) >> 2) + 1 : 8;
        auto join = [&]() {
            for (int k = 0; k < G * 32 - lo; k += 64) {
                const unsigned e = *(LAS unsigned*)(mylist + SUBB + (l + k) * 4);
                if (l + k < G * 32 - lo) *(LAS unsigned*)(mylist + (lo + l + k) * 4) = l + k < hi ? e : PAD;
            }
        };
        const bool early = lo >= 64;
        if (__builtin_expect(!early, 0)) { join(); entries(0, e0); entries(1, e1); }
        if constexpr (first) asm volatile("s_waitcnt lgkmcnt(0)" : "+{v[160:175]}"(qf) :: "memory");
        if (G > 0) dma_group(ring, srd_x, e0, doff);
        if constexpr (first) {
            if (WVLATE) {
                if (G > 1 && w < 6) dma_group(ring + 8192, srd_x, e1, doff);
                wv_resident();
                lds_arrive(smem + L_CNT + 92, l);
                if (w >= 6) { lds_await(smem + L_CNT + 92, 8); if (G > 1) dma_group(ring + 8192, srd_x, e1, doff); }
            } else if (w < 4) {
                if (G > 1) dma_group(ring + 8192, srd_x, e1, doff);
                wv_resident();
                lds_arrive(smem + L_CNT + 92, l);
            } else {
                if (w >= 6) lds_await(smem + L_CNT + 92, 4);
                if (G > 1) dma_group(ring + 8192, srd_x, e1, doff);
            }
        } else if (G > 1) dma_group(ring + 8192, srd_x, e1, doff);
        f32x16 cc;
#pragma unroll
        for (int m = 0; m < 8; ++m) {
            cc[m] = (e0[m] & 0xFF) == keyn ? 0.f : __uint_as_float(inf);
            cc[8 + m] = (e1[m] & 0xFF) == keyn ? 0.f : __uint_as_float(inf);
        }
        if (early) join();
        STAMP(9);
        if (G > 0) gather_loop(acc, qf, addr, u32x4{lista, keyn, inf, 0u}, cc, srd_x, ring, G, nlast);
    };
    batch(std::true_type{});
    while (__builtin_expect(gcur < G1, 0)) {
        unsigned lim = G1 * 32;
        if (gcur >= 0) {
            const u64 m0 = __builtin_amdgcn_ballot_w64(l >= gcur && (int)(*(LAS unsigned*)(smem + L_HDR + l * 4) & hmask) > 4 * CAP1);
            const u64 m1 = __builtin_amdgcn_ballot_w64(l + 64 >= gcur && (int)(*(LAS unsigned*)(smem + L_HDR + 256 + l * 4) & hmask) > 4 * CAP1);
            if (!(m0 | m1)) break;
            gcur = m0 ? __builtin_ctzll(m0) : 64 + __builtin_ctzll(m1);
            unsigned v; const unsigned o = WS_NOVF + gcur * 4;
            asm volatile("buffer_load_dword %0, %1, %2, 0 offen sc1\n\ts_waitcnt vmcnt(0)" : "=&v"(v) : "v"(o), "s"(srd_ws) : "memory");
            lim = rfl(v);
        }
        const unsigned i = pos + t;
        const bool cand = t < CAP2 && i < lim;
        unsigned u;
        { const unsigned o = cand ? (gcur < 0 ? WS_INBOX + b * (G1 * 128) : WS_OVF + gcur * CH * 4) + i * 4 : OOB;
          asm volatile("buffer_load_dword %0, %1, %2, 0 offen sc1\n\ts_waitcnt vmcnt(0)" : "=&v"(u) : "v"(o), "s"(srd_ws) : "memory"); }
        WG_BARRIER();
        if (t < 16) *(LAS unsigned*)(smem + L_CNT + t * 4) = 0;
        WG_BARRIER();
        if (cand) {
            unsigned e; bool v;
            if (gcur < 0) { e = (i & 3) ? u : u ^ mykey; v = ((i & 31) - 2) * 4 < *(LAS unsigned*)(smem + L_HDR + (i >> 5) * 4); }
            else { e = (((u >> 16) + BIAS) << 8) | ((u >> 8) & 0xFF); v = (u & 0xFF) == (unsigned)b; }
            if (v) *(LAS unsigned*)(smem + L_LIST + (e & 7) * (2 * SUBB) + lds_add(smem + L_CNT + (e & 7) * 8, 4)) = e;
        }
        pos += CAP2;
        if ((unsigned)pos >= lim) { ++gcur; pos = 0; }
        WG_BARRIER();
        batch(std::false_type{});
    }
    STAMP(10);

    {
        bf16x8 wl[2][8], ag[4];
#pragma unroll
        for (int u = 0; u < 2; ++u)
#pragma unroll
            for (int a = 0; a < 8; ++a) {
                LAS char* pa = smem + L_WV1 + (cv ^ ((a >> 1) << 6) ^ ((a & 1) << 3)) + u * 8192;
                wl[u][a] = cat(tr_read(pa), tr_read(pa + 4096));
            }
#pragma unroll
        for (int u = 0; u < 4; ++u) {
            const u32x4 v = {pk2(acc[8 * u], acc[8 * u + 1]), pk2(acc[8 * u + 2], acc[8 * u + 3]),
                             pk2(acc[8 * u + 4], acc[8 * u + 5]), pk2(acc[8 * u + 6], acc[8 * u + 7])};
            ag[u] = __builtin_bit_cast(bf16x8, v);
        }
        FENCE();
        f32x4 oc[8];
#pragma unroll
        for (int a = 0; a < 8; ++a) oc[a] = mfma(__builtin_bit_cast(bf16x8, f32x4{wva[4 * a], wva[4 * a + 1], wva[4 * a + 2], wva[4 * a + 3]}), ag[0], f32x4{0.f, 0.f, 0.f, 0.f});
#pragma unroll
        for (int a = 0; a < 8; ++a) oc[a] = mfma(__builtin_bit_cast(bf16x8, f32x4{wvb[4 * a], wvb[4 * a + 1], wvb[4 * a + 2], wvb[4 * a + 3]}), ag[1], oc[a]);
#pragma unroll
        for (int a = 0; a < 8; ++a) oc[a] = mfma(wl[0][a], ag[2], oc[a]);
        FENCE();
        const unsigned ow = ring + n * 256 + ((g ^ n) << 4) + ((g & 1) << 3);
#pragma unroll
        for (int a = 0; a < 8; ++a) oc[a] = mfma(wl[1][a], ag[3], oc[a]);
#pragma unroll
        for (int a = 0; a < 8; ++a)
            *(LAS u32x2*)(size_t)(ow ^ ((a >> 1) << 6) ^ ((a & 1) << 3)) = u32x2{pk2(oc[a][0], oc[a][1]), pk2(oc[a][2], oc[a][3])};
        const auto rs_o = __builtin_amdgcn_make_buffer_rsrc(out, 0, xbytes, 0x00020000);
#pragma unroll
        for (int m = 0; m < 4; ++m) {
            const u32x4 v = *(LAS u32x4*)(size_t)(ring + m * 1024 + l * 16);
            __builtin_amdgcn_raw_buffer_store_b128(v, rs_o, node_row(w, m) + ((n ^ g ^ (4 * m)) << 4), 0, OUT_AUX);
        }
    }
    if (w == 0) { const unsigned o = l == 0 ? WS_KEY + b * 4 : OOB; ST1(mykey, srd_ws, o, " sc1"); }
    STAMP(11);
}

extern "C" void kernel_launch(void* const* d_in, const int* in_sizes, int n_in,
                              void* d_out, int out_size, void* d_ws, size_t ws_size,
                              hipStream_t stream) {
    const int N = in_sizes[0] / 128, E = in_sizes[1];
    k_graph<<<NB, 512, 0, stream>>>((const unsigned short*)d_in[0], (const int*)d_in[1], (const int*)d_in[2],
                                    (const unsigned short*)d_in[3], (const unsigned short*)d_in[4],
                                    (unsigned*)d_ws, (unsigned short*)d_out, N, E);
}
```

```cpp
#define POLL_T 340
#include <hip/hip_runtime.h>
#include <hip/hip_bf16.h>
#include <stdint.h>
#include <type_traits>
#define ORD 1
#define SKIP 0
#define STR2(x) #x
#define STR(x) STR2(x)

typedef __attribute__((ext_vector_type(8))) __bf16 bf16x8;
typedef __attribute__((ext_vector_type(4))) __bf16 bf16x4;
typedef __attribute__((ext_vector_type(2))) __bf16 bf16x2;
typedef __attribute__((ext_vector_type(4))) float f32x4;
typedef __attribute__((ext_vector_type(2))) float f32x2;
typedef __attribute__((ext_vector_type(4))) unsigned u32x4;
typedef __attribute__((ext_vector_type(2))) unsigned u32x2;
typedef __attribute__((ext_vector_type(32))) float f32x32;
typedef __attribute__((ext_vector_type(16))) float f32x16;
typedef __attribute__((ext_vector_type(16))) unsigned u32x16;
typedef __attribute__((ext_vector_type(12))) unsigned u32x12;
typedef __attribute__((ext_vector_type(8))) unsigned u32x8;
typedef unsigned long long u64;
#define LAS __attribute__((address_space(3)))

constexpr int NB = 256;
constexpr int G1 = 128;
constexpr int CH = 3136;
constexpr int CAP1 = 30;
constexpr int CAP2 = 192;
constexpr int SUBB = 784;
#define POLL_AT 3
#define OUT_AUX 16
#define LPRIO 3
#define PKADD 1
#define CODEPF 1
#define CODE_BYTES 21504
#define PARTNER 1
#define XSYNC 1
#define WVLATE 1
#define FASTIN 1
#define W_SLEEP 3
#define PRIO 1
#define SPLIT 16384
constexpr unsigned KEY_STEP = 0x9E3779B1u;
constexpr unsigned WS_KEY = 0;
constexpr unsigned WS_NOVF = 2048;
constexpr unsigned WS_INBOX = 4096;
constexpr unsigned WS_OVF = WS_INBOX + NB * G1 * 128;
constexpr unsigned WS_BYTES = WS_OVF + G1 * CH * 4;
constexpr unsigned OOB = 0x80000000u;
constexpr unsigned BIAS = 32768 - SPLIT;
constexpr unsigned PAD = 0x00FFFFFFu;

constexpr int L_CNT = 0;
constexpr int L_HDR = 128;
constexpr int L_LIST = 768;
constexpr int L_RING = L_LIST + 16 * SUBB;
constexpr int L_IMG = L_RING;
constexpr int L_CNT1 = L_IMG + NB * 144;
constexpr int L_WQ = L_RING + 2 * 16384 + 8192;
constexpr int L_WV0 = L_RING + 6 * 16384 + 8192;
constexpr int L_WV1 = L_RING + 131072;
constexpr int L_DUMP = L_WV1 + 16384;
constexpr int L_TOTAL = L_DUMP + 2048;
static_assert(L_RING % 256 == 0 && L_TOTAL <= 163840, "LDS map");

#define STAMP_DECL
#define STAMP(k)
#define STAMP_FLUSH(base, k0, k1)

__device__ __forceinline__ u32x4 make_srd(const void* p, unsigned bytes, unsigned back = 0) {
    const u64 a = (u64)p - back;
    u32x4 s; s.x = (unsigned)a; s.y = (unsigned)(a >> 32); s.z = bytes + back; s.w = 0x00020000u; return s;
}
#define LD4(dst, srd, off, FL) asm volatile("buffer_load_dwordx4 %0, %1, %2, 0 offen" FL : "=v"(dst) : "v"(off), "s"(srd) : "memory")
#define LD4S(dst, srd, off, so, FL) asm volatile("buffer_load_dwordx4 %0, %1, %2, %3 offen" FL : "=v"(dst) : "v"(off), "s"(srd), "s"(so) : "memory")
#define LD1(dst, srd, off, FL) asm volatile("buffer_load_dword %0, %1, %2, 0 offen" FL : "=v"(dst) : "v"(off), "s"(srd) : "memory")
#define ST1(val, srd, off, FL) asm volatile("buffer_store_dword %0, %1, %2, 0 offen" FL :: "v"(val), "v"(off), "s"(srd) : "memory")
#define PIN(x) asm volatile("" : "+v"(x))
#define VMWAIT(n) asm volatile("s_waitcnt vmcnt(" #n ")" ::: "memory")
#define FENCE() __builtin_amdgcn_sched_barrier(0)
#define WG_BARRIER() do { asm volatile("s_waitcnt lgkmcnt(0)" ::: "memory"); __builtin_amdgcn_s_barrier(); asm volatile("" ::: "memory"); } while (0)

struct Srd4 { u32x4 s0, s1, s2, s3; };
__device__ __forceinline__ Srd4 make_srd4(const void* p, unsigned bytes, unsigned back = 0) {
    return Srd4{make_srd(p, bytes, back), make_srd(p, bytes, back + 1024), make_srd(p, bytes, back + 2048), make_srd(p, bytes, back + 3072)};
}

__device__ __forceinline__ void set_m0(unsigned v) { asm volatile("s_mov_b32 m0, %0" :: "s"(v) : "memory"); }
template <int R> __device__ __forceinline__ void dma_rows(const Srd4& d, unsigned e, unsigned off) {
    unsigned tmp, mask = 0x00FFFF00u;
    asm volatile("v_and_or_b32 %0, %1, %2, %3\n\tbuffer_load_dwordx4 %0, %4, 0 offen offset:%c5 lds"
                 : "=&v"(tmp) : "v"(e), "s"(mask), "v"(off), "s"(R == 0 ? d.s0 : R == 1 ? d.s1 : R == 2 ? d.s2 : d.s3), "i"(R * 1024) : "memory");
}
__device__ __forceinline__ void dma_half(unsigned ldsbase, const Srd4& d, const unsigned* e, const u32x4& off) {
    set_m0(ldsbase);
    dma_rows<0>(d, e[0], off.x); dma_rows<1>(d, e[1], off.y); dma_rows<2>(d, e[2], off.z); dma_rows<3>(d, e[3], off.w);
}
__device__ __forceinline__ void dma_group(unsigned ldsbase, const Srd4& d, const unsigned (&e)[8], const u32x4& off) {
    asm volatile("s_waitcnt lgkmcnt(0)" ::: "memory");
    dma_half(ldsbase, d, e, off); dma_half(ldsbase + 4096, d, e + 4, off);
}

__device__ __forceinline__ void gather_loop(f32x32& acc, const u32x16& qf, const u32x16& addr, u32x4 misc, f32x16 cc,
                                            const Srd4& d, unsigned ring, int G, int nlast) {
    f32x32 t0, t1; u32x12 t2; int tmp, pr; u64 sm; unsigned mask = 0x00FFFF00u;
    asm volatile(
        ".macro GWAIT k\n\ts_cmp_gt_i32 %[t], \\k\n\ts_cbranch_scc1 1f\n\ts_waitcnt vmcnt(\\k)\n\t.endm\n\t"
        ".macro GROWS so\n\t.irp s, 0, 1, 2, 3\n\t"
        "ds_read_b128 v[64+8*\\s:67+8*\\s], v[176+\\s] offset:\\so\n\t"
        "ds_read_b128 v[68+8*\\s:71+8*\\s], v[176+\\s] offset:\\so+4096\n\t.endr\n\t.endm\n\t"
        ".macro GTR c, so\n\t"
        "ds_read_b64_tr_b16 v[96+4*\\c:97+4*\\c], v[180+\\c] offset:\\so\n\t"
        "ds_read_b64_tr_b16 v[98+4*\\c:99+4*\\c], v[180+\\c] offset:\\so+4096\n\t.endm\n\t"
        ".macro GS s, c\n\t"
        "v_mfma_f32_16x16x32_bf16 v[\\c:\\c+3], v[64+8*\\s:67+8*\\s], v[160+4*\\s:163+4*\\s], v[\\c:\\c+3]\n\t"
        "v_mfma_f32_16x16x32_bf16 v[\\c+4:\\c+7], v[68+8*\\s:71+8*\\s], v[160+4*\\s:163+4*\\s], v[\\c+4:\\c+7]\n\t.endm\n\t"
        ".macro GLOAD m, r, srd\n\t"
        "v_and_or_b32 v195, v[212+\\m], %[mask], v[188+\\r]\n\t"
        "buffer_load_dwordx4 v195, \\srd, 0 offen offset:1024*\\r lds\n\t.endm\n\t"
        ".macro GPART m, r, srd\n\ts_cmp_le_i32 %[nl], \\m\n\ts_cbranch_scc1 4f\n\tGLOAD \\m, \\r, \\srd\n\t.endm\n\t"
        ".macro GSUM m\n\tv_mfma_f32_16x16x32_bf16 v[128+4*(\\m):131+4*(\\m)], v[96+4*(\\m):99+4*(\\m)], v[220:223], v[128+4*(\\m):131+4*(\\m)]\n\t.endm\n\t"
        ".macro GCMP m, d\n\tv_cmp_ne_u32_sdwa \\d, v[212+\\m], v193 src0_sel:BYTE_0 src1_sel:DWORD\n\t.endm\n\t"
        ".macro GPAIR m, c\n\t"
        "GSUM \\m\n\tGCMP \\m+1, %[sm]\n\tv_cndmask_b32_e32 v[\\c+\\m], 0, v194, vcc\n\t"
        "GSUM \\m+1\n\t.if \\m < 6\n\tGCMP \\m+2, vcc\n\t.else\n\ts_cmp_gt_i32 %[rem], 0\n\t.endif\n\t"
        "v_cndmask_b32_e64 v[\\c+\\m+1], 0, v194, %[sm]\n\t.endm\n\t"
        ".macro GSTEP so, c, eo\n\t"
        "GTR 0, \\so\n\t"
        "s_waitcnt lgkmcnt(2)\n\tGS 0, \\c\n\t"
        "GTR 1, \\so\n\tGTR 2, \\so\n\tGS 1, \\c\n\t"
        "GTR 3, \\so\n\tGTR 4, \\so\n\tGS 2, \\c\n\t"
        "GTR 5, \\so\n\tGTR 6, \\so\n\tGS 3, \\c\n\t"
        "GTR 7, \\so\n\t"
        "ds_read2_b32 v[212:213], v192 offset0:\\eo offset1:\\eo+4\n\t"
        "ds_read2_b32 v[214:215], v192 offset0:\\eo+8 offset1:\\eo+12\n\t"
        "ds_read2_b32 v[216:217], v192 offset0:\\eo+16 offset1:\\eo+20\n\t"
        "ds_read2_b32 v[218:219], v192 offset0:\\eo+24 offset1:\\eo+28\n\t"
        "s_add_u32 m0, %[ring], \\so\n\t"
        "s_cmp_gt_i32 %[rem], 3\n\t"
        ".irp k, 0, 1, 2, 3, 4, 5, 6, 7\n\tv_exp_f32_e32 v[\\c+\\k], v[\\c+\\k]\n\t.endr\n\t"
        "s_waitcnt lgkmcnt(0)\n\t"
        "s_cbranch_scc0 2f\n\t"
        "GLOAD 0, 0, %[s0]\n\tGLOAD 1, 1, %[s1]\n\tGLOAD 2, 2, %[s2]\n\tGLOAD 3, 3, %[s3]\n\t"
        "s_add_u32 m0, m0, 0x1000\n\t"
        "GLOAD 4, 0, %[s0]\n\tGLOAD 5, 1, %[s1]\n\tGLOAD 6, 2, %[s2]\n\tGLOAD 7, 3, %[s3]\n\t"
        "GSIG \\c\n\t"
        "s_waitcnt vmcnt(8)\n\t"
        "s_branch 1f\n\t"
        "2:\n\t"
        "s_mov_b32 %[t], 0\n\t"
        "s_cmp_eq_u32 %[rem], 3\n\ts_cbranch_scc0 4f\n\t"
        "s_mov_b32 %[t], %[nl]\n\t"
        "GLOAD 0, 0, %[s0]\n\tGPART 1, 1, %[s1]\n\tGPART 2, 2, %[s2]\n\tGPART 3, 3, %[s3]\n\t"
        "s_add_u32 m0, m0, 0x1000\n\t"
        "GPART 4, 0, %[s0]\n\tGPART 5, 1, %[s1]\n\tGPART 6, 2, %[s2]\n\tGPART 7, 3, %[s3]\n\t"
        "4:\n\t"
        "GSIG \\c\n\t"
        "s_waitcnt vmcnt(8)\n\t"
        "GWAIT 7\n\tGWAIT 6\n\tGWAIT 5\n\tGWAIT 4\n\tGWAIT 3\n\tGWAIT 2\n\tGWAIT 1\n\tGWAIT 0\n\t"
        "1:\n\t"
        "GROWS 8192-\\so\n\t"
        "GCMP 0, vcc\n\t"
        "s_add_i32 %[rem], %[rem], -1\n\t"
        "GPAIR 0, \\c\n\tGPAIR 2, \\c\n\tGPAIR 4, \\c\n\tGPAIR 6, \\c\n\t"
        ".endm\n\t"
        ".macro GSIG c\n\t"
        ".if " STR(PKADD) "\n\t.irp k, 0, 2, 4, 6\n\tv_pk_add_f32 v[\\c+\\k:\\c+\\k+1], v[\\c+\\k:\\c+\\k+1], 1.0 op_sel_hi:[1,0]\n\t.endr\n\t.else\n\t"
        ".irp k, 0, 1, 2, 3, 4, 5, 6, 7\n\tv_add_f32_e32 v[\\c+\\k], 1.0, v[\\c+\\k]\n\t.endr\n\t.endif\n\t"
        ".irp k, 0, 1, 2, 3, 4, 5, 6, 7\n\tv_rcp_f32_e32 v[\\c+\\k], v[\\c+\\k]\n\t.endr\n\t"
        ".irp k, 0, 1, 2, 3\n\tv_cvt_pk_bf16_f32 v[220+\\k], v[\\c+2*\\k], v[\\c+2*\\k+1]\n\t.endr\n\t.endm\n\t"
        "s_waitcnt vmcnt(8)\n\t"
        "s_cmp_gt_i32 %[rem], 1\n\ts_cbranch_scc1 1f\n\ts_waitcnt vmcnt(0)\n\t1:\n\t"
        "GROWS 0\n\t"
        ".if " STR(LPRIO) " >= 3\n\ts_mov_b32 %[pr], 0\n\ts_setprio 2\n\t.endif\n\t"
        "0:\n\t"
        ".if " STR(LPRIO) " == 3\n\t"
        "s_cmp_eq_u32 %[pr], 1\n\ts_cbranch_scc0 5f\n\ts_setprio 1\n\t5:\n\t"
        "s_cmp_eq_u32 %[pr], 2\n\ts_cbranch_scc0 5f\n\ts_setprio 0\n\t5:\n\t"
        "s_add_u32 %[pr], %[pr], 1\n\t.endif\n\t"
        "GSTEP 0, 196, 64\n\t"
        "s_cbranch_scc0 9f\n\t"
        "GSTEP 8192, 204, 96\n\t"
        "v_add_u32_e32 v192, 0x100, v192\n\t"
        "s_cbranch_scc1 0b\n\t"
        "9:\n\t"
        "s_waitcnt lgkmcnt(0)\n\ts_nop 6\n\t"
        ".purgem GWAIT\n\t.purgem GROWS\n\t.purgem GTR\n\t.purgem GS\n\t.purgem GLOAD\n\t.purgem GPART\n\t.purgem GSUM\n\t.purgem GCMP\n\t.purgem GPAIR\n\t.purgem GSTEP\n\t.purgem GSIG"
        : "+{v[128:159]}"(acc), "+{v[192:195]}"(misc), "+{v[196:211]}"(cc), "=&{v[64:95]}"(t0), "=&{v[96:127]}"(t1), "=&{v[212:223]}"(t2),
          [rem] "+s"(G), [t] "=&s"(tmp), [pr] "=&s"(pr), [sm] "=&s"(sm)
        : "{v[160:175]}"(qf), "{v[176:191]}"(addr), [s0] "s"(d.s0), [s1] "s"(d.s1), [s2] "s"(d.s2), [s3] "s"(d.s3),
          [mask] "s"(mask), [ring] "s"(ring), [nl] "s"(nlast)
        : "vcc", "scc", "memory");
}

__device__ __forceinline__ u64 rank_edges(u32x16& ed, u32x16& es, u32x16& rk, u64 m0, u64 m1, u64 m2, u64 m3, unsigned keya) {
    u32x16 b4; u64 over; unsigned sel = 0x01050401u, c36 = 36;
    asm volatile(
        ".macro RADD k\n\tv_lshlrev_b32_sdwa v[96+\\k], 2, v[64+\\k] dst_sel:DWORD dst_unused:UNUSED_PAD src0_sel:DWORD src1_sel:BYTE_0\n\t"
        "ds_add_rtn_u32 v[112+\\k], v[96+\\k], v111 offset:%c[cnt]\n\t.endm\n\t"
        ".macro RENT k\n\tv_add_u32_e32 v[80+\\k], %c[bias], v[80+\\k]\n\tv_perm_b32 v[80+\\k], v[80+\\k], v[64+\\k], %[sel]\n\t.endm\n\t"
        ".macro RPUT k\n\tv_min_u32_e32 v110, %c[cap], v[112+\\k]\n\tv_mad_u32_u24 v110, v[96+\\k], %[c36], v110\n\t"
        "ds_write_b32 v110, v[80+\\k] offset:%c[img]\n\t.endm\n\t"
        "v_mov_b32_e32 v111, 4\n\t"
        ".irp k, 0, 2, 4, 6, 8, 10, 12\n\tv_mov_b64_e32 v[112+\\k:113+\\k], 0\n\t.endr\n\t"
        "s_mov_b64 exec, %[m0]\n\ts_waitcnt vmcnt(9)\n\tRADD 0\n\tRADD 1\n\tRADD 2\n\tRADD 3\n\t"
        "s_mov_b64 exec, %[m1]\n\ts_waitcnt vmcnt(8)\n\tRADD 4\n\tRADD 5\n\tRADD 6\n\tRADD 7\n\t"
        "s_mov_b64 exec, %[m2]\n\ts_waitcnt vmcnt(7)\n\tRADD 8\n\tRADD 9\n\tRADD 10\n\tRADD 11\n\t"
        "s_mov_b64 exec, %[m3]\n\ts_waitcnt vmcnt(6)\n\tRADD 12\n\t"
        "s_mov_b64 exec, %[m0]\n\ts_waitcnt vmcnt(5)\n\tRENT 0\n\tRENT 1\n\tRENT 2\n\tRENT 3\n\t"
        "s_waitcnt lgkmcnt(9)\n\tRPUT 0\n\tRPUT 1\n\tRPUT 2\n\tRPUT 3\n\t"
        "s_mov_b64 exec, %[m1]\n\ts_waitcnt vmcnt(4)\n\tRENT 4\n\tRENT 5\n\tRENT 6\n\tRENT 7\n\t"
        "s_waitcnt lgkmcnt(9)\n\tRPUT 4\n\tRPUT 5\n\tRPUT 6\n\tRPUT 7\n\t"
        "s_mov_b64 exec, %[m2]\n\ts_waitcnt vmcnt(3)\n\tRENT 8\n\tRENT 9\n\tRENT 10\n\tRENT 11\n\t"
        "s_waitcnt lgkmcnt(9)\n\tRPUT 8\n\tRPUT 9\n\tRPUT 10\n\tRPUT 11\n\t"
        "s_mov_b64 exec, %[m3]\n\ts_waitcnt vmcnt(0)\n\tRENT 12\n\t"
        "s_waitcnt lgkmcnt(12)\n\tRPUT 12\n\t"
        "s_mov_b64 exec, -1\n\t"
        "v_add_u32_e32 v93, %c[step], v93\n\t"
        "v_max3_u32 v110, v112, v113, v114\n\tv_max3_u32 v109, v115, v116, v117\n\t"
        "ds_write_b32 %[keya], v93 offset:%c[cnt]+1024\n\t"
        "v_max3_u32 v110, v110, v118, v119\n\tv_max3_u32 v109, v109, v120, v121\n\t"
        "v_max3_u32 v110, v110, v122, v123\n\tv_max3_u32 v110, v110, v109, v124\n\t"
        "v_cmp_le_u32_e32 vcc, %c[cap], v110\n\ts_mov_b64 %[over], vcc\n\t"
        ".purgem RADD\n\t.purgem RENT\n\t.purgem RPUT"
        : "+{v[64:79]}"(ed), "+{v[80:95]}"(es), "=&{v[96:111]}"(b4), "=&{v[112:127]}"(rk), [over] "=&s"(over)
        : [m0] "s"(m0), [m1] "s"(m1), [m2] "s"(m2), [m3] "s"(m3), [keya] "v"(keya), [sel] "s"(sel), [c36] "s"(c36),
          [cnt] "i"(L_CNT1), [img] "i"(L_IMG + 8), [bias] "i"(BIAS), [cap] "i"(4 * CAP1), [step] "i"(KEY_STEP)
        : "vcc", "memory");
    return over;
}
__device__ __forceinline__ unsigned read_image(f32x32& img, u32x16& meta, u32x16& sums, unsigned ima, unsigned cna) {
    unsigned no;
    asm volatile(
        "v_mov_b32_e32 v121, 0\n\tds_read_b32 v120, v121 offset:%c[cnt]+2048\n\t"
        ".irp k, 0, 1, 2, 3, 4, 5, 6, 7\n\tds_read_b128 v[64+4*\\k:67+4*\\k], %[ima] offset:4608*\\k\n\t.endr\n\t"
        ".irp k, 0, 1, 2, 3\n\tds_read2_b32 v[96+2*\\k:97+2*\\k], %[cna] offset0:64*\\k offset1:64*\\k+32\n\t.endr\n\t"
        "s_waitcnt lgkmcnt(11)\n\tv_readfirstlane_b32 %[no], v120\n\t"
        "v_add_u32_e32 v121, 0x400, %[cna]\n\t"
        ".irp k, 0, 1, 2, 3\n\tds_read2_b32 v[104+2*\\k:105+2*\\k], v121 offset0:64*\\k offset1:64*\\k+32\n\t.endr\n\t"
        "s_waitcnt lgkmcnt(12)\n\t"
        ".irp k, 0, 1, 2, 3\n\tv_add3_u32 v[112+\\k], v[64+4*\\k], v[65+4*\\k], v[66+4*\\k]\n\tv_add_u32_e32 v[112+\\k], v[112+\\k], v[67+4*\\k]\n\t.endr\n\t"
        "s_waitcnt lgkmcnt(8)\n\t"
        ".irp k, 4, 5, 6, 7\n\tv_add3_u32 v[112+\\k], v[64+4*\\k], v[65+4*\\k], v[66+4*\\k]\n\tv_add_u32_e32 v[112+\\k], v[112+\\k], v[67+4*\\k]\n\t.endr\n\t"
        ".irp k, 0, 1, 2, 3, 4, 5, 6, 7\n\tv_add_u32_dpp v[112+\\k], v[112+\\k], v[112+\\k] quad_perm:[1,0,3,2] row_mask:0xf bank_mask:0xf\n\t.endr\n\t"
        ".irp k, 0, 1, 2, 3, 4, 5, 6, 7\n\tv_add_u32_dpp v[112+\\k], v[112+\\k], v[112+\\k] quad_perm:[2,3,0,1] row_mask:0xf bank_mask:0xf\n\t.endr\n\t"
        ".irp k, 0, 1, 2, 3, 4, 5, 6, 7\n\tv_add_u32_dpp v[112+\\k], v[112+\\k], v[112+\\k] row_half_mirror row_mask:0xf bank_mask:0xf\n\t.endr\n\t"
        "s_waitcnt lgkmcnt(0)"
        : "=&{v[64:95]}"(img), "=&{v[96:111]}"(meta), "=&{v[112:127]}"(sums), [no] "=&s"(no)
        : [ima] "v"(ima), [cna] "v"(cna), [cnt] "i"(L_CNT1) : "memory");
    return no;
}
__device__ __forceinline__ void publish_image(f32x32 img, u32x16 meta, u32x16 sums, const u32x4& srd, unsigned ob) {
    u64 pc0 = 0x0101010101010101ull; unsigned so;
    asm volatile(
        ".macro PUB k\n\ts_mov_b64 exec, %[pc0]\n\tv_mov_b32_e32 v[64+4*\\k], v[96+\\k]\n\tv_mov_b32_e32 v[65+4*\\k], v[112+\\k]\n\t"
        "s_mov_b64 exec, -1\n\tv_xor_b32_e32 v[64+4*\\k], v[64+4*\\k], v[104+\\k]\n\t"
        "buffer_store_dwordx4 v[64+4*\\k:67+4*\\k], %[ob], %[srd], %[so] offen sc1\n\ts_add_u32 %[so], %[so], %c[step]\n\t.endm\n\t"
        "s_mov_b32 %[so], 0\n\t"
        ".irp k, 0, 1, 2, 3, 4, 5, 6, 7\n\tPUB \\k\n\t.endr\n\t.purgem PUB"
        : "+{v[64:95]}"(img), [so] "=&s"(so)
        : "{v[96:111]}"(meta), "{v[112:127]}"(sums), [srd] "s"(srd), [ob] "v"(ob), [pc0] "s"(pc0), [step] "i"(32 * G1 * 128) : "scc", "memory");
}

__device__ __forceinline__ void inbox_request(u32x16& raw, const u32x4& srd, unsigned ib) {
    unsigned o1 = 0x1000, o2 = 0x2000, o3 = 0x3000;
    asm volatile("buffer_load_dwordx4 v[64:67], %1, %2, 0 offen sc1\n\tbuffer_load_dwordx4 v[68:71], %1, %2, %3 offen sc1\n\t"
                 "buffer_load_dwordx4 v[72:75], %1, %2, %4 offen sc1\n\tbuffer_load_dwordx4 v[76:79], %1, %2, %5 offen sc1"
                 : "=&{v[64:79]}"(raw) : "v"(ib), "s"(srd), "s"(o1), "s"(o2), "s"(o3) : "memory");
}
__device__ __forceinline__ unsigned inbox_stage(u32x16 raw, const u32x4& srd, unsigned ib, unsigned key, int i4, unsigned hdra) {
    u32x16 t0, t2; u32x4 t1; u64 m0, m1, m2, m3; unsigned todo, fresh, spin, o1, o2, o3, over;
    u64 pc0 = 0x0101010101010101ull;
    asm volatile(
        ".macro IVAL j, so\n\t"
        "s_bitcmp1_b32 %[todo], \\j\n\ts_cbranch_scc0 2f\n\t"
        "v_xor_b32_e32 v[64+4*\\j], %[key], v[64+4*\\j]\n\t"
        "v_sub_u32_e32 v97, 0, v[65+4*\\j]\n\t"
        "v_add_u32_e32 v96, v[64+4*\\j], v[65+4*\\j]\n\t"
        "v_mov_b32_dpp v[80+\\j], v[64+4*\\j] quad_perm:[0,0,0,0] row_mask:0xf bank_mask:0xf\n\t"
        "v_cndmask_b32_e64 v96, v96, v97, %[pc0]\n\t"
        "s_nop 0\n\t"
        "v_mov_b32_dpp v[80+\\j], v[80+\\j] row_shr:4 row_mask:0xf bank_mask:0xa\n\t"
        "v_cmp_gt_i32_e32 vcc, v[80+\\j], v92\n\t"
        "v_add3_u32 v96, v96, v[66+4*\\j], v[67+4*\\j]\n\t"
        "v_lshrrev_b32_e32 v97, 14, v[80+\\j]\n\t"
        "v_cndmask_b32_e32 v96, 0, v96, vcc\n\t"
        "s_nop 1\n\tv_add_u32_dpp v96, v96, v96 quad_perm:[1,0,3,2] row_mask:0xf bank_mask:0xf\n\t"
        "s_nop 1\n\tv_add_u32_dpp v96, v96, v96 quad_perm:[2,3,0,1] row_mask:0xf bank_mask:0xf\n\t"
        "s_nop 1\n\tv_add_u32_dpp v96, v96, v96 row_half_mirror row_mask:0xf bank_mask:0xf\n\t"
        "v_or_b32_e32 v96, v96, v97\n\t"
        "v_cmp_ne_u32_e32 vcc, 0, v96\n\t"
        "s_cbranch_vccz 1f\n\t"
        "buffer_load_dwordx4 v[64+4*\\j:67+4*\\j], %[ib], %[srd], \\so offen sc1\n\t"
        "s_branch 2f\n\t"
        "1:\n\ts_bitset1_b32 %[fresh], \\j\n\t"
        "2:\n\t.endm\n\t"
        ".macro IRANK j\n\t"
        "s_bitcmp1_b32 %[fresh], \\j\n\ts_cbranch_scc0 2f\n\t"
        "v_cmp_lt_u32_e64 %[m0], v92, v[80+\\j]\n\tv_cmp_lt_u32_e64 %[m1], v93, v[80+\\j]\n\t"
        "v_cmp_lt_u32_e64 %[m2], v94, v[80+\\j]\n\tv_cmp_lt_u32_e64 %[m3], v95, v[80+\\j]\n\t"
        ".irp k, 0, 1, 2, 3\n\tv_lshrrev_b32_e32 v[84+\\k], 21, v[64+4*\\j+\\k]\n\t.endr\n\t"
        ".irp k, 0, 1, 2, 3\n\tv_and_b32_e32 v[84+\\k], 60, v[84+\\k]\n\t.endr\n\t"
        "v_cmp_lt_u32_e32 vcc, %c[cap], v[80+\\j]\n\t"
        "s_mov_b64 exec, %[m0]\n\tds_add_rtn_u32 v88, v84, v98 offset:%c[cnt]\n\t"
        "s_mov_b64 exec, %[m1]\n\tds_add_rtn_u32 v89, v85, v98 offset:%c[cnt]\n\t"
        "s_mov_b64 exec, %[m2]\n\tds_add_rtn_u32 v90, v86, v98 offset:%c[cnt]\n\t"
        "s_mov_b64 exec, %[m3]\n\tds_add_rtn_u32 v91, v87, v98 offset:%c[cnt]\n\t"
        "s_mov_b64 exec, %[pc0]\n\tds_write_b32 %[hdra], v[80+\\j] offset:128*\\j\n\t"
        "s_mov_b64 exec, -1\n\t"
        "s_cbranch_vccz 1f\n\t"
        "s_mov_b32 %[over], 1\n\t"
        "1:\n\ts_waitcnt lgkmcnt(1)\n\t"
        ".irp k, 0, 1, 2, 3\n\tv_min_u32_e32 v[88+\\k], %c[sub]-4, v[88+\\k]\n\t.endr\n\t"
        ".irp k, 0, 1, 2, 3\n\tv_mad_u32_u24 v[88+\\k], v[84+\\k], %[sub4], v[88+\\k]\n\t.endr\n\t"
        "s_mov_b64 exec, %[m0]\n\tds_write_b32 v88, v[64+4*\\j] offset:%c[list]\n\t"
        "s_mov_b64 exec, %[m1]\n\tds_write_b32 v89, v[65+4*\\j] offset:%c[list]\n\t"
        "s_mov_b64 exec, %[m2]\n\tds_write_b32 v90, v[66+4*\\j] offset:%c[list]\n\t"
        "s_mov_b64 exec, %[m3]\n\tds_write_b32 v91, v[67+4*\\j] offset:%c[list]\n\t"
        "s_mov_b64 exec, -1\n\t"
        "2:\n\t.endm\n\t"
        ".macro FADD j, kb, rb\n\t"
        ".irp k, 0, 1, 2, 3\n\tv_lshrrev_b32_e32 v[\\kb+\\k], 21, v[64+4*\\j+\\k]\n\t.endr\n\t"
        ".irp k, 0, 1, 2, 3\n\tv_and_b32_e32 v[\\kb+\\k], 60, v[\\kb+\\k]\n\t.endr\n\t"
        "v_cmpx_lt_u32_e64 %[m0], v94, v[80+\\j]\n\tds_add_rtn_u32 v[\\rb+2], v[\\kb+2], v98 offset:%c[cnt]\n\t"
        "v_cmpx_lt_u32_e64 %[m0], v95, v[80+\\j]\n\tds_add_rtn_u32 v[\\rb+3], v[\\kb+3], v98 offset:%c[cnt]\n\t"
        "s_mov_b64 exec, -1\n\t"
        "v_cmpx_lt_u32_e64 %[m0], v92, v[80+\\j]\n\tds_add_rtn_u32 v[\\rb], v[\\kb], v98 offset:%c[cnt]\n\t"
        "v_cmpx_lt_u32_e64 %[m0], v93, v[80+\\j]\n\tds_add_rtn_u32 v[\\rb+1], v[\\kb+1], v98 offset:%c[cnt]\n\t"
        "s_mov_b64 exec, -1\n\t.endm\n\t"
        ".macro FPUT j, kb, rb, n\n\t"
        "s_waitcnt lgkmcnt(\\n)\n\t"
        ".irp k, 0, 1, 2, 3\n\tv_min_u32_e32 v[\\rb+\\k], %c[sub]-4, v[\\rb+\\k]\n\t.endr\n\t"
        ".irp k, 0, 1, 2, 3\n\tv_mad_u32_u24 v[\\rb+\\k], v[\\kb+\\k], %[sub4], v[\\rb+\\k]\n\t.endr\n\t"
        "v_cmpx_lt_u32_e64 %[m0], v94, v[80+\\j]\n\tds_write_b32 v[\\rb+2], v[66+4*\\j] offset:%c[list]\n\t"
        "v_cmpx_lt_u32_e64 %[m0], v95, v[80+\\j]\n\tds_write_b32 v[\\rb+3], v[67+4*\\j] offset:%c[list]\n\t"
        "s_mov_b64 exec, -1\n\t"
        "v_cmpx_lt_u32_e64 %[m0], v92, v[80+\\j]\n\tds_write_b32 v[\\rb], v[64+4*\\j] offset:%c[list]\n\t"
        "v_cmpx_lt_u32_e64 %[m0], v93, v[80+\\j]\n\tds_write_b32 v[\\rb+1], v[65+4*\\j] offset:%c[list]\n\t"
        "s_mov_b64 exec, -1\n\t.endm\n\t"
        "s_movk_i32 %[o1], 0x1000\n\ts_movk_i32 %[o2], 0x2000\n\ts_movk_i32 %[o3], 0x3000\n\t"
        "v_mov_b32_e32 v92, %[i4]\n\tv_add_u32_e32 v93, 4, %[i4]\n\tv_add_u32_e32 v94, 8, %[i4]\n\tv_add_u32_e32 v95, 12, %[i4]\n\t"
        "v_mov_b32_e32 v98, 4\n\t"
        "s_mov_b32 %[todo], 15\n\ts_mov_b32 %[spin], 0x400000\n\ts_mov_b32 %[over], 0\n\t"
        ".if " STR(FASTIN) "\n\t"
        "s_waitcnt vmcnt(0)\n\t"
        ".irp j, 0, 1, 2, 3\n\tv_xor_b32_e32 v[64+4*\\j], %[key], v[64+4*\\j]\n\t.endr\n\t"
        ".irp j, 0, 1, 2, 3\n\tv_mov_b32_dpp v[80+\\j], v[64+4*\\j] quad_perm:[0,0,0,0] row_mask:0xf bank_mask:0xf\n\t.endr\n\t"
        ".irp j, 0, 1, 2, 3\n\tv_add_u32_e32 v[84+\\j], v[64+4*\\j], v[65+4*\\j]\n\t.endr\n\t"
        ".irp j, 0, 1, 2, 3\n\tv_mov_b32_dpp v[80+\\j], v[80+\\j] row_shr:4 row_mask:0xf bank_mask:0xa\n\t.endr\n\t"
        "s_mov_b64 exec, %[pc0]\n\t"
        ".irp j, 0, 1, 2, 3\n\tv_sub_u32_e32 v[84+\\j], 0, v[65+4*\\j]\n\t.endr\n\t"
        "s_mov_b64 exec, -1\n\t"
        "v_cmp_gt_i32_e64 %[m0], v80, v92\n\tv_cmp_gt_i32_e64 %[m1], v81, v92\n\tv_cmp_gt_i32_e64 %[m2], v82, v92\n\tv_cmp_gt_i32_e64 %[m3], v83, v92\n\t"
        ".irp j, 0, 1, 2, 3\n\tv_add3_u32 v[84+\\j], v[84+\\j], v[66+4*\\j], v[67+4*\\j]\n\t.endr\n\t"
        "v_cndmask_b32_e64 v84, 0, v84, %[m0]\n\tv_cndmask_b32_e64 v85, 0, v85, %[m1]\n\tv_cndmask_b32_e64 v86, 0, v86, %[m2]\n\tv_cndmask_b32_e64 v87, 0, v87, %[m3]\n\t"
        ".irp j, 0, 1, 2, 3\n\tv_add_u32_dpp v[84+\\j], v[84+\\j], v[84+\\j] quad_perm:[1,0,3,2] row_mask:0xf bank_mask:0xf\n\t.endr\n\t"
        ".irp j, 0, 1, 2, 3\n\tv_add_u32_dpp v[84+\\j], v[84+\\j], v[84+\\j] quad_perm:[2,3,0,1] row_mask:0xf bank_mask:0xf\n\t.endr\n\t"
        ".irp j, 0, 1, 2, 3\n\tv_add_u32_dpp v[84+\\j], v[84+\\j], v[84+\\j] row_half_mirror row_mask:0xf bank_mask:0xf\n\t.endr\n\t"
        "v_or3_b32 v96, v80, v81, v82\n\tv_max3_u32 v97, v80, v81, v82\n\tv_or_b32_e32 v96, v96, v83\n\tv_max_u32_e32 v97, v97, v83\n\t"
        "v_lshrrev_b32_e32 v96, 14, v96\n\tv_or3_b32 v96, v96, v84, v85\n\tv_or3_b32 v96, v96, v86, v87\n\t"
        "v_cmp_ne_u32_e32 vcc, 0, v96\n\t"
        "s_cbranch_vccnz 7f\n\t"
        "v_cmp_lt_u32_e32 vcc, %c[cap], v97\n\t"
        "FADD 0, 100, 104\n\tFADD 1, 108, 112\n\t"
        "s_cbranch_vccz 6f\n\ts_mov_b32 %[over], 1\n\t6:\n\t"
        "FPUT 0, 100, 104, 4\n\tFADD 2, 100, 104\n\tFPUT 1, 108, 112, 8\n\tFADD 3, 108, 112\n\tFPUT 2, 100, 104, 8\n\tFPUT 3, 108, 112, 4\n\t"
        "s_mov_b64 exec, %[pc0]\n\t"
        ".irp j, 0, 1, 2, 3\n\tds_write_b32 %[hdra], v[80+\\j] offset:128*\\j\n\t.endr\n\t"
        "s_mov_b64 exec, -1\n\t"
        "s_branch 9f\n\t"
        "7:\n\t.irp j, 0, 1, 2, 3\n\tv_xor_b32_e32 v[64+4*\\j], %[key], v[64+4*\\j]\n\t.endr\n\t"
        ".endif\n\t"
        "0:\n\t"
        "s_mov_b32 %[fresh], 0\n\t"
        "s_waitcnt vmcnt(0)\n\t"
        "IVAL 0, 0\n\tIVAL 1, %[o1]\n\tIVAL 2, %[o2]\n\tIVAL 3, %[o3]\n\t"
        "IRANK 0\n\tIRANK 1\n\tIRANK 2\n\tIRANK 3\n\t"
        "s_andn2_b32 %[todo], %[todo], %[fresh]\n\t"
        "s_cbranch_scc0 9f\n\t"
        "s_sub_u32 %[spin], %[spin], 1\n\t"
        "s_cbranch_scc0 0b\n\t"
        "s_waitcnt vmcnt(0)\n\t"
        "9:\n\t"
        ".purgem IVAL\n\t.purgem IRANK\n\t.purgem FADD\n\t.purgem FPUT"
        : "+{v[64:79]}"(raw), "=&{v[80:95]}"(t0), "=&{v[96:99]}"(t1), "=&{v[100:115]}"(t2), [m0] "=&s"(m0), [m1] "=&s"(m1), [m2] "=&s"(m2), [m3] "=&s"(m3),
          [todo] "=&s"(todo), [fresh] "=&s"(fresh), [spin] "=&s"(spin), [o1] "=&s"(o1), [o2] "=&s"(o2), [o3] "=&s"(o3), [over] "=&s"(over)
        : [srd] "s"(srd), [ib] "v"(ib), [key] "s"(key), [i4] "v"(i4), [hdra] "v"(hdra), [pc0] "s"(pc0), [sub4] "s"(SUBB / 4),
          [cnt] "i"(L_CNT), [list] "i"(L_LIST), [sub] "i"(SUBB), [cap] "i"(4 * CAP1)
        : "vcc", "scc", "memory");
    return over;
}

template <int CTRL, int BANK> __device__ __forceinline__ unsigned dpp(unsigned old, unsigned v) { return __builtin_amdgcn_update_dpp(old, v, CTRL, 0xF, BANK, true); }
__device__ __forceinline__ unsigned bcast8(unsigned v) { v = dpp<0x00, 0xF>(0u, v); return __builtin_amdgcn_update_dpp(v, v, 0x114, 0xF, 0xA, false); }
__device__ __forceinline__ unsigned sum8(unsigned v) { v += dpp<0xB1, 0xF>(0u, v); v += dpp<0x4E, 0xF>(0u, v); return v + dpp<0x141, 0xF>(0u, v); }
__device__ __forceinline__ bf16x4 tr_read(LAS char* p) { return __builtin_amdgcn_ds_read_tr16_b64_v4bf16((LAS bf16x4*)p); }
__device__ __forceinline__ bf16x8 cat(bf16x4 a, bf16x4 b) { return __builtin_shufflevector(a, b, 0, 1, 2, 3, 4, 5, 6, 7); }
__device__ __forceinline__ unsigned pk2(float a, float b) { f32x2 v = {a, b}; bf16x2 r = __builtin_convertvector(v, bf16x2); return __builtin_bit_cast(unsigned, r); }
__device__ __forceinline__ f32x4 mfma(bf16x8 a, bf16x8 b, f32x4 c) { return __builtin_amdgcn_mfma_f32_16x16x32_bf16(a, b, c, 0, 0, 0); }
__device__ __forceinline__ unsigned lds_add(LAS char* p, unsigned v) { return __hip_atomic_fetch_add((LAS unsigned*)p, v, __ATOMIC_RELAXED, __HIP_MEMORY_SCOPE_WORKGROUP); }
__device__ __forceinline__ LAS unsigned* ldsp(unsigned a) { return (LAS unsigned*)(size_t)a; }
__device__ __forceinline__ unsigned byte0_x4(unsigned v) { unsigned r; asm("v_lshlrev_b32_sdwa %0, 2, %1 dst_sel:DWORD dst_unused:UNUSED_PAD src0_sel:DWORD src1_sel:BYTE_0" : "=v"(r) : "v"(v)); return r; }
__device__ __forceinline__ unsigned mad24(unsigned a, unsigned b, unsigned c) { unsigned r; asm("v_mad_u32_u24 %0, %1, %2, %3" : "=v"(r) : "v"(a), "s"(b), "v"(c)); return r; }
__device__ __forceinline__ void lds_arrive(LAS char* p, int lane) {
    asm volatile("s_waitcnt lgkmcnt(0)" ::: "memory");
    if (lane == 0) __hip_atomic_fetch_add((LAS unsigned*)p, 1u, __ATOMIC_RELAXED, __HIP_MEMORY_SCOPE_WORKGROUP);
}
__device__ __forceinline__ void lds_await(LAS char* p, unsigned n) {
    while (__builtin_amdgcn_readfirstlane(*(volatile LAS unsigned*)p) < n) __builtin_amdgcn_s_sleep(1);
    asm volatile("" ::: "memory");
}
__device__ __forceinline__ unsigned rfl(unsigned v) { return __builtin_amdgcn_readfirstlane(v); }

__global__ __launch_bounds__(512, 2) void k_graph(const unsigned short* x_, const int* src_, const int* dst_, const unsigned short* wq_, const unsigned short* wv_,
                                                  unsigned* ws_, unsigned short* out_, int N_, int E_) {
    u32x4 srd_d, srd_s, srd_ws, srd_x0, pw, po;
    u64 t_start;
    asm volatile("s_load_dwordx2 s[40:41], %[ka], 0x10\n\ts_load_dwordx2 s[44:45], %[ka], 0x8\n\ts_load_dwordx4 s[60:63], %[ka], 0x30\n\t"
                 "s_load_dwordx2 s[48:49], %[ka], 0x28\n\ts_load_dwordx2 s[52:53], %[ka], 0x0\n\ts_load_dwordx4 s[56:59], %[ka], 0x18\n\t"
                 "s_memrealtime s[64:65]\n\t"
                 "s_mov_b32 s43, 0x20000\n\ts_mov_b32 s47, 0x20000\n\ts_mov_b32 s51, 0x20000\n\ts_mov_b32 s55, 0x20000\n\ts_mov_b32 s50, %c[wsb]"
                 : "=&{s[40:43]}"(srd_d), "=&{s[44:47]}"(srd_s), "=&{s[48:51]}"(srd_ws), "=&{s[52:55]}"(srd_x0), "=&{s[56:59]}"(pw), "=&{s[60:63]}"(po), "=&{s[64:65]}"(t_start)
                 : [ka] "s"(__builtin_amdgcn_kernarg_segment_ptr()), [wsb] "i"(WS_BYTES));
    FENCE();
    __shared__ __attribute__((aligned(16))) char smem_[L_TOTAL];
    LAS char* smem = (LAS char*)smem_;
    const int t = threadIdx.x, l = t & 63, b = blockIdx.x;
    const int w = rfl(t >> 6);
    const int g = l >> 4, n = l & 15, q = (l >> 2) & 3, p = l & 3, pc = t & 7;
    const unsigned lds0 = (unsigned)(size_t)smem;
    const bool isprod = ((b >> 3) & 1) == 0;
    const int pg = ((b & 7) << 4) | (b >> 4);
    const unsigned e0 = pg * CH + 4 * t, e1 = pg * CH + 3072 + t;

    u32x16 ed, es;
    {
        const unsigned o = e0 * 4, o1 = w == 0 ? e1 * 4 : OOB, ok = WS_KEY + t * 4, om = WS_KEY + b * 4;
        const unsigned oc = w < 4 ? (t < 196 ? ((b - 8) << 8) + ((t >> 1) << 16) + ((t & 1) << 7) : OOB) : (t - 256) * 128, dump = lds0 + L_DUMP + w * 256;
        int role = (w < 4 ? 0 : 2) + (isprod ? 0 : 1);
        unsigned k4 = 4096, k8 = 8192;
        asm volatile("s_cmp_eq_u32 %[role], 0\n\ts_cbranch_scc1 1f\n\t"
                     "s_mov_b32 m0, %[dump]\n\ts_cmp_eq_u32 %[role], 1\n\ts_cbranch_scc1 2f\n\t"
                     "s_getpc_b64 s[68:69]\n\ts_mov_b32 s70, " STR(CODE_BYTES) "\n\ts_mov_b32 s71, 0x20000\n\t"
                     ".if " STR(CODEPF) "\n\tbuffer_load_dword %[oc], s[68:71], 0 offen lds\n\t.endif\n\t"
                     "s_waitcnt lgkmcnt(0)\n\ts_branch 3f\n\t"
                     "2:\n\ts_waitcnt lgkmcnt(0)\n\ts_lshl_b32 s54, s62, 8\n\t"
                     ".if " STR(PARTNER) "\n\tbuffer_load_dword %[oc], s[52:55], 0 offen lds\n\t.endif\n\ts_branch 3f\n\t"
                     "1:\n\ts_waitcnt lgkmcnt(0)\n\ts_lshl_b32 s42, s63, 2\n\ts_lshl_b32 s46, s63, 2\n\t"
                     "buffer_load_dwordx4 v[64:67], %[o], s[40:43], 0 offen\n\tbuffer_load_dwordx4 v[68:71], %[o], s[40:43], %[k4] offen\n\t"
                     "buffer_load_dwordx4 v[72:75], %[o], s[40:43], %[k8] offen\n\tbuffer_load_dword v76, %[o1], s[40:43], 0 offen\n\t"
                     "buffer_load_dwordx4 v[80:83], %[o], s[44:47], 0 offen\n\tbuffer_load_dwordx4 v[84:87], %[o], s[44:47], %[k4] offen\n\t"
                     "buffer_load_dwordx4 v[88:91], %[o], s[44:47], %[k8] offen\n\tbuffer_load_dword v92, %[o1], s[44:47], 0 offen\n\t"
                     "buffer_load_dword v93, %[ok], s[48:51], 0 offen sc1\n\tbuffer_load_dword v94, %[om], s[48:51], 0 offen sc1\n\t"
                     "3:\n\ts_lshl_b32 s54, s62, 8"
                     : "=&{v[64:79]}"(ed), "=&{v[80:95]}"(es)
                     : [o] "v"(o), [o1] "v"(o1), [ok] "v"(ok), [om] "v"(om), [oc] "v"(oc), [role] "s"(role), [dump] "s"(dump), [k4] "s"(k4), [k8] "s"(k8),
                       "{s[40:43]}"(srd_d), "{s[44:47]}"(srd_s), "{s[48:51]}"(srd_ws), "{s[52:55]}"(srd_x0), "{s[56:59]}"(pw), "{s[60:63]}"(po), "{s[64:65]}"(t_start)
                     : "memory", "scc", "s68", "s69", "s70", "s71");
        asm volatile("" : "+{s[48:51]}"(srd_ws), "+{s[52:55]}"(srd_x0), "+{s[56:59]}"(pw), "+{s[60:63]}"(po), "+{s[64:65]}"(t_start));
    }
    const void* const x = (const void*)((u64)srd_x0.x | ((u64)srd_x0.y << 32));
    const void* const wq = (const void*)((u64)pw.x | ((u64)pw.y << 32));
    const void* const wv = (const void*)((u64)pw.z | ((u64)pw.w << 32));
    void* const out = (void*)((u64)po.x | ((u64)po.y << 32));
    const int N = po.z, E = po.w;
    const unsigned xbytes = srd_x0.z;
    STAMP_DECL;
    const unsigned ring = lds0 + L_RING + w * 16384;
    const int jn = 8 * n + w;

    unsigned mykey = 0;
    auto node_row = [&](int tile, int m) { const int nd = b + 256 * (8 * (4 * m + g) + tile); return nd < N ? (unsigned)nd << 8 : OOB; };
    auto zero_staging = [&]() {
        const u32x4 z = {0u, 0u, 0u, 0u};
#pragma unroll
        for (int k = 0; k < 5; ++k) *(LAS u32x4*)(smem + L_IMG + k * 8192 + t * 16) = z;
    };

    u32x16 addr;
    f32x32 acc, wva, wvb;
    unsigned cv, lista, cnta, qa, keyn, inf;
    Srd4 srd_x;
    auto loop_constants = [&]() {
        srd_x = make_srd4(x, xbytes, BIAS * 256);
        lista = lds0 + L_LIST + w * (2 * SUBB) + g * 4; cnta = lds0 + L_CNT + (l & 31) * 4; qa = ring + l * 16; keyn = jn; inf = 0x7F800000u;
        asm volatile("" : "+s"(srd_x.s0), "+s"(srd_x.s1), "+s"(srd_x.s2), "+s"(srd_x.s3));
        asm volatile("" : "+v"(lista), "+v"(cnta), "+v"(qa), "+v"(keyn), "+v"(inf));
        const unsigned o0 = (n ^ (((g & 1) << 3) | (g >> 1))) << 4;
        const unsigned ra = (4 * (n & 3) + (n >> 2)) * 256 + (((4 * g) ^ (((n & 7) << 1) | (n >> 3))) << 4);
        const int i = 4 * g + q, r8 = i & 7;
        const unsigned ta = (4 * q + g) * 256 + 8 * (p & 1) + (((p >> 1) ^ (((i & 7) << 1) | (i >> 3))) << 4);
        cv = i * 256 + (((p ^ ((r8 & 1) | ((r8 & 6) << 1))) << 4) | ((p & 1) << 3));
#pragma unroll
        for (int s = 0; s < 4; ++s) { addr[s] = ring + (ra ^ (s << 4)); addr[12 + s] = o0 ^ (s << 5); }
#pragma unroll
        for (int c = 0; c < 8; ++c) addr[4 + c] = ring + (ta ^ (c << 5));
        asm volatile("" : "+{v[176:191]}"(addr), "+v"(cv));
    };
    auto clear_sums = [&]() { acc = 0.f; asm volatile("" : "+{v[128:159]}"(acc)); };
    auto wv_resident = [&]() {
#pragma unroll
        for (int a = 0; a < 8; ++a) {
            LAS char* pa = smem + L_WV0 + (cv ^ ((a >> 1) << 6) ^ ((a & 1) << 3));
            const f32x4 f0 = __builtin_bit_cast(f32x4, cat(tr_read(pa), tr_read(pa + 4096)));
            const f32x4 f1 = __builtin_bit_cast(f32x4, cat(tr_read(pa + 16384), tr_read(pa + 16384 + 4096)));
#pragma unroll
            for (int k = 0; k < 4; ++k) { wva[4 * a + k] = f0[k]; wvb[4 * a + k] = f1[k]; }
        }
        asm volatile("" : "+{v[224:255]}"(wva), "+{v[32:63]}"(wvb));
    };

    if (isprod) zero_staging();
    if (t < 8) *(LAS u32x4*)(smem + L_CNT + t * 16) = u32x4{0u, 0u, 0u, 0u};
    WG_BARRIER();

    if (__builtin_expect(w < 4, 1)) {
        if (__builtin_expect(isprod, 1)) {
            if (PRIO) __builtin_amdgcn_s_setprio(2);
            unsigned ob = WS_INBOX + ((t >> 3) * G1 + pg) * 128 + pc * 16, ima = lds0 + L_IMG + (t >> 3) * 144 + pc * 16;
            PIN(ob); PIN(ima);
            unsigned cna = lds0 + L_CNT1 + (t >> 3) * 4;
            PIN(cna);
            const bool v0 = e0 < (unsigned)E, v1 = e0 + 1024 < (unsigned)E, v2 = e0 + 2048 < (unsigned)E, v3 = w == 0 && e1 < (unsigned)E;
            STAMP(1);
            u32x16 rk;
            const u64 over = rank_edges(ed, es, rk, __builtin_amdgcn_ballot_w64(v0), __builtin_amdgcn_ballot_w64(v1), __builtin_amdgcn_ballot_w64(v2),
                                        __builtin_amdgcn_ballot_w64(v3), t * 4);
            mykey = es[14];
            if (__builtin_expect(over != 0, 0)) {
#pragma unroll
                for (int k = 0; k < 13; ++k)
                    if (rk[k] >= 4u * CAP1) {
                        const unsigned o = WS_OVF + (pg * CH + lds_add(smem + L_CNT1 + 2048, 1)) * 4;
                        const unsigned v = ((((es[k] >> 8) & 0xFFFF) - BIAS) << 16) | ed[k];
                        ST1(v, srd_ws, o, " sc1");
                    }
            }
            if (XSYNC) { lds_arrive(smem + L_CNT + 96, l); lds_await(smem + L_CNT + 96, 4); } else WG_BARRIER();
            STAMP(3);
            f32x32 img; u32x16 meta, sums;
            const unsigned no = read_image(img, meta, sums, ima, cna);
            lds_arrive(smem + L_CNT + 84, l);
            if (__builtin_expect(no != 0, 0)) {
                const unsigned o = t == 0 ? WS_NOVF + pg * 4 : OOB;
                ST1(no, srd_ws, o, " sc1");
                VMWAIT(0);
                if (XSYNC) { lds_arrive(smem + L_CNT + 100, l); lds_await(smem + L_CNT + 100, 4); } else WG_BARRIER();
            }
            publish_image(img, meta, sums, srd_ws, ob);
            STAMP(4);
        } else {
            { const unsigned om = WS_KEY + b * 4; asm volatile("buffer_load_dword %0, %1, %2, 0 offen sc1\n\ts_waitcnt vmcnt(0)" : "=&v"(mykey) : "v"(om), "s"(srd_ws) : "memory"); }
            STAMP(1);
            if (PRIO) __builtin_amdgcn_s_setprio(2);
            STAMP(2); STAMP(3); STAMP(4);
        }
        STAMP(5);
        mykey = rfl(mykey) + KEY_STEP;
        loop_constants();
        clear_sums();
        unsigned ib = WS_INBOX + b * (G1 * 128) + t * 16;
        PIN(ib);
        while (__builtin_amdgcn_s_memrealtime() < t_start + POLL_T) __builtin_amdgcn_s_sleep(1);
        STAMP(6);
        u32x16 raw;
        inbox_request(raw, srd_ws, ib);
        if (__builtin_expect(inbox_stage(raw, srd_ws, ib, mykey, 16 * pc - 8, lds0 + L_HDR + (t >> 3) * 4) != 0, 0)) {
            LAS unsigned* hp = (LAS unsigned*)(smem + L_HDR) + 32 * (l >> 3 & 3) + 8 * w + (l & 7);
            const unsigned hv = *hp;
            for (u64 m = __builtin_amdgcn_ballot_w64(l < 32 && hv > 4u * CAP1); m; m &= m - 1) {
                const int k = __builtin_ctzll(m), gp = 32 * (k >> 3) + 8 * w + (k & 7);
                unsigned u, no; const unsigned o = WS_OVF + (gp * CH + l) * 4, on = WS_NOVF + gp * 4;
                asm volatile("buffer_load_dword %0, %2, %4, 0 offen sc1\n\tbuffer_load_dword %1, %3, %4, 0 offen sc1\n\ts_waitcnt vmcnt(0)"
                             : "=&v"(u), "=&v"(no) : "v"(o), "v"(on), "s"(srd_ws) : "memory");
                if (rfl(no) <= 64) {
                    if (l < no && (u & 0xFF) == (unsigned)b) {
                        const unsigned e = __builtin_amdgcn_perm((u >> 16) + BIAS, u, 0x01050401u), key = (e >> 21) & 60;
                        *(LAS unsigned*)(smem + L_LIST + key * (SUBB / 4) + min(lds_add(smem + L_CNT + key, 4), SUBB - 4u)) = e;
                    }
                    if (l == k) *hp = hv | 0x80000000u;
                } else *(LAS unsigned*)(smem + L_CNT + 64) = 1;
            }
        }
        if (PRIO) __builtin_amdgcn_s_setprio(0);
        STAMP(7);
    } else {
        const int j = w - 4;
        STAMP(1);
        u32x4 wr[8], xr[8];
        {
            const u32x4 srd_q = make_srd(wq, 32768);
            const unsigned o = j * 8192 + l * 16, k4 = 4096;
            LD4(wr[0], srd_q, o, ""); LD4(wr[1], srd_q, o, " offset:1024"); LD4(wr[2], srd_q, o, " offset:2048"); LD4(wr[3], srd_q, o, " offset:3072");
            LD4S(wr[4], srd_q, o, k4, ""); LD4S(wr[5], srd_q, o, k4, " offset:1024"); LD4S(wr[6], srd_q, o, k4, " offset:2048"); LD4S(wr[7], srd_q, o, k4, " offset:3072");
#pragma unroll
            for (int m = 0; m < 8; ++m) { const unsigned ox = node_row(m < 4 ? j : w, m & 3) + n * 16; LD4(xr[m], srd_x0, ox, ""); }
        }
        unsigned aqa[8], xfa[4];
        unsigned xa = ring + g * 256 + ((n ^ g) << 4), wa = lds0 + L_WQ + j * 16384 + g * 256 + ((n ^ g) << 4) + ((n & 4) << 1);
        {
            const unsigned aq = lds0 + L_WQ + (8 * g + q) * 256 + ((((4 * p) ^ (q | ((g & 1) << 2))) << 4) | ((p & 1) << 3));
#pragma unroll
            for (int s = 0; s < 4; ++s) { xfa[s] = ring + n * 256 + (((4 * s + g) ^ n) << 4); PIN(xfa[s]); }
#pragma unroll
            for (int c = 0; c < 8; ++c) { aqa[c] = aq ^ ((c >> 1) << 4) ^ ((c & 1) << 3); PIN(aqa[c]); }
            PIN(xa); PIN(wa);
        }
        loop_constants();
        STAMP(2);
        if (isprod && !XSYNC) {
            WG_BARRIER();
            if (__builtin_expect(rfl(*(LAS unsigned*)(smem + L_CNT1 + 2048)) != 0, 0)) WG_BARRIER();
            if (W_SLEEP) __builtin_amdgcn_s_sleep(W_SLEEP);
        }
        STAMP(3);
        asm volatile("s_waitcnt vmcnt(%c8)" : "+v"(wr[0]), "+v"(wr[1]), "+v"(wr[2]), "+v"(wr[3]), "+v"(wr[4]), "+v"(wr[5]), "+v"(wr[6]), "+v"(wr[7]) : "i"(8) : "memory");
#pragma unroll
        for (int m = 0; m < 8; ++m) {
            const unsigned a = (wa ^ ((m & 2) << 5)) + m * 1024;
            *(LAS u32x2*)(size_t)a = u32x2{wr[m].x, wr[m].y};
            *(LAS u32x2*)(size_t)(a ^ 8) = u32x2{wr[m].z, wr[m].w};
        }
        asm volatile("s_waitcnt vmcnt(0)" : "+v"(xr[0]), "+v"(xr[1]), "+v"(xr[2]), "+v"(xr[3]), "+v"(xr[4]), "+v"(xr[5]), "+v"(xr[6]), "+v"(xr[7]) :: "memory");
#pragma unroll
        for (int m = 0; m < 8; ++m) *(LAS u32x4*)(size_t)((xa ^ ((m & 3) << 6)) + m * 1024) = xr[m];
        STAMP(4);
        lds_arrive(smem + L_CNT + 80, l);
        lds_await(smem + L_CNT + 80, 4);
        STAMP(5);
        bf16x8 xo[2][4], fr[2][8];
        f32x4 qc[2][8];
        auto wq_frags = [&](int u, bf16x8 (&f)[8]) {
#pragma unroll
            for (int a = 0; a < 8; ++a) {
                LAS char* pa = (LAS char*)(size_t)aqa[a] + u * 16384;
                f[a] = cat(tr_read(pa), tr_read(pa + 1024));
            }
        };
#pragma unroll
        for (int u = 0; u < 4; ++u) { xo[0][u] = *(LAS bf16x8*)(size_t)xfa[u]; xo[1][u] = *(LAS bf16x8*)(size_t)(xfa[u] + 4096); }
        wq_frags(0, fr[0]);
        {
            const Srd4 srd_v = make_srd4(wv, 32768);
            const unsigned r0 = (j * 32 + g) << 8, o0 = (n ^ ((g & 1) | ((g & 2) << 1))) << 4;
            const unsigned e[8] = {r0, r0 + 1024, r0 + 2048, r0 + 3072, r0 + 4096, r0 + 5120, r0 + 6144, r0 + 7168};
            const unsigned base = lds0 + (j < 2 ? L_WV0 + j * 16384 : L_WV1 + (j - 2) * 8192);
            const u32x4 off = {o0, o0 ^ 128, o0, o0 ^ 128};
            dma_half(base, srd_v, e, off); dma_half(base + 4096, srd_v, e + 4, off);
        }
#pragma unroll
        for (int u = 0; u < 4; ++u) {
            if (u < 3) wq_frags(u + 1, fr[(u + 1) & 1]);
            FENCE();
#pragma unroll
            for (int a = 0; a < 8; ++a)
#pragma unroll
                for (int tt = 0; tt < 2; ++tt) qc[tt][a] = mfma(fr[u & 1][a], xo[tt][u], u ? qc[tt][a] : f32x4{0.f, 0.f, 0.f, 0.f});
            FENCE();
            if (!WVLATE && u == 1 && j < 2) { VMWAIT(0); lds_arrive(smem + L_CNT + 88, l); }
        }
        STAMP(6);
        if (isprod) lds_await(smem + L_CNT + 84, 4);
#pragma unroll
        for (int tt = 0; tt < 2; ++tt)
#pragma unroll
            for (int s = 0; s < 4; ++s) {
                const float k = -0.08838834764831845f * 1.4426950408889634f;
                const f32x4 c0 = qc[tt][2 * s] * k, c1 = qc[tt][2 * s + 1] * k;
                *(LAS u32x4*)(size_t)(ring - (tt ? 0 : 65536) + s * 1024 + l * 16) = u32x4{pk2(c0[0], c0[1]), pk2(c0[2], c0[3]), pk2(c1[0], c1[1]), pk2(c1[2], c1[3])};
            }
        clear_sums();
        if (!WVLATE) { lds_await(smem + L_CNT + 88, 2); wv_resident(); }
        VMWAIT(0);
        STAMP(7);
    }
    WG_BARRIER();
    STAMP(8);
    if (LPRIO >= 3) __builtin_amdgcn_s_setprio(3);
    if (LPRIO == 1 && w >= 4) __builtin_amdgcn_s_setprio(1);
    if (LPRIO == 2) { if (w >= 6) __builtin_amdgcn_s_setprio(3); else if (w >= 4) __builtin_amdgcn_s_setprio(2); else if (w >= 2) __builtin_amdgcn_s_setprio(1); }

    u32x16 qf;
    const u32x4 doff = {addr[12], addr[13], addr[14], addr[15]};
    LAS char* mylist = smem + L_LIST + w * (2 * SUBB);
    int gcur = G1, pos = 0;
    unsigned hmask = 0xFFFFFFFFu;
    auto batch = [&](auto first_) {
        constexpr bool first = decltype(first_)::value;
        auto entries = [&](int grp, unsigned (&e)[8]) {
#pragma unroll
            for (int m = 0; m < 8; ++m) e[m] = *(LAS unsigned*)(size_t)(lista + grp * 128 + m * 16);
        };
        unsigned cw = *(LAS unsigned*)(size_t)(first ? cnta : lds0 + L_CNT + (__builtin_amdgcn_mbcnt_lo(~0u, 0u) & 31) * 4), e0[8], e1[8];
        entries(0, e0);
        if constexpr (first) {
#pragma unroll
            for (int s = 0; s < 4; ++s) {
                const u32x4 v = *(LAS u32x4*)(size_t)(qa + s * 1024);
                qf[4 * s] = v.x; qf[4 * s + 1] = v.y; qf[4 * s + 2] = v.z; qf[4 * s + 3] = v.w;
            }
        }
        entries(1, e1);
        FENCE();
        int lo = __builtin_amdgcn_readlane(cw, 2 * w) >> 2, hi = __builtin_amdgcn_readlane(cw, 2 * w + 1) >> 2;
        if constexpr (first) {
            if (__builtin_amdgcn_readlane(cw, 16)) gcur = 0;
            if (__builtin_amdgcn_ballot_w64(cw > 4u * CAP2)) { gcur = -1; lo = 0; hi = 0; hmask = 0x7FFFFFFFu; }
        }
        const int c = lo + hi, G = (c + 31) >> 5;
        const int nlast = G > 2 ? (((c - 1) & 31) >> 2) + 1 : 8;
        auto join = [&]() {
            for (int k = 0; k < G * 32 - lo; k += 64) {
                const unsigned e = *(LAS unsigned*)(mylist + SUBB + (l + k) * 4);
                if (l + k < G * 32 - lo) *(LAS unsigned*)(mylist + (lo + l + k) * 4) = l + k < hi ? e : PAD;
            }
        };
        const bool early = lo >= 64;
        if (__builtin_expect(!early, 0)) { join(); entries(0, e0); entries(1, e1); }
        if constexpr (first) asm volatile("s_waitcnt lgkmcnt(0)" : "+{v[160:175]}"(qf) :: "memory");
        if (G > 0) dma_group(ring, srd_x, e0, doff);
        if constexpr (first) {
            if (WVLATE) {
                if (G > 1 && w < 6) dma_group(ring + 8192, srd_x, e1, doff);
                wv_resident();
                lds_arrive(smem + L_CNT + 92, l);
                if (w >= 6) { lds_await(smem + L_CNT + 92, 8); if (G > 1) dma_group(ring + 8192, srd_x, e1, doff); }
            } else if (w < 4) {
                if (G > 1) dma_group(ring + 8192, srd_x, e1, doff);
                wv_resident();
                lds_arrive(smem + L_CNT + 92, l);
            } else {
                if (w >= 6) lds_await(smem + L_CNT + 92, 4);
                if (G > 1) dma_group(ring + 8192, srd_x, e1, doff);
            }
        } else if (G > 1) dma_group(ring + 8192, srd_x, e1, doff);
        f32x16 cc;
#pragma unroll
        for (int m = 0; m < 8; ++m) {
            cc[m] = (e0[m] & 0xFF) == keyn ? 0.f : __uint_as_float(inf);
            cc[8 + m] = (e1[m] & 0xFF) == keyn ? 0.f : __uint_as_float(inf);
        }
        if (early) join();
        STAMP(9);
        if (G > 0) gather_loop(acc, qf, addr, u32x4{lista, keyn, inf, 0u}, cc, srd_x, ring, G, nlast);
    };
    batch(std::true_type{});
    while (__builtin_expect(gcur < G1, 0)) {
        unsigned lim = G1 * 32;
        if (gcur >= 0) {
            const u64 m0 = __builtin_amdgcn_ballot_w64(l >= gcur && (int)(*(LAS unsigned*)(smem + L_HDR + l * 4) & hmask) > 4 * CAP1);
            const u64 m1 = __builtin_amdgcn_ballot_w64(l + 64 >= gcur && (int)(*(LAS unsigned*)(smem + L_HDR + 256 + l * 4) & hmask) > 4 * CAP1);
            if (!(m0 | m1)) break;
            gcur = m0 ? __builtin_ctzll(m0) : 64 + __builtin_ctzll(m1);
            unsigned v; const unsigned o = WS_NOVF + gcur * 4;
            asm volatile("buffer_load_dword %0, %1, %2, 0 offen sc1\n\ts_waitcnt vmcnt(0)" : "=&v"(v) : "v"(o), "s"(srd_ws) : "memory");
            lim = rfl(v);
        }
        const unsigned i = pos + t;
        const bool cand = t < CAP2 && i < lim;
        unsigned u;
        { const unsigned o = cand ? (gcur < 0 ? WS_INBOX + b * (G1 * 128) : WS_OVF + gcur * CH * 4) + i * 4 : OOB;
          asm volatile("buffer_load_dword %0, %1, %2, 0 offen sc1\n\ts_waitcnt vmcnt(0)" : "=&v"(u) : "v"(o), "s"(srd_ws) : "memory"); }
        WG_BARRIER();
        if (t < 16) *(LAS unsigned*)(smem + L_CNT + t * 4) = 0;
        WG_BARRIER();
        if (cand) {
            unsigned e; bool v;
            if (gcur < 0) { e = (i & 3) ? u : u ^ mykey; v = ((i & 31) - 2) * 4 < *(LAS unsigned*)(smem + L_HDR + (i >> 5) * 4); }
            else { e = (((u >> 16) + BIAS) << 8) | ((u >> 8) & 0xFF); v = (u & 0xFF) == (unsigned)b; }
            if (v) *(LAS unsigned*)(smem + L_LIST + (e & 7) * (2 * SUBB) + lds_add(smem + L_CNT + (e & 7) * 8, 4)) = e;
        }
        pos += CAP2;
        if ((unsigned)pos >= lim) { ++gcur; pos = 0; }
        WG_BARRIER();
        batch(std::false_type{});
    }
    STAMP(10);

    {
        bf16x8 wl[2][8], ag[4];
#pragma unroll
        for (int u = 0; u < 2; ++u)
#pragma unroll
            for (int a = 0; a < 8; ++a) {
                LAS char* pa = smem + L_WV1 + (cv ^ ((a >> 1) << 6) ^ ((a & 1) << 3)) + u * 8192;
                wl[u][a] = cat(tr_read(pa), tr_read(pa + 4096));
            }
#pragma unroll
        for (int u = 0; u < 4; ++u) {
            const u32x4 v = {pk2(acc[8 * u], acc[8 * u + 1]), pk2(acc[8 * u + 2], acc[8 * u + 3]),
                             pk2(acc[8 * u + 4], acc[8 * u + 5]), pk2(acc[8 * u + 6], acc[8 * u + 7])};
            ag[u] = __builtin_bit_cast(bf16x8, v);
        }
        FENCE();
        f32x4 oc[8];
#pragma unroll
        for (int a = 0; a < 8; ++a) oc[a] = mfma(__builtin_bit_cast(bf16x8, f32x4{wva[4 * a], wva[4 * a + 1], wva[4 * a + 2], wva[4 * a + 3]}), ag[0], f32x4{0.f, 0.f, 0.f, 0.f});
#pragma unroll
        for (int a = 0; a < 8; ++a) oc[a] = mfma(__builtin_bit_cast(bf16x8, f32x4{wvb[4 * a], wvb[4 * a + 1], wvb[4 * a + 2], wvb[4 * a + 3]}), ag[1], oc[a]);
#pragma unroll
        for (int a = 0; a < 8; ++a) oc[a] = mfma(wl[0][a], ag[2], oc[a]);
        FENCE();
        const unsigned ow = ring + n * 256 + ((g ^ n) << 4) + ((g & 1) << 3);
#pragma unroll
        for (int a = 0; a < 8; ++a) oc[a] = mfma(wl[1][a], ag[3], oc[a]);
#pragma unroll
        for (int a = 0; a < 8; ++a)
            *(LAS u32x2*)(size_t)(ow ^ ((a >> 1) << 6) ^ ((a & 1) << 3)) = u32x2{pk2(oc[a][0], oc[a][1]), pk2(oc[a][2], oc[a][3])};
        const auto rs_o = __builtin_amdgcn_make_buffer_rsrc(out, 0, xbytes, 0x00020000);
#pragma unroll
        for (int m = 0; m < 4; ++m) {
            const u32x4 v = *(LAS u32x4*)(size_t)(ring + m * 1024 + l * 16);
            __builtin_amdgcn_raw_buffer_store_b128(v, rs_o, node_row(w, m) + ((n ^ g ^ (4 * m)) << 4), 0, OUT_AUX);
        }
    }
    if (w == 0) { const unsigned o = l == 0 ? WS_KEY + b * 4 : OOB; ST1(mykey, srd_ws, o, " sc1"); }
    STAMP(11);
}

extern "C" void kernel_launch(void* const* d_in, const int* in_sizes, int n_in,
                              void* d_out, int out_size, void* d_ws, size_t ws_size,
                              hipStream_t stream) {
    const int N = in_sizes[0] / 128, E = in_sizes[1];
    k_graph<<<NB, 512, 0, stream>>>((const unsigned short*)d_in[0], (const int*)d_in[1], (const int*)d_in[2],
                                    (const unsigned short*)d_in[3], (const unsigned short*)d_in[4],
                                    (unsigned*)d_ws, (unsigned short*)d_out, N, E);
}
```

```cpp
#define STAMPS
#define PROBE_A 0
#define PROBE_B 13
#define PROBE_M 0
#include <hip/hip_runtime.h>
#include <hip/hip_bf16.h>
#include <stdint.h>
#include <type_traits>
#define ORD 1
#define SKIP 0
#define STR2(x) #x
#define STR(x) STR2(x)

typedef __attribute__((ext_vector_type(8))) __bf16 bf16x8;
typedef __attribute__((ext_vector_type(4))) __bf16 bf16x4;
typedef __attribute__((ext_vector_type(2))) __bf16 bf16x2;
typedef __attribute__((ext_vector_type(4))) float f32x4;
typedef __attribute__((ext_vector_type(2))) float f32x2;
typedef __attribute__((ext_vector_type(4))) unsigned u32x4;
typedef __attribute__((ext_vector_type(2))) unsigned u32x2;
typedef __attribute__((ext_vector_type(32))) float f32x32;
typedef __attribute__((ext_vector_type(16))) float f32x16;
typedef __attribute__((ext_vector_type(16))) unsigned u32x16;
typedef __attribute__((ext_vector_type(12))) unsigned u32x12;
typedef __attribute__((ext_vector_type(8))) unsigned u32x8;
typedef unsigned long long u64;
#define LAS __attribute__((address_space(3)))

constexpr int NB = 256;
constexpr int G1 = 128;
constexpr int CH = 3136;
constexpr int CAP1 = 30;
constexpr int CAP2 = 192;
constexpr int SUBB = 784;
#define POLL_AT 3
#define OUT_AUX 16
#define POLL_T 350
#define LPRIO 3
#define PKADD 1
#define CODEPF 1
#define CODE_BYTES 21504
#define PARTNER 1
#define XSYNC 1
#define WVLATE 1
#define FASTIN 1
#define W_SLEEP 3
#define PRIO 1
#define SPLIT 16384
constexpr unsigned KEY_STEP = 0x9E3779B1u;
constexpr unsigned WS_KEY = 0;
constexpr unsigned WS_NOVF = 2048;
constexpr unsigned WS_INBOX = 4096;
constexpr unsigned WS_OVF = WS_INBOX + NB * G1 * 128;
constexpr unsigned WS_BYTES = WS_OVF + G1 * CH * 4;
constexpr unsigned OOB = 0x80000000u;
constexpr unsigned BIAS = 32768 - SPLIT;
constexpr unsigned PAD = 0x00FFFFFFu;

constexpr int L_CNT = 0;
constexpr int L_HDR = 128;
constexpr int L_LIST = 768;
constexpr int L_RING = L_LIST + 16 * SUBB;
constexpr int L_IMG = L_RING;
constexpr int L_CNT1 = L_IMG + NB * 144;
constexpr int L_WQ = L_RING + 2 * 16384 + 8192;
constexpr int L_WV0 = L_RING + 6 * 16384 + 8192;
constexpr int L_WV1 = L_RING + 131072;
constexpr int L_DUMP = L_WV1 + 16384;
constexpr int L_TOTAL = L_DUMP + 2048;
static_assert(L_RING % 256 == 0 && L_TOTAL <= 163840, "LDS map");

#define STAMP_N 24
#define STAMP_WGS 256
#define STAMP_OFF (200u << 20)
#define STAMP_ROLE 0
#define STAMP_VALID(w, k) ((k) < 14 && ((STAMP_MASK >> (k)) & 1) && ((k) == 0 || STAMP_ROLE == 2 || ((((w) >> 3) & 1) == STAMP_ROLE)))
#define STAMP_DECL unsigned long long st_[STAMP_N] = {}
#define STAMP_MASK 0xFFFFFF
#define STAMP(k) do { if (!((STAMP_MASK >> (k)) & 1)) break; __builtin_amdgcn_sched_barrier(0); asm volatile("s_memrealtime %0\n\ts_waitcnt lgkmcnt(0)" : "=s"(st_[k]) :: "memory"); __builtin_amdgcn_sched_barrier(0); } while (0)
#define STAMP_THREAD 0
#define STAMP_FLUSH(base, k0, k1) do { if (threadIdx.x == STAMP_THREAD) { _Pragma("unroll") for (int k_ = k0; k_ < k1; ++k_) ((unsigned long long*)((char*)(base) + STAMP_OFF))[(size_t)blockIdx.x * STAMP_N + k_] = st_[k_]; } } while (0)

__device__ __forceinline__ u32x4 make_srd(const void* p, unsigned bytes, unsigned back = 0) {
    const u64 a = (u64)p - back;
    u32x4 s; s.x = (unsigned)a; s.y = (unsigned)(a >> 32); s.z = bytes + back; s.w = 0x00020000u; return s;
}
#define LD4(dst, srd, off, FL) asm volatile("buffer_load_dwordx4 %0, %1, %2, 0 offen" FL : "=v"(dst) : "v"(off), "s"(srd) : "memory")
#define LD4S(dst, srd, off, so, FL) asm volatile("buffer_load_dwordx4 %0, %1, %2, %3 offen" FL : "=v"(dst) : "v"(off), "s"(srd), "s"(so) : "memory")
#define LD1(dst, srd, off, FL) asm volatile("buffer_load_dword %0, %1, %2, 0 offen" FL : "=v"(dst) : "v"(off), "s"(srd) : "memory")
#define ST1(val, srd, off, FL) asm volatile("buffer_store_dword %0, %1, %2, 0 offen" FL :: "v"(val), "v"(off), "s"(srd) : "memory")
#define PIN(x) asm volatile("" : "+v"(x))
#define VMWAIT(n) asm volatile("s_waitcnt vmcnt(" #n ")" ::: "memory")
#define FENCE() __builtin_amdgcn_sched_barrier(0)
#define WG_BARRIER() do { asm volatile("s_waitcnt lgkmcnt(0)" ::: "memory"); __builtin_amdgcn_s_barrier(); asm volatile("" ::: "memory"); } while (0)

struct Srd4 { u32x4 s0, s1, s2, s3; };
__device__ __forceinline__ Srd4 make_srd4(const void* p, unsigned bytes, unsigned back = 0) {
    return Srd4{make_srd(p, bytes, back), make_srd(p, bytes, back + 1024), make_srd(p, bytes, back + 2048), make_srd(p, bytes, back + 3072)};
}

__device__ __forceinline__ void set_m0(unsigned v) { asm volatile("s_mov_b32 m0, %0" :: "s"(v) : "memory"); }
template <int R> __device__ __forceinline__ void dma_rows(const Srd4& d, unsigned e, unsigned off) {
    unsigned tmp, mask = 0x00FFFF00u;
    asm volatile("v_and_or_b32 %0, %1, %2, %3\n\tbuffer_load_dwordx4 %0, %4, 0 offen offset:%c5 lds"
                 : "=&v"(tmp) : "v"(e), "s"(mask), "v"(off), "s"(R == 0 ? d.s0 : R == 1 ? d.s1 : R == 2 ? d.s2 : d.s3), "i"(R * 1024) : "memory");
}
__device__ __forceinline__ void dma_half(unsigned ldsbase, const Srd4& d, const unsigned* e, const u32x4& off) {
    set_m0(ldsbase);
    dma_rows<0>(d, e[0], off.x); dma_rows<1>(d, e[1], off.y); dma_rows<2>(d, e[2], off.z); dma_rows<3>(d, e[3], off.w);
}
__device__ __forceinline__ void dma_group(unsigned ldsbase, const Srd4& d, const unsigned (&e)[8], const u32x4& off) {
    asm volatile("s_waitcnt lgkmcnt(0)" ::: "memory");
    dma_half(ldsbase, d, e, off); dma_half(ldsbase + 4096, d, e + 4, off);
}

__device__ __forceinline__ void gather_loop(f32x32& acc, const u32x16& qf, const u32x16& addr, u32x4 misc, f32x16 cc,
                                            const Srd4& d, unsigned ring, int G, int nlast) {
    f32x32 t0, t1; u32x12 t2; int tmp, pr; u64 sm; unsigned mask = 0x00FFFF00u;
    asm volatile(
        ".macro GWAIT k\n\ts_cmp_gt_i32 %[t], \\k\n\ts_cbranch_scc1 1f\n\ts_waitcnt vmcnt(\\k)\n\t.endm\n\t"
        ".macro GROWS so\n\t.irp s, 0, 1, 2, 3\n\t"
        "ds_read_b128 v[64+8*\\s:67+8*\\s], v[176+\\s] offset:\\so\n\t"
        "ds_read_b128 v[68+8*\\s:71+8*\\s], v[176+\\s] offset:\\so+4096\n\t.endr\n\t.endm\n\t"
        ".macro GTR c, so\n\t"
        "ds_read_b64_tr_b16 v[96+4*\\c:97+4*\\c], v[180+\\c] offset:\\so\n\t"
        "ds_read_b64_tr_b16 v[98+4*\\c:99+4*\\c], v[180+\\c] offset:\\so+4096\n\t.endm\n\t"
        ".macro GS s, c\n\t"
        "v_mfma_f32_16x16x32_bf16 v[\\c:\\c+3], v[64+8*\\s:67+8*\\s], v[160+4*\\s:163+4*\\s], v[\\c:\\c+3]\n\t"
        "v_mfma_f32_16x16x32_bf16 v[\\c+4:\\c+7], v[68+8*\\s:71+8*\\s], v[160+4*\\s:163+4*\\s], v[\\c+4:\\c+7]\n\t.endm\n\t"
        ".macro GLOAD m, r, srd\n\t"
        "v_and_or_b32 v195, v[212+\\m], %[mask], v[188+\\r]\n\t"
        "buffer_load_dwordx4 v195, \\srd, 0 offen offset:1024*\\r lds\n\t.endm\n\t"
        ".macro GPART m, r, srd\n\ts_cmp_le_i32 %[nl], \\m\n\ts_cbranch_scc1 4f\n\tGLOAD \\m, \\r, \\srd\n\t.endm\n\t"
        ".macro GSUM m\n\tv_mfma_f32_16x16x32_bf16 v[128+4*(\\m):131+4*(\\m)], v[96+4*(\\m):99+4*(\\m)], v[220:223], v[128+4*(\\m):131+4*(\\m)]\n\t.endm\n\t"
        ".macro GCMP m, d\n\tv_cmp_ne_u32_sdwa \\d, v[212+\\m], v193 src0_sel:BYTE_0 src1_sel:DWORD\n\t.endm\n\t"
        ".macro GPAIR m, c\n\t"
        "GSUM \\m\n\tGCMP \\m+1, %[sm]\n\tv_cndmask_b32_e32 v[\\c+\\m], 0, v194, vcc\n\t"
        "GSUM \\m+1\n\t.if \\m < 6\n\tGCMP \\m+2, vcc\n\t.else\n\ts_cmp_gt_i32 %[rem], 0\n\t.endif\n\t"
        "v_cndmask_b32_e64 v[\\c+\\m+1], 0, v194, %[sm]\n\t.endm\n\t"
        ".macro GSTEP so, c, eo\n\t"
        "GTR 0, \\so\n\t"
        "s_waitcnt lgkmcnt(2)\n\tGS 0, \\c\n\t"
        "GTR 1, \\so\n\tGTR 2, \\so\n\tGS 1, \\c\n\t"
        "GTR 3, \\so\n\tGTR 4, \\so\n\tGS 2, \\c\n\t"
        "GTR 5, \\so\n\tGTR 6, \\so\n\tGS 3, \\c\n\t"
        "GTR 7, \\so\n\t"
        "ds_read2_b32 v[212:213], v192 offset0:\\eo offset1:\\eo+4\n\t"
        "ds_read2_b32 v[214:215], v192 offset0:\\eo+8 offset1:\\eo+12\n\t"
        "ds_read2_b32 v[216:217], v192 offset0:\\eo+16 offset1:\\eo+20\n\t"
        "ds_read2_b32 v[218:219], v192 offset0:\\eo+24 offset1:\\eo+28\n\t"
        "s_add_u32 m0, %[ring], \\so\n\t"
        "s_cmp_gt_i32 %[rem], 3\n\t"
        ".irp k, 0, 1, 2, 3, 4, 5, 6, 7\n\tv_exp_f32_e32 v[\\c+\\k], v[\\c+\\k]\n\t.endr\n\t"
        "s_waitcnt lgkmcnt(0)\n\t"
        "s_cbranch_scc0 2f\n\t"
        "GLOAD 0, 0, %[s0]\n\tGLOAD 1, 1, %[s1]\n\tGLOAD 2, 2, %[s2]\n\tGLOAD 3, 3, %[s3]\n\t"
        "s_add_u32 m0, m0, 0x1000\n\t"
        "GLOAD 4, 0, %[s0]\n\tGLOAD 5, 1, %[s1]\n\tGLOAD 6, 2, %[s2]\n\tGLOAD 7, 3, %[s3]\n\t"
        "GSIG \\c\n\t"
        "s_waitcnt vmcnt(8)\n\t"
        "s_branch 1f\n\t"
        "2:\n\t"
        "s_mov_b32 %[t], 0\n\t"
        "s_cmp_eq_u32 %[rem], 3\n\ts_cbranch_scc0 4f\n\t"
        "s_mov_b32 %[t], %[nl]\n\t"
        "GLOAD 0, 0, %[s0]\n\tGPART 1, 1, %[s1]\n\tGPART 2, 2, %[s2]\n\tGPART 3, 3, %[s3]\n\t"
        "s_add_u32 m0, m0, 0x1000\n\t"
        "GPART 4, 0, %[s0]\n\tGPART 5, 1, %[s1]\n\tGPART 6, 2, %[s2]\n\tGPART 7, 3, %[s3]\n\t"
        "4:\n\t"
        "GSIG \\c\n\t"
        "s_waitcnt vmcnt(8)\n\t"
        "GWAIT 7\n\tGWAIT 6\n\tGWAIT 5\n\tGWAIT 4\n\tGWAIT 3\n\tGWAIT 2\n\tGWAIT 1\n\tGWAIT 0\n\t"
        "1:\n\t"
        "GROWS 8192-\\so\n\t"
        "GCMP 0, vcc\n\t"
        "s_add_i32 %[rem], %[rem], -1\n\t"
        "GPAIR 0, \\c\n\tGPAIR 2, \\c\n\tGPAIR 4, \\c\n\tGPAIR 6, \\c\n\t"
        ".endm\n\t"
        ".macro GSIG c\n\t"
        ".if " STR(PKADD) "\n\t.irp k, 0, 2, 4, 6\n\tv_pk_add_f32 v[\\c+\\k:\\c+\\k+1], v[\\c+\\k:\\c+\\k+1], 1.0 op_sel_hi:[1,0]\n\t.endr\n\t.else\n\t"
        ".irp k, 0, 1, 2, 3, 4, 5, 6, 7\n\tv_add_f32_e32 v[\\c+\\k], 1.0, v[\\c+\\k]\n\t.endr\n\t.endif\n\t"
        ".irp k, 0, 1, 2, 3, 4, 5, 6, 7\n\tv_rcp_f32_e32 v[\\c+\\k], v[\\c+\\k]\n\t.endr\n\t"
        ".irp k, 0, 1, 2, 3\n\tv_cvt_pk_bf16_f32 v[220+\\k], v[\\c+2*\\k], v[\\c+2*\\k+1]\n\t.endr\n\t.endm\n\t"
        "s_waitcnt vmcnt(8)\n\t"
        "s_cmp_gt_i32 %[rem], 1\n\ts_cbranch_scc1 1f\n\ts_waitcnt vmcnt(0)\n\t1:\n\t"
        "GROWS 0\n\t"
        ".if " STR(LPRIO) " >= 3\n\ts_mov_b32 %[pr], 0\n\ts_setprio 2\n\t.endif\n\t"
        "0:\n\t"
        ".if " STR(LPRIO) " == 3\n\t"
        "s_cmp_eq_u32 %[pr], 1\n\ts_cbranch_scc0 5f\n\ts_setprio 1\n\t5:\n\t"
        "s_cmp_eq_u32 %[pr], 2\n\ts_cbranch_scc0 5f\n\ts_setprio 0\n\t5:\n\t"
        "s_add_u32 %[pr], %[pr], 1\n\t.endif\n\t"
        "GSTEP 0, 196, 64\n\t"
        "s_cbranch_scc0 9f\n\t"
        "GSTEP 8192, 204, 96\n\t"
        "v_add_u32_e32 v192, 0x100, v192\n\t"
        "s_cbranch_scc1 0b\n\t"
        "9:\n\t"
        "s_waitcnt lgkmcnt(0)\n\ts_nop 6\n\t"
        ".purgem GWAIT\n\t.purgem GROWS\n\t.purgem GTR\n\t.purgem GS\n\t.purgem GLOAD\n\t.purgem GPART\n\t.purgem GSUM\n\t.purgem GCMP\n\t.purgem GPAIR\n\t.purgem GSTEP\n\t.purgem GSIG"
        : "+{v[128:159]}"(acc), "+{v[192:195]}"(misc), "+{v[196:211]}"(cc), "=&{v[64:95]}"(t0), "=&{v[96:127]}"(t1), "=&{v[212:223]}"(t2),
          [rem] "+s"(G), [t] "=&s"(tmp), [pr] "=&s"(pr), [sm] "=&s"(sm)
        : "{v[160:175]}"(qf), "{v[176:191]}"(addr), [s0] "s"(d.s0), [s1] "s"(d.s1), [s2] "s"(d.s2), [s3] "s"(d.s3),
          [mask] "s"(mask), [ring] "s"(ring), [nl] "s"(nlast)
        : "vcc", "scc", "memory");
}

__device__ __forceinline__ u64 rank_edges(u32x16& ed, u32x16& es, u32x16& rk, u64 m0, u64 m1, u64 m2, u64 m3, unsigned keya) {
    u32x16 b4; u64 over; unsigned sel = 0x01050401u, c36 = 36;
    asm volatile(
        ".macro RADD k\n\tv_lshlrev_b32_sdwa v[96+\\k], 2, v[64+\\k] dst_sel:DWORD dst_unused:UNUSED_PAD src0_sel:DWORD src1_sel:BYTE_0\n\t"
        "ds_add_rtn_u32 v[112+\\k], v[96+\\k], v111 offset:%c[cnt]\n\t.endm\n\t"
        ".macro RENT k\n\tv_add_u32_e32 v[80+\\k], %c[bias], v[80+\\k]\n\tv_perm_b32 v[80+\\k], v[80+\\k], v[64+\\k], %[sel]\n\t.endm\n\t"
        ".macro RPUT k\n\tv_min_u32_e32 v110, %c[cap], v[112+\\k]\n\tv_mad_u32_u24 v110, v[96+\\k], %[c36], v110\n\t"
        "ds_write_b32 v110, v[80+\\k] offset:%c[img]\n\t.endm\n\t"
        "v_mov_b32_e32 v111, 4\n\t"
        ".irp k, 0, 2, 4, 6, 8, 10, 12\n\tv_mov_b64_e32 v[112+\\k:113+\\k], 0\n\t.endr\n\t"
        "s_mov_b64 exec, %[m0]\n\ts_waitcnt vmcnt(9)\n\tRADD 0\n\tRADD 1\n\tRADD 2\n\tRADD 3\n\t"
        "s_mov_b64 exec, %[m1]\n\ts_waitcnt vmcnt(8)\n\tRADD 4\n\tRADD 5\n\tRADD 6\n\tRADD 7\n\t"
        "s_mov_b64 exec, %[m2]\n\ts_waitcnt vmcnt(7)\n\tRADD 8\n\tRADD 9\n\tRADD 10\n\tRADD 11\n\t"
        "s_mov_b64 exec, %[m3]\n\ts_waitcnt vmcnt(6)\n\tRADD 12\n\t"
        "s_mov_b64 exec, %[m0]\n\ts_waitcnt vmcnt(5)\n\tRENT 0\n\tRENT 1\n\tRENT 2\n\tRENT 3\n\t"
        "s_waitcnt lgkmcnt(9)\n\tRPUT 0\n\tRPUT 1\n\tRPUT 2\n\tRPUT 3\n\t"
        "s_mov_b64 exec, %[m1]\n\ts_waitcnt vmcnt(4)\n\tRENT 4\n\tRENT 5\n\tRENT 6\n\tRENT 7\n\t"
        "s_waitcnt lgkmcnt(9)\n\tRPUT 4\n\tRPUT 5\n\tRPUT 6\n\tRPUT 7\n\t"
        "s_mov_b64 exec, %[m2]\n\ts_waitcnt vmcnt(3)\n\tRENT 8\n\tRENT 9\n\tRENT 10\n\tRENT 11\n\t"
        "s_waitcnt lgkmcnt(9)\n\tRPUT 8\n\tRPUT 9\n\tRPUT 10\n\tRPUT 11\n\t"
        "s_mov_b64 exec, %[m3]\n\ts_waitcnt vmcnt(0)\n\tRENT 12\n\t"
        "s_waitcnt lgkmcnt(12)\n\tRPUT 12\n\t"
        "s_mov_b64 exec, -1\n\t"
        "v_add_u32_e32 v93, %c[step], v93\n\t"
        "v_max3_u32 v110, v112, v113, v114\n\tv_max3_u32 v109, v115, v116, v117\n\t"
        "ds_write_b32 %[keya], v93 offset:%c[cnt]+1024\n\t"
        "v_max3_u32 v110, v110, v118, v119\n\tv_max3_u32 v109, v109, v120, v121\n\t"
        "v_max3_u32 v110, v110, v122, v123\n\tv_max3_u32 v110, v110, v109, v124\n\t"
        "v_cmp_le_u32_e32 vcc, %c[cap], v110\n\ts_mov_b64 %[over], vcc\n\t"
        ".purgem RADD\n\t.purgem RENT\n\t.purgem RPUT"
        : "+{v[64:79]}"(ed), "+{v[80:95]}"(es), "=&{v[96:111]}"(b4), "=&{v[112:127]}"(rk), [over] "=&s"(over)
        : [m0] "s"(m0), [m1] "s"(m1), [m2] "s"(m2), [m3] "s"(m3), [keya] "v"(keya), [sel] "s"(sel), [c36] "s"(c36),
          [cnt] "i"(L_CNT1), [img] "i"(L_IMG + 8), [bias] "i"(BIAS), [cap] "i"(4 * CAP1), [step] "i"(KEY_STEP)
        : "vcc", "memory");
    return over;
}
__device__ __forceinline__ unsigned read_image(f32x32& img, u32x16& meta, u32x16& sums, unsigned ima, unsigned cna) {
    unsigned no;
    asm volatile(
        "v_mov_b32_e32 v121, 0\n\tds_read_b32 v120, v121 offset:%c[cnt]+2048\n\t"
        ".irp k, 0, 1, 2, 3, 4, 5, 6, 7\n\tds_read_b128 v[64+4*\\k:67+4*\\k], %[ima] offset:4608*\\k\n\t.endr\n\t"
        ".irp k, 0, 1, 2, 3\n\tds_read2_b32 v[96+2*\\k:97+2*\\k], %[cna] offset0:64*\\k offset1:64*\\k+32\n\t.endr\n\t"
        "s_waitcnt lgkmcnt(11)\n\tv_readfirstlane_b32 %[no], v120\n\t"
        "v_add_u32_e32 v121, 0x400, %[cna]\n\t"
        ".irp k, 0, 1, 2, 3\n\tds_read2_b32 v[104+2*\\k:105+2*\\k], v121 offset0:64*\\k offset1:64*\\k+32\n\t.endr\n\t"
        "s_waitcnt lgkmcnt(12)\n\t"
        ".irp k, 0, 1, 2, 3\n\tv_add3_u32 v[112+\\k], v[64+4*\\k], v[65+4*\\k], v[66+4*\\k]\n\tv_add_u32_e32 v[112+\\k], v[112+\\k], v[67+4*\\k]\n\t.endr\n\t"
        "s_waitcnt lgkmcnt(8)\n\t"
        ".irp k, 4, 5, 6, 7\n\tv_add3_u32 v[112+\\k], v[64+4*\\k], v[65+4*\\k], v[66+4*\\k]\n\tv_add_u32_e32 v[112+\\k], v[112+\\k], v[67+4*\\k]\n\t.endr\n\t"
        ".irp k, 0, 1, 2, 3, 4, 5, 6, 7\n\tv_add_u32_dpp v[112+\\k], v[112+\\k], v[112+\\k] quad_perm:[1,0,3,2] row_mask:0xf bank_mask:0xf\n\t.endr\n\t"
        ".irp k, 0, 1, 2, 3, 4, 5, 6, 7\n\tv_add_u32_dpp v[112+\\k], v[112+\\k], v[112+\\k] quad_perm:[2,3,0,1] row_mask:0xf bank_mask:0xf\n\t.endr\n\t"
        ".irp k, 0, 1, 2, 3, 4, 5, 6, 7\n\tv_add_u32_dpp v[112+\\k], v[112+\\k], v[112+\\k] row_half_mirror row_mask:0xf bank_mask:0xf\n\t.endr\n\t"
        "s_waitcnt lgkmcnt(0)"
        : "=&{v[64:95]}"(img), "=&{v[96:111]}"(meta), "=&{v[112:127]}"(sums), [no] "=&s"(no)
        : [ima] "v"(ima), [cna] "v"(cna), [cnt] "i"(L_CNT1) : "memory");
    return no;
}
__device__ __forceinline__ void publish_image(f32x32 img, u32x16 meta, u32x16 sums, const u32x4& srd, unsigned ob) {
    u64 pc0 = 0x0101010101010101ull; unsigned so;
    asm volatile(
        ".macro PUB k\n\ts_mov_b64 exec, %[pc0]\n\tv_mov_b32_e32 v[64+4*\\k], v[96+\\k]\n\tv_mov_b32_e32 v[65+4*\\k], v[112+\\k]\n\t"
        "s_mov_b64 exec, -1\n\tv_xor_b32_e32 v[64+4*\\k], v[64+4*\\k], v[104+\\k]\n\t"
        "buffer_store_dwordx4 v[64+4*\\k:67+4*\\k], %[ob], %[srd], %[so] offen sc1\n\ts_add_u32 %[so], %[so], %c[step]\n\t.endm\n\t"
        "s_mov_b32 %[so], 0\n\t"
        ".irp k, 0, 1, 2, 3, 4, 5, 6, 7\n\tPUB \\k\n\t.endr\n\t.purgem PUB"
        : "+{v[64:95]}"(img), [so] "=&s"(so)
        : "{v[96:111]}"(meta), "{v[112:127]}"(sums), [srd] "s"(srd), [ob] "v"(ob), [pc0] "s"(pc0), [step] "i"(32 * G1 * 128) : "scc", "memory");
}

__device__ __forceinline__ void inbox_request(u32x16& raw, const u32x4& srd, unsigned ib) {
    unsigned o1 = 0x1000, o2 = 0x2000, o3 = 0x3000;
    asm volatile("buffer_load_dwordx4 v[64:67], %1, %2, 0 offen sc1\n\tbuffer_load_dwordx4 v[68:71], %1, %2, %3 offen sc1\n\t"
                 "buffer_load_dwordx4 v[72:75], %1, %2, %4 offen sc1\n\tbuffer_load_dwordx4 v[76:79], %1, %2, %5 offen sc1"
                 : "=&{v[64:79]}"(raw) : "v"(ib), "s"(srd), "s"(o1), "s"(o2), "s"(o3) : "memory");
}
__device__ __forceinline__ unsigned inbox_stage(u32x16 raw, const u32x4& srd, unsigned ib, unsigned key, int i4, unsigned hdra) {
    u32x16 t0, t2; u32x4 t1; u64 m0, m1, m2, m3; unsigned todo, fresh, spin, o1, o2, o3, over;
    u64 pc0 = 0x0101010101010101ull;
    asm volatile(
        ".macro IVAL j, so\n\t"
        "s_bitcmp1_b32 %[todo], \\j\n\ts_cbranch_scc0 2f\n\t"
        "v_xor_b32_e32 v[64+4*\\j], %[key], v[64+4*\\j]\n\t"
        "v_sub_u32_e32 v97, 0, v[65+4*\\j]\n\t"
        "v_add_u32_e32 v96, v[64+4*\\j], v[65+4*\\j]\n\t"
        "v_mov_b32_dpp v[80+\\j], v[64+4*\\j] quad_perm:[0,0,0,0] row_mask:0xf bank_mask:0xf\n\t"
        "v_cndmask_b32_e64 v96, v96, v97, %[pc0]\n\t"
        "s_nop 0\n\t"
        "v_mov_b32_dpp v[80+\\j], v[80+\\j] row_shr:4 row_mask:0xf bank_mask:0xa\n\t"
        "v_cmp_gt_i32_e32 vcc, v[80+\\j], v92\n\t"
        "v_add3_u32 v96, v96, v[66+4*\\j], v[67+4*\\j]\n\t"
        "v_lshrrev_b32_e32 v97, 14, v[80+\\j]\n\t"
        "v_cndmask_b32_e32 v96, 0, v96, vcc\n\t"
        "s_nop 1\n\tv_add_u32_dpp v96, v96, v96 quad_perm:[1,0,3,2] row_mask:0xf bank_mask:0xf\n\t"
        "s_nop 1\n\tv_add_u32_dpp v96, v96, v96 quad_perm:[2,3,0,1] row_mask:0xf bank_mask:0xf\n\t"
        "s_nop 1\n\tv_add_u32_dpp v96, v96, v96 row_half_mirror row_mask:0xf bank_mask:0xf\n\t"
        "v_or_b32_e32 v96, v96, v97\n\t"
        "v_cmp_ne_u32_e32 vcc, 0, v96\n\t"
        "s_cbranch_vccz 1f\n\t"
        "buffer_load_dwordx4 v[64+4*\\j:67+4*\\j], %[ib], %[srd], \\so offen sc1\n\t"
        "s_branch 2f\n\t"
        "1:\n\ts_bitset1_b32 %[fresh], \\j\n\t"
        "2:\n\t.endm\n\t"
        ".macro IRANK j\n\t"
        "s_bitcmp1_b32 %[fresh], \\j\n\ts_cbranch_scc0 2f\n\t"
        "v_cmp_lt_u32_e64 %[m0], v92, v[80+\\j]\n\tv_cmp_lt_u32_e64 %[m1], v93, v[80+\\j]\n\t"
        "v_cmp_lt_u32_e64 %[m2], v94, v[80+\\j]\n\tv_cmp_lt_u32_e64 %[m3], v95, v[80+\\j]\n\t"
        ".irp k, 0, 1, 2, 3\n\tv_lshrrev_b32_e32 v[84+\\k], 21, v[64+4*\\j+\\k]\n\t.endr\n\t"
        ".irp k, 0, 1, 2, 3\n\tv_and_b32_e32 v[84+\\k], 60, v[84+\\k]\n\t.endr\n\t"
        "v_cmp_lt_u32_e32 vcc, %c[cap], v[80+\\j]\n\t"
        "s_mov_b64 exec, %[m0]\n\tds_add_rtn_u32 v88, v84, v98 offset:%c[cnt]\n\t"
        "s_mov_b64 exec, %[m1]\n\tds_add_rtn_u32 v89, v85, v98 offset:%c[cnt]\n\t"
        "s_mov_b64 exec, %[m2]\n\tds_add_rtn_u32 v90, v86, v98 offset:%c[cnt]\n\t"
        "s_mov_b64 exec, %[m3]\n\tds_add_rtn_u32 v91, v87, v98 offset:%c[cnt]\n\t"
        "s_mov_b64 exec, %[pc0]\n\tds_write_b32 %[hdra], v[80+\\j] offset:128*\\j\n\t"
        "s_mov_b64 exec, -1\n\t"
        "s_cbranch_vccz 1f\n\t"
        "s_mov_b32 %[over], 1\n\t"
        "1:\n\ts_waitcnt lgkmcnt(1)\n\t"
        ".irp k, 0, 1, 2, 3\n\tv_min_u32_e32 v[88+\\k], %c[sub]-4, v[88+\\k]\n\t.endr\n\t"
        ".irp k, 0, 1, 2, 3\n\tv_mad_u32_u24 v[88+\\k], v[84+\\k], %[sub4], v[88+\\k]\n\t.endr\n\t"
        "s_mov_b64 exec, %[m0]\n\tds_write_b32 v88, v[64+4*\\j] offset:%c[list]\n\t"
        "s_mov_b64 exec, %[m1]\n\tds_write_b32 v89, v[65+4*\\j] offset:%c[list]\n\t"
        "s_mov_b64 exec, %[m2]\n\tds_write_b32 v90, v[66+4*\\j] offset:%c[list]\n\t"
        "s_mov_b64 exec, %[m3]\n\tds_write_b32 v91, v[67+4*\\j] offset:%c[list]\n\t"
        "s_mov_b64 exec, -1\n\t"
        "2:\n\t.endm\n\t"
        ".macro FADD j, kb, rb\n\t"
        ".irp k, 0, 1, 2, 3\n\tv_lshrrev_b32_e32 v[\\kb+\\k], 21, v[64+4*\\j+\\k]\n\t.endr\n\t"
        ".irp k, 0, 1, 2, 3\n\tv_and_b32_e32 v[\\kb+\\k], 60, v[\\kb+\\k]\n\t.endr\n\t"
        "v_cmpx_lt_u32_e64 %[m0], v94, v[80+\\j]\n\tds_add_rtn_u32 v[\\rb+2], v[\\kb+2], v98 offset:%c[cnt]\n\t"
        "v_cmpx_lt_u32_e64 %[m0], v95, v[80+\\j]\n\tds_add_rtn_u32 v[\\rb+3], v[\\kb+3], v98 offset:%c[cnt]\n\t"
        "s_mov_b64 exec, -1\n\t"
        "v_cmpx_lt_u32_e64 %[m0], v92, v[80+\\j]\n\tds_add_rtn_u32 v[\\rb], v[\\kb], v98 offset:%c[cnt]\n\t"
        "v_cmpx_lt_u32_e64 %[m0], v93, v[80+\\j]\n\tds_add_rtn_u32 v[\\rb+1], v[\\kb+1], v98 offset:%c[cnt]\n\t"
        "s_mov_b64 exec, -1\n\t.endm\n\t"
        ".macro FPUT j, kb, rb, n\n\t"
        "s_waitcnt lgkmcnt(\\n)\n\t"
        ".irp k, 0, 1, 2, 3\n\tv_min_u32_e32 v[\\rb+\\k], %c[sub]-4, v[\\rb+\\k]\n\t.endr\n\t"
        ".irp k, 0, 1, 2, 3\n\tv_mad_u32_u24 v[\\rb+\\k], v[\\kb+\\k], %[sub4], v[\\rb+\\k]\n\t.endr\n\t"
        "v_cmpx_lt_u32_e64 %[m0], v94, v[80+\\j]\n\tds_write_b32 v[\\rb+2], v[66+4*\\j] offset:%c[list]\n\t"
        "v_cmpx_lt_u32_e64 %[m0], v95, v[80+\\j]\n\tds_write_b32 v[\\rb+3], v[67+4*\\j] offset:%c[list]\n\t"
        "s_mov_b64 exec, -1\n\t"
        "v_cmpx_lt_u32_e64 %[m0], v92, v[80+\\j]\n\tds_write_b32 v[\\rb], v[64+4*\\j] offset:%c[list]\n\t"
        "v_cmpx_lt_u32_e64 %[m0], v93, v[80+\\j]\n\tds_write_b32 v[\\rb+1], v[65+4*\\j] offset:%c[list]\n\t"
        "s_mov_b64 exec, -1\n\t.endm\n\t"
        "s_movk_i32 %[o1], 0x1000\n\ts_movk_i32 %[o2], 0x2000\n\ts_movk_i32 %[o3], 0x3000\n\t"
        "v_mov_b32_e32 v92, %[i4]\n\tv_add_u32_e32 v93, 4, %[i4]\n\tv_add_u32_e32 v94, 8, %[i4]\n\tv_add_u32_e32 v95, 12, %[i4]\n\t"
        "v_mov_b32_e32 v98, 4\n\t"
        "s_mov_b32 %[todo], 15\n\ts_mov_b32 %[spin], 0x400000\n\ts_mov_b32 %[over], 0\n\t"
        ".if " STR(FASTIN) "\n\t"
        "s_waitcnt vmcnt(0)\n\t"
        ".irp j, 0, 1, 2, 3\n\tv_xor_b32_e32 v[64+4*\\j], %[key], v[64+4*\\j]\n\t.endr\n\t"
        ".irp j, 0, 1, 2, 3\n\tv_mov_b32_dpp v[80+\\j], v[64+4*\\j] quad_perm:[0,0,0,0] row_mask:0xf bank_mask:0xf\n\t.endr\n\t"
        ".irp j, 0, 1, 2, 3\n\tv_add_u32_e32 v[84+\\j], v[64+4*\\j], v[65+4*\\j]\n\t.endr\n\t"
        ".irp j, 0, 1, 2, 3\n\tv_mov_b32_dpp v[80+\\j], v[80+\\j] row_shr:4 row_mask:0xf bank_mask:0xa\n\t.endr\n\t"
        "s_mov_b64 exec, %[pc0]\n\t"
        ".irp j, 0, 1, 2, 3\n\tv_sub_u32_e32 v[84+\\j], 0, v[65+4*\\j]\n\t.endr\n\t"
        "s_mov_b64 exec, -1\n\t"
        "v_cmp_gt_i32_e64 %[m0], v80, v92\n\tv_cmp_gt_i32_e64 %[m1], v81, v92\n\tv_cmp_gt_i32_e64 %[m2], v82, v92\n\tv_cmp_gt_i32_e64 %[m3], v83, v92\n\t"
        ".irp j, 0, 1, 2, 3\n\tv_add3_u32 v[84+\\j], v[84+\\j], v[66+4*\\j], v[67+4*\\j]\n\t.endr\n\t"
        "v_cndmask_b32_e64 v84, 0, v84, %[m0]\n\tv_cndmask_b32_e64 v85, 0, v85, %[m1]\n\tv_cndmask_b32_e64 v86, 0, v86, %[m2]\n\tv_cndmask_b32_e64 v87, 0, v87, %[m3]\n\t"
        ".irp j, 0, 1, 2, 3\n\tv_add_u32_dpp v[84+\\j], v[84+\\j], v[84+\\j] quad_perm:[1,0,3,2] row_mask:0xf bank_mask:0xf\n\t.endr\n\t"
        ".irp j, 0, 1, 2, 3\n\tv_add_u32_dpp v[84+\\j], v[84+\\j], v[84+\\j] quad_perm:[2,3,0,1] row_mask:0xf bank_mask:0xf\n\t.endr\n\t"
        ".irp j, 0, 1, 2, 3\n\tv_add_u32_dpp v[84+\\j], v[84+\\j], v[84+\\j] row_half_mirror row_mask:0xf bank_mask:0xf\n\t.endr\n\t"
        "v_or3_b32 v96, v80, v81, v82\n\tv_max3_u32 v97, v80, v81, v82\n\tv_or_b32_e32 v96, v96, v83\n\tv_max_u32_e32 v97, v97, v83\n\t"
        "v_lshrrev_b32_e32 v96, 14, v96\n\tv_or3_b32 v96, v96, v84, v85\n\tv_or3_b32 v96, v96, v86, v87\n\t"
        "v_cmp_ne_u32_e32 vcc, 0, v96\n\t"
        "s_cbranch_vccnz 7f\n\t"
        "v_cmp_lt_u32_e32 vcc, %c[cap], v97\n\t"
        "FADD 0, 100, 104\n\tFADD 1, 108, 112\n\t"
        "s_cbranch_vccz 6f\n\ts_mov_b32 %[over], 1\n\t6:\n\t"
        "FPUT 0, 100, 104, 4\n\tFADD 2, 100, 104\n\tFPUT 1, 108, 112, 8\n\tFADD 3, 108, 112\n\tFPUT 2, 100, 104, 8\n\tFPUT 3, 108, 112, 4\n\t"
        "s_mov_b64 exec, %[pc0]\n\t"
        ".irp j, 0, 1, 2, 3\n\tds_write_b32 %[hdra], v[80+\\j] offset:128*\\j\n\t.endr\n\t"
        "s_mov_b64 exec, -1\n\t"
        "s_branch 9f\n\t"
        "7:\n\t.irp j, 0, 1, 2, 3\n\tv_xor_b32_e32 v[64+4*\\j], %[key], v[64+4*\\j]\n\t.endr\n\t"
        ".endif\n\t"
        "0:\n\t"
        "s_mov_b32 %[fresh], 0\n\t"
        "s_waitcnt vmcnt(0)\n\t"
        "IVAL 0, 0\n\tIVAL 1, %[o1]\n\tIVAL 2, %[o2]\n\tIVAL 3, %[o3]\n\t"
        "IRANK 0\n\tIRANK 1\n\tIRANK 2\n\tIRANK 3\n\t"
        "s_andn2_b32 %[todo], %[todo], %[fresh]\n\t"
        "s_cbranch_scc0 9f\n\t"
        "s_sub_u32 %[spin], %[spin], 1\n\t"
        "s_cbranch_scc0 0b\n\t"
        "s_waitcnt vmcnt(0)\n\t"
        "9:\n\t"
        ".purgem IVAL\n\t.purgem IRANK\n\t.purgem FADD\n\t.purgem FPUT"
        : "+{v[64:79]}"(raw), "=&{v[80:95]}"(t0), "=&{v[96:99]}"(t1), "=&{v[100:115]}"(t2), [m0] "=&s"(m0), [m1] "=&s"(m1), [m2] "=&s"(m2), [m3] "=&s"(m3),
          [todo] "=&s"(todo), [fresh] "=&s"(fresh), [spin] "=&s"(spin), [o1] "=&s"(o1), [o2] "=&s"(o2), [o3] "=&s"(o3), [over] "=&s"(over)
        : [srd] "s"(srd), [ib] "v"(ib), [key] "s"(key), [i4] "v"(i4), [hdra] "v"(hdra), [pc0] "s"(pc0), [sub4] "s"(SUBB / 4),
          [cnt] "i"(L_CNT), [list] "i"(L_LIST), [sub] "i"(SUBB), [cap] "i"(4 * CAP1)
        : "vcc", "scc", "memory");
    return over;
}

template <int CTRL, int BANK> __device__ __forceinline__ unsigned dpp(unsigned old, unsigned v) { return __builtin_amdgcn_update_dpp(old, v, CTRL, 0xF, BANK, true); }
__device__ __forceinline__ unsigned bcast8(unsigned v) { v = dpp<0x00, 0xF>(0u, v); return __builtin_amdgcn_update_dpp(v, v, 0x114, 0xF, 0xA, false); }
__device__ __forceinline__ unsigned sum8(unsigned v) { v += dpp<0xB1, 0xF>(0u, v); v += dpp<0x4E, 0xF>(0u, v); return v + dpp<0x141, 0xF>(0u, v); }
__device__ __forceinline__ bf16x4 tr_read(LAS char* p) { return __builtin_amdgcn_ds_read_tr16_b64_v4bf16((LAS bf16x4*)p); }
__device__ __forceinline__ bf16x8 cat(bf16x4 a, bf16x4 b) { return __builtin_shufflevector(a, b, 0, 1, 2, 3, 4, 5, 6, 7); }
__device__ __forceinline__ unsigned pk2(float a, float b) { f32x2 v = {a, b}; bf16x2 r = __builtin_convertvector(v, bf16x2); return __builtin_bit_cast(unsigned, r); }
__device__ __forceinline__ f32x4 mfma(bf16x8 a, bf16x8 b, f32x4 c) { return __builtin_amdgcn_mfma_f32_16x16x32_bf16(a, b, c, 0, 0, 0); }
__device__ __forceinline__ unsigned lds_add(LAS char* p, unsigned v) { return __hip_atomic_fetch_add((LAS unsigned*)p, v, __ATOMIC_RELAXED, __HIP_MEMORY_SCOPE_WORKGROUP); }
__device__ __forceinline__ LAS unsigned* ldsp(unsigned a) { return (LAS unsigned*)(size_t)a; }
__device__ __forceinline__ unsigned byte0_x4(unsigned v) { unsigned r; asm("v_lshlrev_b32_sdwa %0, 2, %1 dst_sel:DWORD dst_unused:UNUSED_PAD src0_sel:DWORD src1_sel:BYTE_0" : "=v"(r) : "v"(v)); return r; }
__device__ __forceinline__ unsigned mad24(unsigned a, unsigned b, unsigned c) { unsigned r; asm("v_mad_u32_u24 %0, %1, %2, %3" : "=v"(r) : "v"(a), "s"(b), "v"(c)); return r; }
__device__ __forceinline__ void lds_arrive(LAS char* p, int lane) {
    asm volatile("s_waitcnt lgkmcnt(0)" ::: "memory");
    if (lane == 0) __hip_atomic_fetch_add((LAS unsigned*)p, 1u, __ATOMIC_RELAXED, __HIP_MEMORY_SCOPE_WORKGROUP);
}
__device__ __forceinline__ void lds_await(LAS char* p, unsigned n) {
    while (__builtin_amdgcn_readfirstlane(*(volatile LAS unsigned*)p) < n) __builtin_amdgcn_s_sleep(1);
    asm volatile("" ::: "memory");
}
__device__ __forceinline__ unsigned rfl(unsigned v) { return __builtin_amdgcn_readfirstlane(v); }

__global__ __launch_bounds__(512, 2) void k_graph(const unsigned short* x_, const int* src_, const int* dst_, const unsigned short* wq_, const unsigned short* wv_,
                                                  unsigned* ws_, unsigned short* out_, int N_, int E_) {
    u32x4 srd_d, srd_s, srd_ws, srd_x0, pw, po;
    u64 t_start;
    asm volatile("s_load_dwordx2 s[40:41], %[ka], 0x10\n\ts_load_dwordx2 s[44:45], %[ka], 0x8\n\ts_load_dwordx4 s[60:63], %[ka], 0x30\n\t"
                 "s_load_dwordx2 s[48:49], %[ka], 0x28\n\ts_load_dwordx2 s[52:53], %[ka], 0x0\n\ts_load_dwordx4 s[56:59], %[ka], 0x18\n\t"
                 "s_memrealtime s[64:65]\n\t"
                 "s_mov_b32 s43, 0x20000\n\ts_mov_b32 s47, 0x20000\n\ts_mov_b32 s51, 0x20000\n\ts_mov_b32 s55, 0x20000\n\ts_mov_b32 s50, %c[wsb]"
                 : "=&{s[40:43]}"(srd_d), "=&{s[44:47]}"(srd_s), "=&{s[48:51]}"(srd_ws), "=&{s[52:55]}"(srd_x0), "=&{s[56:59]}"(pw), "=&{s[60:63]}"(po), "=&{s[64:65]}"(t_start)
                 : [ka] "s"(__builtin_amdgcn_kernarg_segment_ptr()), [wsb] "i"(WS_BYTES));
    FENCE();
    __shared__ __attribute__((aligned(16))) char smem_[L_TOTAL];
    LAS char* smem = (LAS char*)smem_;
    const int t = threadIdx.x, l = t & 63, b = blockIdx.x;
    const int w = rfl(t >> 6);
    const int g = l >> 4, n = l & 15, q = (l >> 2) & 3, p = l & 3, pc = t & 7;
    const unsigned lds0 = (unsigned)(size_t)smem;
    const bool isprod = ((b >> 3) & 1) == 0;
    const int pg = ((b & 7) << 4) | (b >> 4);
    const unsigned e0 = pg * CH + 4 * t, e1 = pg * CH + 3072 + t;

    u32x16 ed, es;
    {
        const unsigned o = e0 * 4, o1 = w == 0 ? e1 * 4 : OOB, ok = WS_KEY + t * 4, om = WS_KEY + b * 4;
        const unsigned oc = w < 4 ? (t < 196 ? ((b - 8) << 8) + ((t >> 1) << 16) + ((t & 1) << 7) : OOB) : (t - 256) * 128, dump = lds0 + L_DUMP + w * 256;
        int role = (w < 4 ? 0 : 2) + (isprod ? 0 : 1);
        unsigned k4 = 4096, k8 = 8192;
        asm volatile("s_cmp_eq_u32 %[role], 0\n\ts_cbranch_scc1 1f\n\t"
                     "s_mov_b32 m0, %[dump]\n\ts_cmp_eq_u32 %[role], 1\n\ts_cbranch_scc1 2f\n\t"
                     "s_getpc_b64 s[68:69]\n\ts_mov_b32 s70, " STR(CODE_BYTES) "\n\ts_mov_b32 s71, 0x20000\n\t"
                     ".if " STR(CODEPF) "\n\tbuffer_load_dword %[oc], s[68:71], 0 offen lds\n\t.endif\n\t"
                     "s_waitcnt lgkmcnt(0)\n\ts_branch 3f\n\t"
                     "2:\n\ts_waitcnt lgkmcnt(0)\n\ts_lshl_b32 s54, s62, 8\n\t"
                     ".if " STR(PARTNER) "\n\tbuffer_load_dword %[oc], s[52:55], 0 offen lds\n\t.endif\n\ts_branch 3f\n\t"
                     "1:\n\ts_waitcnt lgkmcnt(0)\n\ts_lshl_b32 s42, s63, 2\n\ts_lshl_b32 s46, s63, 2\n\t"
                     "buffer_load_dwordx4 v[64:67], %[o], s[40:43], 0 offen\n\tbuffer_load_dwordx4 v[68:71], %[o], s[40:43], %[k4] offen\n\t"
                     "buffer_load_dwordx4 v[72:75], %[o], s[40:43], %[k8] offen\n\tbuffer_load_dword v76, %[o1], s[40:43], 0 offen\n\t"
                     "buffer_load_dwordx4 v[80:83], %[o], s[44:47], 0 offen\n\tbuffer_load_dwordx4 v[84:87], %[o], s[44:47], %[k4] offen\n\t"
                     "buffer_load_dwordx4 v[88:91], %[o], s[44:47], %[k8] offen\n\tbuffer_load_dword v92, %[o1], s[44:47], 0 offen\n\t"
                     "buffer_load_dword v93, %[ok], s[48:51], 0 offen sc1\n\tbuffer_load_dword v94, %[om], s[48:51], 0 offen sc1\n\t"
                     "3:\n\ts_lshl_b32 s54, s62, 8"
                     : "=&{v[64:79]}"(ed), "=&{v[80:95]}"(es)
                     : [o] "v"(o), [o1] "v"(o1), [ok] "v"(ok), [om] "v"(om), [oc] "v"(oc), [role] "s"(role), [dump] "s"(dump), [k4] "s"(k4), [k8] "s"(k8),
                       "{s[40:43]}"(srd_d), "{s[44:47]}"(srd_s), "{s[48:51]}"(srd_ws), "{s[52:55]}"(srd_x0), "{s[56:59]}"(pw), "{s[60:63]}"(po), "{s[64:65]}"(t_start)
                     : "memory", "scc", "s68", "s69", "s70", "s71");
        asm volatile("" : "+{s[48:51]}"(srd_ws), "+{s[52:55]}"(srd_x0), "+{s[56:59]}"(pw), "+{s[60:63]}"(po), "+{s[64:65]}"(t_start));
    }
    const void* const x = (const void*)((u64)srd_x0.x | ((u64)srd_x0.y << 32));
    const void* const wq = (const void*)((u64)pw.x | ((u64)pw.y << 32));
    const void* const wv = (const void*)((u64)pw.z | ((u64)pw.w << 32));
    void* const out = (void*)((u64)po.x | ((u64)po.y << 32));
    const int N = po.z, E = po.w;
    const unsigned xbytes = srd_x0.z;
    unsigned* const ws = (unsigned*)((u64)srd_ws.x | ((u64)srd_ws.y << 32));
    STAMP_DECL;
    st_[0] = t_start;
    const unsigned ring = lds0 + L_RING + w * 16384;
    const int jn = 8 * n + w;

    unsigned mykey = 0;
    auto node_row = [&](int tile, int m) { const int nd = b + 256 * (8 * (4 * m + g) + tile); return nd < N ? (unsigned)nd << 8 : OOB; };
    auto zero_staging = [&]() {
        const u32x4 z = {0u, 0u, 0u, 0u};
#pragma unroll
        for (int k = 0; k < 5; ++k) *(LAS u32x4*)(smem + L_IMG + k * 8192 + t * 16) = z;
    };

    u32x16 addr;
    f32x32 acc, wva, wvb;
    unsigned cv, lista, cnta, qa, keyn, inf;
    Srd4 srd_x;
    auto loop_constants = [&]() {
        srd_x = make_srd4(x, xbytes, BIAS * 256);
        lista = lds0 + L_LIST + w * (2 * SUBB) + g * 4; cnta = lds0 + L_CNT + (l & 31) * 4; qa = ring + l * 16; keyn = jn; inf = 0x7F800000u;
        asm volatile("" : "+s"(srd_x.s0), "+s"(srd_x.s1), "+s"(srd_x.s2), "+s"(srd_x.s3));
        asm volatile("" : "+v"(lista), "+v"(cnta), "+v"(qa), "+v"(keyn), "+v"(inf));
        const unsigned o0 = (n ^ (((g & 1) << 3) | (g >> 1))) << 4;
        const unsigned ra = (4 * (n & 3) + (n >> 2)) * 256 + (((4 * g) ^ (((n & 7) << 1) | (n >> 3))) << 4);
        const int i = 4 * g + q, r8 = i & 7;
        const unsigned ta = (4 * q + g) * 256 + 8 * (p & 1) + (((p >> 1) ^ (((i & 7) << 1) | (i >> 3))) << 4);
        cv = i * 256 + (((p ^ ((r8 & 1) | ((r8 & 6) << 1))) << 4) | ((p & 1) << 3));
#pragma unroll
        for (int s = 0; s < 4; ++s) { addr[s] = ring + (ra ^ (s << 4)); addr[12 + s] = o0 ^ (s << 5); }
#pragma unroll
        for (int c = 0; c < 8; ++c) addr[4 + c] = ring + (ta ^ (c << 5));
        asm volatile("" : "+{v[176:191]}"(addr), "+v"(cv));
    };
    auto clear_sums = [&]() { acc = 0.f; asm volatile("" : "+{v[128:159]}"(acc)); };
    auto wv_resident = [&]() {
#pragma unroll
        for (int a = 0; a < 8; ++a) {
            LAS char* pa = smem + L_WV0 + (cv ^ ((a >> 1) << 6) ^ ((a & 1) << 3));
            const f32x4 f0 = __builtin_bit_cast(f32x4, cat(tr_read(pa), tr_read(pa + 4096)));
            const f32x4 f1 = __builtin_bit_cast(f32x4, cat(tr_read(pa + 16384), tr_read(pa + 16384 + 4096)));
#pragma unroll
            for (int k = 0; k < 4; ++k) { wva[4 * a + k] = f0[k]; wvb[4 * a + k] = f1[k]; }
        }
        asm volatile("" : "+{v[224:255]}"(wva), "+{v[32:63]}"(wvb));
    };

    if (isprod) zero_staging();
    if (t < 8) *(LAS u32x4*)(smem + L_CNT + t * 16) = u32x4{0u, 0u, 0u, 0u};
    WG_BARRIER();

    if (__builtin_expect(w < 4, 1)) {
        if (__builtin_expect(isprod, 1)) {
            if (PRIO) __builtin_amdgcn_s_setprio(2);
            unsigned ob = WS_INBOX + ((t >> 3) * G1 + pg) * 128 + pc * 16, ima = lds0 + L_IMG + (t >> 3) * 144 + pc * 16;
            PIN(ob); PIN(ima);
            unsigned cna = lds0 + L_CNT1 + (t >> 3) * 4;
            PIN(cna);
            const bool v0 = e0 < (unsigned)E, v1 = e0 + 1024 < (unsigned)E, v2 = e0 + 2048 < (unsigned)E, v3 = w == 0 && e1 < (unsigned)E;
            STAMP(1);
            u32x16 rk;
            const u64 over = rank_edges(ed, es, rk, __builtin_amdgcn_ballot_w64(v0), __builtin_amdgcn_ballot_w64(v1), __builtin_amdgcn_ballot_w64(v2),
                                        __builtin_amdgcn_ballot_w64(v3), t * 4);
            mykey = es[14];
            if (__builtin_expect(over != 0, 0)) {
#pragma unroll
                for (int k = 0; k < 13; ++k)
                    if (rk[k] >= 4u * CAP1) {
                        const unsigned o = WS_OVF + (pg * CH + lds_add(smem + L_CNT1 + 2048, 1)) * 4;
                        const unsigned v = ((((es[k] >> 8) & 0xFFFF) - BIAS) << 16) | ed[k];
                        ST1(v, srd_ws, o, " sc1");
                    }
            }
            if (XSYNC) { lds_arrive(smem + L_CNT + 96, l); lds_await(smem + L_CNT + 96, 4); } else WG_BARRIER();
            STAMP(3);
            f32x32 img; u32x16 meta, sums;
            const unsigned no = read_image(img, meta, sums, ima, cna);
            lds_arrive(smem + L_CNT + 84, l);
            if (__builtin_expect(no != 0, 0)) {
                const unsigned o = t == 0 ? WS_NOVF + pg * 4 : OOB;
                ST1(no, srd_ws, o, " sc1");
                VMWAIT(0);
                if (XSYNC) { lds_arrive(smem + L_CNT + 100, l); lds_await(smem + L_CNT + 100, 4); } else WG_BARRIER();
            }
            publish_image(img, meta, sums, srd_ws, ob);
            STAMP(4);
        } else {
            { const unsigned om = WS_KEY + b * 4; asm volatile("buffer_load_dword %0, %1, %2, 0 offen sc1\n\ts_waitcnt vmcnt(0)" : "=&v"(mykey) : "v"(om), "s"(srd_ws) : "memory"); }
            STAMP(1);
            if (PRIO) __builtin_amdgcn_s_setprio(2);
            STAMP(2); STAMP(3); STAMP(4);
        }
        STAMP(5);
        mykey = rfl(mykey) + KEY_STEP;
        loop_constants();
        clear_sums();
        unsigned ib = WS_INBOX + b * (G1 * 128) + t * 16;
        PIN(ib);
        while (__builtin_amdgcn_s_memrealtime() < t_start + POLL_T) __builtin_amdgcn_s_sleep(1);
        STAMP(6);
        u32x16 raw;
        inbox_request(raw, srd_ws, ib);
        if (__builtin_expect(inbox_stage(raw, srd_ws, ib, mykey, 16 * pc - 8, lds0 + L_HDR + (t >> 3) * 4) != 0, 0)) {
            LAS unsigned* hp = (LAS unsigned*)(smem + L_HDR) + 32 * (l >> 3 & 3) + 8 * w + (l & 7);
            const unsigned hv = *hp;
            for (u64 m = __builtin_amdgcn_ballot_w64(l < 32 && hv > 4u * CAP1); m; m &= m - 1) {
                const int k = __builtin_ctzll(m), gp = 32 * (k >> 3) + 8 * w + (k & 7);
                unsigned u, no; const unsigned o = WS_OVF + (gp * CH + l) * 4, on = WS_NOVF + gp * 4;
                asm volatile("buffer_load_dword %0, %2, %4, 0 offen sc1\n\tbuffer_load_dword %1, %3, %4, 0 offen sc1\n\ts_waitcnt vmcnt(0)"
                             : "=&v"(u), "=&v"(no) : "v"(o), "v"(on), "s"(srd_ws) : "memory");
                if (rfl(no) <= 64) {
                    if (l < no && (u & 0xFF) == (unsigned)b) {
                        const unsigned e = __builtin_amdgcn_perm((u >> 16) + BIAS, u, 0x01050401u), key = (e >> 21) & 60;
                        *(LAS unsigned*)(smem + L_LIST + key * (SUBB / 4) + min(lds_add(smem + L_CNT + key, 4), SUBB - 4u)) = e;
                    }
                    if (l == k) *hp = hv | 0x80000000u;
                } else *(LAS unsigned*)(smem + L_CNT + 64) = 1;
            }
        }
        if (PRIO) __builtin_amdgcn_s_setprio(0);
        STAMP(7);
    } else {
        const int j = w - 4;
        STAMP(1);
        u32x4 wr[8], xr[8];
        {
            const u32x4 srd_q = make_srd(wq, 32768);
            const unsigned o = j * 8192 + l * 16, k4 = 4096;
            LD4(wr[0], srd_q, o, ""); LD4(wr[1], srd_q, o, " offset:1024"); LD4(wr[2], srd_q, o, " offset:2048"); LD4(wr[3], srd_q, o, " offset:3072");
            LD4S(wr[4], srd_q, o, k4, ""); LD4S(wr[5], srd_q, o, k4, " offset:1024"); LD4S(wr[6], srd_q, o, k4, " offset:2048"); LD4S(wr[7], srd_q, o, k4, " offset:3072");
#pragma unroll
            for (int m = 0; m < 8; ++m) { const unsigned ox = node_row(m < 4 ? j : w, m & 3) + n * 16; LD4(xr[m], srd_x0, ox, ""); }
        }
        unsigned aqa[8], xfa[4];
        unsigned xa = ring + g * 256 + ((n ^ g) << 4), wa = lds0 + L_WQ + j * 16384 + g * 256 + ((n ^ g) << 4) + ((n & 4) << 1);
        {
            const unsigned aq = lds0 + L_WQ + (8 * g + q) * 256 + ((((4 * p) ^ (q | ((g & 1) << 2))) << 4) | ((p & 1) << 3));
#pragma unroll
            for (int s = 0; s < 4; ++s) { xfa[s] = ring + n * 256 + (((4 * s + g) ^ n) << 4); PIN(xfa[s]); }
#pragma unroll
            for (int c = 0; c < 8; ++c) { aqa[c] = aq ^ ((c >> 1) << 4) ^ ((c & 1) << 3); PIN(aqa[c]); }
            PIN(xa); PIN(wa);
        }
        loop_constants();
        STAMP(2);
        if (isprod && !XSYNC) {
            WG_BARRIER();
            if (__builtin_expect(rfl(*(LAS unsigned*)(smem + L_CNT1 + 2048)) != 0, 0)) WG_BARRIER();
            if (W_SLEEP) __builtin_amdgcn_s_sleep(W_SLEEP);
        }
        STAMP(3);
        asm volatile("s_waitcnt vmcnt(%c8)" : "+v"(wr[0]), "+v"(wr[1]), "+v"(wr[2]), "+v"(wr[3]), "+v"(wr[4]), "+v"(wr[5]), "+v"(wr[6]), "+v"(wr[7]) : "i"(8) : "memory");
#pragma unroll
        for (int m = 0; m < 8; ++m) {
            const unsigned a = (wa ^ ((m & 2) << 5)) + m * 1024;
            *(LAS u32x2*)(size_t)a = u32x2{wr[m].x, wr[m].y};
            *(LAS u32x2*)(size_t)(a ^ 8) = u32x2{wr[m].z, wr[m].w};
        }
        asm volatile("s_waitcnt vmcnt(0)" : "+v"(xr[0]), "+v"(xr[1]), "+v"(xr[2]), "+v"(xr[3]), "+v"(xr[4]), "+v"(xr[5]), "+v"(xr[6]), "+v"(xr[7]) :: "memory");
#pragma unroll
        for (int m = 0; m < 8; ++m) *(LAS u32x4*)(size_t)((xa ^ ((m & 3) << 6)) + m * 1024) = xr[m];
        STAMP(4);
        lds_arrive(smem + L_CNT + 80, l);
        lds_await(smem + L_CNT + 80, 4);
        STAMP(5);
        bf16x8 xo[2][4], fr[2][8];
        f32x4 qc[2][8];
        auto wq_frags = [&](int u, bf16x8 (&f)[8]) {
#pragma unroll
            for (int a = 0; a < 8; ++a) {
                LAS char* pa = (LAS char*)(size_t)aqa[a] + u * 16384;
                f[a] = cat(tr_read(pa), tr_read(pa + 1024));
            }
        };
#pragma unroll
        for (int u = 0; u < 4; ++u) { xo[0][u] = *(LAS bf16x8*)(size_t)xfa[u]; xo[1][u] = *(LAS bf16x8*)(size_t)(xfa[u] + 4096); }
        wq_frags(0, fr[0]);
        {
            const Srd4 srd_v = make_srd4(wv, 32768);
            const unsigned r0 = (j * 32 + g) << 8, o0 = (n ^ ((g & 1) | ((g & 2) << 1))) << 4;
            const unsigned e[8] = {r0, r0 + 1024, r0 + 2048, r0 + 3072, r0 + 4096, r0 + 5120, r0 + 6144, r0 + 7168};
            const unsigned base = lds0 + (j < 2 ? L_WV0 + j * 16384 : L_WV1 + (j - 2) * 8192);
            const u32x4 off = {o0, o0 ^ 128, o0, o0 ^ 128};
            dma_half(base, srd_v, e, off); dma_half(base + 4096, srd_v, e + 4, off);
        }
#pragma unroll
        for (int u = 0; u < 4; ++u) {
            if (u < 3) wq_frags(u + 1, fr[(u + 1) & 1]);
            FENCE();
#pragma unroll
            for (int a = 0; a < 8; ++a)
#pragma unroll
                for (int tt = 0; tt < 2; ++tt) qc[tt][a] = mfma(fr[u & 1][a], xo[tt][u], u ? qc[tt][a] : f32x4{0.f, 0.f, 0.f, 0.f});
            FENCE();
            if (!WVLATE && u == 1 && j < 2) { VMWAIT(0); lds_arrive(smem + L_CNT + 88, l); }
        }
        STAMP(6);
        if (isprod) lds_await(smem + L_CNT + 84, 4);
#pragma unroll
        for (int tt = 0; tt < 2; ++tt)
#pragma unroll
            for (int s = 0; s < 4; ++s) {
                const float k = -0.08838834764831845f * 1.4426950408889634f;
                const f32x4 c0 = qc[tt][2 * s] * k, c1 = qc[tt][2 * s + 1] * k;
                *(LAS u32x4*)(size_t)(ring - (tt ? 0 : 65536) + s * 1024 + l * 16) = u32x4{pk2(c0[0], c0[1]), pk2(c0[2], c0[3]), pk2(c1[0], c1[1]), pk2(c1[2], c1[3])};
            }
        clear_sums();
        if (!WVLATE) { lds_await(smem + L_CNT + 88, 2); wv_resident(); }
        VMWAIT(0);
        STAMP(7);
    }
    WG_BARRIER();
    STAMP(8);
    if (LPRIO >= 3) __builtin_amdgcn_s_setprio(3);
    if (LPRIO == 1 && w >= 4) __builtin_amdgcn_s_setprio(1);
    if (LPRIO == 2) { if (w >= 6) __builtin_amdgcn_s_setprio(3); else if (w >= 4) __builtin_amdgcn_s_setprio(2); else if (w >= 2) __builtin_amdgcn_s_setprio(1); }

    u32x16 qf;
    const u32x4 doff = {addr[12], addr[13], addr[14], addr[15]};
    LAS char* mylist = smem + L_LIST + w * (2 * SUBB);
    int gcur = G1, pos = 0;
    unsigned hmask = 0xFFFFFFFFu;
    auto batch = [&](auto first_) {
        constexpr bool first = decltype(first_)::value;
        auto entries = [&](int grp, unsigned (&e)[8]) {
#pragma unroll
            for (int m = 0; m < 8; ++m) e[m] = *(LAS unsigned*)(size_t)(lista + grp * 128 + m * 16);
        };
        unsigned cw = *(LAS unsigned*)(size_t)(first ? cnta : lds0 + L_CNT + (__builtin_amdgcn_mbcnt_lo(~0u, 0u) & 31) * 4), e0[8], e1[8];
        entries(0, e0);
        if constexpr (first) {
#pragma unroll
            for (int s = 0; s < 4; ++s) {
                const u32x4 v = *(LAS u32x4*)(size_t)(qa + s * 1024);
                qf[4 * s] = v.x; qf[4 * s + 1] = v.y; qf[4 * s + 2] = v.z; qf[4 * s + 3] = v.w;
            }
        }
        entries(1, e1);
        FENCE();
        int lo = __builtin_amdgcn_readlane(cw, 2 * w) >> 2, hi = __builtin_amdgcn_readlane(cw, 2 * w + 1) >> 2;
        if constexpr (first) {
            if (__builtin_amdgcn_readlane(cw, 16)) gcur = 0;
            if (__builtin_amdgcn_ballot_w64(cw > 4u * CAP2)) { gcur = -1; lo = 0; hi = 0; hmask = 0x7FFFFFFFu; }
        }
        const int c = lo + hi, G = (c + 31) >> 5;
        const int nlast = G > 2 ? (((c - 1) & 31) >> 2) + 1 : 8;
        auto join = [&]() {
            for (int k = 0; k < G * 32 - lo; k += 64) {
                const unsigned e = *(LAS unsigned*)(mylist + SUBB + (l + k) * 4);
                if (l + k < G * 32 - lo) *(LAS unsigned*)(mylist + (lo + l + k) * 4) = l + k < hi ? e : PAD;
            }
        };
        const bool early = lo >= 64;
        if (__builtin_expect(!early, 0)) { join(); entries(0, e0); entries(1, e1); }
        if constexpr (first) asm volatile("s_waitcnt lgkmcnt(0)" : "+{v[160:175]}"(qf) :: "memory");
        if (G > 0) dma_group(ring, srd_x, e0, doff);
        if constexpr (first) {
            if (WVLATE) {
                if (G > 1 && w < 6) dma_group(ring + 8192, srd_x, e1, doff);
                wv_resident();
                lds_arrive(smem + L_CNT + 92, l);
                if (w >= 6) { lds_await(smem + L_CNT + 92, 8); if (G > 1) dma_group(ring + 8192, srd_x, e1, doff); }
            } else if (w < 4) {
                if (G > 1) dma_group(ring + 8192, srd_x, e1, doff);
                wv_resident();
                lds_arrive(smem + L_CNT + 92, l);
            } else {
                if (w >= 6) lds_await(smem + L_CNT + 92, 4);
                if (G > 1) dma_group(ring + 8192, srd_x, e1, doff);
            }
        } else if (G > 1) dma_group(ring + 8192, srd_x, e1, doff);
        f32x16 cc;
#pragma unroll
        for (int m = 0; m < 8; ++m) {
            cc[m] = (e0[m] & 0xFF) == keyn ? 0.f : __uint_as_float(inf);
            cc[8 + m] = (e1[m] & 0xFF) == keyn ? 0.f : __uint_as_float(inf);
        }
        if (early) join();
        STAMP(9);
        if (G > 0) gather_loop(acc, qf, addr, u32x4{lista, keyn, inf, 0u}, cc, srd_x, ring, G, nlast);
    };
    batch(std::true_type{});
    while (__builtin_expect(gcur < G1, 0)) {
        unsigned lim = G1 * 32;
        if (gcur >= 0) {
            const u64 m0 = __builtin_amdgcn_ballot_w64(l >= gcur && (int)(*(LAS unsigned*)(smem + L_HDR + l * 4) & hmask) > 4 * CAP1);
            const u64 m1 = __builtin_amdgcn_ballot_w64(l + 64 >= gcur && (int)(*(LAS unsigned*)(smem + L_HDR + 256 + l * 4) & hmask) > 4 * CAP1);
            if (!(m0 | m1)) break;
            gcur = m0 ? __builtin_ctzll(m0) : 64 + __builtin_ctzll(m1);
            unsigned v; const unsigned o = WS_NOVF + gcur * 4;
            asm volatile("buffer_load_dword %0, %1, %2, 0 offen sc1\n\ts_waitcnt vmcnt(0)" : "=&v"(v) : "v"(o), "s"(srd_ws) : "memory");
            lim = rfl(v);
        }
        const unsigned i = pos + t;
        const bool cand = t < CAP2 && i < lim;
        unsigned u;
        { const unsigned o = cand ? (gcur < 0 ? WS_INBOX + b * (G1 * 128) : WS_OVF + gcur * CH * 4) + i * 4 : OOB;
          asm volatile("buffer_load_dword %0, %1, %2, 0 offen sc1\n\ts_waitcnt vmcnt(0)" : "=&v"(u) : "v"(o), "s"(srd_ws) : "memory"); }
        WG_BARRIER();
        if (t < 16) *(LAS unsigned*)(smem + L_CNT + t * 4) = 0;
        WG_BARRIER();
        if (cand) {
            unsigned e; bool v;
            if (gcur < 0) { e = (i & 3) ? u : u ^ mykey; v = ((i & 31) - 2) * 4 < *(LAS unsigned*)(smem + L_HDR + (i >> 5) * 4); }
            else { e = (((u >> 16) + BIAS) << 8) | ((u >> 8) & 0xFF); v = (u & 0xFF) == (unsigned)b; }
            if (v) *(LAS unsigned*)(smem + L_LIST + (e & 7) * (2 * SUBB) + lds_add(smem + L_CNT + (e & 7) * 8, 4)) = e;
        }
        pos += CAP2;
        if ((unsigned)pos >= lim) { ++gcur; pos = 0; }
        WG_BARRIER();
        batch(std::false_type{});
    }
    STAMP(10);

    {
        bf16x8 wl[2][8], ag[4];
#pragma unroll
        for (int u = 0; u < 2; ++u)
#pragma unroll
            for (int a = 0; a < 8; ++a) {
                LAS char* pa = smem + L_WV1 + (cv ^ ((a >> 1) << 6) ^ ((a & 1) << 3)) + u * 8192;
                wl[u][a] = cat(tr_read(pa), tr_read(pa + 4096));
            }
#pragma unroll
        for (int u = 0; u < 4; ++u) {
            const u32x4 v = {pk2(acc[8 * u], acc[8 * u + 1]), pk2(acc[8 * u + 2], acc[8 * u + 3]),
                             pk2(acc[8 * u + 4], acc[8 * u + 5]), pk2(acc[8 * u + 6], acc[8 * u + 7])};
            ag[u] = __builtin_bit_cast(bf16x8, v);
        }
        FENCE();
        f32x4 oc[8];
#pragma unroll
        for (int a = 0; a < 8; ++a) oc[a] = mfma(__builtin_bit_cast(bf16x8, f32x4{wva[4 * a], wva[4 * a + 1], wva[4 * a + 2], wva[4 * a + 3]}), ag[0], f32x4{0.f, 0.f, 0.f, 0.f});
#pragma unroll
        for (int a = 0; a < 8; ++a) oc[a] = mfma(__builtin_bit_cast(bf16x8, f32x4{wvb[4 * a], wvb[4 * a + 1], wvb[4 * a + 2], wvb[4 * a + 3]}), ag[1], oc[a]);
#pragma unroll
        for (int a = 0; a < 8; ++a) oc[a] = mfma(wl[0][a], ag[2], oc[a]);
        FENCE();
        const unsigned ow = ring + n * 256 + ((g ^ n) << 4) + ((g & 1) << 3);
#pragma unroll
        for (int a = 0; a < 8; ++a) oc[a] = mfma(wl[1][a], ag[3], oc[a]);
#pragma unroll
        for (int a = 0; a < 8; ++a)
            *(LAS u32x2*)(size_t)(ow ^ ((a >> 1) << 6) ^ ((a & 1) << 3)) = u32x2{pk2(oc[a][0], oc[a][1]), pk2(oc[a][2], oc[a][3])};
        const auto rs_o = __builtin_amdgcn_make_buffer_rsrc(out, 0, xbytes, 0x00020000);
#pragma unroll
        for (int m = 0; m < 4; ++m) {
            const u32x4 v = *(LAS u32x4*)(size_t)(ring + m * 1024 + l * 16);
            __builtin_amdgcn_raw_buffer_store_b128(v, rs_o, node_row(w, m) + ((n ^ g ^ (4 * m)) << 4), 0, OUT_AUX);
        }
    }
    if (w == 0) { const unsigned o = l == 0 ? WS_KEY + b * 4 : OOB; ST1(mykey, srd_ws, o, " sc1"); }
    STAMP(11);
    asm volatile("s_waitcnt vmcnt(0)" ::: "memory");
    STAMP(12);
    __syncthreads();
    STAMP(13);
    {
        const unsigned long long tgt = st_[13] + (unsigned long long)PROBE_M * (st_[PROBE_B] - st_[PROBE_A]);
        if (threadIdx.x < 64) { int guard = 0; while (wall_clock64() < tgt && ++guard < 200000) __builtin_amdgcn_s_sleep(2); }
    }
}

extern "C" void kernel_launch(void* const* d_in, const int* in_sizes, int n_in,
                              void* d_out, int out_size, void* d_ws, size_t ws_size,
                              hipStream_t stream) {
    const int N = in_sizes[0] / 128, E = in_sizes[1];
    k_graph<<<NB, 512, 0, stream>>>((const unsigned short*)d_in[0], (const int*)d_in[1], (const int*)d_in[2],
                                    (const unsigned short*)d_in[3], (const unsigned short*)d_in[4],
                                    (unsigned*)d_ws, (unsigned short*)d_out, N, E);
}
```

```cpp
#define PFC_KB 768
#include <hip/hip_runtime.h>
#include <hip/hip_bf16.h>
#include <stdint.h>
#include <type_traits>
#define ORD 1
#define SKIP 0
#define STR2(x) #x
#define STR(x) STR2(x)

typedef __attribute__((ext_vector_type(8))) __bf16 bf16x8;
typedef __attribute__((ext_vector_type(4))) __bf16 bf16x4;
typedef __attribute__((ext_vector_type(2))) __bf16 bf16x2;
typedef __attribute__((ext_vector_type(4))) float f32x4;
typedef __attribute__((ext_vector_type(2))) float f32x2;
typedef __attribute__((ext_vector_type(4))) unsigned u32x4;
typedef __attribute__((ext_vector_type(2))) unsigned u32x2;
typedef __attribute__((ext_vector_type(32))) float f32x32;
typedef __attribute__((ext_vector_type(16))) float f32x16;
typedef __attribute__((ext_vector_type(16))) unsigned u32x16;
typedef __attribute__((ext_vector_type(12))) unsigned u32x12;
typedef __attribute__((ext_vector_type(8))) unsigned u32x8;
typedef unsigned long long u64;
#define LAS __attribute__((address_space(3)))

constexpr int NB = 256;
constexpr int G1 = 128;
constexpr int CH = 3136;
constexpr int CAP1 = 30;
constexpr int CAP2 = 192;
constexpr int SUBB = 784;
#define POLL_AT 3
#define OUT_AUX 16
#define POLL_T 350
#define LPRIO 3
#define PKADD 1
#define CODEPF 1
#define CODE_BYTES 21504
#define PARTNER 1
#define XSYNC 1
#define WVLATE 1
#define PFA_KB 0
#define PFB_KB 0
#define FASTIN 1
#define W_SLEEP 3
#define PRIO 1
#define SPLIT 16384
constexpr unsigned KEY_STEP = 0x9E3779B1u;
constexpr unsigned WS_KEY = 0;
constexpr unsigned WS_NOVF = 2048;
constexpr unsigned WS_INBOX = 4096;
constexpr unsigned WS_OVF = WS_INBOX + NB * G1 * 128;
constexpr unsigned WS_BYTES = WS_OVF + G1 * CH * 4;
constexpr unsigned OOB = 0x80000000u;
constexpr unsigned BIAS = 32768 - SPLIT;
constexpr unsigned PAD = 0x00FFFFFFu;

constexpr int L_CNT = 0;
constexpr int L_HDR = 128;
constexpr int L_LIST = 768;
constexpr int L_RING = L_LIST + 16 * SUBB;
constexpr int L_IMG = L_RING;
constexpr int L_CNT1 = L_IMG + NB * 144;
constexpr int L_WQ = L_RING + 2 * 16384 + 8192;
constexpr int L_WV0 = L_RING + 6 * 16384 + 8192;
constexpr int L_WV1 = L_RING + 131072;
constexpr int L_DUMP = L_WV1 + 16384;
constexpr int L_TOTAL = L_DUMP + 2048;
static_assert(L_RING % 256 == 0 && L_TOTAL <= 163840, "LDS map");

#define STAMP_DECL
#define STAMP(k)
#define STAMP_FLUSH(base, k0, k1)

__device__ __forceinline__ u32x4 make_srd(const void* p, unsigned bytes, unsigned back = 0) {
    const u64 a = (u64)p - back;
    u32x4 s; s.x = (unsigned)a; s.y = (unsigned)(a >> 32); s.z = bytes + back; s.w = 0x00020000u; return s;
}
#define LD4(dst, srd, off, FL) asm volatile("buffer_load_dwordx4 %0, %1, %2, 0 offen" FL : "=v"(dst) : "v"(off), "s"(srd) : "memory")
#define LD4S(dst, srd, off, so, FL) asm volatile("buffer_load_dwordx4 %0, %1, %2, %3 offen" FL : "=v"(dst) : "v"(off), "s"(srd), "s"(so) : "memory")
#define LD1(dst, srd, off, FL) asm volatile("buffer_load_dword %0, %1, %2, 0 offen" FL : "=v"(dst) : "v"(off), "s"(srd) : "memory")
#define ST1(val, srd, off, FL) asm volatile("buffer_store_dword %0, %1, %2, 0 offen" FL :: "v"(val), "v"(off), "s"(srd) : "memory")
#define PIN(x) asm volatile("" : "+v"(x))
#define VMWAIT(n) asm volatile("s_waitcnt vmcnt(" #n ")" ::: "memory")
#define FENCE() __builtin_amdgcn_sched_barrier(0)
#define WG_BARRIER() do { asm volatile("s_waitcnt lgkmcnt(0)" ::: "memory"); __builtin_amdgcn_s_barrier(); asm volatile("" ::: "memory"); } while (0)

struct Srd4 { u32x4 s0, s1, s2, s3; };
__device__ __forceinline__ Srd4 make_srd4(const void* p, unsigned bytes, unsigned back = 0) {
    return Srd4{make_srd(p, bytes, back), make_srd(p, bytes, back + 1024), make_srd(p, bytes, back + 2048), make_srd(p, bytes, back + 3072)};
}

__device__ __forceinline__ void set_m0(unsigned v) { asm volatile("s_mov_b32 m0, %0" :: "s"(v) : "memory"); }
template <int R> __device__ __forceinline__ void dma_rows(const Srd4& d, unsigned e, unsigned off) {
    unsigned tmp, mask = 0x00FFFF00u;
    asm volatile("v_and_or_b32 %0, %1, %2, %3\n\tbuffer_load_dwordx4 %0, %4, 0 offen offset:%c5 lds"
                 : "=&v"(tmp) : "v"(e), "s"(mask), "v"(off), "s"(R == 0 ? d.s0 : R == 1 ? d.s1 : R == 2 ? d.s2 : d.s3), "i"(R * 1024) : "memory");
}
__device__ __forceinline__ void dma_half(unsigned ldsbase, const Srd4& d, const unsigned* e, const u32x4& off) {
    set_m0(ldsbase);
    dma_rows<0>(d, e[0], off.x); dma_rows<1>(d, e[1], off.y); dma_rows<2>(d, e[2], off.z); dma_rows<3>(d, e[3], off.w);
}
__device__ __forceinline__ void dma_group(unsigned ldsbase, const Srd4& d, const unsigned (&e)[8], const u32x4& off) {
    asm volatile("s_waitcnt lgkmcnt(0)" ::: "memory");
    dma_half(ldsbase, d, e, off); dma_half(ldsbase + 4096, d, e + 4, off);
}

__device__ __forceinline__ void gather_loop(f32x32& acc, const u32x16& qf, const u32x16& addr, u32x4 misc, f32x16 cc,
                                            const Srd4& d, unsigned ring, int G, int nlast) {
    f32x32 t0, t1; u32x12 t2; int tmp, pr; u64 sm; unsigned mask = 0x00FFFF00u;
    asm volatile(
        ".macro GWAIT k\n\ts_cmp_gt_i32 %[t], \\k\n\ts_cbranch_scc1 1f\n\ts_waitcnt vmcnt(\\k)\n\t.endm\n\t"
        ".macro GROWS so\n\t.irp s, 0, 1, 2, 3\n\t"
        "ds_read_b128 v[64+8*\\s:67+8*\\s], v[176+\\s] offset:\\so\n\t"
        "ds_read_b128 v[68+8*\\s:71+8*\\s], v[176+\\s] offset:\\so+4096\n\t.endr\n\t.endm\n\t"
        ".macro GTR c, so\n\t"
        "ds_read_b64_tr_b16 v[96+4*\\c:97+4*\\c], v[180+\\c] offset:\\so\n\t"
        "ds_read_b64_tr_b16 v[98+4*\\c:99+4*\\c], v[180+\\c] offset:\\so+4096\n\t.endm\n\t"
        ".macro GS s, c\n\t"
        "v_mfma_f32_16x16x32_bf16 v[\\c:\\c+3], v[64+8*\\s:67+8*\\s], v[160+4*\\s:163+4*\\s], v[\\c:\\c+3]\n\t"
        "v_mfma_f32_16x16x32_bf16 v[\\c+4:\\c+7], v[68+8*\\s:71+8*\\s], v[160+4*\\s:163+4*\\s], v[\\c+4:\\c+7]\n\t.endm\n\t"
        ".macro GLOAD m, r, srd\n\t"
        "v_and_or_b32 v195, v[212+\\m], %[mask], v[188+\\r]\n\t"
        "buffer_load_dwordx4 v195, \\srd, 0 offen offset:1024*\\r lds\n\t.endm\n\t"
        ".macro GPART m, r, srd\n\ts_cmp_le_i32 %[nl], \\m\n\ts_cbranch_scc1 4f\n\tGLOAD \\m, \\r, \\srd\n\t.endm\n\t"
        ".macro GSUM m\n\tv_mfma_f32_16x16x32_bf16 v[128+4*(\\m):131+4*(\\m)], v[96+4*(\\m):99+4*(\\m)], v[220:223], v[128+4*(\\m):131+4*(\\m)]\n\t.endm\n\t"
        ".macro GCMP m, d\n\tv_cmp_ne_u32_sdwa \\d, v[212+\\m], v193 src0_sel:BYTE_0 src1_sel:DWORD\n\t.endm\n\t"
        ".macro GPAIR m, c\n\t"
        "GSUM \\m\n\tGCMP \\m+1, %[sm]\n\tv_cndmask_b32_e32 v[\\c+\\m], 0, v194, vcc\n\t"
        "GSUM \\m+1\n\t.if \\m < 6\n\tGCMP \\m+2, vcc\n\t.else\n\ts_cmp_gt_i32 %[rem], 0\n\t.endif\n\t"
        "v_cndmask_b32_e64 v[\\c+\\m+1], 0, v194, %[sm]\n\t.endm\n\t"
        ".macro GSTEP so, c, eo\n\t"
        "GTR 0, \\so\n\t"
        "s_waitcnt lgkmcnt(2)\n\tGS 0, \\c\n\t"
        "GTR 1, \\so\n\tGTR 2, \\so\n\tGS 1, \\c\n\t"
        "GTR 3, \\so\n\tGTR 4, \\so\n\tGS 2, \\c\n\t"
        "GTR 5, \\so\n\tGTR 6, \\so\n\tGS 3, \\c\n\t"
        "GTR 7, \\so\n\t"
        "ds_read2_b32 v[212:213], v192 offset0:\\eo offset1:\\eo+4\n\t"
        "ds_read2_b32 v[214:215], v192 offset0:\\eo+8 offset1:\\eo+12\n\t"
        "ds_read2_b32 v[216:217], v192 offset0:\\eo+16 offset1:\\eo+20\n\t"
        "ds_read2_b32 v[218:219], v192 offset0:\\eo+24 offset1:\\eo+28\n\t"
        "s_add_u32 m0, %[ring], \\so\n\t"
        "s_cmp_gt_i32 %[rem], 3\n\t"
        ".irp k, 0, 1, 2, 3, 4, 5, 6, 7\n\tv_exp_f32_e32 v[\\c+\\k], v[\\c+\\k]\n\t.endr\n\t"
        "s_waitcnt lgkmcnt(0)\n\t"
        "s_cbranch_scc0 2f\n\t"
        "GLOAD 0, 0, %[s0]\n\tGLOAD 1, 1, %[s1]\n\tGLOAD 2, 2, %[s2]\n\tGLOAD 3, 3, %[s3]\n\t"
        "s_add_u32 m0, m0, 0x1000\n\t"
        "GLOAD 4, 0, %[s0]\n\tGLOAD 5, 1, %[s1]\n\tGLOAD 6, 2, %[s2]\n\tGLOAD 7, 3, %[s3]\n\t"
        "GSIG \\c\n\t"
        "s_waitcnt vmcnt(8)\n\t"
        "s_branch 1f\n\t"
        "2:\n\t"
        "s_mov_b32 %[t], 0\n\t"
        "s_cmp_eq_u32 %[rem], 3\n\ts_cbranch_scc0 4f\n\t"
        "s_mov_b32 %[t], %[nl]\n\t"
        "GLOAD 0, 0, %[s0]\n\tGPART 1, 1, %[s1]\n\tGPART 2, 2, %[s2]\n\tGPART 3, 3, %[s3]\n\t"
        "s_add_u32 m0, m0, 0x1000\n\t"
        "GPART 4, 0, %[s0]\n\tGPART 5, 1, %[s1]\n\tGPART 6, 2, %[s2]\n\tGPART 7, 3, %[s3]\n\t"
        "4:\n\t"
        "GSIG \\c\n\t"
        "s_waitcnt vmcnt(8)\n\t"
        "GWAIT 7\n\tGWAIT 6\n\tGWAIT 5\n\tGWAIT 4\n\tGWAIT 3\n\tGWAIT 2\n\tGWAIT 1\n\tGWAIT 0\n\t"
        "1:\n\t"
        "GROWS 8192-\\so\n\t"
        "GCMP 0, vcc\n\t"
        "s_add_i32 %[rem], %[rem], -1\n\t"
        "GPAIR 0, \\c\n\tGPAIR 2, \\c\n\tGPAIR 4, \\c\n\tGPAIR 6, \\c\n\t"
        ".endm\n\t"
        ".macro GSIG c\n\t"
        ".if " STR(PKADD) "\n\t.irp k, 0, 2, 4, 6\n\tv_pk_add_f32 v[\\c+\\k:\\c+\\k+1], v[\\c+\\k:\\c+\\k+1], 1.0 op_sel_hi:[1,0]\n\t.endr\n\t.else\n\t"
        ".irp k, 0, 1, 2, 3, 4, 5, 6, 7\n\tv_add_f32_e32 v[\\c+\\k], 1.0, v[\\c+\\k]\n\t.endr\n\t.endif\n\t"
        ".irp k, 0, 1, 2, 3, 4, 5, 6, 7\n\tv_rcp_f32_e32 v[\\c+\\k], v[\\c+\\k]\n\t.endr\n\t"
        ".irp k, 0, 1, 2, 3\n\tv_cvt_pk_bf16_f32 v[220+\\k], v[\\c+2*\\k], v[\\c+2*\\k+1]\n\t.endr\n\t.endm\n\t"
        "s_waitcnt vmcnt(8)\n\t"
        "s_cmp_gt_i32 %[rem], 1\n\ts_cbranch_scc1 1f\n\ts_waitcnt vmcnt(0)\n\t1:\n\t"
        "GROWS 0\n\t"
        ".if " STR(LPRIO) " >= 3\n\ts_mov_b32 %[pr], 0\n\ts_setprio 2\n\t.endif\n\t"
        "0:\n\t"
        ".if " STR(LPRIO) " == 3\n\t"
        "s_cmp_eq_u32 %[pr], 1\n\ts_cbranch_scc0 5f\n\ts_setprio 1\n\t5:\n\t"
        "s_cmp_eq_u32 %[pr], 2\n\ts_cbranch_scc0 5f\n\ts_setprio 0\n\t5:\n\t"
        "s_add_u32 %[pr], %[pr], 1\n\t.endif\n\t"
        "GSTEP 0, 196, 64\n\t"
        "s_cbranch_scc0 9f\n\t"
        "GSTEP 8192, 204, 96\n\t"
        "v_add_u32_e32 v192, 0x100, v192\n\t"
        "s_cbranch_scc1 0b\n\t"
        "9:\n\t"
        "s_waitcnt lgkmcnt(0)\n\ts_nop 6\n\t"
        ".purgem GWAIT\n\t.purgem GROWS\n\t.purgem GTR\n\t.purgem GS\n\t.purgem GLOAD\n\t.purgem GPART\n\t.purgem GSUM\n\t.purgem GCMP\n\t.purgem GPAIR\n\t.purgem GSTEP\n\t.purgem GSIG"
        : "+{v[128:159]}"(acc), "+{v[192:195]}"(misc), "+{v[196:211]}"(cc), "=&{v[64:95]}"(t0), "=&{v[96:127]}"(t1), "=&{v[212:223]}"(t2),
          [rem] "+s"(G), [t] "=&s"(tmp), [pr] "=&s"(pr), [sm] "=&s"(sm)
        : "{v[160:175]}"(qf), "{v[176:191]}"(addr), [s0] "s"(d.s0), [s1] "s"(d.s1), [s2] "s"(d.s2), [s3] "s"(d.s3),
          [mask] "s"(mask), [ring] "s"(ring), [nl] "s"(nlast)
        : "vcc", "scc", "memory");
}

__device__ __forceinline__ u64 rank_edges(u32x16& ed, u32x16& es, u32x16& rk, u64 m0, u64 m1, u64 m2, u64 m3, unsigned keya) {
    u32x16 b4; u64 over; unsigned sel = 0x01050401u, c36 = 36;
    asm volatile(
        ".macro RADD k\n\tv_lshlrev_b32_sdwa v[96+\\k], 2, v[64+\\k] dst_sel:DWORD dst_unused:UNUSED_PAD src0_sel:DWORD src1_sel:BYTE_0\n\t"
        "ds_add_rtn_u32 v[112+\\k], v[96+\\k], v111 offset:%c[cnt]\n\t.endm\n\t"
        ".macro RENT k\n\tv_add_u32_e32 v[80+\\k], %c[bias], v[80+\\k]\n\tv_perm_b32 v[80+\\k], v[80+\\k], v[64+\\k], %[sel]\n\t.endm\n\t"
        ".macro RPUT k\n\tv_min_u32_e32 v110, %c[cap], v[112+\\k]\n\tv_mad_u32_u24 v110, v[96+\\k], %[c36], v110\n\t"
        "ds_write_b32 v110, v[80+\\k] offset:%c[img]\n\t.endm\n\t"
        "v_mov_b32_e32 v111, 4\n\t"
        ".irp k, 0, 2, 4, 6, 8, 10, 12\n\tv_mov_b64_e32 v[112+\\k:113+\\k], 0\n\t.endr\n\t"
        "s_mov_b64 exec, %[m0]\n\ts_waitcnt vmcnt(9)\n\tRADD 0\n\tRADD 1\n\tRADD 2\n\tRADD 3\n\t"
        "s_mov_b64 exec, %[m1]\n\ts_waitcnt vmcnt(8)\n\tRADD 4\n\tRADD 5\n\tRADD 6\n\tRADD 7\n\t"
        "s_mov_b64 exec, %[m2]\n\ts_waitcnt vmcnt(7)\n\tRADD 8\n\tRADD 9\n\tRADD 10\n\tRADD 11\n\t"
        "s_mov_b64 exec, %[m3]\n\ts_waitcnt vmcnt(6)\n\tRADD 12\n\t"
        "s_mov_b64 exec, %[m0]\n\ts_waitcnt vmcnt(5)\n\tRENT 0\n\tRENT 1\n\tRENT 2\n\tRENT 3\n\t"
        "s_waitcnt lgkmcnt(9)\n\tRPUT 0\n\tRPUT 1\n\tRPUT 2\n\tRPUT 3\n\t"
        "s_mov_b64 exec, %[m1]\n\ts_waitcnt vmcnt(4)\n\tRENT 4\n\tRENT 5\n\tRENT 6\n\tRENT 7\n\t"
        "s_waitcnt lgkmcnt(9)\n\tRPUT 4\n\tRPUT 5\n\tRPUT 6\n\tRPUT 7\n\t"
        "s_mov_b64 exec, %[m2]\n\ts_waitcnt vmcnt(3)\n\tRENT 8\n\tRENT 9\n\tRENT 10\n\tRENT 11\n\t"
        "s_waitcnt lgkmcnt(9)\n\tRPUT 8\n\tRPUT 9\n\tRPUT 10\n\tRPUT 11\n\t"
        "s_mov_b64 exec, %[m3]\n\ts_waitcnt vmcnt(0)\n\tRENT 12\n\t"
        "s_waitcnt lgkmcnt(12)\n\tRPUT 12\n\t"
        "s_mov_b64 exec, -1\n\t"
        "v_add_u32_e32 v93, %c[step], v93\n\t"
        "v_max3_u32 v110, v112, v113, v114\n\tv_max3_u32 v109, v115, v116, v117\n\t"
        "ds_write_b32 %[keya], v93 offset:%c[cnt]+1024\n\t"
        "v_max3_u32 v110, v110, v118, v119\n\tv_max3_u32 v109, v109, v120, v121\n\t"
        "v_max3_u32 v110, v110, v122, v123\n\tv_max3_u32 v110, v110, v109, v124\n\t"
        "v_cmp_le_u32_e32 vcc, %c[cap], v110\n\ts_mov_b64 %[over], vcc\n\t"
        ".purgem RADD\n\t.purgem RENT\n\t.purgem RPUT"
        : "+{v[64:79]}"(ed), "+{v[80:95]}"(es), "=&{v[96:111]}"(b4), "=&{v[112:127]}"(rk), [over] "=&s"(over)
        : [m0] "s"(m0), [m1] "s"(m1), [m2] "s"(m2), [m3] "s"(m3), [keya] "v"(keya), [sel] "s"(sel), [c36] "s"(c36),
          [cnt] "i"(L_CNT1), [img] "i"(L_IMG + 8), [bias] "i"(BIAS), [cap] "i"(4 * CAP1), [step] "i"(KEY_STEP)
        : "vcc", "memory");
    return over;
}
__device__ __forceinline__ unsigned read_image(f32x32& img, u32x16& meta, u32x16& sums, unsigned ima, unsigned cna) {
    unsigned no;
    asm volatile(
        "v_mov_b32_e32 v121, 0\n\tds_read_b32 v120, v121 offset:%c[cnt]+2048\n\t"
        ".irp k, 0, 1, 2, 3, 4, 5, 6, 7\n\tds_read_b128 v[64+4*\\k:67+4*\\k], %[ima] offset:4608*\\k\n\t.endr\n\t"
        ".irp k, 0, 1, 2, 3\n\tds_read2_b32 v[96+2*\\k:97+2*\\k], %[cna] offset0:64*\\k offset1:64*\\k+32\n\t.endr\n\t"
        "s_waitcnt lgkmcnt(11)\n\tv_readfirstlane_b32 %[no], v120\n\t"
        "v_add_u32_e32 v121, 0x400, %[cna]\n\t"
        ".irp k, 0, 1, 2, 3\n\tds_read2_b32 v[104+2*\\k:105+2*\\k], v121 offset0:64*\\k offset1:64*\\k+32\n\t.endr\n\t"
        "s_waitcnt lgkmcnt(12)\n\t"
        ".irp k, 0, 1, 2, 3\n\tv_add3_u32 v[112+\\k], v[64+4*\\k], v[65+4*\\k], v[66+4*\\k]\n\tv_add_u32_e32 v[112+\\k], v[112+\\k], v[67+4*\\k]\n\t.endr\n\t"
        "s_waitcnt lgkmcnt(8)\n\t"
        ".irp k, 4, 5, 6, 7\n\tv_add3_u32 v[112+\\k], v[64+4*\\k], v[65+4*\\k], v[66+4*\\k]\n\tv_add_u32_e32 v[112+\\k], v[112+\\k], v[67+4*\\k]\n\t.endr\n\t"
        ".irp k, 0, 1, 2, 3, 4, 5, 6, 7\n\tv_add_u32_dpp v[112+\\k], v[112+\\k], v[112+\\k] quad_perm:[1,0,3,2] row_mask:0xf bank_mask:0xf\n\t.endr\n\t"
        ".irp k, 0, 1, 2, 3, 4, 5, 6, 7\n\tv_add_u32_dpp v[112+\\k], v[112+\\k], v[112+\\k] quad_perm:[2,3,0,1] row_mask:0xf bank_mask:0xf\n\t.endr\n\t"
        ".irp k, 0, 1, 2, 3, 4, 5, 6, 7\n\tv_add_u32_dpp v[112+\\k], v[112+\\k], v[112+\\k] row_half_mirror row_mask:0xf bank_mask:0xf\n\t.endr\n\t"
        "s_waitcnt lgkmcnt(0)"
        : "=&{v[64:95]}"(img), "=&{v[96:111]}"(meta), "=&{v[112:127]}"(sums), [no] "=&s"(no)
        : [ima] "v"(ima), [cna] "v"(cna), [cnt] "i"(L_CNT1) : "memory");
    return no;
}
__device__ __forceinline__ void publish_image(f32x32 img, u32x16 meta, u32x16 sums, const u32x4& srd, unsigned ob) {
    u64 pc0 = 0x0101010101010101ull; unsigned so;
    asm volatile(
        ".macro PUB k\n\ts_mov_b64 exec, %[pc0]\n\tv_mov_b32_e32 v[64+4*\\k], v[96+\\k]\n\tv_mov_b32_e32 v[65+4*\\k], v[112+\\k]\n\t"
        "s_mov_b64 exec, -1\n\tv_xor_b32_e32 v[64+4*\\k], v[64+4*\\k], v[104+\\k]\n\t"
        "buffer_store_dwordx4 v[64+4*\\k:67+4*\\k], %[ob], %[srd], %[so] offen sc1\n\ts_add_u32 %[so], %[so], %c[step]\n\t.endm\n\t"
        "s_mov_b32 %[so], 0\n\t"
        ".irp k, 0, 1, 2, 3, 4, 5, 6, 7\n\tPUB \\k\n\t.endr\n\t.purgem PUB"
        : "+{v[64:95]}"(img), [so] "=&s"(so)
        : "{v[96:111]}"(meta), "{v[112:127]}"(sums), [srd] "s"(srd), [ob] "v"(ob), [pc0] "s"(pc0), [step] "i"(32 * G1 * 128) : "scc", "memory");
}

__device__ __forceinline__ void inbox_request(u32x16& raw, const u32x4& srd, unsigned ib) {
    unsigned o1 = 0x1000, o2 = 0x2000, o3 = 0x3000;
    asm volatile("buffer_load_dwordx4 v[64:67], %1, %2, 0 offen sc1\n\tbuffer_load_dwordx4 v[68:71], %1, %2, %3 offen sc1\n\t"
                 "buffer_load_dwordx4 v[72:75], %1, %2, %4 offen sc1\n\tbuffer_load_dwordx4 v[76:79], %1, %2, %5 offen sc1"
                 : "=&{v[64:79]}"(raw) : "v"(ib), "s"(srd), "s"(o1), "s"(o2), "s"(o3) : "memory");
}
__device__ __forceinline__ unsigned inbox_stage(u32x16 raw, const u32x4& srd, unsigned ib, unsigned key, int i4, unsigned hdra) {
    u32x16 t0; f32x32 t2; unsigned four = 4; u64 m0, m1, m2, m3; unsigned todo, fresh, spin, o1, o2, o3, over;
    u64 pc0 = 0x0101010101010101ull;
    asm volatile(
        ".macro IVAL j, so\n\t"
        "s_bitcmp1_b32 %[todo], \\j\n\ts_cbranch_scc0 2f\n\t"
        "v_xor_b32_e32 v[64+4*\\j], %[key], v[64+4*\\j]\n\t"
        "v_sub_u32_e32 v97, 0, v[65+4*\\j]\n\t"
        "v_add_u32_e32 v96, v[64+4*\\j], v[65+4*\\j]\n\t"
        "v_mov_b32_dpp v[80+\\j], v[64+4*\\j] quad_perm:[0,0,0,0] row_mask:0xf bank_mask:0xf\n\t"
        "v_cndmask_b32_e64 v96, v96, v97, %[pc0]\n\t"
        "s_nop 0\n\t"
        "v_mov_b32_dpp v[80+\\j], v[80+\\j] row_shr:4 row_mask:0xf bank_mask:0xa\n\t"
        "v_cmp_gt_i32_e32 vcc, v[80+\\j], v92\n\t"
        "v_add3_u32 v96, v96, v[66+4*\\j], v[67+4*\\j]\n\t"
        "v_lshrrev_b32_e32 v97, 14, v[80+\\j]\n\t"
        "v_cndmask_b32_e32 v96, 0, v96, vcc\n\t"
        "s_nop 1\n\tv_add_u32_dpp v96, v96, v96 quad_perm:[1,0,3,2] row_mask:0xf bank_mask:0xf\n\t"
        "s_nop 1\n\tv_add_u32_dpp v96, v96, v96 quad_perm:[2,3,0,1] row_mask:0xf bank_mask:0xf\n\t"
        "s_nop 1\n\tv_add_u32_dpp v96, v96, v96 row_half_mirror row_mask:0xf bank_mask:0xf\n\t"
        "v_or_b32_e32 v96, v96, v97\n\t"
        "v_cmp_ne_u32_e32 vcc, 0, v96\n\t"
        "s_cbranch_vccz 1f\n\t"
        "buffer_load_dwordx4 v[64+4*\\j:67+4*\\j], %[ib], %[srd], \\so offen sc1\n\t"
        "s_branch 2f\n\t"
        "1:\n\ts_bitset1_b32 %[fresh], \\j\n\t"
        "2:\n\t.endm\n\t"
        ".macro IRANK j\n\t"
        "s_bitcmp1_b32 %[fresh], \\j\n\ts_cbranch_scc0 2f\n\t"
        "v_cmp_lt_u32_e64 %[m0], v92, v[80+\\j]\n\tv_cmp_lt_u32_e64 %[m1], v93, v[80+\\j]\n\t"
        "v_cmp_lt_u32_e64 %[m2], v94, v[80+\\j]\n\tv_cmp_lt_u32_e64 %[m3], v95, v[80+\\j]\n\t"
        ".irp k, 0, 1, 2, 3\n\tv_lshrrev_b32_e32 v[84+\\k], 21, v[64+4*\\j+\\k]\n\t.endr\n\t"
        ".irp k, 0, 1, 2, 3\n\tv_and_b32_e32 v[84+\\k], 60, v[84+\\k]\n\t.endr\n\t"
        "v_cmp_lt_u32_e32 vcc, %c[cap], v[80+\\j]\n\t"
        "s_mov_b64 exec, %[m0]\n\tds_add_rtn_u32 v88, v84, %[four] offset:%c[cnt]\n\t"
        "s_mov_b64 exec, %[m1]\n\tds_add_rtn_u32 v89, v85, %[four] offset:%c[cnt]\n\t"
        "s_mov_b64 exec, %[m2]\n\tds_add_rtn_u32 v90, v86, %[four] offset:%c[cnt]\n\t"
        "s_mov_b64 exec, %[m3]\n\tds_add_rtn_u32 v91, v87, %[four] offset:%c[cnt]\n\t"
        "s_mov_b64 exec, %[pc0]\n\tds_write_b32 %[hdra], v[80+\\j] offset:128*\\j\n\t"
        "s_mov_b64 exec, -1\n\t"
        "s_cbranch_vccz 1f\n\t"
        "s_mov_b32 %[over], 1\n\t"
        "1:\n\ts_waitcnt lgkmcnt(1)\n\t"
        ".irp k, 0, 1, 2, 3\n\tv_min_u32_e32 v[88+\\k], %c[sub]-4, v[88+\\k]\n\t.endr\n\t"
        ".irp k, 0, 1, 2, 3\n\tv_mad_u32_u24 v[88+\\k], v[84+\\k], %[sub4], v[88+\\k]\n\t.endr\n\t"
        "s_mov_b64 exec, %[m0]\n\tds_write_b32 v88, v[64+4*\\j] offset:%c[list]\n\t"
        "s_mov_b64 exec, %[m1]\n\tds_write_b32 v89, v[65+4*\\j] offset:%c[list]\n\t"
        "s_mov_b64 exec, %[m2]\n\tds_write_b32 v90, v[66+4*\\j] offset:%c[list]\n\t"
        "s_mov_b64 exec, %[m3]\n\tds_write_b32 v91, v[67+4*\\j] offset:%c[list]\n\t"
        "s_mov_b64 exec, -1\n\t"
        "2:\n\t.endm\n\t"
        ".macro FADD j\n\t"
        ".irp k, 0, 1, 2, 3\n\tv_lshrrev_b32_e32 v[96+4*\\j+\\k], 21, v[64+4*\\j+\\k]\n\t.endr\n\t"
        ".irp k, 0, 1, 2, 3\n\tv_and_b32_e32 v[96+4*\\j+\\k], 60, v[96+4*\\j+\\k]\n\t.endr\n\t"
        "v_cmpx_lt_u32_e64 s[72+8*\\j:73+8*\\j], v94, v[80+\\j]\n\tds_add_rtn_u32 v[114+4*\\j], v[98+4*\\j], %[four] offset:%c[cnt]\n\t"
        "v_cmpx_lt_u32_e64 s[74+8*\\j:75+8*\\j], v95, v[80+\\j]\n\tds_add_rtn_u32 v[115+4*\\j], v[99+4*\\j], %[four] offset:%c[cnt]\n\t"
        "s_mov_b64 exec, -1\n\t"
        "v_cmpx_lt_u32_e64 s[68+8*\\j:69+8*\\j], v92, v[80+\\j]\n\tds_add_rtn_u32 v[112+4*\\j], v[96+4*\\j], %[four] offset:%c[cnt]\n\t"
        "v_cmpx_lt_u32_e64 s[70+8*\\j:71+8*\\j], v93, v[80+\\j]\n\tds_add_rtn_u32 v[113+4*\\j], v[97+4*\\j], %[four] offset:%c[cnt]\n\t"
        "s_mov_b64 exec, -1\n\t.endm\n\t"
        ".macro FPUT j\n\t"
        "s_waitcnt lgkmcnt(12)\n\t"
        ".irp k, 0, 1, 2, 3\n\tv_min_u32_e32 v[112+4*\\j+\\k], %c[sub]-4, v[112+4*\\j+\\k]\n\t.endr\n\t"
        ".irp k, 0, 1, 2, 3\n\tv_mad_u32_u24 v[112+4*\\j+\\k], v[96+4*\\j+\\k], %[sub4], v[112+4*\\j+\\k]\n\t.endr\n\t"
        ".irp k, 0, 1, 2, 3\n\ts_mov_b64 exec, s[68+8*\\j+2*\\k:69+8*\\j+2*\\k]\n\tds_write_b32 v[112+4*\\j+\\k], v[64+4*\\j+\\k] offset:%c[list]\n\t.endr\n\t"
        "s_mov_b64 exec, -1\n\t.endm\n\t"
        "s_movk_i32 %[o1], 0x1000\n\ts_movk_i32 %[o2], 0x2000\n\ts_movk_i32 %[o3], 0x3000\n\t"
        "v_mov_b32_e32 v92, %[i4]\n\tv_add_u32_e32 v93, 4, %[i4]\n\tv_add_u32_e32 v94, 8, %[i4]\n\tv_add_u32_e32 v95, 12, %[i4]\n\t"
        "s_mov_b32 %[todo], 15\n\ts_mov_b32 %[spin], 0x400000\n\ts_mov_b32 %[over], 0\n\t"
        ".if " STR(FASTIN) "\n\t"
        "s_waitcnt vmcnt(0)\n\t"
        ".irp j, 0, 1, 2, 3\n\tv_xor_b32_e32 v[64+4*\\j], %[key], v[64+4*\\j]\n\t.endr\n\t"
        ".irp j, 0, 1, 2, 3\n\tv_mov_b32_dpp v[80+\\j], v[64+4*\\j] quad_perm:[0,0,0,0] row_mask:0xf bank_mask:0xf\n\t.endr\n\t"
        ".irp j, 0, 1, 2, 3\n\tv_add_u32_e32 v[84+\\j], v[64+4*\\j], v[65+4*\\j]\n\t.endr\n\t"
        ".irp j, 0, 1, 2, 3\n\tv_mov_b32_dpp v[80+\\j], v[80+\\j] row_shr:4 row_mask:0xf bank_mask:0xa\n\t.endr\n\t"
        "s_mov_b64 exec, %[pc0]\n\t"
        ".irp j, 0, 1, 2, 3\n\tv_sub_u32_e32 v[84+\\j], 0, v[65+4*\\j]\n\t.endr\n\t"
        "s_mov_b64 exec, -1\n\t"
        "v_cmp_gt_i32_e64 %[m0], v80, v92\n\tv_cmp_gt_i32_e64 %[m1], v81, v92\n\tv_cmp_gt_i32_e64 %[m2], v82, v92\n\tv_cmp_gt_i32_e64 %[m3], v83, v92\n\t"
        ".irp j, 0, 1, 2, 3\n\tv_add3_u32 v[84+\\j], v[84+\\j], v[66+4*\\j], v[67+4*\\j]\n\t.endr\n\t"
        "v_cndmask_b32_e64 v84, 0, v84, %[m0]\n\tv_cndmask_b32_e64 v85, 0, v85, %[m1]\n\tv_cndmask_b32_e64 v86, 0, v86, %[m2]\n\tv_cndmask_b32_e64 v87, 0, v87, %[m3]\n\t"
        ".irp j, 0, 1, 2, 3\n\tv_add_u32_dpp v[84+\\j], v[84+\\j], v[84+\\j] quad_perm:[1,0,3,2] row_mask:0xf bank_mask:0xf\n\t.endr\n\t"
        ".irp j, 0, 1, 2, 3\n\tv_add_u32_dpp v[84+\\j], v[84+\\j], v[84+\\j] quad_perm:[2,3,0,1] row_mask:0xf bank_mask:0xf\n\t.endr\n\t"
        ".irp j, 0, 1, 2, 3\n\tv_add_u32_dpp v[84+\\j], v[84+\\j], v[84+\\j] row_half_mirror row_mask:0xf bank_mask:0xf\n\t.endr\n\t"
        "v_or3_b32 v96, v80, v81, v82\n\tv_max3_u32 v97, v80, v81, v82\n\tv_or_b32_e32 v96, v96, v83\n\tv_max_u32_e32 v97, v97, v83\n\t"
        "v_lshrrev_b32_e32 v96, 14, v96\n\tv_or3_b32 v96, v96, v84, v85\n\tv_or3_b32 v96, v96, v86, v87\n\t"
        "v_cmp_ne_u32_e32 vcc, 0, v96\n\t"
        "s_cbranch_vccnz 7f\n\t"
        "v_cmp_lt_u32_e32 vcc, %c[cap], v97\n\t"
        "FADD 0\n\tFADD 1\n\tFADD 2\n\tFADD 3\n\t"
        "s_cbranch_vccz 6f\n\ts_mov_b32 %[over], 1\n\t6:\n\t"
        "FPUT 0\n\tFPUT 1\n\tFPUT 2\n\tFPUT 3\n\t"
        "s_mov_b64 exec, %[pc0]\n\t"
        ".irp j, 0, 1, 2, 3\n\tds_write_b32 %[hdra], v[80+\\j] offset:128*\\j\n\t.endr\n\t"
        "s_mov_b64 exec, -1\n\t"
        "s_branch 9f\n\t"
        "7:\n\t.irp j, 0, 1, 2, 3\n\tv_xor_b32_e32 v[64+4*\\j], %[key], v[64+4*\\j]\n\t.endr\n\t"
        ".endif\n\t"
        "0:\n\t"
        "s_mov_b32 %[fresh], 0\n\t"
        "s_waitcnt vmcnt(0)\n\t"
        "IVAL 0, 0\n\tIVAL 1, %[o1]\n\tIVAL 2, %[o2]\n\tIVAL 3, %[o3]\n\t"
        "IRANK 0\n\tIRANK 1\n\tIRANK 2\n\tIRANK 3\n\t"
        "s_andn2_b32 %[todo], %[todo], %[fresh]\n\t"
        "s_cbranch_scc0 9f\n\t"
        "s_sub_u32 %[spin], %[spin], 1\n\t"
        "s_cbranch_scc0 0b\n\t"
        "s_waitcnt vmcnt(0)\n\t"
        "9:\n\t"
        ".purgem IVAL\n\t.purgem IRANK\n\t.purgem FADD\n\t.purgem FPUT"
        : "+{v[64:79]}"(raw), "=&{v[80:95]}"(t0), "=&{v[96:127]}"(t2), [m0] "=&s"(m0), [m1] "=&s"(m1), [m2] "=&s"(m2), [m3] "=&s"(m3),
          [todo] "=&s"(todo), [fresh] "=&s"(fresh), [spin] "=&s"(spin), [o1] "=&s"(o1), [o2] "=&s"(o2), [o3] "=&s"(o3), [over] "=&s"(over)
        : [srd] "s"(srd), [ib] "v"(ib), [key] "s"(key), [i4] "v"(i4), [hdra] "v"(hdra), [pc0] "s"(pc0), [sub4] "s"(SUBB / 4), [four] "v"(four),
          [cnt] "i"(L_CNT), [list] "i"(L_LIST), [sub] "i"(SUBB), [cap] "i"(4 * CAP1)
        : "vcc", "scc", "memory", "s68", "s69", "s70", "s71", "s72", "s73", "s74", "s75", "s76", "s77", "s78", "s79", "s80", "s81", "s82", "s83",
          "s84", "s85", "s86", "s87", "s88", "s89", "s90", "s91", "s92", "s93", "s94", "s95", "s96", "s97", "s98", "s99");
    return over;
}

template <int CTRL, int BANK> __device__ __forceinline__ unsigned dpp(unsigned old, unsigned v) { return __builtin_amdgcn_update_dpp(old, v, CTRL, 0xF, BANK, true); }
__device__ __forceinline__ unsigned bcast8(unsigned v) { v = dpp<0x00, 0xF>(0u, v); return __builtin_amdgcn_update_dpp(v, v, 0x114, 0xF, 0xA, false); }
__device__ __forceinline__ unsigned sum8(unsigned v) { v += dpp<0xB1, 0xF>(0u, v); v += dpp<0x4E, 0xF>(0u, v); return v + dpp<0x141, 0xF>(0u, v); }
__device__ __forceinline__ bf16x4 tr_read(LAS char* p) { return __builtin_amdgcn_ds_read_tr16_b64_v4bf16((LAS bf16x4*)p); }
__device__ __forceinline__ bf16x8 cat(bf16x4 a, bf16x4 b) { return __builtin_shufflevector(a, b, 0, 1, 2, 3, 4, 5, 6, 7); }
__device__ __forceinline__ unsigned pk2(float a, float b) { f32x2 v = {a, b}; bf16x2 r = __builtin_convertvector(v, bf16x2); return __builtin_bit_cast(unsigned, r); }
__device__ __forceinline__ f32x4 mfma(bf16x8 a, bf16x8 b, f32x4 c) { return __builtin_amdgcn_mfma_f32_16x16x32_bf16(a, b, c, 0, 0, 0); }
__device__ __forceinline__ unsigned lds_add(LAS char* p, unsigned v) { return __hip_atomic_fetch_add((LAS unsigned*)p, v, __ATOMIC_RELAXED, __HIP_MEMORY_SCOPE_WORKGROUP); }
__device__ __forceinline__ LAS unsigned* ldsp(unsigned a) { return (LAS unsigned*)(size_t)a; }
__device__ __forceinline__ unsigned byte0_x4(unsigned v) { unsigned r; asm("v_lshlrev_b32_sdwa %0, 2, %1 dst_sel:DWORD dst_unused:UNUSED_PAD src0_sel:DWORD src1_sel:BYTE_0" : "=v"(r) : "v"(v)); return r; }
__device__ __forceinline__ unsigned mad24(unsigned a, unsigned b, unsigned c) { unsigned r; asm("v_mad_u32_u24 %0, %1, %2, %3" : "=v"(r) : "v"(a), "s"(b), "v"(c)); return r; }
__device__ __forceinline__ void lds_arrive(LAS char* p, int lane) {
    asm volatile("s_waitcnt lgkmcnt(0)" ::: "memory");
    if (lane == 0) __hip_atomic_fetch_add((LAS unsigned*)p, 1u, __ATOMIC_RELAXED, __HIP_MEMORY_SCOPE_WORKGROUP);
}
__device__ __forceinline__ void lds_await(LAS char* p, unsigned n) {
    while (__builtin_amdgcn_readfirstlane(*(volatile LAS unsigned*)p) < n) __builtin_amdgcn_s_sleep(1);
    asm volatile("" ::: "memory");
}
__device__ __forceinline__ unsigned rfl(unsigned v) { return __builtin_amdgcn_readfirstlane(v); }

__global__ __launch_bounds__(512, 2) void k_graph(const unsigned short* x_, const int* src_, const int* dst_, const unsigned short* wq_, const unsigned short* wv_,
                                                  unsigned* ws_, unsigned short* out_, int N_, int E_) {
    u32x4 srd_d, srd_s, srd_ws, srd_x0, pw, po;
    u64 t_start;
    asm volatile("s_load_dwordx2 s[40:41], %[ka], 0x10\n\ts_load_dwordx2 s[44:45], %[ka], 0x8\n\ts_load_dwordx4 s[60:63], %[ka], 0x30\n\t"
                 "s_load_dwordx2 s[48:49], %[ka], 0x28\n\ts_load_dwordx2 s[52:53], %[ka], 0x0\n\ts_load_dwordx4 s[56:59], %[ka], 0x18\n\t"
                 "s_memrealtime s[64:65]\n\t"
                 "s_mov_b32 s43, 0x20000\n\ts_mov_b32 s47, 0x20000\n\ts_mov_b32 s51, 0x20000\n\ts_mov_b32 s55, 0x20000\n\ts_mov_b32 s50, %c[wsb]"
                 : "=&{s[40:43]}"(srd_d), "=&{s[44:47]}"(srd_s), "=&{s[48:51]}"(srd_ws), "=&{s[52:55]}"(srd_x0), "=&{s[56:59]}"(pw), "=&{s[60:63]}"(po), "=&{s[64:65]}"(t_start)
                 : [ka] "s"(__builtin_amdgcn_kernarg_segment_ptr()), [wsb] "i"(WS_BYTES));
    FENCE();
    __shared__ __attribute__((aligned(16))) char smem_[L_TOTAL];
    LAS char* smem = (LAS char*)smem_;
    const int t = threadIdx.x, l = t & 63, b = blockIdx.x;
    const int w = rfl(t >> 6);
    const int g = l >> 4, n = l & 15, q = (l >> 2) & 3, p = l & 3, pc = t & 7;
    const unsigned lds0 = (unsigned)(size_t)smem;
    const bool isprod = ((b >> 3) & 1) == 0;
    const int pg = ((b & 7) << 4) | (b >> 4);
    const unsigned e0 = pg * CH + 4 * t, e1 = pg * CH + 3072 + t;

    u32x16 ed, es;
    {
        const unsigned o = e0 * 4, o1 = w == 0 ? e1 * 4 : OOB, ok = WS_KEY + t * 4, om = WS_KEY + b * 4;
        const unsigned oc = w < 4 ? (t < 196 ? ((b - 8) << 8) + ((t >> 1) << 16) + ((t & 1) << 7) : OOB) : (t - 256) * 128, dump = lds0 + L_DUMP + w * 256;
        int role = (w < 4 ? 0 : 2) + (isprod ? 0 : 1);
        unsigned k4 = 4096, k8 = 8192;
        asm volatile("s_cmp_eq_u32 %[role], 0\n\ts_cbranch_scc1 1f\n\t"
                     "s_mov_b32 m0, %[dump]\n\ts_cmp_eq_u32 %[role], 1\n\ts_cbranch_scc1 2f\n\t"
                     "s_getpc_b64 s[68:69]\n\ts_mov_b32 s70, " STR(CODE_BYTES) "\n\ts_mov_b32 s71, 0x20000\n\t"
                     ".if " STR(CODEPF) "\n\tbuffer_load_dword %[oc], s[68:71], 0 offen lds\n\t.endif\n\t"
                     "s_waitcnt lgkmcnt(0)\n\ts_branch 3f\n\t"
                     "2:\n\ts_waitcnt lgkmcnt(0)\n\ts_lshl_b32 s54, s62, 8\n\t"
                     ".if " STR(PARTNER) "\n\tbuffer_load_dword %[oc], s[52:55], 0 offen lds\n\t.endif\n\ts_branch 3f\n\t"
                     "1:\n\ts_waitcnt lgkmcnt(0)\n\ts_lshl_b32 s42, s63, 2\n\ts_lshl_b32 s46, s63, 2\n\t"
                     "buffer_load_dwordx4 v[64:67], %[o], s[40:43], 0 offen\n\tbuffer_load_dwordx4 v[68:71], %[o], s[40:43], %[k4] offen\n\t"
                     "buffer_load_dwordx4 v[72:75], %[o], s[40:43], %[k8] offen\n\tbuffer_load_dword v76, %[o1], s[40:43], 0 offen\n\t"
                     "buffer_load_dwordx4 v[80:83], %[o], s[44:47], 0 offen\n\tbuffer_load_dwordx4 v[84:87], %[o], s[44:47], %[k4] offen\n\t"
                     "buffer_load_dwordx4 v[88:91], %[o], s[44:47], %[k8] offen\n\tbuffer_load_dword v92, %[o1], s[44:47], 0 offen\n\t"
                     "buffer_load_dword v93, %[ok], s[48:51], 0 offen sc1\n\tbuffer_load_dword v94, %[om], s[48:51], 0 offen sc1\n\t"
                     "3:\n\ts_lshl_b32 s54, s62, 8"
                     : "=&{v[64:79]}"(ed), "=&{v[80:95]}"(es)
                     : [o] "v"(o), [o1] "v"(o1), [ok] "v"(ok), [om] "v"(om), [oc] "v"(oc), [role] "s"(role), [dump] "s"(dump), [k4] "s"(k4), [k8] "s"(k8),
                       "{s[40:43]}"(srd_d), "{s[44:47]}"(srd_s), "{s[48:51]}"(srd_ws), "{s[52:55]}"(srd_x0), "{s[56:59]}"(pw), "{s[60:63]}"(po), "{s[64:65]}"(t_start)
                     : "memory", "scc", "s68", "s69", "s70", "s71");
        asm volatile("" : "+{s[48:51]}"(srd_ws), "+{s[52:55]}"(srd_x0), "+{s[56:59]}"(pw), "+{s[60:63]}"(po), "+{s[64:65]}"(t_start));
    }
    const void* const x = (const void*)((u64)srd_x0.x | ((u64)srd_x0.y << 32));
    const void* const wq = (const void*)((u64)pw.x | ((u64)pw.y << 32));
    const void* const wv = (const void*)((u64)pw.z | ((u64)pw.w << 32));
    void* const out = (void*)((u64)po.x | ((u64)po.y << 32));
    const int N = po.z, E = po.w;
    const unsigned xbytes = srd_x0.z;
    STAMP_DECL;
    const unsigned ring = lds0 + L_RING + w * 16384;
    const int jn = 8 * n + w;

    unsigned mykey = 0;
    auto node_row = [&](int tile, int m) { const int nd = b + 256 * (8 * (4 * m + g) + tile); return nd < N ? (unsigned)nd << 8 : OOB; };
    auto zero_staging = [&]() {
        const u32x4 z = {0u, 0u, 0u, 0u};
#pragma unroll
        for (int k = 0; k < 5; ++k) *(LAS u32x4*)(smem + L_IMG + k * 8192 + t * 16) = z;
    };

    u32x16 addr;
    f32x32 acc, wva, wvb;
    unsigned cv, lista, cnta, qa, keyn, inf;
    Srd4 srd_x;
    auto loop_constants = [&]() {
        srd_x = make_srd4(x, xbytes, BIAS * 256);
        lista = lds0 + L_LIST + w * (2 * SUBB) + g * 4; cnta = lds0 + L_CNT + (l & 31) * 4; qa = ring + l * 16; keyn = jn; inf = 0x7F800000u;
        asm volatile("" : "+s"(srd_x.s0), "+s"(srd_x.s1), "+s"(srd_x.s2), "+s"(srd_x.s3));
        asm volatile("" : "+v"(lista), "+v"(cnta), "+v"(qa), "+v"(keyn), "+v"(inf));
        const unsigned o0 = (n ^ (((g & 1) << 3) | (g >> 1))) << 4;
        const unsigned ra = (4 * (n & 3) + (n >> 2)) * 256 + (((4 * g) ^ (((n & 7) << 1) | (n >> 3))) << 4);
        const int i = 4 * g + q, r8 = i & 7;
        const unsigned ta = (4 * q + g) * 256 + 8 * (p & 1) + (((p >> 1) ^ (((i & 7) << 1) | (i >> 3))) << 4);
        cv = i * 256 + (((p ^ ((r8 & 1) | ((r8 & 6) << 1))) << 4) | ((p & 1) << 3));
#pragma unroll
        for (int s = 0; s < 4; ++s) { addr[s] = ring + (ra ^ (s << 4)); addr[12 + s] = o0 ^ (s << 5); }
#pragma unroll
        for (int c = 0; c < 8; ++c) addr[4 + c] = ring + (ta ^ (c << 5));
        asm volatile("" : "+{v[176:191]}"(addr), "+v"(cv));
    };
    auto clear_sums = [&]() { acc = 0.f; asm volatile("" : "+{v[128:159]}"(acc)); };
    auto wv_resident = [&]() {
#pragma unroll
        for (int a = 0; a < 8; ++a) {
            LAS char* pa = smem + L_WV0 + (cv ^ ((a >> 1) << 6) ^ ((a & 1) << 3));
            const f32x4 f0 = __builtin_bit_cast(f32x4, cat(tr_read(pa), tr_read(pa + 4096)));
            const f32x4 f1 = __builtin_bit_cast(f32x4, cat(tr_read(pa + 16384), tr_read(pa + 16384 + 4096)));
#pragma unroll
            for (int k = 0; k < 4; ++k) { wva[4 * a + k] = f0[k]; wvb[4 * a + k] = f1[k]; }
        }
        asm volatile("" : "+{v[224:255]}"(wva), "+{v[32:63]}"(wvb));
    };

    if (isprod) zero_staging();
    if (t < 8) *(LAS u32x4*)(smem + L_CNT + t * 16) = u32x4{0u, 0u, 0u, 0u};
    WG_BARRIER();

    if (__builtin_expect(w < 4, 1)) {
        if (__builtin_expect(isprod, 1)) {
            if (PRIO) __builtin_amdgcn_s_setprio(2);
            unsigned ob = WS_INBOX + ((t >> 3) * G1 + pg) * 128 + pc * 16, ima = lds0 + L_IMG + (t >> 3) * 144 + pc * 16;
            PIN(ob); PIN(ima);
            unsigned cna = lds0 + L_CNT1 + (t >> 3) * 4;
            PIN(cna);
            const bool v0 = e0 < (unsigned)E, v1 = e0 + 1024 < (unsigned)E, v2 = e0 + 2048 < (unsigned)E, v3 = w == 0 && e1 < (unsigned)E;
            STAMP(1);
            u32x16 rk;
            const u64 over = rank_edges(ed, es, rk, __builtin_amdgcn_ballot_w64(v0), __builtin_amdgcn_ballot_w64(v1), __builtin_amdgcn_ballot_w64(v2),
                                        __builtin_amdgcn_ballot_w64(v3), t * 4);
            mykey = es[14];
            if (__builtin_expect(over != 0, 0)) {
#pragma unroll
                for (int k = 0; k < 13; ++k)
                    if (rk[k] >= 4u * CAP1) {
                        const unsigned o = WS_OVF + (pg * CH + lds_add(smem + L_CNT1 + 2048, 1)) * 4;
                        const unsigned v = ((((es[k] >> 8) & 0xFFFF) - BIAS) << 16) | ed[k];
                        ST1(v, srd_ws, o, " sc1");
                    }
            }
            if (XSYNC) { lds_arrive(smem + L_CNT + 96, l); lds_await(smem + L_CNT + 96, 4); } else WG_BARRIER();
            STAMP(3);
            f32x32 img; u32x16 meta, sums;
            const unsigned no = read_image(img, meta, sums, ima, cna);
            lds_arrive(smem + L_CNT + 84, l);
            if (__builtin_expect(no != 0, 0)) {
                const unsigned o = t == 0 ? WS_NOVF + pg * 4 : OOB;
                ST1(no, srd_ws, o, " sc1");
                VMWAIT(0);
                if (XSYNC) { lds_arrive(smem + L_CNT + 100, l); lds_await(smem + L_CNT + 100, 4); } else WG_BARRIER();
            }
            publish_image(img, meta, sums, srd_ws, ob);
            STAMP(4);
        } else {
            { const unsigned om = WS_KEY + b * 4; asm volatile("buffer_load_dword %0, %1, %2, 0 offen sc1\n\ts_waitcnt vmcnt(0)" : "=&v"(mykey) : "v"(om), "s"(srd_ws) : "memory"); }
            STAMP(1);
            if (PRIO) __builtin_amdgcn_s_setprio(2);
            STAMP(2); STAMP(3); STAMP(4);
        }
        STAMP(5);
        mykey = rfl(mykey) + KEY_STEP;
        loop_constants();
        clear_sums();
        unsigned ib = WS_INBOX + b * (G1 * 128) + t * 16;
        PIN(ib);
        while (__builtin_amdgcn_s_memrealtime() < t_start + POLL_T) __builtin_amdgcn_s_sleep(1);
        STAMP(6);
        u32x16 raw;
        inbox_request(raw, srd_ws, ib);
        if (__builtin_expect(inbox_stage(raw, srd_ws, ib, mykey, 16 * pc - 8, lds0 + L_HDR + (t >> 3) * 4) != 0, 0)) {
            LAS unsigned* hp = (LAS unsigned*)(smem + L_HDR) + 32 * (l >> 3 & 3) + 8 * w + (l & 7);
            const unsigned hv = *hp;
            for (u64 m = __builtin_amdgcn_ballot_w64(l < 32 && hv > 4u * CAP1); m; m &= m - 1) {
                const int k = __builtin_ctzll(m), gp = 32 * (k >> 3) + 8 * w + (k & 7);
                unsigned u, no; const unsigned o = WS_OVF + (gp * CH + l) * 4, on = WS_NOVF + gp * 4;
                asm volatile("buffer_load_dword %0, %2, %4, 0 offen sc1\n\tbuffer_load_dword %1, %3, %4, 0 offen sc1\n\ts_waitcnt vmcnt(0)"
                             : "=&v"(u), "=&v"(no) : "v"(o), "v"(on), "s"(srd_ws) : "memory");
                if (rfl(no) <= 64) {
                    if (l < no && (u & 0xFF) == (unsigned)b) {
                        const unsigned e = __builtin_amdgcn_perm((u >> 16) + BIAS, u, 0x01050401u), key = (e >> 21) & 60;
                        *(LAS unsigned*)(smem + L_LIST + key * (SUBB / 4) + min(lds_add(smem + L_CNT + key, 4), SUBB - 4u)) = e;
                    }
                    if (l == k) *hp = hv | 0x80000000u;
                } else *(LAS unsigned*)(smem + L_CNT + 64) = 1;
            }
        }
        if (PRIO) __builtin_amdgcn_s_setprio(0);
        STAMP(7);
    } else {
        const int j = w - 4;
        auto prefetch = [&](int k0, int kn) {
            const unsigned ln = (b >> 3) * (kn / 4) + (t - 256), o = t - 256 < kn / 4 ? k0 * 1024 + ln * 128 : OOB;
            set_m0(lds0 + L_DUMP + w * 256);
            asm volatile("s_nop 0\n\tbuffer_load_dword %1, %0, 0 offen lds" :: "s"(srd_x0), "v"(o) : "memory");
        };
        STAMP(1);
        u32x4 wr[8], xr[8];
        {
            const u32x4 srd_q = make_srd(wq, 32768);
            const unsigned o = j * 8192 + l * 16, k4 = 4096;
            LD4(wr[0], srd_q, o, ""); LD4(wr[1], srd_q, o, " offset:1024"); LD4(wr[2], srd_q, o, " offset:2048"); LD4(wr[3], srd_q, o, " offset:3072");
            LD4S(wr[4], srd_q, o, k4, ""); LD4S(wr[5], srd_q, o, k4, " offset:1024"); LD4S(wr[6], srd_q, o, k4, " offset:2048"); LD4S(wr[7], srd_q, o, k4, " offset:3072");
#pragma unroll
            for (int m = 0; m < 8; ++m) { const unsigned ox = node_row(m < 4 ? j : w, m & 3) + n * 16; LD4(xr[m], srd_x0, ox, ""); }
        }
        unsigned aqa[8], xfa[4];
        unsigned xa = ring + g * 256 + ((n ^ g) << 4), wa = lds0 + L_WQ + j * 16384 + g * 256 + ((n ^ g) << 4) + ((n & 4) << 1);
        {
            const unsigned aq = lds0 + L_WQ + (8 * g + q) * 256 + ((((4 * p) ^ (q | ((g & 1) << 2))) << 4) | ((p & 1) << 3));
#pragma unroll
            for (int s = 0; s < 4; ++s) { xfa[s] = ring + n * 256 + (((4 * s + g) ^ n) << 4); PIN(xfa[s]); }
#pragma unroll
            for (int c = 0; c < 8; ++c) { aqa[c] = aq ^ ((c >> 1) << 4) ^ ((c & 1) << 3); PIN(aqa[c]); }
            PIN(xa); PIN(wa);
        }
        loop_constants();
        STAMP(2);
        if (isprod && !XSYNC) {
            WG_BARRIER();
            if (__builtin_expect(rfl(*(LAS unsigned*)(smem + L_CNT1 + 2048)) != 0, 0)) WG_BARRIER();
            if (W_SLEEP) __builtin_amdgcn_s_sleep(W_SLEEP);
        }
        STAMP(3);
        asm volatile("s_waitcnt vmcnt(%c8)" : "+v"(wr[0]), "+v"(wr[1]), "+v"(wr[2]), "+v"(wr[3]), "+v"(wr[4]), "+v"(wr[5]), "+v"(wr[6]), "+v"(wr[7]) : "i"(8) : "memory");
#pragma unroll
        for (int m = 0; m < 8; ++m) {
            const unsigned a = (wa ^ ((m & 2) << 5)) + m * 1024;
            *(LAS u32x2*)(size_t)a = u32x2{wr[m].x, wr[m].y};
            *(LAS u32x2*)(size_t)(a ^ 8) = u32x2{wr[m].z, wr[m].w};
        }
        asm volatile("s_waitcnt vmcnt(0)" : "+v"(xr[0]), "+v"(xr[1]), "+v"(xr[2]), "+v"(xr[3]), "+v"(xr[4]), "+v"(xr[5]), "+v"(xr[6]), "+v"(xr[7]) :: "memory");
#pragma unroll
        for (int m = 0; m < 8; ++m) *(LAS u32x4*)(size_t)((xa ^ ((m & 3) << 6)) + m * 1024) = xr[m];
        STAMP(4);
        lds_arrive(smem + L_CNT + 80, l);
        lds_await(smem + L_CNT + 80, 4);
        STAMP(5);
        bf16x8 xo[2][4], fr[2][8];
        f32x4 qc[2][8];
        auto wq_frags = [&](int u, bf16x8 (&f)[8]) {
#pragma unroll
            for (int a = 0; a < 8; ++a) {
                LAS char* pa = (LAS char*)(size_t)aqa[a] + u * 16384;
                f[a] = cat(tr_read(pa), tr_read(pa + 1024));
            }
        };
#pragma unroll
        for (int u = 0; u < 4; ++u) { xo[0][u] = *(LAS bf16x8*)(size_t)xfa[u]; xo[1][u] = *(LAS bf16x8*)(size_t)(xfa[u] + 4096); }
        wq_frags(0, fr[0]);
        {
            const Srd4 srd_v = make_srd4(wv, 32768);
            const unsigned r0 = (j * 32 + g) << 8, o0 = (n ^ ((g & 1) | ((g & 2) << 1))) << 4;
            const unsigned e[8] = {r0, r0 + 1024, r0 + 2048, r0 + 3072, r0 + 4096, r0 + 5120, r0 + 6144, r0 + 7168};
            const unsigned base = lds0 + (j < 2 ? L_WV0 + j * 16384 : L_WV1 + (j - 2) * 8192);
            const u32x4 off = {o0, o0 ^ 128, o0, o0 ^ 128};
            dma_half(base, srd_v, e, off); dma_half(base + 4096, srd_v, e + 4, off);
        }
        if (PFA_KB) prefetch(0, PFA_KB);
#pragma unroll
        for (int u = 0; u < 4; ++u) {
            if (u < 3) wq_frags(u + 1, fr[(u + 1) & 1]);
            FENCE();
#pragma unroll
            for (int a = 0; a < 8; ++a)
#pragma unroll
                for (int tt = 0; tt < 2; ++tt) qc[tt][a] = mfma(fr[u & 1][a], xo[tt][u], u ? qc[tt][a] : f32x4{0.f, 0.f, 0.f, 0.f});
            FENCE();
            if (!WVLATE && u == 1 && j < 2) { VMWAIT(0); lds_arrive(smem + L_CNT + 88, l); }
        }
        STAMP(6);
        if (PFB_KB) { asm volatile("s_waitcnt vmcnt(%c0)" :: "i"(PFA_KB ? 1 : 0) : "memory"); prefetch(PFA_KB, PFB_KB); }
        if (isprod) lds_await(smem + L_CNT + 84, 4);
#pragma unroll
        for (int tt = 0; tt < 2; ++tt)
#pragma unroll
            for (int s = 0; s < 4; ++s) {
                const float k = -0.08838834764831845f * 1.4426950408889634f;
                const f32x4 c0 = qc[tt][2 * s] * k, c1 = qc[tt][2 * s + 1] * k;
                *(LAS u32x4*)(size_t)(ring - (tt ? 0 : 65536) + s * 1024 + l * 16) = u32x4{pk2(c0[0], c0[1]), pk2(c0[2], c0[3]), pk2(c1[0], c1[1]), pk2(c1[2], c1[3])};
            }
        clear_sums();
        if (!WVLATE) { lds_await(smem + L_CNT + 88, 2); wv_resident(); }
        if (!PFB_KB) asm volatile("s_waitcnt vmcnt(%c0)" :: "i"(PFA_KB ? 1 : 0) : "memory");
        if (PFC_KB) prefetch(PFA_KB + PFB_KB, PFC_KB);
        STAMP(7);
    }
    WG_BARRIER();
    STAMP(8);
    if (LPRIO >= 3) __builtin_amdgcn_s_setprio(3);
    if (LPRIO == 1 && w >= 4) __builtin_amdgcn_s_setprio(1);
    if (LPRIO == 2) { if (w >= 6) __builtin_amdgcn_s_setprio(3); else if (w >= 4) __builtin_amdgcn_s_setprio(2); else if (w >= 2) __builtin_amdgcn_s_setprio(1); }

    u32x16 qf;
    const u32x4 doff = {addr[12], addr[13], addr[14], addr[15]};
    LAS char* mylist = smem + L_LIST + w * (2 * SUBB);
    int gcur = G1, pos = 0;
    unsigned hmask = 0xFFFFFFFFu;
    auto batch = [&](auto first_) {
        constexpr bool first = decltype(first_)::value;
        auto entries = [&](int grp, unsigned (&e)[8]) {
#pragma unroll
            for (int m = 0; m < 8; ++m) e[m] = *(LAS unsigned*)(size_t)(lista + grp * 128 + m * 16);
        };
        unsigned cw = *(LAS unsigned*)(size_t)(first ? cnta : lds0 + L_CNT + (__builtin_amdgcn_mbcnt_lo(~0u, 0u) & 31) * 4), e0[8], e1[8];
        entries(0, e0);
        if constexpr (first) {
#pragma unroll
            for (int s = 0; s < 4; ++s) {
                const u32x4 v = *(LAS u32x4*)(size_t)(qa + s * 1024);
                qf[4 * s] = v.x; qf[4 * s + 1] = v.y; qf[4 * s + 2] = v.z; qf[4 * s + 3] = v.w;
            }
        }
        entries(1, e1);
        FENCE();
        int lo = __builtin_amdgcn_readlane(cw, 2 * w) >> 2, hi = __builtin_amdgcn_readlane(cw, 2 * w + 1) >> 2;
        if constexpr (first) {
            if (__builtin_amdgcn_readlane(cw, 16)) gcur = 0;
            if (__builtin_amdgcn_ballot_w64(cw > 4u * CAP2)) { gcur = -1; lo = 0; hi = 0; hmask = 0x7FFFFFFFu; }
        }
        const int c = lo + hi, G = (c + 31) >> 5;
        const int nlast = G > 2 ? (((c - 1) & 31) >> 2) + 1 : 8;
        auto join = [&]() {
            for (int k = 0; k < G * 32 - lo; k += 64) {
                const unsigned e = *(LAS unsigned*)(mylist + SUBB + (l + k) * 4);
                if (l + k < G * 32 - lo) *(LAS unsigned*)(mylist + (lo + l + k) * 4) = l + k < hi ? e : PAD;
            }
        };
        const bool early = lo >= 64;
        if (__builtin_expect(!early, 0)) { join(); entries(0, e0); entries(1, e1); }
        if constexpr (first) asm volatile("s_waitcnt lgkmcnt(0)" : "+{v[160:175]}"(qf) :: "memory");
        if (G > 0) dma_group(ring, srd_x, e0, doff);
        if constexpr (first) {
            if (WVLATE == 1) {
                if (G > 1 && w < 6) dma_group(ring + 8192, srd_x, e1, doff);
                wv_resident();
                lds_arrive(smem + L_CNT + 92, l);
                if (w >= 6) { lds_await(smem + L_CNT + 92, 8); if (G > 1) dma_group(ring + 8192, srd_x, e1, doff); }
            } else if (WVLATE == 2) {
                wv_resident();
                lds_arrive(smem + L_CNT + 92, l);
                if (w >= 6) lds_await(smem + L_CNT + 92, 8);
                if (G > 1) dma_group(ring + 8192, srd_x, e1, doff);
            } else {
                if (w < 4) { wv_resident(); lds_arrive(smem + L_CNT + 92, l); }
                else if (w >= 6) lds_await(smem + L_CNT + 92, 4);
                if (G > 1) dma_group(ring + 8192, srd_x, e1, doff);
            }
        } else if (G > 1) dma_group(ring + 8192, srd_x, e1, doff);
        f32x16 cc;
#pragma unroll
        for (int m = 0; m < 8; ++m) {
            cc[m] = (e0[m] & 0xFF) == keyn ? 0.f : __uint_as_float(inf);
            cc[8 + m] = (e1[m] & 0xFF) == keyn ? 0.f : __uint_as_float(inf);
        }
        if (early) join();
        STAMP(9);
        if (G > 0) gather_loop(acc, qf, addr, u32x4{lista, keyn, inf, 0u}, cc, srd_x, ring, G, nlast);
    };
    batch(std::true_type{});
    while (__builtin_expect(gcur < G1, 0)) {
        unsigned lim = G1 * 32;
        if (gcur >= 0) {
            const u64 m0 = __builtin_amdgcn_ballot_w64(l >= gcur && (int)(*(LAS unsigned*)(smem + L_HDR + l * 4) & hmask) > 4 * CAP1);
            const u64 m1 = __builtin_amdgcn_ballot_w64(l + 64 >= gcur && (int)(*(LAS unsigned*)(smem + L_HDR + 256 + l * 4) & hmask) > 4 * CAP1);
            if (!(m0 | m1)) break;
            gcur = m0 ? __builtin_ctzll(m0) : 64 + __builtin_ctzll(m1);
            unsigned v; const unsigned o = WS_NOVF + gcur * 4;
            asm volatile("buffer_load_dword %0, %1, %2, 0 offen sc1\n\ts_waitcnt vmcnt(0)" : "=&v"(v) : "v"(o), "s"(srd_ws) : "memory");
            lim = rfl(v);
        }
        const unsigned i = pos + t;
        const bool cand = t < CAP2 && i < lim;
        unsigned u;
        { const unsigned o = cand ? (gcur < 0 ? WS_INBOX + b * (G1 * 128) : WS_OVF + gcur * CH * 4) + i * 4 : OOB;
          asm volatile("buffer_load_dword %0, %1, %2, 0 offen sc1\n\ts_waitcnt vmcnt(0)" : "=&v"(u) : "v"(o), "s"(srd_ws) : "memory"); }
        WG_BARRIER();
        if (t < 16) *(LAS unsigned*)(smem + L_CNT + t * 4) = 0;
        WG_BARRIER();
        if (cand) {
            unsigned e; bool v;
            if (gcur < 0) { e = (i & 3) ? u : u ^ mykey; v = ((i & 31) - 2) * 4 < *(LAS unsigned*)(smem + L_HDR + (i >> 5) * 4); }
            else { e = (((u >> 16) + BIAS) << 8) | ((u >> 8) & 0xFF); v = (u & 0xFF) == (unsigned)b; }
            if (v) *(LAS unsigned*)(smem + L_LIST + (e & 7) * (2 * SUBB) + lds_add(smem + L_CNT + (e & 7) * 8, 4)) = e;
        }
        pos += CAP2;
        if ((unsigned)pos >= lim) { ++gcur; pos = 0; }
        WG_BARRIER();
        batch(std::false_type{});
    }
    STAMP(10);

    {
        bf16x8 wl[2][8], ag[4];
#pragma unroll
        for (int u = 0; u < 2; ++u)
#pragma unroll
            for (int a = 0; a < 8; ++a) {
                LAS char* pa = smem + L_WV1 + (cv ^ ((a >> 1) << 6) ^ ((a & 1) << 3)) + u * 8192;
                wl[u][a] = cat(tr_read(pa), tr_read(pa + 4096));
            }
#pragma unroll
        for (int u = 0; u < 4; ++u) {
            const u32x4 v = {pk2(acc[8 * u], acc[8 * u + 1]), pk2(acc[8 * u + 2], acc[8 * u + 3]),
                             pk2(acc[8 * u + 4], acc[8 * u + 5]), pk2(acc[8 * u + 6], acc[8 * u + 7])};
            ag[u] = __builtin_bit_cast(bf16x8, v);
        }
        FENCE();
        f32x4 oc[8];
#pragma unroll
        for (int a = 0; a < 8; ++a) oc[a] = mfma(__builtin_bit_cast(bf16x8, f32x4{wva[4 * a], wva[4 * a + 1], wva[4 * a + 2], wva[4 * a + 3]}), ag[0], f32x4{0.f, 0.f, 0.f, 0.f});
#pragma unroll
        for (int a = 0; a < 8; ++a) oc[a] = mfma(__builtin_bit_cast(bf16x8, f32x4{wvb[4 * a], wvb[4 * a + 1], wvb[4 * a + 2], wvb[4 * a + 3]}), ag[1], oc[a]);
#pragma unroll
        for (int a = 0; a < 8; ++a) oc[a] = mfma(wl[0][a], ag[2], oc[a]);
        FENCE();
        const unsigned ow = ring + n * 256 + ((g ^ n) << 4) + ((g & 1) << 3);
#pragma unroll
        for (int a = 0; a < 8; ++a) oc[a] = mfma(wl[1][a], ag[3], oc[a]);
#pragma unroll
        for (int a = 0; a < 8; ++a)
            *(LAS u32x2*)(size_t)(ow ^ ((a >> 1) << 6) ^ ((a & 1) << 3)) = u32x2{pk2(oc[a][0], oc[a][1]), pk2(oc[a][2], oc[a][3])};
        const auto rs_o = __builtin_amdgcn_make_buffer_rsrc(out, 0, xbytes, 0x00020000);
#pragma unroll
        for (int m = 0; m < 4; ++m) {
            const u32x4 v = *(LAS u32x4*)(size_t)(ring + m * 1024 + l * 16);
            __builtin_amdgcn_raw_buffer_store_b128(v, rs_o, node_row(w, m) + ((n ^ g ^ (4 * m)) << 4), 0, OUT_AUX);
        }
    }
    if (w == 0) { const unsigned o = l == 0 ? WS_KEY + b * 4 : OOB; ST1(mykey, srd_ws, o, " sc1"); }
    STAMP(11);
}

extern "C" void kernel_launch(void* const* d_in, const int* in_sizes, int n_in,
                              void* d_out, int out_size, void* d_ws, size_t ws_size,
                              hipStream_t stream) {
    const int N = in_sizes[0] / 128, E = in_sizes[1];
    k_graph<<<NB, 512, 0, stream>>>((const unsigned short*)d_in[0], (const int*)d_in[1], (const int*)d_in[2],
                                    (const unsigned short*)d_in[3], (const unsigned short*)d_in[4],
                                    (unsigned*)d_ws, (unsigned short*)d_out, N, E);
}
```

```cpp
#define PFC_KB 1024
#include <hip/hip_runtime.h>
#include <hip/hip_bf16.h>
#include <stdint.h>
#include <type_traits>
#define ORD 1
#define SKIP 0
#define STR2(x) #x
#define STR(x) STR2(x)

typedef __attribute__((ext_vector_type(8))) __bf16 bf16x8;
typedef __attribute__((ext_vector_type(4))) __bf16 bf16x4;
typedef __attribute__((ext_vector_type(2))) __bf16 bf16x2;
typedef __attribute__((ext_vector_type(4))) float f32x4;
typedef __attribute__((ext_vector_type(2))) float f32x2;
typedef __attribute__((ext_vector_type(4))) unsigned u32x4;
typedef __attribute__((ext_vector_type(2))) unsigned u32x2;
typedef __attribute__((ext_vector_type(32))) float f32x32;
typedef __attribute__((ext_vector_type(16))) float f32x16;
typedef __attribute__((ext_vector_type(16))) unsigned u32x16;
typedef __attribute__((ext_vector_type(12))) unsigned u32x12;
typedef __attribute__((ext_vector_type(8))) unsigned u32x8;
typedef unsigned long long u64;
#define LAS __attribute__((address_space(3)))

constexpr int NB = 256;
constexpr int G1 = 128;
constexpr int CH = 3136;
constexpr int CAP1 = 30;
constexpr int CAP2 = 192;
constexpr int SUBB = 784;
#define POLL_AT 3
#define OUT_AUX 16
#define POLL_T 350
#define LPRIO 3
#define PKADD 1
#define CODEPF 1
#define CODE_BYTES 21504
#define PARTNER 1
#define XSYNC 1
#define WVLATE 1
#define PFA_KB 0
#define PFB_KB 0
#define X_T 0
#define XAFTER 0
#define POLL_D 60
#define FASTIN 1
#define W_SLEEP 3
#define PRIO 1
#define SPLIT 16384
constexpr unsigned KEY_STEP = 0x9E3779B1u;
constexpr unsigned WS_KEY = 0;
constexpr unsigned WS_NOVF = 2048;
constexpr unsigned WS_INBOX = 4096;
constexpr unsigned WS_OVF = WS_INBOX + NB * G1 * 128;
constexpr unsigned WS_BYTES = WS_OVF + G1 * CH * 4;
constexpr unsigned OOB = 0x80000000u;
constexpr unsigned BIAS = 32768 - SPLIT;
constexpr unsigned PAD = 0x00FFFFFFu;

constexpr int L_CNT = 0;
constexpr int L_HDR = 128;
constexpr int L_LIST = 768;
constexpr int L_RING = L_LIST + 16 * SUBB;
constexpr int L_IMG = L_RING;
constexpr int L_CNT1 = L_IMG + NB * 144;
constexpr int L_WQ = L_RING + 2 * 16384 + 8192;
constexpr int L_WV0 = L_RING + 6 * 16384 + 8192;
constexpr int L_WV1 = L_RING + 131072;
constexpr int L_DUMP = L_WV1 + 16384;
constexpr int L_TOTAL = L_DUMP + 2048;
static_assert(L_RING % 256 == 0 && L_TOTAL <= 163840, "LDS map");

#define STAMP_DECL
#define STAMP(k)
#define STAMP_FLUSH(base, k0, k1)

__device__ __forceinline__ u32x4 make_srd(const void* p, unsigned bytes, unsigned back = 0) {
    const u64 a = (u64)p - back;
    u32x4 s; s.x = (unsigned)a; s.y = (unsigned)(a >> 32); s.z = bytes + back; s.w = 0x00020000u; return s;
}
#define LD4(dst, srd, off, FL) asm volatile("buffer_load_dwordx4 %0, %1, %2, 0 offen" FL : "=v"(dst) : "v"(off), "s"(srd) : "memory")
#define LD4S(dst, srd, off, so, FL) asm volatile("buffer_load_dwordx4 %0, %1, %2, %3 offen" FL : "=v"(dst) : "v"(off), "s"(srd), "s"(so) : "memory")
#define LD1(dst, srd, off, FL) asm volatile("buffer_load_dword %0, %1, %2, 0 offen" FL : "=v"(dst) : "v"(off), "s"(srd) : "memory")
#define ST1(val, srd, off, FL) asm volatile("buffer_store_dword %0, %1, %2, 0 offen" FL :: "v"(val), "v"(off), "s"(srd) : "memory")
#define PIN(x) asm volatile("" : "+v"(x))
#define VMWAIT(n) asm volatile("s_waitcnt vmcnt(" #n ")" ::: "memory")
#define FENCE() __builtin_amdgcn_sched_barrier(0)
#define WG_BARRIER() do { asm volatile("s_waitcnt lgkmcnt(0)" ::: "memory"); __builtin_amdgcn_s_barrier(); asm volatile("" ::: "memory"); } while (0)

struct Srd4 { u32x4 s0, s1, s2, s3; };
__device__ __forceinline__ Srd4 make_srd4(const void* p, unsigned bytes, unsigned back = 0) {
    return Srd4{make_srd(p, bytes, back), make_srd(p, bytes, back + 1024), make_srd(p, bytes, back + 2048), make_srd(p, bytes, back + 3072)};
}

__device__ __forceinline__ void set_m0(unsigned v) { asm volatile("s_mov_b32 m0, %0" :: "s"(v) : "memory"); }
template <int R> __device__ __forceinline__ void dma_rows(const Srd4& d, unsigned e, unsigned off) {
    unsigned tmp, mask = 0x00FFFF00u;
    asm volatile("v_and_or_b32 %0, %1, %2, %3\n\tbuffer_load_dwordx4 %0, %4, 0 offen offset:%c5 lds"
                 : "=&v"(tmp) : "v"(e), "s"(mask), "v"(off), "s"(R == 0 ? d.s0 : R == 1 ? d.s1 : R == 2 ? d.s2 : d.s3), "i"(R * 1024) : "memory");
}
__device__ __forceinline__ void dma_half(unsigned ldsbase, const Srd4& d, const unsigned* e, const u32x4& off) {
    set_m0(ldsbase);
    dma_rows<0>(d, e[0], off.x); dma_rows<1>(d, e[1], off.y); dma_rows<2>(d, e[2], off.z); dma_rows<3>(d, e[3], off.w);
}
__device__ __forceinline__ void dma_group(unsigned ldsbase, const Srd4& d, const unsigned (&e)[8], const u32x4& off) {
    asm volatile("s_waitcnt lgkmcnt(0)" ::: "memory");
    dma_half(ldsbase, d, e, off); dma_half(ldsbase + 4096, d, e + 4, off);
}

__device__ __forceinline__ void gather_loop(f32x32& acc, const u32x16& qf, const u32x16& addr, u32x4 misc, f32x16 cc,
                                            const Srd4& d, unsigned ring, int G, int nlast) {
    f32x32 t0, t1; u32x12 t2; int tmp, pr; u64 sm; unsigned mask = 0x00FFFF00u;
    asm volatile(
        ".macro GWAIT k\n\ts_cmp_gt_i32 %[t], \\k\n\ts_cbranch_scc1 1f\n\ts_waitcnt vmcnt(\\k)\n\t.endm\n\t"
        ".macro GROWS so\n\t.irp s, 0, 1, 2, 3\n\t"
        "ds_read_b128 v[64+8*\\s:67+8*\\s], v[176+\\s] offset:\\so\n\t"
        "ds_read_b128 v[68+8*\\s:71+8*\\s], v[176+\\s] offset:\\so+4096\n\t.endr\n\t.endm\n\t"
        ".macro GTR c, so\n\t"
        "ds_read_b64_tr_b16 v[96+4*\\c:97+4*\\c], v[180+\\c] offset:\\so\n\t"
        "ds_read_b64_tr_b16 v[98+4*\\c:99+4*\\c], v[180+\\c] offset:\\so+4096\n\t.endm\n\t"
        ".macro GS s, c\n\t"
        "v_mfma_f32_16x16x32_bf16 v[\\c:\\c+3], v[64+8*\\s:67+8*\\s], v[160+4*\\s:163+4*\\s], v[\\c:\\c+3]\n\t"
        "v_mfma_f32_16x16x32_bf16 v[\\c+4:\\c+7], v[68+8*\\s:71+8*\\s], v[160+4*\\s:163+4*\\s], v[\\c+4:\\c+7]\n\t.endm\n\t"
        ".macro GLOAD m, r, srd\n\t"
        "v_and_or_b32 v195, v[212+\\m], %[mask], v[188+\\r]\n\t"
        "buffer_load_dwordx4 v195, \\srd, 0 offen offset:1024*\\r lds\n\t.endm\n\t"
        ".macro GPART m, r, srd\n\ts_cmp_le_i32 %[nl], \\m\n\ts_cbranch_scc1 4f\n\tGLOAD \\m, \\r, \\srd\n\t.endm\n\t"
        ".macro GSUM m\n\tv_mfma_f32_16x16x32_bf16 v[128+4*(\\m):131+4*(\\m)], v[96+4*(\\m):99+4*(\\m)], v[220:223], v[128+4*(\\m):131+4*(\\m)]\n\t.endm\n\t"
        ".macro GCMP m, d\n\tv_cmp_ne_u32_sdwa \\d, v[212+\\m], v193 src0_sel:BYTE_0 src1_sel:DWORD\n\t.endm\n\t"
        ".macro GPAIR m, c\n\t"
        "GSUM \\m\n\tGCMP \\m+1, %[sm]\n\tv_cndmask_b32_e32 v[\\c+\\m], 0, v194, vcc\n\t"
        "GSUM \\m+1\n\t.if \\m < 6\n\tGCMP \\m+2, vcc\n\t.else\n\ts_cmp_gt_i32 %[rem], 0\n\t.endif\n\t"
        "v_cndmask_b32_e64 v[\\c+\\m+1], 0, v194, %[sm]\n\t.endm\n\t"
        ".macro GSTEP so, c, eo\n\t"
        "GTR 0, \\so\n\t"
        "s_waitcnt lgkmcnt(2)\n\tGS 0, \\c\n\t"
        "GTR 1, \\so\n\tGTR 2, \\so\n\tGS 1, \\c\n\t"
        "GTR 3, \\so\n\tGTR 4, \\so\n\tGS 2, \\c\n\t"
        "GTR 5, \\so\n\tGTR 6, \\so\n\tGS 3, \\c\n\t"
        "GTR 7, \\so\n\t"
        "ds_read2_b32 v[212:213], v192 offset0:\\eo offset1:\\eo+4\n\t"
        "ds_read2_b32 v[214:215], v192 offset0:\\eo+8 offset1:\\eo+12\n\t"
        "ds_read2_b32 v[216:217], v192 offset0:\\eo+16 offset1:\\eo+20\n\t"
        "ds_read2_b32 v[218:219], v192 offset0:\\eo+24 offset1:\\eo+28\n\t"
        "s_add_u32 m0, %[ring], \\so\n\t"
        "s_cmp_gt_i32 %[rem], 3\n\t"
        ".irp k, 0, 1, 2, 3, 4, 5, 6, 7\n\tv_exp_f32_e32 v[\\c+\\k], v[\\c+\\k]\n\t.endr\n\t"
        "s_waitcnt lgkmcnt(0)\n\t"
        "s_cbranch_scc0 2f\n\t"
        "GLOAD 0, 0, %[s0]\n\tGLOAD 1, 1, %[s1]\n\tGLOAD 2, 2, %[s2]\n\tGLOAD 3, 3, %[s3]\n\t"
        "s_add_u32 m0, m0, 0x1000\n\t"
        "GLOAD 4, 0, %[s0]\n\tGLOAD 5, 1, %[s1]\n\tGLOAD 6, 2, %[s2]\n\tGLOAD 7, 3, %[s3]\n\t"
        "GSIG \\c\n\t"
        "s_waitcnt vmcnt(8)\n\t"
        "s_branch 1f\n\t"
        "2:\n\t"
        "s_mov_b32 %[t], 0\n\t"
        "s_cmp_eq_u32 %[rem], 3\n\ts_cbranch_scc0 4f\n\t"
        "s_mov_b32 %[t], %[nl]\n\t"
        "GLOAD 0, 0, %[s0]\n\tGPART 1, 1, %[s1]\n\tGPART 2, 2, %[s2]\n\tGPART 3, 3, %[s3]\n\t"
        "s_add_u32 m0, m0, 0x1000\n\t"
        "GPART 4, 0, %[s0]\n\tGPART 5, 1, %[s1]\n\tGPART 6, 2, %[s2]\n\tGPART 7, 3, %[s3]\n\t"
        "4:\n\t"
        "GSIG \\c\n\t"
        "s_waitcnt vmcnt(8)\n\t"
        "GWAIT 7\n\tGWAIT 6\n\tGWAIT 5\n\tGWAIT 4\n\tGWAIT 3\n\tGWAIT 2\n\tGWAIT 1\n\tGWAIT 0\n\t"
        "1:\n\t"
        "GROWS 8192-\\so\n\t"
        "GCMP 0, vcc\n\t"
        "s_add_i32 %[rem], %[rem], -1\n\t"
        "GPAIR 0, \\c\n\tGPAIR 2, \\c\n\tGPAIR 4, \\c\n\tGPAIR 6, \\c\n\t"
        ".endm\n\t"
        ".macro GSIG c\n\t"
        ".if " STR(PKADD) "\n\t.irp k, 0, 2, 4, 6\n\tv_pk_add_f32 v[\\c+\\k:\\c+\\k+1], v[\\c+\\k:\\c+\\k+1], 1.0 op_sel_hi:[1,0]\n\t.endr\n\t.else\n\t"
        ".irp k, 0, 1, 2, 3, 4, 5, 6, 7\n\tv_add_f32_e32 v[\\c+\\k], 1.0, v[\\c+\\k]\n\t.endr\n\t.endif\n\t"
        ".irp k, 0, 1, 2, 3, 4, 5, 6, 7\n\tv_rcp_f32_e32 v[\\c+\\k], v[\\c+\\k]\n\t.endr\n\t"
        ".irp k, 0, 1, 2, 3\n\tv_cvt_pk_bf16_f32 v[220+\\k], v[\\c+2*\\k], v[\\c+2*\\k+1]\n\t.endr\n\t.endm\n\t"
        "s_waitcnt vmcnt(8)\n\t"
        "s_cmp_gt_i32 %[rem], 1\n\ts_cbranch_scc1 1f\n\ts_waitcnt vmcnt(0)\n\t1:\n\t"
        "GROWS 0\n\t"
        ".if " STR(LPRIO) " >= 3\n\ts_mov_b32 %[pr], 0\n\ts_setprio 2\n\t.endif\n\t"
        "0:\n\t"
        ".if " STR(LPRIO) " == 3\n\t"
        "s_cmp_eq_u32 %[pr], 1\n\ts_cbranch_scc0 5f\n\ts_setprio 1\n\t5:\n\t"
        "s_cmp_eq_u32 %[pr], 2\n\ts_cbranch_scc0 5f\n\ts_setprio 0\n\t5:\n\t"
        "s_add_u32 %[pr], %[pr], 1\n\t.endif\n\t"
        "GSTEP 0, 196, 64\n\t"
        "s_cbranch_scc0 9f\n\t"
        "GSTEP 8192, 204, 96\n\t"
        "v_add_u32_e32 v192, 0x100, v192\n\t"
        "s_cbranch_scc1 0b\n\t"
        "9:\n\t"
        "s_waitcnt lgkmcnt(0)\n\ts_nop 6\n\t"
        ".purgem GWAIT\n\t.purgem GROWS\n\t.purgem GTR\n\t.purgem GS\n\t.purgem GLOAD\n\t.purgem GPART\n\t.purgem GSUM\n\t.purgem GCMP\n\t.purgem GPAIR\n\t.purgem GSTEP\n\t.purgem GSIG"
        : "+{v[128:159]}"(acc), "+{v[192:195]}"(misc), "+{v[196:211]}"(cc), "=&{v[64:95]}"(t0), "=&{v[96:127]}"(t1), "=&{v[212:223]}"(t2),
          [rem] "+s"(G), [t] "=&s"(tmp), [pr] "=&s"(pr), [sm] "=&s"(sm)
        : "{v[160:175]}"(qf), "{v[176:191]}"(addr), [s0] "s"(d.s0), [s1] "s"(d.s1), [s2] "s"(d.s2), [s3] "s"(d.s3),
          [mask] "s"(mask), [ring] "s"(ring), [nl] "s"(nlast)
        : "vcc", "scc", "memory");
}

__device__ __forceinline__ u64 rank_edges(u32x16& ed, u32x16& es, u32x16& rk, u64 m0, u64 m1, u64 m2, u64 m3, unsigned keya) {
    u32x16 b4; u64 over; unsigned sel = 0x01050401u, c36 = 36;
    asm volatile(
        ".macro RADD k\n\tv_lshlrev_b32_sdwa v[96+\\k], 2, v[64+\\k] dst_sel:DWORD dst_unused:UNUSED_PAD src0_sel:DWORD src1_sel:BYTE_0\n\t"
        "ds_add_rtn_u32 v[112+\\k], v[96+\\k], v111 offset:%c[cnt]\n\t.endm\n\t"
        ".macro RENT k\n\tv_add_u32_e32 v[80+\\k], %c[bias], v[80+\\k]\n\tv_perm_b32 v[80+\\k], v[80+\\k], v[64+\\k], %[sel]\n\t.endm\n\t"
        ".macro RPUT k\n\tv_min_u32_e32 v110, %c[cap], v[112+\\k]\n\tv_mad_u32_u24 v110, v[96+\\k], %[c36], v110\n\t"
        "ds_write_b32 v110, v[80+\\k] offset:%c[img]\n\t.endm\n\t"
        "v_mov_b32_e32 v111, 4\n\t"
        ".irp k, 0, 2, 4, 6, 8, 10, 12\n\tv_mov_b64_e32 v[112+\\k:113+\\k], 0\n\t.endr\n\t"
        "s_mov_b64 exec, %[m0]\n\ts_waitcnt vmcnt(9)\n\tRADD 0\n\tRADD 1\n\tRADD 2\n\tRADD 3\n\t"
        "s_mov_b64 exec, %[m1]\n\ts_waitcnt vmcnt(8)\n\tRADD 4\n\tRADD 5\n\tRADD 6\n\tRADD 7\n\t"
        "s_mov_b64 exec, %[m2]\n\ts_waitcnt vmcnt(7)\n\tRADD 8\n\tRADD 9\n\tRADD 10\n\tRADD 11\n\t"
        "s_mov_b64 exec, %[m3]\n\ts_waitcnt vmcnt(6)\n\tRADD 12\n\t"
        "s_mov_b64 exec, %[m0]\n\ts_waitcnt vmcnt(5)\n\tRENT 0\n\tRENT 1\n\tRENT 2\n\tRENT 3\n\t"
        "s_waitcnt lgkmcnt(9)\n\tRPUT 0\n\tRPUT 1\n\tRPUT 2\n\tRPUT 3\n\t"
        "s_mov_b64 exec, %[m1]\n\ts_waitcnt vmcnt(4)\n\tRENT 4\n\tRENT 5\n\tRENT 6\n\tRENT 7\n\t"
        "s_waitcnt lgkmcnt(9)\n\tRPUT 4\n\tRPUT 5\n\tRPUT 6\n\tRPUT 7\n\t"
        "s_mov_b64 exec, %[m2]\n\ts_waitcnt vmcnt(3)\n\tRENT 8\n\tRENT 9\n\tRENT 10\n\tRENT 11\n\t"
        "s_waitcnt lgkmcnt(9)\n\tRPUT 8\n\tRPUT 9\n\tRPUT 10\n\tRPUT 11\n\t"
        "s_mov_b64 exec, %[m3]\n\ts_waitcnt vmcnt(0)\n\tRENT 12\n\t"
        "s_waitcnt lgkmcnt(12)\n\tRPUT 12\n\t"
        "s_mov_b64 exec, -1\n\t"
        "v_add_u32_e32 v93, %c[step], v93\n\t"
        "v_max3_u32 v110, v112, v113, v114\n\tv_max3_u32 v109, v115, v116, v117\n\t"
        "ds_write_b32 %[keya], v93 offset:%c[cnt]+1024\n\t"
        "v_max3_u32 v110, v110, v118, v119\n\tv_max3_u32 v109, v109, v120, v121\n\t"
        "v_max3_u32 v110, v110, v122, v123\n\tv_max3_u32 v110, v110, v109, v124\n\t"
        "v_cmp_le_u32_e32 vcc, %c[cap], v110\n\ts_mov_b64 %[over], vcc\n\t"
        ".purgem RADD\n\t.purgem RENT\n\t.purgem RPUT"
        : "+{v[64:79]}"(ed), "+{v[80:95]}"(es), "=&{v[96:111]}"(b4), "=&{v[112:127]}"(rk), [over] "=&s"(over)
        : [m0] "s"(m0), [m1] "s"(m1), [m2] "s"(m2), [m3] "s"(m3), [keya] "v"(keya), [sel] "s"(sel), [c36] "s"(c36),
          [cnt] "i"(L_CNT1), [img] "i"(L_IMG + 8), [bias] "i"(BIAS), [cap] "i"(4 * CAP1), [step] "i"(KEY_STEP)
        : "vcc", "memory");
    return over;
}
__device__ __forceinline__ unsigned read_image(f32x32& img, u32x16& meta, u32x16& sums, unsigned ima, unsigned cna) {
    unsigned no;
    asm volatile(
        "v_mov_b32_e32 v121, 0\n\tds_read_b32 v120, v121 offset:%c[cnt]+2048\n\t"
        ".irp k, 0, 1, 2, 3, 4, 5, 6, 7\n\tds_read_b128 v[64+4*\\k:67+4*\\k], %[ima] offset:4608*\\k\n\t.endr\n\t"
        ".irp k, 0, 1, 2, 3\n\tds_read2_b32 v[96+2*\\k:97+2*\\k], %[cna] offset0:64*\\k offset1:64*\\k+32\n\t.endr\n\t"
        "s_waitcnt lgkmcnt(11)\n\tv_readfirstlane_b32 %[no], v120\n\t"
        "v_add_u32_e32 v121, 0x400, %[cna]\n\t"
        ".irp k, 0, 1, 2, 3\n\tds_read2_b32 v[104+2*\\k:105+2*\\k], v121 offset0:64*\\k offset1:64*\\k+32\n\t.endr\n\t"
        "s_waitcnt lgkmcnt(12)\n\t"
        ".irp k, 0, 1, 2, 3\n\tv_add3_u32 v[112+\\k], v[64+4*\\k], v[65+4*\\k], v[66+4*\\k]\n\tv_add_u32_e32 v[112+\\k], v[112+\\k], v[67+4*\\k]\n\t.endr\n\t"
        "s_waitcnt lgkmcnt(8)\n\t"
        ".irp k, 4, 5, 6, 7\n\tv_add3_u32 v[112+\\k], v[64+4*\\k], v[65+4*\\k], v[66+4*\\k]\n\tv_add_u32_e32 v[112+\\k], v[112+\\k], v[67+4*\\k]\n\t.endr\n\t"
        ".irp k, 0, 1, 2, 3, 4, 5, 6, 7\n\tv_add_u32_dpp v[112+\\k], v[112+\\k], v[112+\\k] quad_perm:[1,0,3,2] row_mask:0xf bank_mask:0xf\n\t.endr\n\t"
        ".irp k, 0, 1, 2, 3, 4, 5, 6, 7\n\tv_add_u32_dpp v[112+\\k], v[112+\\k], v[112+\\k] quad_perm:[2,3,0,1] row_mask:0xf bank_mask:0xf\n\t.endr\n\t"
        ".irp k, 0, 1, 2, 3, 4, 5, 6, 7\n\tv_add_u32_dpp v[112+\\k], v[112+\\k], v[112+\\k] row_half_mirror row_mask:0xf bank_mask:0xf\n\t.endr\n\t"
        "s_waitcnt lgkmcnt(0)"
        : "=&{v[64:95]}"(img), "=&{v[96:111]}"(meta), "=&{v[112:127]}"(sums), [no] "=&s"(no)
        : [ima] "v"(ima), [cna] "v"(cna), [cnt] "i"(L_CNT1) : "memory");
    return no;
}
__device__ __forceinline__ void publish_image(f32x32 img, u32x16 meta, u32x16 sums, const u32x4& srd, unsigned ob) {
    u64 pc0 = 0x0101010101010101ull; unsigned so;
    asm volatile(
        ".macro PUB k\n\ts_mov_b64 exec, %[pc0]\n\tv_mov_b32_e32 v[64+4*\\k], v[96+\\k]\n\tv_mov_b32_e32 v[65+4*\\k], v[112+\\k]\n\t"
        "s_mov_b64 exec, -1\n\tv_xor_b32_e32 v[64+4*\\k], v[64+4*\\k], v[104+\\k]\n\t"
        "buffer_store_dwordx4 v[64+4*\\k:67+4*\\k], %[ob], %[srd], %[so] offen sc1\n\ts_add_u32 %[so], %[so], %c[step]\n\t.endm\n\t"
        "s_mov_b32 %[so], 0\n\t"
        ".irp k, 0, 1, 2, 3, 4, 5, 6, 7\n\tPUB \\k\n\t.endr\n\t.purgem PUB"
        : "+{v[64:95]}"(img), [so] "=&s"(so)
        : "{v[96:111]}"(meta), "{v[112:127]}"(sums), [srd] "s"(srd), [ob] "v"(ob), [pc0] "s"(pc0), [step] "i"(32 * G1 * 128) : "scc", "memory");
}

__device__ __forceinline__ void inbox_request(u32x16& raw, const u32x4& srd, unsigned ib) {
    unsigned o1 = 0x1000, o2 = 0x2000, o3 = 0x3000;
    asm volatile("buffer_load_dwordx4 v[64:67], %1, %2, 0 offen sc1\n\tbuffer_load_dwordx4 v[68:71], %1, %2, %3 offen sc1\n\t"
                 "buffer_load_dwordx4 v[72:75], %1, %2, %4 offen sc1\n\tbuffer_load_dwordx4 v[76:79], %1, %2, %5 offen sc1"
                 : "=&{v[64:79]}"(raw) : "v"(ib), "s"(srd), "s"(o1), "s"(o2), "s"(o3) : "memory");
}
__device__ __forceinline__ unsigned inbox_stage(u32x16 raw, const u32x4& srd, unsigned ib, unsigned key, int i4, unsigned hdra) {
    u32x16 t0; f32x32 t2; unsigned four = 4; u64 m0, m1, m2, m3; unsigned todo, fresh, spin, o1, o2, o3, over;
    u64 pc0 = 0x0101010101010101ull;
    asm volatile(
        ".macro IVAL j, so\n\t"
        "s_bitcmp1_b32 %[todo], \\j\n\ts_cbranch_scc0 2f\n\t"
        "v_xor_b32_e32 v[64+4*\\j], %[key], v[64+4*\\j]\n\t"
        "v_sub_u32_e32 v97, 0, v[65+4*\\j]\n\t"
        "v_add_u32_e32 v96, v[64+4*\\j], v[65+4*\\j]\n\t"
        "v_mov_b32_dpp v[80+\\j], v[64+4*\\j] quad_perm:[0,0,0,0] row_mask:0xf bank_mask:0xf\n\t"
        "v_cndmask_b32_e64 v96, v96, v97, %[pc0]\n\t"
        "s_nop 0\n\t"
        "v_mov_b32_dpp v[80+\\j], v[80+\\j] row_shr:4 row_mask:0xf bank_mask:0xa\n\t"
        "v_cmp_gt_i32_e32 vcc, v[80+\\j], v92\n\t"
        "v_add3_u32 v96, v96, v[66+4*\\j], v[67+4*\\j]\n\t"
        "v_lshrrev_b32_e32 v97, 14, v[80+\\j]\n\t"
        "v_cndmask_b32_e32 v96, 0, v96, vcc\n\t"
        "s_nop 1\n\tv_add_u32_dpp v96, v96, v96 quad_perm:[1,0,3,2] row_mask:0xf bank_mask:0xf\n\t"
        "s_nop 1\n\tv_add_u32_dpp v96, v96, v96 quad_perm:[2,3,0,1] row_mask:0xf bank_mask:0xf\n\t"
        "s_nop 1\n\tv_add_u32_dpp v96, v96, v96 row_half_mirror row_mask:0xf bank_mask:0xf\n\t"
        "v_or_b32_e32 v96, v96, v97\n\t"
        "v_cmp_ne_u32_e32 vcc, 0, v96\n\t"
        "s_cbranch_vccz 1f\n\t"
        "buffer_load_dwordx4 v[64+4*\\j:67+4*\\j], %[ib], %[srd], \\so offen sc1\n\t"
        "s_branch 2f\n\t"
        "1:\n\ts_bitset1_b32 %[fresh], \\j\n\t"
        "2:\n\t.endm\n\t"
        ".macro IRANK j\n\t"
        "s_bitcmp1_b32 %[fresh], \\j\n\ts_cbranch_scc0 2f\n\t"
        "v_cmp_lt_u32_e64 %[m0], v92, v[80+\\j]\n\tv_cmp_lt_u32_e64 %[m1], v93, v[80+\\j]\n\t"
        "v_cmp_lt_u32_e64 %[m2], v94, v[80+\\j]\n\tv_cmp_lt_u32_e64 %[m3], v95, v[80+\\j]\n\t"
        ".irp k, 0, 1, 2, 3\n\tv_lshrrev_b32_e32 v[84+\\k], 21, v[64+4*\\j+\\k]\n\t.endr\n\t"
        ".irp k, 0, 1, 2, 3\n\tv_and_b32_e32 v[84+\\k], 60, v[84+\\k]\n\t.endr\n\t"
        "v_cmp_lt_u32_e32 vcc, %c[cap], v[80+\\j]\n\t"
        "s_mov_b64 exec, %[m0]\n\tds_add_rtn_u32 v88, v84, %[four] offset:%c[cnt]\n\t"
        "s_mov_b64 exec, %[m1]\n\tds_add_rtn_u32 v89, v85, %[four] offset:%c[cnt]\n\t"
        "s_mov_b64 exec, %[m2]\n\tds_add_rtn_u32 v90, v86, %[four] offset:%c[cnt]\n\t"
        "s_mov_b64 exec, %[m3]\n\tds_add_rtn_u32 v91, v87, %[four] offset:%c[cnt]\n\t"
        "s_mov_b64 exec, %[pc0]\n\tds_write_b32 %[hdra], v[80+\\j] offset:128*\\j\n\t"
        "s_mov_b64 exec, -1\n\t"
        "s_cbranch_vccz 1f\n\t"
        "s_mov_b32 %[over], 1\n\t"
        "1:\n\ts_waitcnt lgkmcnt(1)\n\t"
        ".irp k, 0, 1, 2, 3\n\tv_min_u32_e32 v[88+\\k], %c[sub]-4, v[88+\\k]\n\t.endr\n\t"
        ".irp k, 0, 1, 2, 3\n\tv_mad_u32_u24 v[88+\\k], v[84+\\k], %[sub4], v[88+\\k]\n\t.endr\n\t"
        "s_mov_b64 exec, %[m0]\n\tds_write_b32 v88, v[64+4*\\j] offset:%c[list]\n\t"
        "s_mov_b64 exec, %[m1]\n\tds_write_b32 v89, v[65+4*\\j] offset:%c[list]\n\t"
        "s_mov_b64 exec, %[m2]\n\tds_write_b32 v90, v[66+4*\\j] offset:%c[list]\n\t"
        "s_mov_b64 exec, %[m3]\n\tds_write_b32 v91, v[67+4*\\j] offset:%c[list]\n\t"
        "s_mov_b64 exec, -1\n\t"
        "2:\n\t.endm\n\t"
        ".macro FADD j\n\t"
        ".irp k, 0, 1, 2, 3\n\tv_lshrrev_b32_e32 v[96+4*\\j+\\k], 21, v[64+4*\\j+\\k]\n\t.endr\n\t"
        ".irp k, 0, 1, 2, 3\n\tv_and_b32_e32 v[96+4*\\j+\\k], 60, v[96+4*\\j+\\k]\n\t.endr\n\t"
        "v_cmpx_lt_u32_e64 s[72+8*\\j:73+8*\\j], v94, v[80+\\j]\n\tds_add_rtn_u32 v[114+4*\\j], v[98+4*\\j], %[four] offset:%c[cnt]\n\t"
        "v_cmpx_lt_u32_e64 s[74+8*\\j:75+8*\\j], v95, v[80+\\j]\n\tds_add_rtn_u32 v[115+4*\\j], v[99+4*\\j], %[four] offset:%c[cnt]\n\t"
        "s_mov_b64 exec, -1\n\t"
        "v_cmpx_lt_u32_e64 s[68+8*\\j:69+8*\\j], v92, v[80+\\j]\n\tds_add_rtn_u32 v[112+4*\\j], v[96+4*\\j], %[four] offset:%c[cnt]\n\t"
        "v_cmpx_lt_u32_e64 s[70+8*\\j:71+8*\\j], v93, v[80+\\j]\n\tds_add_rtn_u32 v[113+4*\\j], v[97+4*\\j], %[four] offset:%c[cnt]\n\t"
        "s_mov_b64 exec, -1\n\t.endm\n\t"
        ".macro FPUT j\n\t"
        "s_waitcnt lgkmcnt(12)\n\t"
        ".irp k, 0, 1, 2, 3\n\tv_min_u32_e32 v[112+4*\\j+\\k], %c[sub]-4, v[112+4*\\j+\\k]\n\t.endr\n\t"
        ".irp k, 0, 1, 2, 3\n\tv_mad_u32_u24 v[112+4*\\j+\\k], v[96+4*\\j+\\k], %[sub4], v[112+4*\\j+\\k]\n\t.endr\n\t"
        ".irp k, 0, 1, 2, 3\n\ts_mov_b64 exec, s[68+8*\\j+2*\\k:69+8*\\j+2*\\k]\n\tds_write_b32 v[112+4*\\j+\\k], v[64+4*\\j+\\k] offset:%c[list]\n\t.endr\n\t"
        "s_mov_b64 exec, -1\n\t.endm\n\t"
        "s_movk_i32 %[o1], 0x1000\n\ts_movk_i32 %[o2], 0x2000\n\ts_movk_i32 %[o3], 0x3000\n\t"
        "v_mov_b32_e32 v92, %[i4]\n\tv_add_u32_e32 v93, 4, %[i4]\n\tv_add_u32_e32 v94, 8, %[i4]\n\tv_add_u32_e32 v95, 12, %[i4]\n\t"
        "s_mov_b32 %[todo], 15\n\ts_mov_b32 %[spin], 0x400000\n\ts_mov_b32 %[over], 0\n\t"
        ".if " STR(FASTIN) "\n\t"
        "s_waitcnt vmcnt(0)\n\t"
        ".irp j, 0, 1, 2, 3\n\tv_xor_b32_e32 v[64+4*\\j], %[key], v[64+4*\\j]\n\t.endr\n\t"
        ".irp j, 0, 1, 2, 3\n\tv_mov_b32_dpp v[80+\\j], v[64+4*\\j] quad_perm:[0,0,0,0] row_mask:0xf bank_mask:0xf\n\t.endr\n\t"
        ".irp j, 0, 1, 2, 3\n\tv_add_u32_e32 v[84+\\j], v[64+4*\\j], v[65+4*\\j]\n\t.endr\n\t"
        ".irp j, 0, 1, 2, 3\n\tv_mov_b32_dpp v[80+\\j], v[80+\\j] row_shr:4 row_mask:0xf bank_mask:0xa\n\t.endr\n\t"
        "s_mov_b64 exec, %[pc0]\n\t"
        ".irp j, 0, 1, 2, 3\n\tv_sub_u32_e32 v[84+\\j], 0, v[65+4*\\j]\n\t.endr\n\t"
        "s_mov_b64 exec, -1\n\t"
        "v_cmp_gt_i32_e64 %[m0], v80, v92\n\tv_cmp_gt_i32_e64 %[m1], v81, v92\n\tv_cmp_gt_i32_e64 %[m2], v82, v92\n\tv_cmp_gt_i32_e64 %[m3], v83, v92\n\t"
        ".irp j, 0, 1, 2, 3\n\tv_add3_u32 v[84+\\j], v[84+\\j], v[66+4*\\j], v[67+4*\\j]\n\t.endr\n\t"
        "v_cndmask_b32_e64 v84, 0, v84, %[m0]\n\tv_cndmask_b32_e64 v85, 0, v85, %[m1]\n\tv_cndmask_b32_e64 v86, 0, v86, %[m2]\n\tv_cndmask_b32_e64 v87, 0, v87, %[m3]\n\t"
        ".irp j, 0, 1, 2, 3\n\tv_add_u32_dpp v[84+\\j], v[84+\\j], v[84+\\j] quad_perm:[1,0,3,2] row_mask:0xf bank_mask:0xf\n\t.endr\n\t"
        ".irp j, 0, 1, 2, 3\n\tv_add_u32_dpp v[84+\\j], v[84+\\j], v[84+\\j] quad_perm:[2,3,0,1] row_mask:0xf bank_mask:0xf\n\t.endr\n\t"
        ".irp j, 0, 1, 2, 3\n\tv_add_u32_dpp v[84+\\j], v[84+\\j], v[84+\\j] row_half_mirror row_mask:0xf bank_mask:0xf\n\t.endr\n\t"
        "v_or3_b32 v96, v80, v81, v82\n\tv_max3_u32 v97, v80, v81, v82\n\tv_or_b32_e32 v96, v96, v83\n\tv_max_u32_e32 v97, v97, v83\n\t"
        "v_lshrrev_b32_e32 v96, 14, v96\n\tv_or3_b32 v96, v96, v84, v85\n\tv_or3_b32 v96, v96, v86, v87\n\t"
        "v_cmp_ne_u32_e32 vcc, 0, v96\n\t"
        "s_cbranch_vccnz 7f\n\t"
        "v_cmp_lt_u32_e32 vcc, %c[cap], v97\n\t"
        "FADD 0\n\tFADD 1\n\tFADD 2\n\tFADD 3\n\t"
        "s_cbranch_vccz 6f\n\ts_mov_b32 %[over], 1\n\t6:\n\t"
        "FPUT 0\n\tFPUT 1\n\tFPUT 2\n\tFPUT 3\n\t"
        "s_mov_b64 exec, %[pc0]\n\t"
        ".irp j, 0, 1, 2, 3\n\tds_write_b32 %[hdra], v[80+\\j] offset:128*\\j\n\t.endr\n\t"
        "s_mov_b64 exec, -1\n\t"
        "s_branch 9f\n\t"
        "7:\n\t.irp j, 0, 1, 2, 3\n\tv_xor_b32_e32 v[64+4*\\j], %[key], v[64+4*\\j]\n\t.endr\n\t"
        ".endif\n\t"
        "0:\n\t"
        "s_mov_b32 %[fresh], 0\n\t"
        "s_waitcnt vmcnt(0)\n\t"
        "IVAL 0, 0\n\tIVAL 1, %[o1]\n\tIVAL 2, %[o2]\n\tIVAL 3, %[o3]\n\t"
        "IRANK 0\n\tIRANK 1\n\tIRANK 2\n\tIRANK 3\n\t"
        "s_andn2_b32 %[todo], %[todo], %[fresh]\n\t"
        "s_cbranch_scc0 9f\n\t"
        "s_sub_u32 %[spin], %[spin], 1\n\t"
        "s_cbranch_scc0 0b\n\t"
        "s_waitcnt vmcnt(0)\n\t"
        "9:\n\t"
        ".purgem IVAL\n\t.purgem IRANK\n\t.purgem FADD\n\t.purgem FPUT"
        : "+{v[64:79]}"(raw), "=&{v[80:95]}"(t0), "=&{v[96:127]}"(t2), [m0] "=&s"(m0), [m1] "=&s"(m1), [m2] "=&s"(m2), [m3] "=&s"(m3),
          [todo] "=&s"(todo), [fresh] "=&s"(fresh), [spin] "=&s"(spin), [o1] "=&s"(o1), [o2] "=&s"(o2), [o3] "=&s"(o3), [over] "=&s"(over)
        : [srd] "s"(srd), [ib] "v"(ib), [key] "s"(key), [i4] "v"(i4), [hdra] "v"(hdra), [pc0] "s"(pc0), [sub4] "s"(SUBB / 4), [four] "v"(four),
          [cnt] "i"(L_CNT), [list] "i"(L_LIST), [sub] "i"(SUBB), [cap] "i"(4 * CAP1)
        : "vcc", "scc", "memory", "s68", "s69", "s70", "s71", "s72", "s73", "s74", "s75", "s76", "s77", "s78", "s79", "s80", "s81", "s82", "s83",
          "s84", "s85", "s86", "s87", "s88", "s89", "s90", "s91", "s92", "s93", "s94", "s95", "s96", "s97", "s98", "s99");
    return over;
}

template <int CTRL, int BANK> __device__ __forceinline__ unsigned dpp(unsigned old, unsigned v) { return __builtin_amdgcn_update_dpp(old, v, CTRL, 0xF, BANK, true); }
__device__ __forceinline__ unsigned bcast8(unsigned v) { v = dpp<0x00, 0xF>(0u, v); return __builtin_amdgcn_update_dpp(v, v, 0x114, 0xF, 0xA, false); }
__device__ __forceinline__ unsigned sum8(unsigned v) { v += dpp<0xB1, 0xF>(0u, v); v += dpp<0x4E, 0xF>(0u, v); return v + dpp<0x141, 0xF>(0u, v); }
__device__ __forceinline__ bf16x4 tr_read(LAS char* p) { return __builtin_amdgcn_ds_read_tr16_b64_v4bf16((LAS bf16x4*)p); }
__device__ __forceinline__ bf16x8 cat(bf16x4 a, bf16x4 b) { return __builtin_shufflevector(a, b, 0, 1, 2, 3, 4, 5, 6, 7); }
__device__ __forceinline__ unsigned pk2(float a, float b) { f32x2 v = {a, b}; bf16x2 r = __builtin_convertvector(v, bf16x2); return __builtin_bit_cast(unsigned, r); }
__device__ __forceinline__ f32x4 mfma(bf16x8 a, bf16x8 b, f32x4 c) { return __builtin_amdgcn_mfma_f32_16x16x32_bf16(a, b, c, 0, 0, 0); }
__device__ __forceinline__ unsigned lds_add(LAS char* p, unsigned v) { return __hip_atomic_fetch_add((LAS unsigned*)p, v, __ATOMIC_RELAXED, __HIP_MEMORY_SCOPE_WORKGROUP); }
__device__ __forceinline__ LAS unsigned* ldsp(unsigned a) { return (LAS unsigned*)(size_t)a; }
__device__ __forceinline__ unsigned byte0_x4(unsigned v) { unsigned r; asm("v_lshlrev_b32_sdwa %0, 2, %1 dst_sel:DWORD dst_unused:UNUSED_PAD src0_sel:DWORD src1_sel:BYTE_0" : "=v"(r) : "v"(v)); return r; }
__device__ __forceinline__ unsigned mad24(unsigned a, unsigned b, unsigned c) { unsigned r; asm("v_mad_u32_u24 %0, %1, %2, %3" : "=v"(r) : "v"(a), "s"(b), "v"(c)); return r; }
__device__ __forceinline__ void lds_arrive(LAS char* p, int lane) {
    asm volatile("s_waitcnt lgkmcnt(0)" ::: "memory");
    if (lane == 0) __hip_atomic_fetch_add((LAS unsigned*)p, 1u, __ATOMIC_RELAXED, __HIP_MEMORY_SCOPE_WORKGROUP);
}
__device__ __forceinline__ void lds_await(LAS char* p, unsigned n) {
    while (__builtin_amdgcn_readfirstlane(*(volatile LAS unsigned*)p) < n) __builtin_amdgcn_s_sleep(1);
    asm volatile("" ::: "memory");
}
__device__ __forceinline__ unsigned rfl(unsigned v) { return __builtin_amdgcn_readfirstlane(v); }

__global__ __launch_bounds__(512, 2) void k_graph(const unsigned short* x_, const int* src_, const int* dst_, const unsigned short* wq_, const unsigned short* wv_,
                                                  unsigned* ws_, unsigned short* out_, int N_, int E_) {
    u32x4 srd_d, srd_s, srd_ws, srd_x0, pw, po;
    u64 t_start;
    asm volatile("s_load_dwordx2 s[40:41], %[ka], 0x10\n\ts_load_dwordx2 s[44:45], %[ka], 0x8\n\ts_load_dwordx4 s[60:63], %[ka], 0x30\n\t"
                 "s_load_dwordx2 s[48:49], %[ka], 0x28\n\ts_load_dwordx2 s[52:53], %[ka], 0x0\n\ts_load_dwordx4 s[56:59], %[ka], 0x18\n\t"
                 "s_memrealtime s[64:65]\n\t"
                 "s_mov_b32 s43, 0x20000\n\ts_mov_b32 s47, 0x20000\n\ts_mov_b32 s51, 0x20000\n\ts_mov_b32 s55, 0x20000\n\ts_mov_b32 s50, %c[wsb]"
                 : "=&{s[40:43]}"(srd_d), "=&{s[44:47]}"(srd_s), "=&{s[48:51]}"(srd_ws), "=&{s[52:55]}"(srd_x0), "=&{s[56:59]}"(pw), "=&{s[60:63]}"(po), "=&{s[64:65]}"(t_start)
                 : [ka] "s"(__builtin_amdgcn_kernarg_segment_ptr()), [wsb] "i"(WS_BYTES));
    FENCE();
    __shared__ __attribute__((aligned(16))) char smem_[L_TOTAL];
    LAS char* smem = (LAS char*)smem_;
    const int t = threadIdx.x, l = t & 63, b = blockIdx.x;
    const int w = rfl(t >> 6);
    const int g = l >> 4, n = l & 15, q = (l >> 2) & 3, p = l & 3, pc = t & 7;
    const unsigned lds0 = (unsigned)(size_t)smem;
    const bool isprod = ((b >> 3) & 1) == 0;
    const int pg = ((b & 7) << 4) | (b >> 4);
    const unsigned e0 = pg * CH + 4 * t, e1 = pg * CH + 3072 + t;

    u32x16 ed, es;
    {
        const unsigned o = e0 * 4, o1 = w == 0 ? e1 * 4 : OOB, ok = WS_KEY + t * 4, om = WS_KEY + b * 4;
        const unsigned oc = (t - 256) * 128, dump = lds0 + L_DUMP + w * 256;
        int role = (w < 4 ? 0 : 2) + (isprod ? 0 : 1);
        unsigned k4 = 4096, k8 = 8192;
        asm volatile("s_cmp_eq_u32 %[role], 0\n\ts_cbranch_scc1 1f\n\t"
                     "s_mov_b32 m0, %[dump]\n\ts_cmp_eq_u32 %[role], 1\n\ts_cbranch_scc1 2f\n\t"
                     "s_getpc_b64 s[68:69]\n\ts_mov_b32 s70, " STR(CODE_BYTES) "\n\ts_mov_b32 s71, 0x20000\n\t"
                     ".if " STR(CODEPF) "\n\tbuffer_load_dword %[oc], s[68:71], 0 offen lds\n\t.endif\n\t"
                     "s_waitcnt lgkmcnt(0)\n\ts_branch 3f\n\t"
                     "2:\n\ts_waitcnt lgkmcnt(0)\n\ts_branch 3f\n\t"
                     "1:\n\ts_waitcnt lgkmcnt(0)\n\ts_lshl_b32 s42, s63, 2\n\ts_lshl_b32 s46, s63, 2\n\t"
                     "buffer_load_dwordx4 v[64:67], %[o], s[40:43], 0 offen\n\tbuffer_load_dwordx4 v[68:71], %[o], s[40:43], %[k4] offen\n\t"
                     "buffer_load_dwordx4 v[72:75], %[o], s[40:43], %[k8] offen\n\tbuffer_load_dword v76, %[o1], s[40:43], 0 offen\n\t"
                     "buffer_load_dwordx4 v[80:83], %[o], s[44:47], 0 offen\n\tbuffer_load_dwordx4 v[84:87], %[o], s[44:47], %[k4] offen\n\t"
                     "buffer_load_dwordx4 v[88:91], %[o], s[44:47], %[k8] offen\n\tbuffer_load_dword v92, %[o1], s[44:47], 0 offen\n\t"
                     "buffer_load_dword v93, %[ok], s[48:51], 0 offen sc1\n\tbuffer_load_dword v94, %[om], s[48:51], 0 offen sc1\n\t"
                     "3:\n\ts_lshl_b32 s54, s62, 8"
                     : "=&{v[64:79]}"(ed), "=&{v[80:95]}"(es)
                     : [o] "v"(o), [o1] "v"(o1), [ok] "v"(ok), [om] "v"(om), [oc] "v"(oc), [role] "s"(role), [dump] "s"(dump), [k4] "s"(k4), [k8] "s"(k8),
                       "{s[40:43]}"(srd_d), "{s[44:47]}"(srd_s), "{s[48:51]}"(srd_ws), "{s[52:55]}"(srd_x0), "{s[56:59]}"(pw), "{s[60:63]}"(po), "{s[64:65]}"(t_start)
                     : "memory", "scc", "s68", "s69", "s70", "s71");
        asm volatile("" : "+{s[48:51]}"(srd_ws), "+{s[52:55]}"(srd_x0), "+{s[56:59]}"(pw), "+{s[60:63]}"(po), "+{s[64:65]}"(t_start));
    }
    const void* const x = (const void*)((u64)srd_x0.x | ((u64)srd_x0.y << 32));
    const void* const wq = (const void*)((u64)pw.x | ((u64)pw.y << 32));
    const void* const wv = (const void*)((u64)pw.z | ((u64)pw.w << 32));
    void* const out = (void*)((u64)po.x | ((u64)po.y << 32));
    const int N = po.z, E = po.w;
    const unsigned xbytes = srd_x0.z;
    STAMP_DECL;
    const unsigned ring = lds0 + L_RING + w * 16384;
    const int jn = 8 * n + w;

    unsigned mykey = 0;
    auto node_row = [&](int tile, int m) { const int nd = b + 256 * (8 * (4 * m + g) + tile); return nd < N ? (unsigned)nd << 8 : OOB; };
    auto zero_staging = [&]() {
        const u32x4 z = {0u, 0u, 0u, 0u};
#pragma unroll
        for (int k = 0; k < 5; ++k) *(LAS u32x4*)(smem + L_IMG + k * 8192 + t * 16) = z;
    };

    u32x16 addr;
    f32x32 acc, wva, wvb;
    unsigned cv, lista, cnta, qa, keyn, inf;
    Srd4 srd_x;
    auto loop_constants = [&]() {
        srd_x = make_srd4(x, xbytes, BIAS * 256);
        lista = lds0 + L_LIST + w * (2 * SUBB) + g * 4; cnta = lds0 + L_CNT + (l & 31) * 4; qa = ring + l * 16; keyn = jn; inf = 0x7F800000u;
        asm volatile("" : "+s"(srd_x.s0), "+s"(srd_x.s1), "+s"(srd_x.s2), "+s"(srd_x.s3));
        asm volatile("" : "+v"(lista), "+v"(cnta), "+v"(qa), "+v"(keyn), "+v"(inf));
        const unsigned o0 = (n ^ (((g & 1) << 3) | (g >> 1))) << 4;
        const unsigned ra = (4 * (n & 3) + (n >> 2)) * 256 + (((4 * g) ^ (((n & 7) << 1) | (n >> 3))) << 4);
        const int i = 4 * g + q, r8 = i & 7;
        const unsigned ta = (4 * q + g) * 256 + 8 * (p & 1) + (((p >> 1) ^ (((i & 7) << 1) | (i >> 3))) << 4);
        cv = i * 256 + (((p ^ ((r8 & 1) | ((r8 & 6) << 1))) << 4) | ((p & 1) << 3));
#pragma unroll
        for (int s = 0; s < 4; ++s) { addr[s] = ring + (ra ^ (s << 4)); addr[12 + s] = o0 ^ (s << 5); }
#pragma unroll
        for (int c = 0; c < 8; ++c) addr[4 + c] = ring + (ta ^ (c << 5));
        asm volatile("" : "+{v[176:191]}"(addr), "+v"(cv));
    };
    auto clear_sums = [&]() { acc = 0.f; asm volatile("" : "+{v[128:159]}"(acc)); };
    auto wv_resident = [&]() {
#pragma unroll
        for (int a = 0; a < 8; ++a) {
            LAS char* pa = smem + L_WV0 + (cv ^ ((a >> 1) << 6) ^ ((a & 1) << 3));
            const f32x4 f0 = __builtin_bit_cast(f32x4, cat(tr_read(pa), tr_read(pa + 4096)));
            const f32x4 f1 = __builtin_bit_cast(f32x4, cat(tr_read(pa + 16384), tr_read(pa + 16384 + 4096)));
#pragma unroll
            for (int k = 0; k < 4; ++k) { wva[4 * a + k] = f0[k]; wvb[4 * a + k] = f1[k]; }
        }
        asm volatile("" : "+{v[224:255]}"(wva), "+{v[32:63]}"(wvb));
    };

    if (isprod) zero_staging();
    if (t < 8) *(LAS u32x4*)(smem + L_CNT + t * 16) = u32x4{0u, 0u, 0u, 0u};
    WG_BARRIER();

    if (__builtin_expect(w < 4, 1)) {
        if (__builtin_expect(isprod, 1)) {
            if (PRIO) __builtin_amdgcn_s_setprio(2);
            unsigned ob = WS_INBOX + ((t >> 3) * G1 + pg) * 128 + pc * 16, ima = lds0 + L_IMG + (t >> 3) * 144 + pc * 16;
            PIN(ob); PIN(ima);
            unsigned cna = lds0 + L_CNT1 + (t >> 3) * 4;
            PIN(cna);
            const bool v0 = e0 < (unsigned)E, v1 = e0 + 1024 < (unsigned)E, v2 = e0 + 2048 < (unsigned)E, v3 = w == 0 && e1 < (unsigned)E;
            STAMP(1);
            u32x16 rk;
            const u64 over = rank_edges(ed, es, rk, __builtin_amdgcn_ballot_w64(v0), __builtin_amdgcn_ballot_w64(v1), __builtin_amdgcn_ballot_w64(v2),
                                        __builtin_amdgcn_ballot_w64(v3), t * 4);
            mykey = es[14];
            if (__builtin_expect(over != 0, 0)) {
#pragma unroll
                for (int k = 0; k < 13; ++k)
                    if (rk[k] >= 4u * CAP1) {
                        const unsigned o = WS_OVF + (pg * CH + lds_add(smem + L_CNT1 + 2048, 1)) * 4;
                        const unsigned v = ((((es[k] >> 8) & 0xFFFF) - BIAS) << 16) | ed[k];
                        ST1(v, srd_ws, o, " sc1");
                    }
            }
            if (XSYNC) { lds_arrive(smem + L_CNT + 96, l); lds_await(smem + L_CNT + 96, 4); } else WG_BARRIER();
            STAMP(3);
            f32x32 img; u32x16 meta, sums;
            const unsigned no = read_image(img, meta, sums, ima, cna);
            lds_arrive(smem + L_CNT + 84, l);
            if (__builtin_expect(no != 0, 0)) {
                const unsigned o = t == 0 ? WS_NOVF + pg * 4 : OOB;
                ST1(no, srd_ws, o, " sc1");
                VMWAIT(0);
                if (XSYNC) { lds_arrive(smem + L_CNT + 100, l); lds_await(smem + L_CNT + 100, 4); } else WG_BARRIER();
            }
            publish_image(img, meta, sums, srd_ws, ob);
            if (XAFTER) lds_arrive(smem + L_CNT + 104, l);
            STAMP(4);
        } else {
            { const unsigned om = WS_KEY + b * 4; asm volatile("buffer_load_dword %0, %1, %2, 0 offen sc1\n\ts_waitcnt vmcnt(0)" : "=&v"(mykey) : "v"(om), "s"(srd_ws) : "memory"); }
            STAMP(1);
            if (PARTNER) {
                const unsigned o = t < 196 ? ((b - 8) << 8) + ((t >> 1) << 16) + ((t & 1) << 7) : OOB;
                while (__builtin_amdgcn_s_memrealtime() < t_start + X_T) __builtin_amdgcn_s_sleep(1);
                asm volatile("buffer_load_dword %1, %0, 0 offen lds" :: "s"(srd_x0), "v"(o) : "memory");
            }
            if (PRIO) __builtin_amdgcn_s_setprio(2);
            STAMP(2); STAMP(3); STAMP(4);
        }
        STAMP(5);
        mykey = rfl(mykey) + KEY_STEP;
        loop_constants();
        clear_sums();
        unsigned ib = WS_INBOX + b * (G1 * 128) + t * 16;
        PIN(ib);
        u64 t_go = t_start + POLL_T;
        if (POLL_D) { lds_await(smem + L_CNT + 80, 4); t_go = max(t_go, __builtin_amdgcn_s_memrealtime() + POLL_D); }
        while (__builtin_amdgcn_s_memrealtime() < t_go) __builtin_amdgcn_s_sleep(1);
        STAMP(6);
        u32x16 raw;
        inbox_request(raw, srd_ws, ib);
        if (__builtin_expect(inbox_stage(raw, srd_ws, ib, mykey, 16 * pc - 8, lds0 + L_HDR + (t >> 3) * 4) != 0, 0)) {
            LAS unsigned* hp = (LAS unsigned*)(smem + L_HDR) + 32 * (l >> 3 & 3) + 8 * w + (l & 7);
            const unsigned hv = *hp;
            for (u64 m = __builtin_amdgcn_ballot_w64(l < 32 && hv > 4u * CAP1); m; m &= m - 1) {
                const int k = __builtin_ctzll(m), gp = 32 * (k >> 3) + 8 * w + (k & 7);
                unsigned u, no; const unsigned o = WS_OVF + (gp * CH + l) * 4, on = WS_NOVF + gp * 4;
                asm volatile("buffer_load_dword %0, %2, %4, 0 offen sc1\n\tbuffer_load_dword %1, %3, %4, 0 offen sc1\n\ts_waitcnt vmcnt(0)"
                             : "=&v"(u), "=&v"(no) : "v"(o), "v"(on), "s"(srd_ws) : "memory");
                if (rfl(no) <= 64) {
                    if (l < no && (u & 0xFF) == (unsigned)b) {
                        const unsigned e = __builtin_amdgcn_perm((u >> 16) + BIAS, u, 0x01050401u), key = (e >> 21) & 60;
                        *(LAS unsigned*)(smem + L_LIST + key * (SUBB / 4) + min(lds_add(smem + L_CNT + key, 4), SUBB - 4u)) = e;
                    }
                    if (l == k) *hp = hv | 0x80000000u;
                } else *(LAS unsigned*)(smem + L_CNT + 64) = 1;
            }
        }
        if (PRIO) __builtin_amdgcn_s_setprio(0);
        STAMP(7);
    } else {
        const int j = w - 4;
        auto prefetch = [&](int k0, int kn) {
            const unsigned ln = (b >> 3) * (kn / 4) + (t - 256), o = t - 256 < kn / 4 ? k0 * 1024 + ln * 128 : OOB;
            set_m0(lds0 + L_DUMP + w * 256);
            asm volatile("s_nop 0\n\tbuffer_load_dword %1, %0, 0 offen lds" :: "s"(srd_x0), "v"(o) : "memory");
        };
        STAMP(1);
        u32x4 wr[8], xr[8];
        {
            const u32x4 srd_q = make_srd(wq, 32768);
            const unsigned o = j * 8192 + l * 16, k4 = 4096;
            LD4(wr[0], srd_q, o, ""); LD4(wr[1], srd_q, o, " offset:1024"); LD4(wr[2], srd_q, o, " offset:2048"); LD4(wr[3], srd_q, o, " offset:3072");
            LD4S(wr[4], srd_q, o, k4, ""); LD4S(wr[5], srd_q, o, k4, " offset:1024"); LD4S(wr[6], srd_q, o, k4, " offset:2048"); LD4S(wr[7], srd_q, o, k4, " offset:3072");
        }
        unsigned ox[8];
#pragma unroll
        for (int m = 0; m < 8; ++m) { ox[m] = node_row(m < 4 ? j : w, m & 3) + n * 16; PIN(ox[m]); }
        unsigned aqa[8], xfa[4];
        unsigned xa = ring + g * 256 + ((n ^ g) << 4), wa = lds0 + L_WQ + j * 16384 + g * 256 + ((n ^ g) << 4) + ((n & 4) << 1);
        {
            const unsigned aq = lds0 + L_WQ + (8 * g + q) * 256 + ((((4 * p) ^ (q | ((g & 1) << 2))) << 4) | ((p & 1) << 3));
#pragma unroll
            for (int s = 0; s < 4; ++s) { xfa[s] = ring + n * 256 + (((4 * s + g) ^ n) << 4); PIN(xfa[s]); }
#pragma unroll
            for (int c = 0; c < 8; ++c) { aqa[c] = aq ^ ((c >> 1) << 4) ^ ((c & 1) << 3); PIN(aqa[c]); }
            PIN(xa); PIN(wa);
        }
        loop_constants();
        STAMP(2);
        if (isprod && !XSYNC) {
            WG_BARRIER();
            if (__builtin_expect(rfl(*(LAS unsigned*)(smem + L_CNT1 + 2048)) != 0, 0)) WG_BARRIER();
            if (W_SLEEP) __builtin_amdgcn_s_sleep(W_SLEEP);
        }
        STAMP(3);
        auto row_loads = [&]() {
#pragma unroll
            for (int m = 0; m < 8; ++m) LD4(xr[m], srd_x0, ox[m], "");
        };
        asm volatile("s_waitcnt vmcnt(0)" : "+v"(wr[0]), "+v"(wr[1]), "+v"(wr[2]), "+v"(wr[3]), "+v"(wr[4]), "+v"(wr[5]), "+v"(wr[6]), "+v"(wr[7]) :: "memory");
        if (isprod && XAFTER) lds_await(smem + L_CNT + 104, 4);
        else while (__builtin_amdgcn_s_memrealtime() < t_start + X_T) __builtin_amdgcn_s_sleep(1);
        row_loads();
#pragma unroll
        for (int m = 0; m < 8; ++m) {
            const unsigned a = (wa ^ ((m & 2) << 5)) + m * 1024;
            *(LAS u32x2*)(size_t)a = u32x2{wr[m].x, wr[m].y};
            *(LAS u32x2*)(size_t)(a ^ 8) = u32x2{wr[m].z, wr[m].w};
        }
        asm volatile("s_waitcnt vmcnt(0)" : "+v"(xr[0]), "+v"(xr[1]), "+v"(xr[2]), "+v"(xr[3]), "+v"(xr[4]), "+v"(xr[5]), "+v"(xr[6]), "+v"(xr[7]) :: "memory");
#pragma unroll
        for (int m = 0; m < 8; ++m) *(LAS u32x4*)(size_t)((xa ^ ((m & 3) << 6)) + m * 1024) = xr[m];
        STAMP(4);
        lds_arrive(smem + L_CNT + 80, l);
        lds_await(smem + L_CNT + 80, 4);
        STAMP(5);
        bf16x8 xo[2][4], fr[2][8];
        f32x4 qc[2][8];
        auto wq_frags = [&](int u, bf16x8 (&f)[8]) {
#pragma unroll
            for (int a = 0; a < 8; ++a) {
                LAS char* pa = (LAS char*)(size_t)aqa[a] + u * 16384;
                f[a] = cat(tr_read(pa), tr_read(pa + 1024));
            }
        };
#pragma unroll
        for (int u = 0; u < 4; ++u) { xo[0][u] = *(LAS bf16x8*)(size_t)xfa[u]; xo[1][u] = *(LAS bf16x8*)(size_t)(xfa[u] + 4096); }
        wq_frags(0, fr[0]);
        {
            const Srd4 srd_v = make_srd4(wv, 32768);
            const unsigned r0 = (j * 32 + g) << 8, o0 = (n ^ ((g & 1) | ((g & 2) << 1))) << 4;
            const unsigned e[8] = {r0, r0 + 1024, r0 + 2048, r0 + 3072, r0 + 4096, r0 + 5120, r0 + 6144, r0 + 7168};
            const unsigned base = lds0 + (j < 2 ? L_WV0 + j * 16384 : L_WV1 + (j - 2) * 8192);
            const u32x4 off = {o0, o0 ^ 128, o0, o0 ^ 128};
            dma_half(base, srd_v, e, off); dma_half(base + 4096, srd_v, e + 4, off);
        }
        if (PFA_KB) prefetch(0, PFA_KB);
#pragma unroll
        for (int u = 0; u < 4; ++u) {
            if (u < 3) wq_frags(u + 1, fr[(u + 1) & 1]);
            FENCE();
#pragma unroll
            for (int a = 0; a < 8; ++a)
#pragma unroll
                for (int tt = 0; tt < 2; ++tt) qc[tt][a] = mfma(fr[u & 1][a], xo[tt][u], u ? qc[tt][a] : f32x4{0.f, 0.f, 0.f, 0.f});
            FENCE();
            if (!WVLATE && u == 1 && j < 2) { VMWAIT(0); lds_arrive(smem + L_CNT + 88, l); }
        }
        STAMP(6);
        if (PFB_KB) { asm volatile("s_waitcnt vmcnt(%c0)" :: "i"(PFA_KB ? 1 : 0) : "memory"); prefetch(PFA_KB, PFB_KB); }
        if (isprod) lds_await(smem + L_CNT + 84, 4);
#pragma unroll
        for (int tt = 0; tt < 2; ++tt)
#pragma unroll
            for (int s = 0; s < 4; ++s) {
                const float k = -0.08838834764831845f * 1.4426950408889634f;
                const f32x4 c0 = qc[tt][2 * s] * k, c1 = qc[tt][2 * s + 1] * k;
                *(LAS u32x4*)(size_t)(ring - (tt ? 0 : 65536) + s * 1024 + l * 16) = u32x4{pk2(c0[0], c0[1]), pk2(c0[2], c0[3]), pk2(c1[0], c1[1]), pk2(c1[2], c1[3])};
            }
        clear_sums();
        if (!WVLATE) { lds_await(smem + L_CNT + 88, 2); wv_resident(); }
        if (!PFB_KB) asm volatile("s_waitcnt vmcnt(%c0)" :: "i"(PFA_KB ? 1 : 0) : "memory");
        if (PFC_KB) prefetch(PFA_KB + PFB_KB, PFC_KB);
        STAMP(7);
    }
    WG_BARRIER();
    STAMP(8);
    if (LPRIO >= 3) __builtin_amdgcn_s_setprio(3);
    if (LPRIO == 1 && w >= 4) __builtin_amdgcn_s_setprio(1);
    if (LPRIO == 2) { if (w >= 6) __builtin_amdgcn_s_setprio(3); else if (w >= 4) __builtin_amdgcn_s_setprio(2); else if (w >= 2) __builtin_amdgcn_s_setprio(1); }

    u32x16 qf;
    const u32x4 doff = {addr[12], addr[13], addr[14], addr[15]};
    LAS char* mylist = smem + L_LIST + w * (2 * SUBB);
    int gcur = G1, pos = 0;
    unsigned hmask = 0xFFFFFFFFu;
    auto batch = [&](auto first_) {
        constexpr bool first = decltype(first_)::value;
        auto entries = [&](int grp, unsigned (&e)[8]) {
#pragma unroll
            for (int m = 0; m < 8; ++m) e[m] = *(LAS unsigned*)(size_t)(lista + grp * 128 + m * 16);
        };
        unsigned cw = *(LAS unsigned*)(size_t)(first ? cnta : lds0 + L_CNT + (__builtin_amdgcn_mbcnt_lo(~0u, 0u) & 31) * 4), e0[8], e1[8];
        entries(0, e0);
        if constexpr (first) {
#pragma unroll
            for (int s = 0; s < 4; ++s) {
                const u32x4 v = *(LAS u32x4*)(size_t)(qa + s * 1024);
                qf[4 * s] = v.x; qf[4 * s + 1] = v.y; qf[4 * s + 2] = v.z; qf[4 * s + 3] = v.w;
            }
        }
        entries(1, e1);
        FENCE();
        int lo = __builtin_amdgcn_readlane(cw, 2 * w) >> 2, hi = __builtin_amdgcn_readlane(cw, 2 * w + 1) >> 2;
        if constexpr (first) {
            if (__builtin_amdgcn_readlane(cw, 16)) gcur = 0;
            if (__builtin_amdgcn_ballot_w64(cw > 4u * CAP2)) { gcur = -1; lo = 0; hi = 0; hmask = 0x7FFFFFFFu; }
        }
        const int c = lo + hi, G = (c + 31) >> 5;
        const int nlast = G > 2 ? (((c - 1) & 31) >> 2) + 1 : 8;
        auto join = [&]() {
            for (int k = 0; k < G * 32 - lo; k += 64) {
                const unsigned e = *(LAS unsigned*)(mylist + SUBB + (l + k) * 4);
                if (l + k < G * 32 - lo) *(LAS unsigned*)(mylist + (lo + l + k) * 4) = l + k < hi ? e : PAD;
            }
        };
        const bool early = lo >= 64;
        if (__builtin_expect(!early, 0)) { join(); entries(0, e0); entries(1, e1); }
        if constexpr (first) asm volatile("s_waitcnt lgkmcnt(0)" : "+{v[160:175]}"(qf) :: "memory");
        if (G > 0) dma_group(ring, srd_x, e0, doff);
        if constexpr (first) {
            if (WVLATE == 1) {
                if (G > 1 && w < 6) dma_group(ring + 8192, srd_x, e1, doff);
                wv_resident();
                lds_arrive(smem + L_CNT + 92, l);
                if (w >= 6) { lds_await(smem + L_CNT + 92, 8); if (G > 1) dma_group(ring + 8192, srd_x, e1, doff); }
            } else if (WVLATE == 2) {
                wv_resident();
                lds_arrive(smem + L_CNT + 92, l);
                if (w >= 6) lds_await(smem + L_CNT + 92, 8);
                if (G > 1) dma_group(ring + 8192, srd_x, e1, doff);
            } else {
                if (w < 4) { wv_resident(); lds_arrive(smem + L_CNT + 92, l); }
                else if (w >= 6) lds_await(smem + L_CNT + 92, 4);
                if (G > 1) dma_group(ring + 8192, srd_x, e1, doff);
            }
        } else if (G > 1) dma_group(ring + 8192, srd_x, e1, doff);
        f32x16 cc;
#pragma unroll
        for (int m = 0; m < 8; ++m) {
            cc[m] = (e0[m] & 0xFF) == keyn ? 0.f : __uint_as_float(inf);
            cc[8 + m] = (e1[m] & 0xFF) == keyn ? 0.f : __uint_as_float(inf);
        }
        if (early) join();
        STAMP(9);
        if (G > 0) gather_loop(acc, qf, addr, u32x4{lista, keyn, inf, 0u}, cc, srd_x, ring, G, nlast);
    };
    batch(std::true_type{});
    while (__builtin_expect(gcur < G1, 0)) {
        unsigned lim = G1 * 32;
        if (gcur >= 0) {
            const u64 m0 = __builtin_amdgcn_ballot_w64(l >= gcur && (int)(*(LAS unsigned*)(smem + L_HDR + l * 4) & hmask) > 4 * CAP1);
            const u64 m1 = __builtin_amdgcn_ballot_w64(l + 64 >= gcur && (int)(*(LAS unsigned*)(smem + L_HDR + 256 + l * 4) & hmask) > 4 * CAP1);
            if (!(m0 | m1)) break;
            gcur = m0 ? __builtin_ctzll(m0) : 64 + __builtin_ctzll(m1);
            unsigned v; const unsigned o = WS_NOVF + gcur * 4;
            asm volatile("buffer_load_dword %0, %1, %2, 0 offen sc1\n\ts_waitcnt vmcnt(0)" : "=&v"(v) : "v"(o), "s"(srd_ws) : "memory");
            lim = rfl(v);
        }
        const unsigned i = pos + t;
        const bool cand = t < CAP2 && i < lim;
        unsigned u;
        { const unsigned o = cand ? (gcur < 0 ? WS_INBOX + b * (G1 * 128) : WS_OVF + gcur * CH * 4) + i * 4 : OOB;
          asm volatile("buffer_load_dword %0, %1, %2, 0 offen sc1\n\ts_waitcnt vmcnt(0)" : "=&v"(u) : "v"(o), "s"(srd_ws) : "memory"); }
        WG_BARRIER();
        if (t < 16) *(LAS unsigned*)(smem + L_CNT + t * 4) = 0;
        WG_BARRIER();
        if (cand) {
            unsigned e; bool v;
            if (gcur < 0) { e = (i & 3) ? u : u ^ mykey; v = ((i & 31) - 2) * 4 < *(LAS unsigned*)(smem + L_HDR + (i >> 5) * 4); }
            else { e = (((u >> 16) + BIAS) << 8) | ((u >> 8) & 0xFF); v = (u & 0xFF) == (unsigned)b; }
            if (v) *(LAS unsigned*)(smem + L_LIST + (e & 7) * (2 * SUBB) + lds_add(smem + L_CNT + (e & 7) * 8, 4)) = e;
        }
        pos += CAP2;
        if ((unsigned)pos >= lim) { ++gcur; pos = 0; }
        WG_BARRIER();
        batch(std::false_type{});
    }
    STAMP(10);

    {
        bf16x8 wl[2][8], ag[4];
#pragma unroll
        for (int u = 0; u < 2; ++u)
#pragma unroll
            for (int a = 0; a < 8; ++a) {
                LAS char* pa = smem + L_WV1 + (cv ^ ((a >> 1) << 6) ^ ((a & 1) << 3)) + u * 8192;
                wl[u][a] = cat(tr_read(pa), tr_read(pa + 4096));
            }
#pragma unroll
        for (int u = 0; u < 4; ++u) {
            const u32x4 v = {pk2(acc[8 * u], acc[8 * u + 1]), pk2(acc[8 * u + 2], acc[8 * u + 3]),
                             pk2(acc[8 * u + 4], acc[8 * u + 5]), pk2(acc[8 * u + 6], acc[8 * u + 7])};
            ag[u] = __builtin_bit_cast(bf16x8, v);
        }
        FENCE();
        f32x4 oc[8];
#pragma unroll
        for (int a = 0; a < 8; ++a) oc[a] = mfma(__builtin_bit_cast(bf16x8, f32x4{wva[4 * a], wva[4 * a + 1], wva[4 * a + 2], wva[4 * a + 3]}), ag[0], f32x4{0.f, 0.f, 0.f, 0.f});
#pragma unroll
        for (int a = 0; a < 8; ++a) oc[a] = mfma(__builtin_bit_cast(bf16x8, f32x4{wvb[4 * a], wvb[4 * a + 1], wvb[4 * a + 2], wvb[4 * a + 3]}), ag[1], oc[a]);
#pragma unroll
        for (int a = 0; a < 8; ++a) oc[a] = mfma(wl[0][a], ag[2], oc[a]);
        FENCE();
        const unsigned ow = ring + n * 256 + ((g ^ n) << 4) + ((g & 1) << 3);
#pragma unroll
        for (int a = 0; a < 8; ++a) oc[a] = mfma(wl[1][a], ag[3], oc[a]);
#pragma unroll
        for (int a = 0; a < 8; ++a)
            *(LAS u32x2*)(size_t)(ow ^ ((a >> 1) << 6) ^ ((a & 1) << 3)) = u32x2{pk2(oc[a][0], oc[a][1]), pk2(oc[a][2], oc[a][3])};
        const auto rs_o = __builtin_amdgcn_make_buffer_rsrc(out, 0, xbytes, 0x00020000);
#pragma unroll
        for (int m = 0; m < 4; ++m) {
            const u32x4 v = *(LAS u32x4*)(size_t)(ring + m * 1024 + l * 16);
            __builtin_amdgcn_raw_buffer_store_b128(v, rs_o, node_row(w, m) + ((n ^ g ^ (4 * m)) << 4), 0, OUT_AUX);
        }
    }
    if (w == 0) { const unsigned o = l == 0 ? WS_KEY + b * 4 : OOB; ST1(mykey, srd_ws, o, " sc1"); }
    STAMP(11);
}

extern "C" void kernel_launch(void* const* d_in, const int* in_sizes, int n_in,
                              void* d_out, int out_size, void* d_ws, size_t ws_size,
                              hipStream_t stream) {
    const int N = in_sizes[0] / 128, E = in_sizes[1];
    k_graph<<<NB, 512, 0, stream>>>((const unsigned short*)d_in[0], (const int*)d_in[1], (const int*)d_in[2],
                                    (const unsigned short*)d_in[3], (const unsigned short*)d_in[4],
                                    (unsigned*)d_ws, (unsigned short*)d_out, N, E);
}
```

```cpp
#define POLL_T 290
#include <hip/hip_runtime.h>
#include <hip/hip_bf16.h>
#include <stdint.h>
#include <type_traits>
#define ORD 1
#define SKIP 0
#define STR2(x) #x
#define STR(x) STR2(x)

typedef __attribute__((ext_vector_type(8))) __bf16 bf16x8;
typedef __attribute__((ext_vector_type(4))) __bf16 bf16x4;
typedef __attribute__((ext_vector_type(2))) __bf16 bf16x2;
typedef __attribute__((ext_vector_type(4))) float f32x4;
typedef __attribute__((ext_vector_type(2))) float f32x2;
typedef __attribute__((ext_vector_type(4))) unsigned u32x4;
typedef __attribute__((ext_vector_type(2))) unsigned u32x2;
typedef __attribute__((ext_vector_type(32))) float f32x32;
typedef __attribute__((ext_vector_type(16))) float f32x16;
typedef __attribute__((ext_vector_type(16))) unsigned u32x16;
typedef __attribute__((ext_vector_type(12))) unsigned u32x12;
typedef __attribute__((ext_vector_type(8))) unsigned u32x8;
typedef unsigned long long u64;
#define LAS __attribute__((address_space(3)))

constexpr int NB = 256;
constexpr int G1 = 128;
constexpr int CH = 3136;
constexpr int CAP1 = 30;
constexpr int CAP2 = 192;
constexpr int SUBB = 784;
#define POLL_AT 3
#define OUT_AUX 16
#define LPRIO 3
#define PKADD 1
#define CODE_BYTES 24576
#define PARTNER 1
#define XSYNC 1
#define WVLATE 1
#define PFA_KB 0
#define PFB_KB 0
#define PFC_KB 1024
#define X_T 0
#define XAFTER 0
#define POLL_D 60
#define FASTIN 1
#define W_SLEEP 3
#define PRIO 1
#define SPLIT 16384
constexpr unsigned KEY_STEP = 0x9E3779B1u;
constexpr unsigned WS_KEY = 0;
constexpr unsigned WS_NOVF = 2048;
constexpr unsigned WS_INBOX = 4096;
constexpr unsigned WS_OVF = WS_INBOX + NB * G1 * 128;
constexpr unsigned WS_BYTES = WS_OVF + G1 * CH * 4;
constexpr unsigned OOB = 0x80000000u;
constexpr unsigned BIAS = 32768 - SPLIT;
constexpr unsigned PAD = 0x00FFFFFFu;

constexpr int L_CNT = 0;
constexpr int L_HDR = 128;
constexpr int L_LIST = 768;
constexpr int L_RING = L_LIST + 16 * SUBB;
constexpr int L_IMG = L_RING;
constexpr int L_CNT1 = L_IMG + NB * 144;
constexpr int L_WQ = L_RING + 2 * 16384 + 8192;
constexpr int L_WV0 = L_RING + 6 * 16384 + 8192;
constexpr int L_WV1 = L_RING + 131072;
constexpr int L_DUMP = L_WV1 + 16384;
constexpr int L_TOTAL = L_DUMP + 2048;
static_assert(WS_KEY == 0 && CH == 3072 + 64 && L_RING % 256 == 0 && L_TOTAL <= 163840, "LDS map");

#define STAMP_DECL
#define STAMP(k)
#define STAMP_FLUSH(base, k0, k1)

__device__ __forceinline__ u32x4 make_srd(const void* p, unsigned bytes, unsigned back = 0) {
    const u64 a = (u64)p - back;
    u32x4 s; s.x = (unsigned)a; s.y = (unsigned)(a >> 32); s.z = bytes + back; s.w = 0x00020000u; return s;
}
#define LD4(dst, srd, off, FL) asm volatile("buffer_load_dwordx4 %0, %1, %2, 0 offen" FL : "=v"(dst) : "v"(off), "s"(srd) : "memory")
#define LD4S(dst, srd, off, so, FL) asm volatile("buffer_load_dwordx4 %0, %1, %2, %3 offen" FL : "=v"(dst) : "v"(off), "s"(srd), "s"(so) : "memory")
#define LD1(dst, srd, off, FL) asm volatile("buffer_load_dword %0, %1, %2, 0 offen" FL : "=v"(dst) : "v"(off), "s"(srd) : "memory")
#define ST1(val, srd, off, FL) asm volatile("buffer_store_dword %0, %1, %2, 0 offen" FL :: "v"(val), "v"(off), "s"(srd) : "memory")
#define PIN(x) asm volatile("" : "+v"(x))
#define VMWAIT(n) asm volatile("s_waitcnt vmcnt(" #n ")" ::: "memory")
#define FENCE() __builtin_amdgcn_sched_barrier(0)
#define WG_BARRIER() do { asm volatile("s_waitcnt lgkmcnt(0)" ::: "memory"); __builtin_amdgcn_s_barrier(); asm volatile("" ::: "memory"); } while (0)

struct Srd4 { u32x4 s0, s1, s2, s3; };
__device__ __forceinline__ Srd4 make_srd4(const void* p, unsigned bytes, unsigned back = 0) {
    return Srd4{make_srd(p, bytes, back), make_srd(p, bytes, back + 1024), make_srd(p, bytes, back + 2048), make_srd(p, bytes, back + 3072)};
}

__device__ __forceinline__ void set_m0(unsigned v) { asm volatile("s_mov_b32 m0, %0" :: "s"(v) : "memory"); }
template <int R> __device__ __forceinline__ void dma_rows(const Srd4& d, unsigned e, unsigned off) {
    unsigned tmp, mask = 0x00FFFF00u;
    asm volatile("v_and_or_b32 %0, %1, %2, %3\n\tbuffer_load_dwordx4 %0, %4, 0 offen offset:%c5 lds"
                 : "=&v"(tmp) : "v"(e), "s"(mask), "v"(off), "s"(R == 0 ? d.s0 : R == 1 ? d.s1 : R == 2 ? d.s2 : d.s3), "i"(R * 1024) : "memory");
}
__device__ __forceinline__ void dma_half(unsigned ldsbase, const Srd4& d, const unsigned* e, const u32x4& off) {
    set_m0(ldsbase);
    dma_rows<0>(d, e[0], off.x); dma_rows<1>(d, e[1], off.y); dma_rows<2>(d, e[2], off.z); dma_rows<3>(d, e[3], off.w);
}
__device__ __forceinline__ void dma_group(unsigned ldsbase, const Srd4& d, const unsigned (&e)[8], const u32x4& off) {
    asm volatile("s_waitcnt lgkmcnt(0)" ::: "memory");
    dma_half(ldsbase, d, e, off); dma_half(ldsbase + 4096, d, e + 4, off);
}

__device__ __forceinline__ void gather_loop(f32x32& acc, const u32x16& qf, const u32x16& addr, u32x4 misc, f32x16 cc,
                                            const Srd4& d, unsigned ring, int G, int nlast) {
    f32x32 t0, t1; u32x12 t2; int tmp, pr; u64 sm; unsigned mask = 0x00FFFF00u;
    asm volatile(
        ".macro GWAIT k\n\ts_cmp_gt_i32 %[t], \\k\n\ts_cbranch_scc1 1f\n\ts_waitcnt vmcnt(\\k)\n\t.endm\n\t"
        ".macro GROWS so\n\t.irp s, 0, 1, 2, 3\n\t"
        "ds_read_b128 v[64+8*\\s:67+8*\\s], v[176+\\s] offset:\\so\n\t"
        "ds_read_b128 v[68+8*\\s:71+8*\\s], v[176+\\s] offset:\\so+4096\n\t.endr\n\t.endm\n\t"
        ".macro GTR c, so\n\t"
        "ds_read_b64_tr_b16 v[96+4*\\c:97+4*\\c], v[180+\\c] offset:\\so\n\t"
        "ds_read_b64_tr_b16 v[98+4*\\c:99+4*\\c], v[180+\\c] offset:\\so+4096\n\t.endm\n\t"
        ".macro GS s, c\n\t"
        "v_mfma_f32_16x16x32_bf16 v[\\c:\\c+3], v[64+8*\\s:67+8*\\s], v[160+4*\\s:163+4*\\s], v[\\c:\\c+3]\n\t"
        "v_mfma_f32_16x16x32_bf16 v[\\c+4:\\c+7], v[68+8*\\s:71+8*\\s], v[160+4*\\s:163+4*\\s], v[\\c+4:\\c+7]\n\t.endm\n\t"
        ".macro GLOAD m, r, srd\n\t"
        "v_and_or_b32 v195, v[212+\\m], %[mask], v[188+\\r]\n\t"
        "buffer_load_dwordx4 v195, \\srd, 0 offen offset:1024*\\r lds\n\t.endm\n\t"
        ".macro GPART m, r, srd\n\ts_cmp_le_i32 %[nl], \\m\n\ts_cbranch_scc1 4f\n\tGLOAD \\m, \\r, \\srd\n\t.endm\n\t"
        ".macro GSUM m\n\tv_mfma_f32_16x16x32_bf16 v[128+4*(\\m):131+4*(\\m)], v[96+4*(\\m):99+4*(\\m)], v[220:223], v[128+4*(\\m):131+4*(\\m)]\n\t.endm\n\t"
        ".macro GCMP m, d\n\tv_cmp_ne_u32_sdwa \\d, v[212+\\m], v193 src0_sel:BYTE_0 src1_sel:DWORD\n\t.endm\n\t"
        ".macro GPAIR m, c\n\t"
        "GSUM \\m\n\tGCMP \\m+1, %[sm]\n\tv_cndmask_b32_e32 v[\\c+\\m], 0, v194, vcc\n\t"
        "GSUM \\m+1\n\t.if \\m < 6\n\tGCMP \\m+2, vcc\n\t.else\n\ts_cmp_gt_i32 %[rem], 0\n\t.endif\n\t"
        "v_cndmask_b32_e64 v[\\c+\\m+1], 0, v194, %[sm]\n\t.endm\n\t"
        ".macro GSTEP so, c, eo\n\t"
        "GTR 0, \\so\n\t"
        "s_waitcnt lgkmcnt(2)\n\tGS 0, \\c\n\t"
        "GTR 1, \\so\n\tGTR 2, \\so\n\tGS 1, \\c\n\t"
        "GTR 3, \\so\n\tGTR 4, \\so\n\tGS 2, \\c\n\t"
        "GTR 5, \\so\n\tGTR 6, \\so\n\tGS 3, \\c\n\t"
        "GTR 7, \\so\n\t"
        "ds_read2_b32 v[212:213], v192 offset0:\\eo offset1:\\eo+4\n\t"
        "ds_read2_b32 v[214:215], v192 offset0:\\eo+8 offset1:\\eo+12\n\t"
        "ds_read2_b32 v[216:217], v192 offset0:\\eo+16 offset1:\\eo+20\n\t"
        "ds_read2_b32 v[218:219], v192 offset0:\\eo+24 offset1:\\eo+28\n\t"
        "s_add_u32 m0, %[ring], \\so\n\t"
        "s_cmp_gt_i32 %[rem], 3\n\t"
        ".irp k, 0, 1, 2, 3, 4, 5, 6, 7\n\tv_exp_f32_e32 v[\\c+\\k], v[\\c+\\k]\n\t.endr\n\t"
        "s_waitcnt lgkmcnt(0)\n\t"
        "s_cbranch_scc0 2f\n\t"
        "GLOAD 0, 0, %[s0]\n\tGLOAD 1, 1, %[s1]\n\tGLOAD 2, 2, %[s2]\n\tGLOAD 3, 3, %[s3]\n\t"
        "s_add_u32 m0, m0, 0x1000\n\t"
        "GLOAD 4, 0, %[s0]\n\tGLOAD 5, 1, %[s1]\n\tGLOAD 6, 2, %[s2]\n\tGLOAD 7, 3, %[s3]\n\t"
        "GSIG \\c\n\t"
        "s_waitcnt vmcnt(8)\n\t"
        "s_branch 1f\n\t"
        "2:\n\t"
        "s_mov_b32 %[t], 0\n\t"
        "s_cmp_eq_u32 %[rem], 3\n\ts_cbranch_scc0 4f\n\t"
        "s_mov_b32 %[t], %[nl]\n\t"
        "GLOAD 0, 0, %[s0]\n\tGPART 1, 1, %[s1]\n\tGPART 2, 2, %[s2]\n\tGPART 3, 3, %[s3]\n\t"
        "s_add_u32 m0, m0, 0x1000\n\t"
        "GPART 4, 0, %[s0]\n\tGPART 5, 1, %[s1]\n\tGPART 6, 2, %[s2]\n\tGPART 7, 3, %[s3]\n\t"
        "4:\n\t"
        "GSIG \\c\n\t"
        "s_waitcnt vmcnt(8)\n\t"
        "GWAIT 7\n\tGWAIT 6\n\tGWAIT 5\n\tGWAIT 4\n\tGWAIT 3\n\tGWAIT 2\n\tGWAIT 1\n\tGWAIT 0\n\t"
        "1:\n\t"
        "GROWS 8192-\\so\n\t"
        "GCMP 0, vcc\n\t"
        "s_add_i32 %[rem], %[rem], -1\n\t"
        "GPAIR 0, \\c\n\tGPAIR 2, \\c\n\tGPAIR 4, \\c\n\tGPAIR 6, \\c\n\t"
        ".endm\n\t"
        ".macro GSIG c\n\t"
        ".if " STR(PKADD) "\n\t.irp k, 0, 2, 4, 6\n\tv_pk_add_f32 v[\\c+\\k:\\c+\\k+1], v[\\c+\\k:\\c+\\k+1], 1.0 op_sel_hi:[1,0]\n\t.endr\n\t.else\n\t"
        ".irp k, 0, 1, 2, 3, 4, 5, 6, 7\n\tv_add_f32_e32 v[\\c+\\k], 1.0, v[\\c+\\k]\n\t.endr\n\t.endif\n\t"
        ".irp k, 0, 1, 2, 3, 4, 5, 6, 7\n\tv_rcp_f32_e32 v[\\c+\\k], v[\\c+\\k]\n\t.endr\n\t"
        ".irp k, 0, 1, 2, 3\n\tv_cvt_pk_bf16_f32 v[220+\\k], v[\\c+2*\\k], v[\\c+2*\\k+1]\n\t.endr\n\t.endm\n\t"
        "s_waitcnt vmcnt(8)\n\t"
        "s_cmp_gt_i32 %[rem], 1\n\ts_cbranch_scc1 1f\n\ts_waitcnt vmcnt(0)\n\t1:\n\t"
        "GROWS 0\n\t"
        ".if " STR(LPRIO) " >= 3\n\ts_mov_b32 %[pr], 0\n\ts_setprio 2\n\t.endif\n\t"
        "0:\n\t"
        ".if " STR(LPRIO) " == 3\n\t"
        "s_cmp_eq_u32 %[pr], 1\n\ts_cbranch_scc0 5f\n\ts_setprio 1\n\t5:\n\t"
        "s_cmp_eq_u32 %[pr], 2\n\ts_cbranch_scc0 5f\n\ts_setprio 0\n\t5:\n\t"
        "s_add_u32 %[pr], %[pr], 1\n\t.endif\n\t"
        "GSTEP 0, 196, 64\n\t"
        "s_cbranch_scc0 9f\n\t"
        "GSTEP 8192, 204, 96\n\t"
        "v_add_u32_e32 v192, 0x100, v192\n\t"
        "s_cbranch_scc1 0b\n\t"
        "9:\n\t"
        "s_waitcnt lgkmcnt(0)\n\ts_nop 6\n\t"
        ".purgem GWAIT\n\t.purgem GROWS\n\t.purgem GTR\n\t.purgem GS\n\t.purgem GLOAD\n\t.purgem GPART\n\t.purgem GSUM\n\t.purgem GCMP\n\t.purgem GPAIR\n\t.purgem GSTEP\n\t.purgem GSIG"
        : "+{v[128:159]}"(acc), "+{v[192:195]}"(misc), "+{v[196:211]}"(cc), "=&{v[64:95]}"(t0), "=&{v[96:127]}"(t1), "=&{v[212:223]}"(t2),
          [rem] "+s"(G), [t] "=&s"(tmp), [pr] "=&s"(pr), [sm] "=&s"(sm)
        : "{v[160:175]}"(qf), "{v[176:191]}"(addr), [s0] "s"(d.s0), [s1] "s"(d.s1), [s2] "s"(d.s2), [s3] "s"(d.s3),
          [mask] "s"(mask), [ring] "s"(ring), [nl] "s"(nlast)
        : "vcc", "scc", "memory");
}

__device__ __forceinline__ u64 rank_edges(u32x16& ed, u32x16& es, u32x16& rk, u64 m0, u64 m1, u64 m2, u64 m3, unsigned keya) {
    u32x16 b4; u64 over; unsigned sel = 0x01050401u, c36 = 36;
    asm volatile(
        ".macro RADD k\n\tv_lshlrev_b32_sdwa v[96+\\k], 2, v[64+\\k] dst_sel:DWORD dst_unused:UNUSED_PAD src0_sel:DWORD src1_sel:BYTE_0\n\t"
        "ds_add_rtn_u32 v[112+\\k], v[96+\\k], v111 offset:%c[cnt]\n\t.endm\n\t"
        ".macro RENT k\n\tv_add_u32_e32 v[80+\\k], %c[bias], v[80+\\k]\n\tv_perm_b32 v[80+\\k], v[80+\\k], v[64+\\k], %[sel]\n\t.endm\n\t"
        ".macro RPUT k\n\tv_min_u32_e32 v110, %c[cap], v[112+\\k]\n\tv_mad_u32_u24 v110, v[96+\\k], %[c36], v110\n\t"
        "ds_write_b32 v110, v[80+\\k] offset:%c[img]\n\t.endm\n\t"
        "v_mov_b32_e32 v111, 4\n\t"
        ".irp k, 0, 2, 4, 6, 8, 10, 12\n\tv_mov_b64_e32 v[112+\\k:113+\\k], 0\n\t.endr\n\t"
        "s_mov_b64 exec, %[m0]\n\ts_waitcnt vmcnt(9)\n\tRADD 0\n\tRADD 1\n\tRADD 2\n\tRADD 3\n\t"
        "s_mov_b64 exec, %[m1]\n\ts_waitcnt vmcnt(8)\n\tRADD 4\n\tRADD 5\n\tRADD 6\n\tRADD 7\n\t"
        "s_mov_b64 exec, %[m2]\n\ts_waitcnt vmcnt(7)\n\tRADD 8\n\tRADD 9\n\tRADD 10\n\tRADD 11\n\t"
        "s_mov_b64 exec, %[m3]\n\ts_waitcnt vmcnt(6)\n\tRADD 12\n\t"
        "s_mov_b64 exec, %[m0]\n\ts_waitcnt vmcnt(5)\n\tRENT 0\n\tRENT 1\n\tRENT 2\n\tRENT 3\n\t"
        "s_waitcnt lgkmcnt(9)\n\tRPUT 0\n\tRPUT 1\n\tRPUT 2\n\tRPUT 3\n\t"
        "s_mov_b64 exec, %[m1]\n\ts_waitcnt vmcnt(4)\n\tRENT 4\n\tRENT 5\n\tRENT 6\n\tRENT 7\n\t"
        "s_waitcnt lgkmcnt(9)\n\tRPUT 4\n\tRPUT 5\n\tRPUT 6\n\tRPUT 7\n\t"
        "s_mov_b64 exec, %[m2]\n\ts_waitcnt vmcnt(3)\n\tRENT 8\n\tRENT 9\n\tRENT 10\n\tRENT 11\n\t"
        "s_waitcnt lgkmcnt(9)\n\tRPUT 8\n\tRPUT 9\n\tRPUT 10\n\tRPUT 11\n\t"
        "s_mov_b64 exec, %[m3]\n\ts_waitcnt vmcnt(0)\n\tRENT 12\n\t"
        "s_waitcnt lgkmcnt(12)\n\tRPUT 12\n\t"
        "s_mov_b64 exec, -1\n\t"
        "v_add_u32_e32 v93, %c[step], v93\n\t"
        "v_max3_u32 v110, v112, v113, v114\n\tv_max3_u32 v109, v115, v116, v117\n\t"
        "ds_write_b32 %[keya], v93 offset:%c[cnt]+1024\n\t"
        "v_max3_u32 v110, v110, v118, v119\n\tv_max3_u32 v109, v109, v120, v121\n\t"
        "v_max3_u32 v110, v110, v122, v123\n\tv_max3_u32 v110, v110, v109, v124\n\t"
        "v_cmp_le_u32_e32 vcc, %c[cap], v110\n\ts_mov_b64 %[over], vcc\n\t"
        ".purgem RADD\n\t.purgem RENT\n\t.purgem RPUT"
        : "+{v[64:79]}"(ed), "+{v[80:95]}"(es), "=&{v[96:111]}"(b4), "=&{v[112:127]}"(rk), [over] "=&s"(over)
        : [m0] "s"(m0), [m1] "s"(m1), [m2] "s"(m2), [m3] "s"(m3), [keya] "v"(keya), [sel] "s"(sel), [c36] "s"(c36),
          [cnt] "i"(L_CNT1), [img] "i"(L_IMG + 8), [bias] "i"(BIAS), [cap] "i"(4 * CAP1), [step] "i"(KEY_STEP)
        : "vcc", "memory");
    return over;
}
__device__ __forceinline__ unsigned read_image(f32x32& img, u32x16& meta, u32x16& sums, unsigned ima, unsigned cna) {
    unsigned no;
    asm volatile(
        "v_mov_b32_e32 v121, 0\n\tds_read_b32 v120, v121 offset:%c[cnt]+2048\n\t"
        ".irp k, 0, 1, 2, 3, 4, 5, 6, 7\n\tds_read_b128 v[64+4*\\k:67+4*\\k], %[ima] offset:4608*\\k\n\t.endr\n\t"
        ".irp k, 0, 1, 2, 3\n\tds_read2_b32 v[96+2*\\k:97+2*\\k], %[cna] offset0:64*\\k offset1:64*\\k+32\n\t.endr\n\t"
        "s_waitcnt lgkmcnt(11)\n\tv_readfirstlane_b32 %[no], v120\n\t"
        "v_add_u32_e32 v121, 0x400, %[cna]\n\t"
        ".irp k, 0, 1, 2, 3\n\tds_read2_b32 v[104+2*\\k:105+2*\\k], v121 offset0:64*\\k offset1:64*\\k+32\n\t.endr\n\t"
        "s_waitcnt lgkmcnt(12)\n\t"
        ".irp k, 0, 1, 2, 3\n\tv_add3_u32 v[112+\\k], v[64+4*\\k], v[65+4*\\k], v[66+4*\\k]\n\tv_add_u32_e32 v[112+\\k], v[112+\\k], v[67+4*\\k]\n\t.endr\n\t"
        "s_waitcnt lgkmcnt(8)\n\t"
        ".irp k, 4, 5, 6, 7\n\tv_add3_u32 v[112+\\k], v[64+4*\\k], v[65+4*\\k], v[66+4*\\k]\n\tv_add_u32_e32 v[112+\\k], v[112+\\k], v[67+4*\\k]\n\t.endr\n\t"
        ".irp k, 0, 1, 2, 3, 4, 5, 6, 7\n\tv_add_u32_dpp v[112+\\k], v[112+\\k], v[112+\\k] quad_perm:[1,0,3,2] row_mask:0xf bank_mask:0xf\n\t.endr\n\t"
        ".irp k, 0, 1, 2, 3, 4, 5, 6, 7\n\tv_add_u32_dpp v[112+\\k], v[112+\\k], v[112+\\k] quad_perm:[2,3,0,1] row_mask:0xf bank_mask:0xf\n\t.endr\n\t"
        ".irp k, 0, 1, 2, 3, 4, 5, 6, 7\n\tv_add_u32_dpp v[112+\\k], v[112+\\k], v[112+\\k] row_half_mirror row_mask:0xf bank_mask:0xf\n\t.endr\n\t"
        "s_waitcnt lgkmcnt(0)"
        : "=&{v[64:95]}"(img), "=&{v[96:111]}"(meta), "=&{v[112:127]}"(sums), [no] "=&s"(no)
        : [ima] "v"(ima), [cna] "v"(cna), [cnt] "i"(L_CNT1) : "memory");
    return no;
}
__device__ __forceinline__ void publish_image(f32x32 img, u32x16 meta, u32x16 sums, const u32x4& srd, unsigned ob) {
    u64 pc0 = 0x0101010101010101ull; unsigned so;
    asm volatile(
        ".macro PUB k\n\ts_mov_b64 exec, %[pc0]\n\tv_mov_b32_e32 v[64+4*\\k], v[96+\\k]\n\tv_mov_b32_e32 v[65+4*\\k], v[112+\\k]\n\t"
        "s_mov_b64 exec, -1\n\tv_xor_b32_e32 v[64+4*\\k], v[64+4*\\k], v[104+\\k]\n\t"
        "buffer_store_dwordx4 v[64+4*\\k:67+4*\\k], %[ob], %[srd], %[so] offen sc1\n\ts_add_u32 %[so], %[so], %c[step]\n\t.endm\n\t"
        "s_mov_b32 %[so], 0\n\t"
        ".irp k, 0, 1, 2, 3, 4, 5, 6, 7\n\tPUB \\k\n\t.endr\n\t.purgem PUB"
        : "+{v[64:95]}"(img), [so] "=&s"(so)
        : "{v[96:111]}"(meta), "{v[112:127]}"(sums), [srd] "s"(srd), [ob] "v"(ob), [pc0] "s"(pc0), [step] "i"(32 * G1 * 128) : "scc", "memory");
}

__device__ __forceinline__ void inbox_request(u32x16& raw, const u32x4& srd, unsigned ib) {
    unsigned o1 = 0x1000, o2 = 0x2000, o3 = 0x3000;
    asm volatile("buffer_load_dwordx4 v[64:67], %1, %2, 0 offen sc1\n\tbuffer_load_dwordx4 v[68:71], %1, %2, %3 offen sc1\n\t"
                 "buffer_load_dwordx4 v[72:75], %1, %2, %4 offen sc1\n\tbuffer_load_dwordx4 v[76:79], %1, %2, %5 offen sc1"
                 : "=&{v[64:79]}"(raw) : "v"(ib), "s"(srd), "s"(o1), "s"(o2), "s"(o3) : "memory");
}
__device__ __forceinline__ unsigned inbox_stage(u32x16 raw, const u32x4& srd, unsigned ib, unsigned key, int i4, unsigned hdra) {
    u32x16 t0; f32x32 t2; unsigned four = 4; u64 m0, m1, m2, m3; unsigned todo, fresh, spin, o1, o2, o3, over;
    u64 pc0 = 0x0101010101010101ull;
    asm volatile(
        ".macro IVAL j, so\n\t"
        "s_bitcmp1_b32 %[todo], \\j\n\ts_cbranch_scc0 2f\n\t"
        "v_xor_b32_e32 v[64+4*\\j], %[key], v[64+4*\\j]\n\t"
        "v_sub_u32_e32 v97, 0, v[65+4*\\j]\n\t"
        "v_add_u32_e32 v96, v[64+4*\\j], v[65+4*\\j]\n\t"
        "v_mov_b32_dpp v[80+\\j], v[64+4*\\j] quad_perm:[0,0,0,0] row_mask:0xf bank_mask:0xf\n\t"
        "v_cndmask_b32_e64 v96, v96, v97, %[pc0]\n\t"
        "s_nop 0\n\t"
        "v_mov_b32_dpp v[80+\\j], v[80+\\j] row_shr:4 row_mask:0xf bank_mask:0xa\n\t"
        "v_cmp_gt_i32_e32 vcc, v[80+\\j], v92\n\t"
        "v_add3_u32 v96, v96, v[66+4*\\j], v[67+4*\\j]\n\t"
        "v_lshrrev_b32_e32 v97, 14, v[80+\\j]\n\t"
        "v_cndmask_b32_e32 v96, 0, v96, vcc\n\t"
        "s_nop 1\n\tv_add_u32_dpp v96, v96, v96 quad_perm:[1,0,3,2] row_mask:0xf bank_mask:0xf\n\t"
        "s_nop 1\n\tv_add_u32_dpp v96, v96, v96 quad_perm:[2,3,0,1] row_mask:0xf bank_mask:0xf\n\t"
        "s_nop 1\n\tv_add_u32_dpp v96, v96, v96 row_half_mirror row_mask:0xf bank_mask:0xf\n\t"
        "v_or_b32_e32 v96, v96, v97\n\t"
        "v_cmp_ne_u32_e32 vcc, 0, v96\n\t"
        "s_cbranch_vccz 1f\n\t"
        "buffer_load_dwordx4 v[64+4*\\j:67+4*\\j], %[ib], %[srd], \\so offen sc1\n\t"
        "s_branch 2f\n\t"
        "1:\n\ts_bitset1_b32 %[fresh], \\j\n\t"
        "2:\n\t.endm\n\t"
        ".macro IRANK j\n\t"
        "s_bitcmp1_b32 %[fresh], \\j\n\ts_cbranch_scc0 2f\n\t"
        "v_cmp_lt_u32_e64 %[m0], v92, v[80+\\j]\n\tv_cmp_lt_u32_e64 %[m1], v93, v[80+\\j]\n\t"
        "v_cmp_lt_u32_e64 %[m2], v94, v[80+\\j]\n\tv_cmp_lt_u32_e64 %[m3], v95, v[80+\\j]\n\t"
        ".irp k, 0, 1, 2, 3\n\tv_lshrrev_b32_e32 v[84+\\k], 21, v[64+4*\\j+\\k]\n\t.endr\n\t"
        ".irp k, 0, 1, 2, 3\n\tv_and_b32_e32 v[84+\\k], 60, v[84+\\k]\n\t.endr\n\t"
        "v_cmp_lt_u32_e32 vcc, %c[cap], v[80+\\j]\n\t"
        "s_mov_b64 exec, %[m0]\n\tds_add_rtn_u32 v88, v84, %[four] offset:%c[cnt]\n\t"
        "s_mov_b64 exec, %[m1]\n\tds_add_rtn_u32 v89, v85, %[four] offset:%c[cnt]\n\t"
        "s_mov_b64 exec, %[m2]\n\tds_add_rtn_u32 v90, v86, %[four] offset:%c[cnt]\n\t"
        "s_mov_b64 exec, %[m3]\n\tds_add_rtn_u32 v91, v87, %[four] offset:%c[cnt]\n\t"
        "s_mov_b64 exec, %[pc0]\n\tds_write_b32 %[hdra], v[80+\\j] offset:128*\\j\n\t"
        "s_mov_b64 exec, -1\n\t"
        "s_cbranch_vccz 1f\n\t"
        "s_mov_b32 %[over], 1\n\t"
        "1:\n\ts_waitcnt lgkmcnt(1)\n\t"
        ".irp k, 0, 1, 2, 3\n\tv_min_u32_e32 v[88+\\k], %c[sub]-4, v[88+\\k]\n\t.endr\n\t"
        ".irp k, 0, 1, 2, 3\n\tv_mad_u32_u24 v[88+\\k], v[84+\\k], %[sub4], v[88+\\k]\n\t.endr\n\t"
        "s_mov_b64 exec, %[m0]\n\tds_write_b32 v88, v[64+4*\\j] offset:%c[list]\n\t"
        "s_mov_b64 exec, %[m1]\n\tds_write_b32 v89, v[65+4*\\j] offset:%c[list]\n\t"
        "s_mov_b64 exec, %[m2]\n\tds_write_b32 v90, v[66+4*\\j] offset:%c[list]\n\t"
        "s_mov_b64 exec, %[m3]\n\tds_write_b32 v91, v[67+4*\\j] offset:%c[list]\n\t"
        "s_mov_b64 exec, -1\n\t"
        "2:\n\t.endm\n\t"
        ".macro FADD j\n\t"
        ".irp k, 0, 1, 2, 3\n\tv_lshrrev_b32_e32 v[96+4*\\j+\\k], 21, v[64+4*\\j+\\k]\n\t.endr\n\t"
        ".irp k, 0, 1, 2, 3\n\tv_and_b32_e32 v[96+4*\\j+\\k], 60, v[96+4*\\j+\\k]\n\t.endr\n\t"
        "v_cmpx_lt_u32_e64 s[72+8*\\j:73+8*\\j], v94, v[80+\\j]\n\tds_add_rtn_u32 v[114+4*\\j], v[98+4*\\j], %[four] offset:%c[cnt]\n\t"
        "v_cmpx_lt_u32_e64 s[74+8*\\j:75+8*\\j], v95, v[80+\\j]\n\tds_add_rtn_u32 v[115+4*\\j], v[99+4*\\j], %[four] offset:%c[cnt]\n\t"
        "s_mov_b64 exec, -1\n\t"
        "v_cmpx_lt_u32_e64 s[68+8*\\j:69+8*\\j], v92, v[80+\\j]\n\tds_add_rtn_u32 v[112+4*\\j], v[96+4*\\j], %[four] offset:%c[cnt]\n\t"
        "v_cmpx_lt_u32_e64 s[70+8*\\j:71+8*\\j], v93, v[80+\\j]\n\tds_add_rtn_u32 v[113+4*\\j], v[97+4*\\j], %[four] offset:%c[cnt]\n\t"
        "s_mov_b64 exec, -1\n\t.endm\n\t"
        ".macro FPUT j\n\t"
        "s_waitcnt lgkmcnt(12)\n\t"
        ".irp k, 0, 1, 2, 3\n\tv_min_u32_e32 v[112+4*\\j+\\k], %c[sub]-4, v[112+4*\\j+\\k]\n\t.endr\n\t"
        ".irp k, 0, 1, 2, 3\n\tv_mad_u32_u24 v[112+4*\\j+\\k], v[96+4*\\j+\\k], %[sub4], v[112+4*\\j+\\k]\n\t.endr\n\t"
        ".irp k, 0, 1, 2, 3\n\ts_mov_b64 exec, s[68+8*\\j+2*\\k:69+8*\\j+2*\\k]\n\tds_write_b32 v[112+4*\\j+\\k], v[64+4*\\j+\\k] offset:%c[list]\n\t.endr\n\t"
        "s_mov_b64 exec, -1\n\t.endm\n\t"
        "s_movk_i32 %[o1], 0x1000\n\ts_movk_i32 %[o2], 0x2000\n\ts_movk_i32 %[o3], 0x3000\n\t"
        "v_mov_b32_e32 v92, %[i4]\n\tv_add_u32_e32 v93, 4, %[i4]\n\tv_add_u32_e32 v94, 8, %[i4]\n\tv_add_u32_e32 v95, 12, %[i4]\n\t"
        "s_mov_b32 %[todo], 15\n\ts_mov_b32 %[spin], 0x400000\n\ts_mov_b32 %[over], 0\n\t"
        ".if " STR(FASTIN) "\n\t"
        "s_waitcnt vmcnt(0)\n\t"
        ".irp j, 0, 1, 2, 3\n\tv_xor_b32_e32 v[64+4*\\j], %[key], v[64+4*\\j]\n\t.endr\n\t"
        ".irp j, 0, 1, 2, 3\n\tv_mov_b32_dpp v[80+\\j], v[64+4*\\j] quad_perm:[0,0,0,0] row_mask:0xf bank_mask:0xf\n\t.endr\n\t"
        ".irp j, 0, 1, 2, 3\n\tv_add_u32_e32 v[84+\\j], v[64+4*\\j], v[65+4*\\j]\n\t.endr\n\t"
        ".irp j, 0, 1, 2, 3\n\tv_mov_b32_dpp v[80+\\j], v[80+\\j] row_shr:4 row_mask:0xf bank_mask:0xa\n\t.endr\n\t"
        "s_mov_b64 exec, %[pc0]\n\t"
        ".irp j, 0, 1, 2, 3\n\tv_sub_u32_e32 v[84+\\j], 0, v[65+4*\\j]\n\t.endr\n\t"
        "s_mov_b64 exec, -1\n\t"
        "v_cmp_gt_i32_e64 %[m0], v80, v92\n\tv_cmp_gt_i32_e64 %[m1], v81, v92\n\tv_cmp_gt_i32_e64 %[m2], v82, v92\n\tv_cmp_gt_i32_e64 %[m3], v83, v92\n\t"
        ".irp j, 0, 1, 2, 3\n\tv_add3_u32 v[84+\\j], v[84+\\j], v[66+4*\\j], v[67+4*\\j]\n\t.endr\n\t"
        "v_cndmask_b32_e64 v84, 0, v84, %[m0]\n\tv_cndmask_b32_e64 v85, 0, v85, %[m1]\n\tv_cndmask_b32_e64 v86, 0, v86, %[m2]\n\tv_cndmask_b32_e64 v87, 0, v87, %[m3]\n\t"
        ".irp j, 0, 1, 2, 3\n\tv_add_u32_dpp v[84+\\j], v[84+\\j], v[84+\\j] quad_perm:[1,0,3,2] row_mask:0xf bank_mask:0xf\n\t.endr\n\t"
        ".irp j, 0, 1, 2, 3\n\tv_add_u32_dpp v[84+\\j], v[84+\\j], v[84+\\j] quad_perm:[2,3,0,1] row_mask:0xf bank_mask:0xf\n\t.endr\n\t"
        ".irp j, 0, 1, 2, 3\n\tv_add_u32_dpp v[84+\\j], v[84+\\j], v[84+\\j] row_half_mirror row_mask:0xf bank_mask:0xf\n\t.endr\n\t"
        "v_or3_b32 v96, v80, v81, v82\n\tv_max3_u32 v97, v80, v81, v82\n\tv_or_b32_e32 v96, v96, v83\n\tv_max_u32_e32 v97, v97, v83\n\t"
        "v_lshrrev_b32_e32 v96, 14, v96\n\tv_or3_b32 v96, v96, v84, v85\n\tv_or3_b32 v96, v96, v86, v87\n\t"
        "v_cmp_ne_u32_e32 vcc, 0, v96\n\t"
        "s_cbranch_vccnz 7f\n\t"
        "v_cmp_lt_u32_e32 vcc, %c[cap], v97\n\t"
        "FADD 0\n\tFADD 1\n\tFADD 2\n\tFADD 3\n\t"
        "s_cbranch_vccz 6f\n\ts_mov_b32 %[over], 1\n\t6:\n\t"
        "FPUT 0\n\tFPUT 1\n\tFPUT 2\n\tFPUT 3\n\t"
        "s_mov_b64 exec, %[pc0]\n\t"
        ".irp j, 0, 1, 2, 3\n\tds_write_b32 %[hdra], v[80+\\j] offset:128*\\j\n\t.endr\n\t"
        "s_mov_b64 exec, -1\n\t"
        "s_branch 9f\n\t"
        "7:\n\t.irp j, 0, 1, 2, 3\n\tv_xor_b32_e32 v[64+4*\\j], %[key], v[64+4*\\j]\n\t.endr\n\t"
        ".endif\n\t"
        "0:\n\t"
        "s_mov_b32 %[fresh], 0\n\t"
        "s_waitcnt vmcnt(0)\n\t"
        "IVAL 0, 0\n\tIVAL 1, %[o1]\n\tIVAL 2, %[o2]\n\tIVAL 3, %[o3]\n\t"
        "IRANK 0\n\tIRANK 1\n\tIRANK 2\n\tIRANK 3\n\t"
        "s_andn2_b32 %[todo], %[todo], %[fresh]\n\t"
        "s_cbranch_scc0 9f\n\t"
        "s_sub_u32 %[spin], %[spin], 1\n\t"
        "s_cbranch_scc0 0b\n\t"
        "s_waitcnt vmcnt(0)\n\t"
        "9:\n\t"
        ".purgem IVAL\n\t.purgem IRANK\n\t.purgem FADD\n\t.purgem FPUT"
        : "+{v[64:79]}"(raw), "=&{v[80:95]}"(t0), "=&{v[96:127]}"(t2), [m0] "=&s"(m0), [m1] "=&s"(m1), [m2] "=&s"(m2), [m3] "=&s"(m3),
          [todo] "=&s"(todo), [fresh] "=&s"(fresh), [spin] "=&s"(spin), [o1] "=&s"(o1), [o2] "=&s"(o2), [o3] "=&s"(o3), [over] "=&s"(over)
        : [srd] "s"(srd), [ib] "v"(ib), [key] "s"(key), [i4] "v"(i4), [hdra] "v"(hdra), [pc0] "s"(pc0), [sub4] "s"(SUBB / 4), [four] "v"(four),
          [cnt] "i"(L_CNT), [list] "i"(L_LIST), [sub] "i"(SUBB), [cap] "i"(4 * CAP1)
        : "vcc", "scc", "memory", "s68", "s69", "s70", "s71", "s72", "s73", "s74", "s75", "s76", "s77", "s78", "s79", "s80", "s81", "s82", "s83",
          "s84", "s85", "s86", "s87", "s88", "s89", "s90", "s91", "s92", "s93", "s94", "s95", "s96", "s97", "s98", "s99");
    return over;
}

template <int CTRL, int BANK> __device__ __forceinline__ unsigned dpp(unsigned old, unsigned v) { return __builtin_amdgcn_update_dpp(old, v, CTRL, 0xF, BANK, true); }
__device__ __forceinline__ unsigned bcast8(unsigned v) { v = dpp<0x00, 0xF>(0u, v); return __builtin_amdgcn_update_dpp(v, v, 0x114, 0xF, 0xA, false); }
__device__ __forceinline__ unsigned sum8(unsigned v) { v += dpp<0xB1, 0xF>(0u, v); v += dpp<0x4E, 0xF>(0u, v); return v + dpp<0x141, 0xF>(0u, v); }
__device__ __forceinline__ bf16x4 tr_read(LAS char* p) { return __builtin_amdgcn_ds_read_tr16_b64_v4bf16((LAS bf16x4*)p); }
__device__ __forceinline__ bf16x8 cat(bf16x4 a, bf16x4 b) { return __builtin_shufflevector(a, b, 0, 1, 2, 3, 4, 5, 6, 7); }
__device__ __forceinline__ unsigned pk2(float a, float b) { f32x2 v = {a, b}; bf16x2 r = __builtin_convertvector(v, bf16x2); return __builtin_bit_cast(unsigned, r); }
__device__ __forceinline__ f32x4 mfma(bf16x8 a, bf16x8 b, f32x4 c) { return __builtin_amdgcn_mfma_f32_16x16x32_bf16(a, b, c, 0, 0, 0); }
__device__ __forceinline__ unsigned lds_add(LAS char* p, unsigned v) { return __hip_atomic_fetch_add((LAS unsigned*)p, v, __ATOMIC_RELAXED, __HIP_MEMORY_SCOPE_WORKGROUP); }
__device__ __forceinline__ LAS unsigned* ldsp(unsigned a) { return (LAS unsigned*)(size_t)a; }
__device__ __forceinline__ unsigned byte0_x4(unsigned v) { unsigned r; asm("v_lshlrev_b32_sdwa %0, 2, %1 dst_sel:DWORD dst_unused:UNUSED_PAD src0_sel:DWORD src1_sel:BYTE_0" : "=v"(r) : "v"(v)); return r; }
__device__ __forceinline__ unsigned mad24(unsigned a, unsigned b, unsigned c) { unsigned r; asm("v_mad_u32_u24 %0, %1, %2, %3" : "=v"(r) : "v"(a), "s"(b), "v"(c)); return r; }
__device__ __forceinline__ void lds_arrive(LAS char* p, int lane) {
    asm volatile("s_waitcnt lgkmcnt(0)" ::: "memory");
    if (lane == 0) __hip_atomic_fetch_add((LAS unsigned*)p, 1u, __ATOMIC_RELAXED, __HIP_MEMORY_SCOPE_WORKGROUP);
}
__device__ __forceinline__ void lds_await(LAS char* p, unsigned n) {
    while (__builtin_amdgcn_readfirstlane(*(volatile LAS unsigned*)p) < n) __builtin_amdgcn_s_sleep(1);
    asm volatile("" ::: "memory");
}
__device__ __forceinline__ unsigned rfl(unsigned v) { return __builtin_amdgcn_readfirstlane(v); }

__global__ __launch_bounds__(512, 2) void k_graph(const unsigned short* x_, const int* src_, const int* dst_, const unsigned short* wq_, const unsigned short* wv_,
                                                  unsigned* ws_, unsigned short* out_, int N_, int E_) {
    u32x4 srd_ws, srd_x0, pw, po;
    u32x16 ed, es;
    u64 t_start;
    asm volatile("s_load_dwordx2 s[40:41], %[ka], 0x10\n\ts_load_dwordx2 s[44:45], %[ka], 0x8\n\ts_load_dwordx4 s[60:63], %[ka], 0x30\n\t"
                 "s_getpc_b64 s[68:69]\n\ts_movk_i32 s70, " STR(CODE_BYTES) "\n\ts_mov_b32 s71, 0x20000\n\ts_mov_b32 m0, %c[dump]\n\t"
                 "s_movk_i32 s66, 0x8000\n\tv_lshl_add_u32 v77, %[t], 7, s66\n\t"
                 "buffer_load_dword v77, s[68:71], 0 offen lds\n\t"
                 "s_load_dwordx2 s[48:49], %[ka], 0x28\n\ts_load_dwordx2 s[52:53], %[ka], 0x0\n\ts_load_dwordx4 s[56:59], %[ka], 0x18\n\t"
                 "s_memrealtime s[64:65]\n\t"
                 "v_readfirstlane_b32 s72, %[t]\n\t"
                 "s_and_b32 s66, %[b], 7\n\ts_lshl_b32 s66, s66, 4\n\ts_lshr_b32 s67, %[b], 4\n\ts_or_b32 s66, s66, s67\n\ts_mulk_i32 s66, %c[ch4]\n\t"
                 "s_mov_b32 s43, s71\n\ts_mov_b32 s47, s71\n\t"
                 "v_lshl_add_u32 v77, %[t], 4, s66\n\t"
                 "s_lshr_b32 s67, s72, 5\n\ts_or_b32 s67, s67, %[b]\n\t"
                 "s_movk_i32 s73, 0x1000\n\ts_movk_i32 s74, 0x2000\n\t"
                 "s_bitcmp1_b32 s67, 3\n\ts_waitcnt lgkmcnt(0)\n\ts_cbranch_scc1 1f\n\t"
                 "s_lshl_b32 s42, s63, 2\n\ts_lshl_b32 s46, s63, 2\n\t"
                 "buffer_load_dwordx4 v[64:67], v77, s[40:43], 0 offen\n\tbuffer_load_dwordx4 v[68:71], v77, s[40:43], s73 offen\n\t"
                 "buffer_load_dwordx4 v[72:75], v77, s[40:43], s74 offen\n\t"
                 "s_add_u32 s66, s66, 0x3000\n\ts_cmp_lt_u32 s72, 64\n\ts_cselect_b32 s66, s66, %c[oob]\n\t"
                 "v_lshl_add_u32 v78, %[t], 2, s66\n\t"
                 "buffer_load_dword v76, v78, s[40:43], 0 offen\n\t"
                 "buffer_load_dwordx4 v[80:83], v77, s[44:47], 0 offen\n\tbuffer_load_dwordx4 v[84:87], v77, s[44:47], s73 offen\n\t"
                 "buffer_load_dwordx4 v[88:91], v77, s[44:47], s74 offen\n\tbuffer_load_dword v92, v78, s[44:47], 0 offen\n\t"
                 "s_mov_b32 s51, s71\n\ts_mov_b32 s50, %c[wsb]\n\ts_lshl_b32 s66, %[b], 2\n\t"
                 "v_lshlrev_b32_e32 v77, 2, %[t]\n\tv_mov_b32_e32 v78, s66\n\t"
                 "buffer_load_dword v93, v77, s[48:51], 0 offen sc1\n\tbuffer_load_dword v94, v78, s[48:51], 0 offen sc1\n\t"
                 "1:\n\ts_mov_b32 s51, s71\n\ts_mov_b32 s50, %c[wsb]\n\ts_mov_b32 s55, s71\n\ts_lshl_b32 s54, s62, 8"
                 : "=&{v[64:79]}"(ed), "=&{v[80:95]}"(es)
                 : [ka] "s"(__builtin_amdgcn_kernarg_segment_ptr()), [t] "v"(threadIdx.x), [b] "s"(blockIdx.x),
                   [wsb] "i"(WS_BYTES), [dump] "i"(L_DUMP), [ch4] "i"(CH * 4), [oob] "i"(OOB)
                 : "memory", "scc", "s40", "s41", "s42", "s43", "s44", "s45", "s46", "s47", "s48", "s49", "s50", "s51", "s52", "s53", "s54", "s55",
                   "s56", "s57", "s58", "s59", "s60", "s61", "s62", "s63", "s64", "s65", "s66", "s67", "s68", "s69", "s70", "s71", "s72", "s73", "s74");
    asm volatile("" : "={s[48:51]}"(srd_ws), "={s[52:55]}"(srd_x0), "={s[56:59]}"(pw), "={s[60:63]}"(po), "={s[64:65]}"(t_start));
    FENCE();
    __shared__ __attribute__((aligned(16))) char smem_[L_TOTAL];
    LAS char* smem = (LAS char*)smem_;
    const int t = threadIdx.x, l = t & 63, b = blockIdx.x;
    const int w = rfl(t >> 6);
    const int g = l >> 4, n = l & 15, q = (l >> 2) & 3, p = l & 3, pc = t & 7;
    const unsigned lds0 = (unsigned)(size_t)smem;
    const bool isprod = ((b >> 3) & 1) == 0;
    const int pg = ((b & 7) << 4) | (b >> 4);
    const unsigned e0 = pg * CH + 4 * t, e1 = pg * CH + 3072 + t;

    const void* const x = (const void*)((u64)srd_x0.x | ((u64)srd_x0.y << 32));
    const void* const wq = (const void*)((u64)pw.x | ((u64)pw.y << 32));
    const void* const wv = (const void*)((u64)pw.z | ((u64)pw.w << 32));
    void* const out = (void*)((u64)po.x | ((u64)po.y << 32));
    const int N = po.z, E = po.w;
    const unsigned xbytes = srd_x0.z;
    STAMP_DECL;
    const unsigned ring = lds0 + L_RING + w * 16384;
    const int jn = 8 * n + w;

    unsigned mykey = 0;
    auto node_row = [&](int tile, int m) { const int nd = b + 256 * (8 * (4 * m + g) + tile); return nd < N ? (unsigned)nd << 8 : OOB; };
    auto zero_staging = [&]() {
        const u32x4 z = {0u, 0u, 0u, 0u};
#pragma unroll
        for (int k = 0; k < 5; ++k) *(LAS u32x4*)(smem + L_IMG + k * 8192 + t * 16) = z;
    };

    u32x16 addr;
    f32x32 acc, wva, wvb;
    unsigned cv, lista, cnta, qa, keyn, inf;
    Srd4 srd_x;
    auto loop_constants = [&]() {
        srd_x = make_srd4(x, xbytes, BIAS * 256);
        lista = lds0 + L_LIST + w * (2 * SUBB) + g * 4; cnta = lds0 + L_CNT + (l & 31) * 4; qa = ring + l * 16; keyn = jn; inf = 0x7F800000u;
        asm volatile("" : "+s"(srd_x.s0), "+s"(srd_x.s1), "+s"(srd_x.s2), "+s"(srd_x.s3));
        asm volatile("" : "+v"(lista), "+v"(cnta), "+v"(qa), "+v"(keyn), "+v"(inf));
        const unsigned o0 = (n ^ (((g & 1) << 3) | (g >> 1))) << 4;
        const unsigned ra = (4 * (n & 3) + (n >> 2)) * 256 + (((4 * g) ^ (((n & 7) << 1) | (n >> 3))) << 4);
        const int i = 4 * g + q, r8 = i & 7;
        const unsigned ta = (4 * q + g) * 256 + 8 * (p & 1) + (((p >> 1) ^ (((i & 7) << 1) | (i >> 3))) << 4);
        cv = i * 256 + (((p ^ ((r8 & 1) | ((r8 & 6) << 1))) << 4) | ((p & 1) << 3));
#pragma unroll
        for (int s = 0; s < 4; ++s) { addr[s] = ring + (ra ^ (s << 4)); addr[12 + s] = o0 ^ (s << 5); }
#pragma unroll
        for (int c = 0; c < 8; ++c) addr[4 + c] = ring + (ta ^ (c << 5));
        asm volatile("" : "+{v[176:191]}"(addr), "+v"(cv));
    };
    auto clear_sums = [&]() { acc = 0.f; asm volatile("" : "+{v[128:159]}"(acc)); };
    auto wv_resident = [&]() {
#pragma unroll
        for (int a = 0; a < 8; ++a) {
            LAS char* pa = smem + L_WV0 + (cv ^ ((a >> 1) << 6) ^ ((a & 1) << 3));
            const f32x4 f0 = __builtin_bit_cast(f32x4, cat(tr_read(pa), tr_read(pa + 4096)));
            const f32x4 f1 = __builtin_bit_cast(f32x4, cat(tr_read(pa + 16384), tr_read(pa + 16384 + 4096)));
#pragma unroll
            for (int k = 0; k < 4; ++k) { wva[4 * a + k] = f0[k]; wvb[4 * a + k] = f1[k]; }
        }
        asm volatile("" : "+{v[224:255]}"(wva), "+{v[32:63]}"(wvb));
    };

    if (isprod) zero_staging();
    if (t < 8) *(LAS u32x4*)(smem + L_CNT + t * 16) = u32x4{0u, 0u, 0u, 0u};
    WG_BARRIER();

    if (__builtin_expect(w < 4, 1)) {
        if (__builtin_expect(isprod, 1)) {
            if (PRIO) __builtin_amdgcn_s_setprio(2);
            unsigned ob = WS_INBOX + ((t >> 3) * G1 + pg) * 128 + pc * 16, ima = lds0 + L_IMG + (t >> 3) * 144 + pc * 16;
            PIN(ob); PIN(ima);
            unsigned cna = lds0 + L_CNT1 + (t >> 3) * 4;
            PIN(cna);
            const bool v0 = e0 < (unsigned)E, v1 = e0 + 1024 < (unsigned)E, v2 = e0 + 2048 < (unsigned)E, v3 = w == 0 && e1 < (unsigned)E;
            STAMP(1);
            u32x16 rk;
            const u64 over = rank_edges(ed, es, rk, __builtin_amdgcn_ballot_w64(v0), __builtin_amdgcn_ballot_w64(v1), __builtin_amdgcn_ballot_w64(v2),
                                        __builtin_amdgcn_ballot_w64(v3), t * 4);
            mykey = es[14];
            if (__builtin_expect(over != 0, 0)) {
#pragma unroll
                for (int k = 0; k < 13; ++k)
                    if (rk[k] >= 4u * CAP1) {
                        const unsigned o = WS_OVF + (pg * CH + lds_add(smem + L_CNT1 + 2048, 1)) * 4;
                        const unsigned v = ((((es[k] >> 8) & 0xFFFF) - BIAS) << 16) | ed[k];
                        ST1(v, srd_ws, o, " sc1");
                    }
            }
            if (XSYNC) { lds_arrive(smem + L_CNT + 96, l); lds_await(smem + L_CNT + 96, 4); } else WG_BARRIER();
            STAMP(3);
            f32x32 img; u32x16 meta, sums;
            const unsigned no = read_image(img, meta, sums, ima, cna);
            lds_arrive(smem + L_CNT + 84, l);
            if (__builtin_expect(no != 0, 0)) {
                const unsigned o = t == 0 ? WS_NOVF + pg * 4 : OOB;
                ST1(no, srd_ws, o, " sc1");
                VMWAIT(0);
                if (XSYNC) { lds_arrive(smem + L_CNT + 100, l); lds_await(smem + L_CNT + 100, 4); } else WG_BARRIER();
            }
            publish_image(img, meta, sums, srd_ws, ob);
            if (XAFTER) lds_arrive(smem + L_CNT + 104, l);
            STAMP(4);
        } else {
            { const unsigned om = WS_KEY + b * 4; asm volatile("buffer_load_dword %0, %1, %2, 0 offen sc1\n\ts_waitcnt vmcnt(0)" : "=&v"(mykey) : "v"(om), "s"(srd_ws) : "memory"); }
            STAMP(1);
            if (PARTNER) {
                set_m0(lds0 + L_DUMP + w * 256);
                const unsigned o = t < 196 ? ((b - 8) << 8) + ((t >> 1) << 16) + ((t & 1) << 7) : OOB;
                while (__builtin_amdgcn_s_memrealtime() < t_start + X_T) __builtin_amdgcn_s_sleep(1);
                asm volatile("buffer_load_dword %1, %0, 0 offen lds" :: "s"(srd_x0), "v"(o) : "memory");
            }
            if (PRIO) __builtin_amdgcn_s_setprio(2);
            STAMP(2); STAMP(3); STAMP(4);
        }
        STAMP(5);
        mykey = rfl(mykey) + KEY_STEP;
        loop_constants();
        clear_sums();
        unsigned ib = WS_INBOX + b * (G1 * 128) + t * 16;
        PIN(ib);
        u64 t_go = t_start + POLL_T;
        if (POLL_D) { lds_await(smem + L_CNT + 80, 4); t_go = max(t_go, __builtin_amdgcn_s_memrealtime() + POLL_D); }
        while (__builtin_amdgcn_s_memrealtime() < t_go) __builtin_amdgcn_s_sleep(1);
        STAMP(6);
        u32x16 raw;
        inbox_request(raw, srd_ws, ib);
        if (__builtin_expect(inbox_stage(raw, srd_ws, ib, mykey, 16 * pc - 8, lds0 + L_HDR + (t >> 3) * 4) != 0, 0)) {
            LAS unsigned* hp = (LAS unsigned*)(smem + L_HDR) + 32 * (l >> 3 & 3) + 8 * w + (l & 7);
            const unsigned hv = *hp;
            for (u64 m = __builtin_amdgcn_ballot_w64(l < 32 && hv > 4u * CAP1); m; m &= m - 1) {
                const int k = __builtin_ctzll(m), gp = 32 * (k >> 3) + 8 * w + (k & 7);
                unsigned u, no; const unsigned o = WS_OVF + (gp * CH + l) * 4, on = WS_NOVF + gp * 4;
                asm volatile("buffer_load_dword %0, %2, %4, 0 offen sc1\n\tbuffer_load_dword %1, %3, %4, 0 offen sc1\n\ts_waitcnt vmcnt(0)"
                             : "=&v"(u), "=&v"(no) : "v"(o), "v"(on), "s"(srd_ws) : "memory");
                if (rfl(no) <= 64) {
                    if (l < no && (u & 0xFF) == (unsigned)b) {
                        const unsigned e = __builtin_amdgcn_perm((u >> 16) + BIAS, u, 0x01050401u), key = (e >> 21) & 60;
                        *(LAS unsigned*)(smem + L_LIST + key * (SUBB / 4) + min(lds_add(smem + L_CNT + key, 4), SUBB - 4u)) = e;
                    }
                    if (l == k) *hp = hv | 0x80000000u;
                } else *(LAS unsigned*)(smem + L_CNT + 64) = 1;
            }
        }
        if (PRIO) __builtin_amdgcn_s_setprio(0);
        STAMP(7);
    } else {
        const int j = w - 4;
        auto prefetch = [&](int k0, int kn) {
            const unsigned ln = (b >> 3) * (kn / 4) + (t - 256), o = t - 256 < kn / 4 ? k0 * 1024 + ln * 128 : OOB;
            set_m0(lds0 + L_DUMP + w * 256);
            asm volatile("s_nop 0\n\tbuffer_load_dword %1, %0, 0 offen lds" :: "s"(srd_x0), "v"(o) : "memory");
        };
        STAMP(1);
        u32x4 wr[8], xr[8];
        {
            const u32x4 srd_q = make_srd(wq, 32768);
            const unsigned o = j * 8192 + l * 16, k4 = 4096;
            LD4(wr[0], srd_q, o, ""); LD4(wr[1], srd_q, o, " offset:1024"); LD4(wr[2], srd_q, o, " offset:2048"); LD4(wr[3], srd_q, o, " offset:3072");
            LD4S(wr[4], srd_q, o, k4, ""); LD4S(wr[5], srd_q, o, k4, " offset:1024"); LD4S(wr[6], srd_q, o, k4, " offset:2048"); LD4S(wr[7], srd_q, o, k4, " offset:3072");
        }
        unsigned ox[8];
#pragma unroll
        for (int m = 0; m < 8; ++m) { ox[m] = node_row(m < 4 ? j : w, m & 3) + n * 16; PIN(ox[m]); }
        unsigned aqa[8], xfa[4];
        unsigned xa = ring + g * 256 + ((n ^ g) << 4), wa = lds0 + L_WQ + j * 16384 + g * 256 + ((n ^ g) << 4) + ((n & 4) << 1);
        {
            const unsigned aq = lds0 + L_WQ + (8 * g + q) * 256 + ((((4 * p) ^ (q | ((g & 1) << 2))) << 4) | ((p & 1) << 3));
#pragma unroll
            for (int s = 0; s < 4; ++s) { xfa[s] = ring + n * 256 + (((4 * s + g) ^ n) << 4); PIN(xfa[s]); }
#pragma unroll
            for (int c = 0; c < 8; ++c) { aqa[c] = aq ^ ((c >> 1) << 4) ^ ((c & 1) << 3); PIN(aqa[c]); }
            PIN(xa); PIN(wa);
        }
        loop_constants();
        STAMP(2);
        if (isprod && !XSYNC) {
            WG_BARRIER();
            if (__builtin_expect(rfl(*(LAS unsigned*)(smem + L_CNT1 + 2048)) != 0, 0)) WG_BARRIER();
            if (W_SLEEP) __builtin_amdgcn_s_sleep(W_SLEEP);
        }
        STAMP(3);
        auto row_loads = [&]() {
#pragma unroll
            for (int m = 0; m < 8; ++m) LD4(xr[m], srd_x0, ox[m], "");
        };
        asm volatile("s_waitcnt vmcnt(0)" : "+v"(wr[0]), "+v"(wr[1]), "+v"(wr[2]), "+v"(wr[3]), "+v"(wr[4]), "+v"(wr[5]), "+v"(wr[6]), "+v"(wr[7]) :: "memory");
        if (isprod && XAFTER) lds_await(smem + L_CNT + 104, 4);
        else while (__builtin_amdgcn_s_memrealtime() < t_start + X_T) __builtin_amdgcn_s_sleep(1);
        row_loads();
#pragma unroll
        for (int m = 0; m < 8; ++m) {
            const unsigned a = (wa ^ ((m & 2) << 5)) + m * 1024;
            *(LAS u32x2*)(size_t)a = u32x2{wr[m].x, wr[m].y};
            *(LAS u32x2*)(size_t)(a ^ 8) = u32x2{wr[m].z, wr[m].w};
        }
        asm volatile("s_waitcnt vmcnt(0)" : "+v"(xr[0]), "+v"(xr[1]), "+v"(xr[2]), "+v"(xr[3]), "+v"(xr[4]), "+v"(xr[5]), "+v"(xr[6]), "+v"(xr[7]) :: "memory");
#pragma unroll
        for (int m = 0; m < 8; ++m) *(LAS u32x4*)(size_t)((xa ^ ((m & 3) << 6)) + m * 1024) = xr[m];
        STAMP(4);
        lds_arrive(smem + L_CNT + 80, l);
        lds_await(smem + L_CNT + 80, 4);
        STAMP(5);
        bf16x8 xo[2][4], fr[2][8];
        f32x4 qc[2][8];
        auto wq_frags = [&](int u, bf16x8 (&f)[8]) {
#pragma unroll
            for (int a = 0; a < 8; ++a) {
                LAS char* pa = (LAS char*)(size_t)aqa[a] + u * 16384;
                f[a] = cat(tr_read(pa), tr_read(pa + 1024));
            }
        };
#pragma unroll
        for (int u = 0; u < 4; ++u) { xo[0][u] = *(LAS bf16x8*)(size_t)xfa[u]; xo[1][u] = *(LAS bf16x8*)(size_t)(xfa[u] + 4096); }
        wq_frags(0, fr[0]);
        {
            const Srd4 srd_v = make_srd4(wv, 32768);
            const unsigned r0 = (j * 32 + g) << 8, o0 = (n ^ ((g & 1) | ((g & 2) << 1))) << 4;
            const unsigned e[8] = {r0, r0 + 1024, r0 + 2048, r0 + 3072, r0 + 4096, r0 + 5120, r0 + 6144, r0 + 7168};
            const unsigned base = lds0 + (j < 2 ? L_WV0 + j * 16384 : L_WV1 + (j - 2) * 8192);
            const u32x4 off = {o0, o0 ^ 128, o0, o0 ^ 128};
            dma_half(base, srd_v, e, off); dma_half(base + 4096, srd_v, e + 4, off);
        }
        if (PFA_KB) prefetch(0, PFA_KB);
#pragma unroll
        for (int u = 0; u < 4; ++u) {
            if (u < 3) wq_frags(u + 1, fr[(u + 1) & 1]);
            FENCE();
#pragma unroll
            for (int a = 0; a < 8; ++a)
#pragma unroll
                for (int tt = 0; tt < 2; ++tt) qc[tt][a] = mfma(fr[u & 1][a], xo[tt][u], u ? qc[tt][a] : f32x4{0.f, 0.f, 0.f, 0.f});
            FENCE();
            if (!WVLATE && u == 1 && j < 2) { VMWAIT(0); lds_arrive(smem + L_CNT + 88, l); }
        }
        STAMP(6);
        if (PFB_KB) { asm volatile("s_waitcnt vmcnt(%c0)" :: "i"(PFA_KB ? 1 : 0) : "memory"); prefetch(PFA_KB, PFB_KB); }
        if (isprod) lds_await(smem + L_CNT + 84, 4);
#pragma unroll
        for (int tt = 0; tt < 2; ++tt)
#pragma unroll
            for (int s = 0; s < 4; ++s) {
                const float k = -0.08838834764831845f * 1.4426950408889634f;
                const f32x4 c0 = qc[tt][2 * s] * k, c1 = qc[tt][2 * s + 1] * k;
                *(LAS u32x4*)(size_t)(ring - (tt ? 0 : 65536) + s * 1024 + l * 16) = u32x4{pk2(c0[0], c0[1]), pk2(c0[2], c0[3]), pk2(c1[0], c1[1]), pk2(c1[2], c1[3])};
            }
        clear_sums();
        if (!WVLATE) { lds_await(smem + L_CNT + 88, 2); wv_resident(); }
        if (!PFB_KB) asm volatile("s_waitcnt vmcnt(%c0)" :: "i"(PFA_KB ? 1 : 0) : "memory");
        if (PFC_KB) prefetch(PFA_KB + PFB_KB, PFC_KB);
        STAMP(7);
    }
    WG_BARRIER();
    STAMP(8);
    if (LPRIO >= 3) __builtin_amdgcn_s_setprio(3);
    if (LPRIO == 1 && w >= 4) __builtin_amdgcn_s_setprio(1);
    if (LPRIO == 2) { if (w >= 6) __builtin_amdgcn_s_setprio(3); else if (w >= 4) __builtin_amdgcn_s_setprio(2); else if (w >= 2) __builtin_amdgcn_s_setprio(1); }

    u32x16 qf;
    const u32x4 doff = {addr[12], addr[13], addr[14], addr[15]};
    LAS char* mylist = smem + L_LIST + w * (2 * SUBB);
    int gcur = G1, pos = 0;
    unsigned hmask = 0xFFFFFFFFu;
    auto batch = [&](auto first_) {
        constexpr bool first = decltype(first_)::value;
        auto entries = [&](int grp, unsigned (&e)[8]) {
#pragma unroll
            for (int m = 0; m < 8; ++m) e[m] = *(LAS unsigned*)(size_t)(lista + grp * 128 + m * 16);
        };
        unsigned cw = *(LAS unsigned*)(size_t)(first ? cnta : lds0 + L_CNT + (__builtin_amdgcn_mbcnt_lo(~0u, 0u) & 31) * 4), e0[8], e1[8];
        entries(0, e0);
        if constexpr (first) {
#pragma unroll
            for (int s = 0; s < 4; ++s) {
                const u32x4 v = *(LAS u32x4*)(size_t)(qa + s * 1024);
                qf[4 * s] = v.x; qf[4 * s + 1] = v.y; qf[4 * s + 2] = v.z; qf[4 * s + 3] = v.w;
            }
        }
        entries(1, e1);
        FENCE();
        int lo = __builtin_amdgcn_readlane(cw, 2 * w) >> 2, hi = __builtin_amdgcn_readlane(cw, 2 * w + 1) >> 2;
        if constexpr (first) {
            if (__builtin_amdgcn_readlane(cw, 16)) gcur = 0;
            if (__builtin_amdgcn_ballot_w64(cw > 4u * CAP2)) { gcur = -1; lo = 0; hi = 0; hmask = 0x7FFFFFFFu; }
        }
        const int c = lo + hi, G = (c + 31) >> 5;
        const int nlast = G > 2 ? (((c - 1) & 31) >> 2) + 1 : 8;
        auto join = [&]() {
            for (int k = 0; k < G * 32 - lo; k += 64) {
                const unsigned e = *(LAS unsigned*)(mylist + SUBB + (l + k) * 4);
                if (l + k < G * 32 - lo) *(LAS unsigned*)(mylist + (lo + l + k) * 4) = l + k < hi ? e : PAD;
            }
        };
        const bool early = lo >= 64;
        if (__builtin_expect(!early, 0)) { join(); entries(0, e0); entries(1, e1); }
        if constexpr (first) asm volatile("s_waitcnt lgkmcnt(0)" : "+{v[160:175]}"(qf) :: "memory");
        if (G > 0) dma_group(ring, srd_x, e0, doff);
        if constexpr (first) {
            if (WVLATE == 1) {
                if (G > 1 && w < 6) dma_group(ring + 8192, srd_x, e1, doff);
                wv_resident();
                lds_arrive(smem + L_CNT + 92, l);
                if (w >= 6) { lds_await(smem + L_CNT + 92, 8); if (G > 1) dma_group(ring + 8192, srd_x, e1, doff); }
            } else if (WVLATE == 2) {
                wv_resident();
                lds_arrive(smem + L_CNT + 92, l);
                if (w >= 6) lds_await(smem + L_CNT + 92, 8);
                if (G > 1) dma_group(ring + 8192, srd_x, e1, doff);
            } else {
                if (w < 4) { wv_resident(); lds_arrive(smem + L_CNT + 92, l); }
                else if (w >= 6) lds_await(smem + L_CNT + 92, 4);
                if (G > 1) dma_group(ring + 8192, srd_x, e1, doff);
            }
        } else if (G > 1) dma_group(ring + 8192, srd_x, e1, doff);
        f32x16 cc;
#pragma unroll
        for (int m = 0; m < 8; ++m) {
            cc[m] = (e0[m] & 0xFF) == keyn ? 0.f : __uint_as_float(inf);
            cc[8 + m] = (e1[m] & 0xFF) == keyn ? 0.f : __uint_as_float(inf);
        }
        if (early) join();
        STAMP(9);
        if (G > 0) gather_loop(acc, qf, addr, u32x4{lista, keyn, inf, 0u}, cc, srd_x, ring, G, nlast);
    };
    batch(std::true_type{});
    while (__builtin_expect(gcur < G1, 0)) {
        unsigned lim = G1 * 32;
        if (gcur >= 0) {
            const u64 m0 = __builtin_amdgcn_ballot_w64(l >= gcur && (int)(*(LAS unsigned*)(smem + L_HDR + l * 4) & hmask) > 4 * CAP1);
            const u64 m1 = __builtin_amdgcn_ballot_w64(l + 64 >= gcur && (int)(*(LAS unsigned*)(smem + L_HDR + 256 + l * 4) & hmask) > 4 * CAP1);
            if (!(m0 | m1)) break;
            gcur = m0 ? __builtin_ctzll(m0) : 64 + __builtin_ctzll(m1);
            unsigned v; const unsigned o = WS_NOVF + gcur * 4;
            asm volatile("buffer_load_dword %0, %1, %2, 0 offen sc1\n\ts_waitcnt vmcnt(0)" : "=&v"(v) : "v"(o), "s"(srd_ws) : "memory");
            lim = rfl(v);
        }
        const unsigned i = pos + t;
        const bool cand = t < CAP2 && i < lim;
        unsigned u;
        { const unsigned o = cand ? (gcur < 0 ? WS_INBOX + b * (G1 * 128) : WS_OVF + gcur * CH * 4) + i * 4 : OOB;
          asm volatile("buffer_load_dword %0, %1, %2, 0 offen sc1\n\ts_waitcnt vmcnt(0)" : "=&v"(u) : "v"(o), "s"(srd_ws) : "memory"); }
        WG_BARRIER();
        if (t < 16) *(LAS unsigned*)(smem + L_CNT + t * 4) = 0;
        WG_BARRIER();
        if (cand) {
            unsigned e; bool v;
            if (gcur < 0) { e = (i & 3) ? u : u ^ mykey; v = ((i & 31) - 2) * 4 < *(LAS unsigned*)(smem + L_HDR + (i >> 5) * 4); }
            else { e = (((u >> 16) + BIAS) << 8) | ((u >> 8) & 0xFF); v = (u & 0xFF) == (unsigned)b; }
            if (v) *(LAS unsigned*)(smem + L_LIST + (e & 7) * (2 * SUBB) + lds_add(smem + L_CNT + (e & 7) * 8, 4)) = e;
        }
        pos += CAP2;
        if ((unsigned)pos >= lim) { ++gcur; pos = 0; }
        WG_BARRIER();
        batch(std::false_type{});
    }
    STAMP(10);

    {
        bf16x8 wl[2][8], ag[4];
#pragma unroll
        for (int u = 0; u < 2; ++u)
#pragma unroll
            for (int a = 0; a < 8; ++a) {
                LAS char* pa = smem + L_WV1 + (cv ^ ((a >> 1) << 6) ^ ((a & 1) << 3)) + u * 8192;
                wl[u][a] = cat(tr_read(pa), tr_read(pa + 4096));
            }
#pragma unroll
        for (int u = 0; u < 4; ++u) {
            const u32x4 v = {pk2(acc[8 * u], acc[8 * u + 1]), pk2(acc[8 * u + 2], acc[8 * u + 3]),
                             pk2(acc[8 * u + 4], acc[8 * u + 5]), pk2(acc[8 * u + 6], acc[8 * u + 7])};
            ag[u] = __builtin_bit_cast(bf16x8, v);
        }
        FENCE();
        f32x4 oc[8];
#pragma unroll
        for (int a = 0; a < 8; ++a) oc[a] = mfma(__builtin_bit_cast(bf16x8, f32x4{wva[4 * a], wva[4 * a + 1], wva[4 * a + 2], wva[4 * a + 3]}), ag[0], f32x4{0.f, 0.f, 0.f, 0.f});
#pragma unroll
        for (int a = 0; a < 8; ++a) oc[a] = mfma(__builtin_bit_cast(bf16x8, f32x4{wvb[4 * a], wvb[4 * a + 1], wvb[4 * a + 2], wvb[4 * a + 3]}), ag[1], oc[a]);
#pragma unroll
        for (int a = 0; a < 8; ++a) oc[a] = mfma(wl[0][a], ag[2], oc[a]);
        FENCE();
        const unsigned ow = ring + n * 256 + ((g ^ n) << 4) + ((g & 1) << 3);
#pragma unroll
        for (int a = 0; a < 8; ++a) oc[a] = mfma(wl[1][a], ag[3], oc[a]);
#pragma unroll
        for (int a = 0; a < 8; ++a)
            *(LAS u32x2*)(size_t)(ow ^ ((a >> 1) << 6) ^ ((a & 1) << 3)) = u32x2{pk2(oc[a][0], oc[a][1]), pk2(oc[a][2], oc[a][3])};
        const auto rs_o = __builtin_amdgcn_make_buffer_rsrc(out, 0, xbytes, 0x00020000);
#pragma unroll
        for (int m = 0; m < 4; ++m) {
            const u32x4 v = *(LAS u32x4*)(size_t)(ring + m * 1024 + l * 16);
            __builtin_amdgcn_raw_buffer_store_b128(v, rs_o, node_row(w, m) + ((n ^ g ^ (4 * m)) << 4), 0, OUT_AUX);
        }
    }
    if (w == 0) { const unsigned o = l == 0 ? WS_KEY + b * 4 : OOB; ST1(mykey, srd_ws, o, " sc1"); }
    STAMP(11);
}

extern "C" void kernel_launch(void* const* d_in, const int* in_sizes, int n_in,
                              void* d_out, int out_size, void* d_ws, size_t ws_size,
                              hipStream_t stream) {
    const int N = in_sizes[0] / 128, E = in_sizes[1];
    k_graph<<<NB, 512, 0, stream>>>((const unsigned short*)d_in[0], (const int*)d_in[1], (const int*)d_in[2],
                                    (const unsigned short*)d_in[3], (const unsigned short*)d_in[4],
                                    (unsigned*)d_ws, (unsigned short*)d_out, N, E);
}
```

```cpp
#define POLL_T 260
#define POLL_D 40
#include <hip/hip_runtime.h>
#include <hip/hip_bf16.h>
#include <stdint.h>
#include <type_traits>
#define ORD 1
#define SKIP 0
#define STR2(x) #x
#define STR(x) STR2(x)

typedef __attribute__((ext_vector_type(8))) __bf16 bf16x8;
typedef __attribute__((ext_vector_type(4))) __bf16 bf16x4;
typedef __attribute__((ext_vector_type(2))) __bf16 bf16x2;
typedef __attribute__((ext_vector_type(4))) float f32x4;
typedef __attribute__((ext_vector_type(2))) float f32x2;
typedef __attribute__((ext_vector_type(4))) unsigned u32x4;
typedef __attribute__((ext_vector_type(2))) unsigned u32x2;
typedef __attribute__((ext_vector_type(32))) float f32x32;
typedef __attribute__((ext_vector_type(16))) float f32x16;
typedef __attribute__((ext_vector_type(16))) unsigned u32x16;
typedef __attribute__((ext_vector_type(12))) unsigned u32x12;
typedef __attribute__((ext_vector_type(8))) unsigned u32x8;
typedef unsigned long long u64;
#define LAS __attribute__((address_space(3)))

constexpr int NB = 256;
constexpr int G1 = 128;
constexpr int CH = 3136;
constexpr int CAP1 = 30;
constexpr int CAP2 = 192;
constexpr int SUBB = 784;
#define POLL_AT 3
#define LPRIO 3
#define PKADD 1
#define CODE_BYTES 23552
#define PARTNER 1
#define XSYNC 1
#define WVLATE 1
#define PFA_KB 0
#define PFB_KB 0
#define PFC_KB 1024
#define X_T 0
#define XAFTER 0
#define FASTIN 1
#define W_SLEEP 3
#define PRIO 1
#define SPLIT 16384
constexpr unsigned KEY_STEP = 0x9E3779B1u;
constexpr unsigned WS_KEY = 0;
constexpr unsigned WS_NOVF = 2048;
constexpr unsigned WS_INBOX = 4096;
constexpr unsigned WS_OVF = WS_INBOX + NB * G1 * 128;
constexpr unsigned WS_BYTES = WS_OVF + G1 * CH * 4;
constexpr unsigned OOB = 0x80000000u;
constexpr unsigned BIAS = 32768 - SPLIT;
constexpr unsigned PAD = 0x00FFFFFFu;

constexpr int L_CNT = 0;
constexpr int L_HDR = 128;
constexpr int L_LIST = 768;
constexpr int L_RING = L_LIST + 16 * SUBB;
constexpr int L_IMG = L_RING;
constexpr int L_CNT1 = L_IMG + NB * 144;
constexpr int L_WQ = L_RING + 2 * 16384 + 8192;
constexpr int L_WV0 = L_RING + 6 * 16384 + 8192;
constexpr int L_WV1 = L_RING + 131072;
constexpr int L_DUMP = L_WV1 + 16384;
constexpr int L_TOTAL = L_DUMP + 2048;
static_assert(WS_KEY == 0 && L_WV1 % 256 == 0 && CH == 3072 + 64 && L_RING % 256 == 0 && L_TOTAL <= 163840, "LDS map");

#define STAMP_DECL
#define STAMP(k)
#define STAMP_FLUSH(base, k0, k1)

__device__ __forceinline__ u32x4 make_srd(const void* p, unsigned bytes, unsigned back = 0) {
    const u64 a = (u64)p - back;
    u32x4 s; s.x = (unsigned)a; s.y = (unsigned)(a >> 32); s.z = bytes + back; s.w = 0x00020000u; return s;
}
#define LD4(dst, srd, off, FL) asm volatile("buffer_load_dwordx4 %0, %1, %2, 0 offen" FL : "=v"(dst) : "v"(off), "s"(srd) : "memory")
#define LD4S(dst, srd, off, so, FL) asm volatile("buffer_load_dwordx4 %0, %1, %2, %3 offen" FL : "=v"(dst) : "v"(off), "s"(srd), "s"(so) : "memory")
#define LD1(dst, srd, off, FL) asm volatile("buffer_load_dword %0, %1, %2, 0 offen" FL : "=v"(dst) : "v"(off), "s"(srd) : "memory")
#define ST1(val, srd, off, FL) asm volatile("buffer_store_dword %0, %1, %2, 0 offen" FL :: "v"(val), "v"(off), "s"(srd) : "memory")
#define PIN(x) asm volatile("" : "+v"(x))
#define VMWAIT(n) asm volatile("s_waitcnt vmcnt(" #n ")" ::: "memory")
#define FENCE() __builtin_amdgcn_sched_barrier(0)
#define WG_BARRIER() do { asm volatile("s_waitcnt lgkmcnt(0)" ::: "memory"); __builtin_amdgcn_s_barrier(); asm volatile("" ::: "memory"); } while (0)

struct Srd4 { u32x4 s0, s1, s2, s3; };
__device__ __forceinline__ Srd4 make_srd4(const void* p, unsigned bytes, unsigned back = 0) {
    return Srd4{make_srd(p, bytes, back), make_srd(p, bytes, back + 1024), make_srd(p, bytes, back + 2048), make_srd(p, bytes, back + 3072)};
}

__device__ __forceinline__ void set_m0(unsigned v) { asm volatile("s_mov_b32 m0, %0" :: "s"(v) : "memory"); }
template <int R> __device__ __forceinline__ void dma_rows(const Srd4& d, unsigned e, unsigned off) {
    unsigned tmp, mask = 0x00FFFF00u;
    asm volatile("v_and_or_b32 %0, %1, %2, %3\n\tbuffer_load_dwordx4 %0, %4, 0 offen offset:%c5 lds"
                 : "=&v"(tmp) : "v"(e), "s"(mask), "v"(off), "s"(R == 0 ? d.s0 : R == 1 ? d.s1 : R == 2 ? d.s2 : d.s3), "i"(R * 1024) : "memory");
}
__device__ __forceinline__ void dma_half(unsigned ldsbase, const Srd4& d, const unsigned* e, const u32x4& off) {
    set_m0(ldsbase);
    dma_rows<0>(d, e[0], off.x); dma_rows<1>(d, e[1], off.y); dma_rows<2>(d, e[2], off.z); dma_rows<3>(d, e[3], off.w);
}
__device__ __forceinline__ void dma_group(unsigned ldsbase, const Srd4& d, const unsigned (&e)[8], const u32x4& off) {
    asm volatile("s_waitcnt lgkmcnt(0)" ::: "memory");
    dma_half(ldsbase, d, e, off); dma_half(ldsbase + 4096, d, e + 4, off);
}

__device__ __forceinline__ void gather_loop(f32x32& acc, const u32x16& qf, const u32x16& addr, u32x4 misc, f32x16 cc,
                                            const Srd4& d, unsigned ring, int G, int nlast) {
    f32x32 t0, t1; u32x12 t2; int tmp, pr; u64 sm; unsigned mask = 0x00FFFF00u;
    asm volatile(
        ".macro GWAIT k\n\ts_cmp_gt_i32 %[t], \\k\n\ts_cbranch_scc1 1f\n\ts_waitcnt vmcnt(\\k)\n\t.endm\n\t"
        ".macro GROWS so\n\t.irp s, 0, 1, 2, 3\n\t"
        "ds_read_b128 v[64+8*\\s:67+8*\\s], v[176+\\s] offset:\\so\n\t"
        "ds_read_b128 v[68+8*\\s:71+8*\\s], v[176+\\s] offset:\\so+4096\n\t.endr\n\t.endm\n\t"
        ".macro GTR c, so\n\t"
        "ds_read_b64_tr_b16 v[96+4*\\c:97+4*\\c], v[180+\\c] offset:\\so\n\t"
        "ds_read_b64_tr_b16 v[98+4*\\c:99+4*\\c], v[180+\\c] offset:\\so+4096\n\t.endm\n\t"
        ".macro GS s, c\n\t"
        "v_mfma_f32_16x16x32_bf16 v[\\c:\\c+3], v[64+8*\\s:67+8*\\s], v[160+4*\\s:163+4*\\s], v[\\c:\\c+3]\n\t"
        "v_mfma_f32_16x16x32_bf16 v[\\c+4:\\c+7], v[68+8*\\s:71+8*\\s], v[160+4*\\s:163+4*\\s], v[\\c+4:\\c+7]\n\t.endm\n\t"
        ".macro GLOAD m, r, srd\n\t"
        "v_and_or_b32 v195, v[212+\\m], %[mask], v[188+\\r]\n\t"
        "buffer_load_dwordx4 v195, \\srd, 0 offen offset:1024*\\r lds\n\t.endm\n\t"
        ".macro GPART m, r, srd\n\ts_cmp_le_i32 %[nl], \\m\n\ts_cbranch_scc1 4f\n\tGLOAD \\m, \\r, \\srd\n\t.endm\n\t"
        ".macro GSUM m\n\tv_mfma_f32_16x16x32_bf16 v[128+4*(\\m):131+4*(\\m)], v[96+4*(\\m):99+4*(\\m)], v[220:223], v[128+4*(\\m):131+4*(\\m)]\n\t.endm\n\t"
        ".macro GCMP m, d\n\tv_cmp_ne_u32_sdwa \\d, v[212+\\m], v193 src0_sel:BYTE_0 src1_sel:DWORD\n\t.endm\n\t"
        ".macro GPAIR m, c\n\t"
        "GSUM \\m\n\tGCMP \\m+1, %[sm]\n\tv_cndmask_b32_e32 v[\\c+\\m], 0, v194, vcc\n\t"
        "GSUM \\m+1\n\t.if \\m < 6\n\tGCMP \\m+2, vcc\n\t.else\n\ts_cmp_gt_i32 %[rem], 0\n\t.endif\n\t"
        "v_cndmask_b32_e64 v[\\c+\\m+1], 0, v194, %[sm]\n\t.endm\n\t"
        ".macro GSTEP so, c, eo\n\t"
        "GTR 0, \\so\n\t"
        "s_waitcnt lgkmcnt(2)\n\tGS 0, \\c\n\t"
        "GTR 1, \\so\n\tGTR 2, \\so\n\tGS 1, \\c\n\t"
        "GTR 3, \\so\n\tGTR 4, \\so\n\tGS 2, \\c\n\t"
        "GTR 5, \\so\n\tGTR 6, \\so\n\tGS 3, \\c\n\t"
        "GTR 7, \\so\n\t"
        "ds_read2_b32 v[212:213], v192 offset0:\\eo offset1:\\eo+4\n\t"
        "ds_read2_b32 v[214:215], v192 offset0:\\eo+8 offset1:\\eo+12\n\t"
        "ds_read2_b32 v[216:217], v192 offset0:\\eo+16 offset1:\\eo+20\n\t"
        "ds_read2_b32 v[218:219], v192 offset0:\\eo+24 offset1:\\eo+28\n\t"
        "s_add_u32 m0, %[ring], \\so\n\t"
        "s_cmp_gt_i32 %[rem], 3\n\t"
        ".irp k, 0, 1, 2, 3, 4, 5, 6, 7\n\tv_exp_f32_e32 v[\\c+\\k], v[\\c+\\k]\n\t.endr\n\t"
        "s_waitcnt lgkmcnt(0)\n\t"
        "s_cbranch_scc0 2f\n\t"
        "GLOAD 0, 0, %[s0]\n\tGLOAD 1, 1, %[s1]\n\tGLOAD 2, 2, %[s2]\n\tGLOAD 3, 3, %[s3]\n\t"
        "s_add_u32 m0, m0, 0x1000\n\t"
        "GLOAD 4, 0, %[s0]\n\tGLOAD 5, 1, %[s1]\n\tGLOAD 6, 2, %[s2]\n\tGLOAD 7, 3, %[s3]\n\t"
        "GSIG \\c\n\t"
        "s_waitcnt vmcnt(8)\n\t"
        "s_branch 1f\n\t"
        "2:\n\t"
        "s_mov_b32 %[t], 0\n\t"
        "s_cmp_eq_u32 %[rem], 3\n\ts_cbranch_scc0 4f\n\t"
        "s_mov_b32 %[t], %[nl]\n\t"
        "GLOAD 0, 0, %[s0]\n\tGPART 1, 1, %[s1]\n\tGPART 2, 2, %[s2]\n\tGPART 3, 3, %[s3]\n\t"
        "s_add_u32 m0, m0, 0x1000\n\t"
        "GPART 4, 0, %[s0]\n\tGPART 5, 1, %[s1]\n\tGPART 6, 2, %[s2]\n\tGPART 7, 3, %[s3]\n\t"
        "4:\n\t"
        "GSIG \\c\n\t"
        "s_waitcnt vmcnt(8)\n\t"
        "GWAIT 7\n\tGWAIT 6\n\tGWAIT 5\n\tGWAIT 4\n\tGWAIT 3\n\tGWAIT 2\n\tGWAIT 1\n\tGWAIT 0\n\t"
        "1:\n\t"
        "GROWS 8192-\\so\n\t"
        "GCMP 0, vcc\n\t"
        "s_add_i32 %[rem], %[rem], -1\n\t"
        "GPAIR 0, \\c\n\tGPAIR 2, \\c\n\tGPAIR 4, \\c\n\tGPAIR 6, \\c\n\t"
        ".endm\n\t"
        ".macro GSIG c\n\t"
        ".if " STR(PKADD) "\n\t.irp k, 0, 2, 4, 6\n\tv_pk_add_f32 v[\\c+\\k:\\c+\\k+1], v[\\c+\\k:\\c+\\k+1], 1.0 op_sel_hi:[1,0]\n\t.endr\n\t.else\n\t"
        ".irp k, 0, 1, 2, 3, 4, 5, 6, 7\n\tv_add_f32_e32 v[\\c+\\k], 1.0, v[\\c+\\k]\n\t.endr\n\t.endif\n\t"
        ".irp k, 0, 1, 2, 3, 4, 5, 6, 7\n\tv_rcp_f32_e32 v[\\c+\\k], v[\\c+\\k]\n\t.endr\n\t"
        ".irp k, 0, 1, 2, 3\n\tv_cvt_pk_bf16_f32 v[220+\\k], v[\\c+2*\\k], v[\\c+2*\\k+1]\n\t.endr\n\t.endm\n\t"
        "s_waitcnt vmcnt(8)\n\t"
        "s_cmp_gt_i32 %[rem], 1\n\ts_cbranch_scc1 1f\n\ts_waitcnt vmcnt(0)\n\t1:\n\t"
        "GROWS 0\n\t"
        ".if " STR(LPRIO) " >= 3\n\ts_mov_b32 %[pr], 0\n\ts_setprio 2\n\t.endif\n\t"
        "0:\n\t"
        ".if " STR(LPRIO) " == 3\n\t"
        "s_cmp_eq_u32 %[pr], 1\n\ts_cbranch_scc0 5f\n\ts_setprio 1\n\t5:\n\t"
        "s_cmp_eq_u32 %[pr], 2\n\ts_cbranch_scc0 5f\n\ts_setprio 0\n\t5:\n\t"
        "s_add_u32 %[pr], %[pr], 1\n\t.endif\n\t"
        "GSTEP 0, 196, 64\n\t"
        "s_cbranch_scc0 9f\n\t"
        "GSTEP 8192, 204, 96\n\t"
        "v_add_u32_e32 v192, 0x100, v192\n\t"
        "s_cbranch_scc1 0b\n\t"
        "9:\n\t"
        "s_waitcnt lgkmcnt(0)\n\ts_nop 6\n\t"
        ".purgem GWAIT\n\t.purgem GROWS\n\t.purgem GTR\n\t.purgem GS\n\t.purgem GLOAD\n\t.purgem GPART\n\t.purgem GSUM\n\t.purgem GCMP\n\t.purgem GPAIR\n\t.purgem GSTEP\n\t.purgem GSIG"
        : "+{v[128:159]}"(acc), "+{v[192:195]}"(misc), "+{v[196:211]}"(cc), "=&{v[64:95]}"(t0), "=&{v[96:127]}"(t1), "=&{v[212:223]}"(t2),
          [rem] "+s"(G), [t] "=&s"(tmp), [pr] "=&s"(pr), [sm] "=&s"(sm)
        : "{v[160:175]}"(qf), "{v[176:191]}"(addr), [s0] "s"(d.s0), [s1] "s"(d.s1), [s2] "s"(d.s2), [s3] "s"(d.s3),
          [mask] "s"(mask), [ring] "s"(ring), [nl] "s"(nlast)
        : "vcc", "scc", "memory");
}

__device__ __forceinline__ void output_tile(f32x32 acc, f32x32 wva, f32x32 wvb, unsigned wa, unsigned ow, unsigned ra, unsigned go, const u32x4& srd) {
    f32x32 t0, t1, t2, t3; unsigned o1, o2, o3;
    asm volatile(
        ".macro VPK k\n\tv_cvt_pk_bf16_f32 v[196+\\k], v[128+2*(\\k)], v[129+2*(\\k)]\n\t.endm\n\t"
        ".macro VRD a, u\n\tds_read_b64_tr_b16 v[64+32*\\u+4*\\a:65+32*\\u+4*\\a], v[212+\\a] offset:8192*\\u\n\t"
        "ds_read_b64_tr_b16 v[66+32*\\u+4*\\a:67+32*\\u+4*\\a], v[212+\\a] offset:8192*\\u+4096\n\t.endm\n\t"
        ".macro VMM a, w, u\n\tv_mfma_f32_16x16x32_bf16 v[160+4*\\a:163+4*\\a], v[\\w+4*\\a:\\w+4*\\a+3], v[196+4*\\u:199+4*\\u], v[160+4*\\a:163+4*\\a]\n\t.endm\n\t"
        ".macro VADR a, r\n\tv_xor_b32_e32 v[212+\\a], ((\\a >> 1) << 6) | ((\\a & 1) << 3), \\r\n\t.endm\n\t"
        ".macro VOUT a\n\tv_cvt_pk_bf16_f32 v[160+4*\\a], v[160+4*\\a], v[161+4*\\a]\n\tv_cvt_pk_bf16_f32 v[161+4*\\a], v[162+4*\\a], v[163+4*\\a]\n\t"
        "ds_write_b64 v[212+\\a], v[160+4*\\a:161+4*\\a]\n\t.endm\n\t"
        "VPK 0\n\tVPK 1\n\tVPK 2\n\tVPK 3\n\t"
        ".irp a, 0, 1, 2, 3, 4, 5, 6, 7\n\tVADR \\a, %[wa]\n\t.endr\n\t"
        ".irp a, 0, 1, 2, 3, 4, 5, 6, 7\n\t"
        "v_mfma_f32_16x16x32_bf16 v[160+4*\\a:163+4*\\a], v[224+4*\\a:227+4*\\a], v[196:199], 0\n\tVRD \\a, 0\n\tVPK 4+\\a\n\t.endr\n\t"
        ".irp a, 0, 1, 2, 3\n\tVMM \\a, 32, 1\n\tVRD \\a, 1\n\tVPK 12+\\a\n\t.endr\n\t"
        "VMM 4, 32, 1\n\tVRD 4, 1\n\ts_mov_b32 %[o1], 0x200000\n\t"
        "VMM 5, 32, 1\n\tVRD 5, 1\n\ts_mov_b32 %[o2], 0x400000\n\t"
        "VMM 6, 32, 1\n\tVRD 6, 1\n\ts_mov_b32 %[o3], 0x600000\n\t"
        "VMM 7, 32, 1\n\tVRD 7, 1\n\ts_waitcnt lgkmcnt(15)\n\t"
        ".irp a, 0, 1, 2, 3, 4, 5, 6, 7\n\tVMM \\a, 64, 2\n\tVADR \\a, %[ow]\n\t.endr\n\t"
        "s_waitcnt lgkmcnt(0)\n\t"
        "VMM 0, 96, 3\n\tv_xor_b32_e32 v220, 64, %[go]\n\tv_xor_b32_e32 v221, 0x80, %[go]\n\tv_xor_b32_e32 v222, 0xc0, %[go]\n\t"
        "VMM 1, 96, 3\n\ts_nop 2\n\t"
        "VMM 2, 96, 3\n\tVOUT 0\n\tVMM 3, 96, 3\n\tVOUT 1\n\tVMM 4, 96, 3\n\tVOUT 2\n\tVMM 5, 96, 3\n\tVOUT 3\n\t"
        "VMM 6, 96, 3\n\tVOUT 4\n\tVMM 7, 96, 3\n\tVOUT 5\n\ts_nop 0\n\tVOUT 6\n\ts_nop 0\n\tVOUT 7\n\t"
        ".irp m, 0, 1, 2, 3\n\tds_read_b128 v[64+4*\\m:67+4*\\m], %[ra] offset:1024*\\m\n\t.endr\n\t"
        "s_waitcnt lgkmcnt(3)\n\tbuffer_store_dwordx4 v[64:67], %[go], %[srd], 0 offen sc1\n\t"
        "s_waitcnt lgkmcnt(2)\n\tbuffer_store_dwordx4 v[68:71], v220, %[srd], %[o1] offen sc1\n\t"
        "s_waitcnt lgkmcnt(1)\n\tbuffer_store_dwordx4 v[72:75], v221, %[srd], %[o2] offen sc1\n\t"
        "s_waitcnt lgkmcnt(0)\n\tbuffer_store_dwordx4 v[76:79], v222, %[srd], %[o3] offen sc1\n\ts_nop 1\n\t"
        ".purgem VPK\n\t.purgem VRD\n\t.purgem VMM\n\t.purgem VADR\n\t.purgem VOUT"
        : "=&{v[64:95]}"(t0), "=&{v[96:127]}"(t1), "=&{v[160:191]}"(t2), "=&{v[192:223]}"(t3), [o1] "=&s"(o1), [o2] "=&s"(o2), [o3] "=&s"(o3)
        : "{v[128:159]}"(acc), "{v[224:255]}"(wva), "{v[32:63]}"(wvb), [wa] "v"(wa), [ow] "v"(ow), [ra] "v"(ra), [go] "v"(go), [srd] "s"(srd)
        : "memory");
}

__device__ __forceinline__ u64 rank_edges(u32x16& ed, u32x16& es, u32x16& rk, u64 m0, u64 m1, u64 m2, u64 m3, unsigned keya) {
    u32x16 b4; u64 over; unsigned sel = 0x01050401u, c36 = 36;
    asm volatile(
        ".macro RADD k\n\tv_lshlrev_b32_sdwa v[96+\\k], 2, v[64+\\k] dst_sel:DWORD dst_unused:UNUSED_PAD src0_sel:DWORD src1_sel:BYTE_0\n\t"
        "ds_add_rtn_u32 v[112+\\k], v[96+\\k], v111 offset:%c[cnt]\n\t.endm\n\t"
        ".macro RENT k\n\tv_add_u32_e32 v[80+\\k], %c[bias], v[80+\\k]\n\tv_perm_b32 v[80+\\k], v[80+\\k], v[64+\\k], %[sel]\n\t.endm\n\t"
        ".macro RPUT k\n\tv_min_u32_e32 v110, %c[cap], v[112+\\k]\n\tv_mad_u32_u24 v110, v[96+\\k], %[c36], v110\n\t"
        "ds_write_b32 v110, v[80+\\k] offset:%c[img]\n\t.endm\n\t"
        "v_mov_b32_e32 v111, 4\n\t"
        ".irp k, 0, 2, 4, 6, 8, 10, 12\n\tv_mov_b64_e32 v[112+\\k:113+\\k], 0\n\t.endr\n\t"
        "s_mov_b64 exec, %[m0]\n\ts_waitcnt vmcnt(9)\n\tRADD 0\n\tRADD 1\n\tRADD 2\n\tRADD 3\n\t"
        "s_mov_b64 exec, %[m1]\n\ts_waitcnt vmcnt(8)\n\tRADD 4\n\tRADD 5\n\tRADD 6\n\tRADD 7\n\t"
        "s_mov_b64 exec, %[m2]\n\ts_waitcnt vmcnt(7)\n\tRADD 8\n\tRADD 9\n\tRADD 10\n\tRADD 11\n\t"
        "s_mov_b64 exec, %[m3]\n\ts_waitcnt vmcnt(6)\n\tRADD 12\n\t"
        "s_mov_b64 exec, %[m0]\n\ts_waitcnt vmcnt(5)\n\tRENT 0\n\tRENT 1\n\tRENT 2\n\tRENT 3\n\t"
        "s_waitcnt lgkmcnt(9)\n\tRPUT 0\n\tRPUT 1\n\tRPUT 2\n\tRPUT 3\n\t"
        "s_mov_b64 exec, %[m1]\n\ts_waitcnt vmcnt(4)\n\tRENT 4\n\tRENT 5\n\tRENT 6\n\tRENT 7\n\t"
        "s_waitcnt lgkmcnt(9)\n\tRPUT 4\n\tRPUT 5\n\tRPUT 6\n\tRPUT 7\n\t"
        "s_mov_b64 exec, %[m2]\n\ts_waitcnt vmcnt(3)\n\tRENT 8\n\tRENT 9\n\tRENT 10\n\tRENT 11\n\t"
        "s_waitcnt lgkmcnt(9)\n\tRPUT 8\n\tRPUT 9\n\tRPUT 10\n\tRPUT 11\n\t"
        "s_mov_b64 exec, %[m3]\n\ts_waitcnt vmcnt(0)\n\tRENT 12\n\t"
        "s_waitcnt lgkmcnt(12)\n\tRPUT 12\n\t"
        "s_mov_b64 exec, -1\n\t"
        "v_add_u32_e32 v93, %c[step], v93\n\t"
        "v_max3_u32 v110, v112, v113, v114\n\tv_max3_u32 v109, v115, v116, v117\n\t"
        "ds_write_b32 %[keya], v93 offset:%c[cnt]+1024\n\t"
        "v_max3_u32 v110, v110, v118, v119\n\tv_max3_u32 v109, v109, v120, v121\n\t"
        "v_max3_u32 v110, v110, v122, v123\n\tv_max3_u32 v110, v110, v109, v124\n\t"
        "v_cmp_le_u32_e32 vcc, %c[cap], v110\n\ts_mov_b64 %[over], vcc\n\t"
        ".purgem RADD\n\t.purgem RENT\n\t.purgem RPUT"
        : "+{v[64:79]}"(ed), "+{v[80:95]}"(es), "=&{v[96:111]}"(b4), "=&{v[112:127]}"(rk), [over] "=&s"(over)
        : [m0] "s"(m0), [m1] "s"(m1), [m2] "s"(m2), [m3] "s"(m3), [keya] "v"(keya), [sel] "s"(sel), [c36] "s"(c36),
          [cnt] "i"(L_CNT1), [img] "i"(L_IMG + 8), [bias] "i"(BIAS), [cap] "i"(4 * CAP1), [step] "i"(KEY_STEP)
        : "vcc", "memory");
    return over;
}
__device__ __forceinline__ unsigned read_image(f32x32& img, u32x16& meta, u32x16& sums, unsigned ima, unsigned cna) {
    unsigned no;
    asm volatile(
        "v_mov_b32_e32 v121, 0\n\tds_read_b32 v120, v121 offset:%c[cnt]+2048\n\t"
        ".irp k, 0, 1, 2, 3, 4, 5, 6, 7\n\tds_read_b128 v[64+4*\\k:67+4*\\k], %[ima] offset:4608*\\k\n\t.endr\n\t"
        ".irp k, 0, 1, 2, 3\n\tds_read2_b32 v[96+2*\\k:97+2*\\k], %[cna] offset0:64*\\k offset1:64*\\k+32\n\t.endr\n\t"
        "s_waitcnt lgkmcnt(11)\n\tv_readfirstlane_b32 %[no], v120\n\t"
        "v_add_u32_e32 v121, 0x400, %[cna]\n\t"
        ".irp k, 0, 1, 2, 3\n\tds_read2_b32 v[104+2*\\k:105+2*\\k], v121 offset0:64*\\k offset1:64*\\k+32\n\t.endr\n\t"
        "s_waitcnt lgkmcnt(12)\n\t"
        ".irp k, 0, 1, 2, 3\n\tv_add3_u32 v[112+\\k], v[64+4*\\k], v[65+4*\\k], v[66+4*\\k]\n\tv_add_u32_e32 v[112+\\k], v[112+\\k], v[67+4*\\k]\n\t.endr\n\t"
        "s_waitcnt lgkmcnt(8)\n\t"
        ".irp k, 4, 5, 6, 7\n\tv_add3_u32 v[112+\\k], v[64+4*\\k], v[65+4*\\k], v[66+4*\\k]\n\tv_add_u32_e32 v[112+\\k], v[112+\\k], v[67+4*\\k]\n\t.endr\n\t"
        ".irp k, 0, 1, 2, 3, 4, 5, 6, 7\n\tv_add_u32_dpp v[112+\\k], v[112+\\k], v[112+\\k] quad_perm:[1,0,3,2] row_mask:0xf bank_mask:0xf\n\t.endr\n\t"
        ".irp k, 0, 1, 2, 3, 4, 5, 6, 7\n\tv_add_u32_dpp v[112+\\k], v[112+\\k], v[112+\\k] quad_perm:[2,3,0,1] row_mask:0xf bank_mask:0xf\n\t.endr\n\t"
        ".irp k, 0, 1, 2, 3, 4, 5, 6, 7\n\tv_add_u32_dpp v[112+\\k], v[112+\\k], v[112+\\k] row_half_mirror row_mask:0xf bank_mask:0xf\n\t.endr\n\t"
        "s_waitcnt lgkmcnt(0)"
        : "=&{v[64:95]}"(img), "=&{v[96:111]}"(meta), "=&{v[112:127]}"(sums), [no] "=&s"(no)
        : [ima] "v"(ima), [cna] "v"(cna), [cnt] "i"(L_CNT1) : "memory");
    return no;
}
__device__ __forceinline__ void publish_image(f32x32 img, u32x16 meta, u32x16 sums, const u32x4& srd, unsigned ob) {
    u64 pc0 = 0x0101010101010101ull; unsigned so;
    asm volatile(
        ".macro PUB k\n\ts_mov_b64 exec, %[pc0]\n\tv_mov_b32_e32 v[64+4*\\k], v[96+\\k]\n\tv_mov_b32_e32 v[65+4*\\k], v[112+\\k]\n\t"
        "s_mov_b64 exec, -1\n\tv_xor_b32_e32 v[64+4*\\k], v[64+4*\\k], v[104+\\k]\n\t"
        "buffer_store_dwordx4 v[64+4*\\k:67+4*\\k], %[ob], %[srd], %[so] offen sc1\n\ts_add_u32 %[so], %[so], %c[step]\n\t.endm\n\t"
        "s_mov_b32 %[so], 0\n\t"
        ".irp k, 0, 1, 2, 3, 4, 5, 6, 7\n\tPUB \\k\n\t.endr\n\t.purgem PUB"
        : "+{v[64:95]}"(img), [so] "=&s"(so)
        : "{v[96:111]}"(meta), "{v[112:127]}"(sums), [srd] "s"(srd), [ob] "v"(ob), [pc0] "s"(pc0), [step] "i"(32 * G1 * 128) : "scc", "memory");
}

__device__ __forceinline__ void inbox_request(u32x16& raw, const u32x4& srd, unsigned ib) {
    unsigned o1 = 0x1000, o2 = 0x2000, o3 = 0x3000;
    asm volatile("buffer_load_dwordx4 v[64:67], %1, %2, 0 offen sc1\n\tbuffer_load_dwordx4 v[68:71], %1, %2, %3 offen sc1\n\t"
                 "buffer_load_dwordx4 v[72:75], %1, %2, %4 offen sc1\n\tbuffer_load_dwordx4 v[76:79], %1, %2, %5 offen sc1"
                 : "=&{v[64:79]}"(raw) : "v"(ib), "s"(srd), "s"(o1), "s"(o2), "s"(o3) : "memory");
}
__device__ __forceinline__ unsigned inbox_stage(u32x16 raw, const u32x4& srd, unsigned ib, unsigned key, int i4, unsigned hdra) {
    u32x16 t0; f32x32 t2; unsigned four = 4; u64 m0, m1, m2, m3; unsigned todo, fresh, spin, o1, o2, o3, over;
    u64 pc0 = 0x0101010101010101ull;
    asm volatile(
        ".macro IVAL j, so\n\t"
        "s_bitcmp1_b32 %[todo], \\j\n\ts_cbranch_scc0 2f\n\t"
        "v_xor_b32_e32 v[64+4*\\j], %[key], v[64+4*\\j]\n\t"
        "v_sub_u32_e32 v97, 0, v[65+4*\\j]\n\t"
        "v_add_u32_e32 v96, v[64+4*\\j], v[65+4*\\j]\n\t"
        "v_mov_b32_dpp v[80+\\j], v[64+4*\\j] quad_perm:[0,0,0,0] row_mask:0xf bank_mask:0xf\n\t"
        "v_cndmask_b32_e64 v96, v96, v97, %[pc0]\n\t"
        "s_nop 0\n\t"
        "v_mov_b32_dpp v[80+\\j], v[80+\\j] row_shr:4 row_mask:0xf bank_mask:0xa\n\t"
        "v_cmp_gt_i32_e32 vcc, v[80+\\j], v92\n\t"
        "v_add3_u32 v96, v96, v[66+4*\\j], v[67+4*\\j]\n\t"
        "v_lshrrev_b32_e32 v97, 14, v[80+\\j]\n\t"
        "v_cndmask_b32_e32 v96, 0, v96, vcc\n\t"
        "s_nop 1\n\tv_add_u32_dpp v96, v96, v96 quad_perm:[1,0,3,2] row_mask:0xf bank_mask:0xf\n\t"
        "s_nop 1\n\tv_add_u32_dpp v96, v96, v96 quad_perm:[2,3,0,1] row_mask:0xf bank_mask:0xf\n\t"
        "s_nop 1\n\tv_add_u32_dpp v96, v96, v96 row_half_mirror row_mask:0xf bank_mask:0xf\n\t"
        "v_or_b32_e32 v96, v96, v97\n\t"
        "v_cmp_ne_u32_e32 vcc, 0, v96\n\t"
        "s_cbranch_vccz 1f\n\t"
        "buffer_load_dwordx4 v[64+4*\\j:67+4*\\j], %[ib], %[srd], \\so offen sc1\n\t"
        "s_branch 2f\n\t"
        "1:\n\ts_bitset1_b32 %[fresh], \\j\n\t"
        "2:\n\t.endm\n\t"
        ".macro IRANK j\n\t"
        "s_bitcmp1_b32 %[fresh], \\j\n\ts_cbranch_scc0 2f\n\t"
        "v_cmp_lt_u32_e64 %[m0], v92, v[80+\\j]\n\tv_cmp_lt_u32_e64 %[m1], v93, v[80+\\j]\n\t"
        "v_cmp_lt_u32_e64 %[m2], v94, v[80+\\j]\n\tv_cmp_lt_u32_e64 %[m3], v95, v[80+\\j]\n\t"
        ".irp k, 0, 1, 2, 3\n\tv_lshrrev_b32_e32 v[84+\\k], 21, v[64+4*\\j+\\k]\n\t.endr\n\t"
        ".irp k, 0, 1, 2, 3\n\tv_and_b32_e32 v[84+\\k], 60, v[84+\\k]\n\t.endr\n\t"
        "v_cmp_lt_u32_e32 vcc, %c[cap], v[80+\\j]\n\t"
        "s_mov_b64 exec, %[m0]\n\tds_add_rtn_u32 v88, v84, %[four] offset:%c[cnt]\n\t"
        "s_mov_b64 exec, %[m1]\n\tds_add_rtn_u32 v89, v85, %[four] offset:%c[cnt]\n\t"
        "s_mov_b64 exec, %[m2]\n\tds_add_rtn_u32 v90, v86, %[four] offset:%c[cnt]\n\t"
        "s_mov_b64 exec, %[m3]\n\tds_add_rtn_u32 v91, v87, %[four] offset:%c[cnt]\n\t"
        "s_mov_b64 exec, %[pc0]\n\tds_write_b32 %[hdra], v[80+\\j] offset:128*\\j\n\t"
        "s_mov_b64 exec, -1\n\t"
        "s_cbranch_vccz 1f\n\t"
        "s_mov_b32 %[over], 1\n\t"
        "1:\n\ts_waitcnt lgkmcnt(1)\n\t"
        ".irp k, 0, 1, 2, 3\n\tv_min_u32_e32 v[88+\\k], %c[sub]-4, v[88+\\k]\n\t.endr\n\t"
        ".irp k, 0, 1, 2, 3\n\tv_mad_u32_u24 v[88+\\k], v[84+\\k], %[sub4], v[88+\\k]\n\t.endr\n\t"
        "s_mov_b64 exec, %[m0]\n\tds_write_b32 v88, v[64+4*\\j] offset:%c[list]\n\t"
        "s_mov_b64 exec, %[m1]\n\tds_write_b32 v89, v[65+4*\\j] offset:%c[list]\n\t"
        "s_mov_b64 exec, %[m2]\n\tds_write_b32 v90, v[66+4*\\j] offset:%c[list]\n\t"
        "s_mov_b64 exec, %[m3]\n\tds_write_b32 v91, v[67+4*\\j] offset:%c[list]\n\t"
        "s_mov_b64 exec, -1\n\t"
        "2:\n\t.endm\n\t"
        ".macro FADD j\n\t"
        ".irp k, 0, 1, 2, 3\n\tv_lshrrev_b32_e32 v[96+4*\\j+\\k], 21, v[64+4*\\j+\\k]\n\t.endr\n\t"
        ".irp k, 0, 1, 2, 3\n\tv_and_b32_e32 v[96+4*\\j+\\k], 60, v[96+4*\\j+\\k]\n\t.endr\n\t"
        "v_cmpx_lt_u32_e64 s[72+8*\\j:73+8*\\j], v94, v[80+\\j]\n\tds_add_rtn_u32 v[114+4*\\j], v[98+4*\\j], %[four] offset:%c[cnt]\n\t"
        "v_cmpx_lt_u32_e64 s[74+8*\\j:75+8*\\j], v95, v[80+\\j]\n\tds_add_rtn_u32 v[115+4*\\j], v[99+4*\\j], %[four] offset:%c[cnt]\n\t"
        "s_mov_b64 exec, -1\n\t"
        "v_cmpx_lt_u32_e64 s[68+8*\\j:69+8*\\j], v92, v[80+\\j]\n\tds_add_rtn_u32 v[112+4*\\j], v[96+4*\\j], %[four] offset:%c[cnt]\n\t"
        "v_cmpx_lt_u32_e64 s[70+8*\\j:71+8*\\j], v93, v[80+\\j]\n\tds_add_rtn_u32 v[113+4*\\j], v[97+4*\\j], %[four] offset:%c[cnt]\n\t"
        "s_mov_b64 exec, -1\n\t.endm\n\t"
        ".macro FPUT j\n\t"
        "s_waitcnt lgkmcnt(12)\n\t"
        ".irp k, 0, 1, 2, 3\n\tv_min_u32_e32 v[112+4*\\j+\\k], %c[sub]-4, v[112+4*\\j+\\k]\n\t.endr\n\t"
        ".irp k, 0, 1, 2, 3\n\tv_mad_u32_u24 v[112+4*\\j+\\k], v[96+4*\\j+\\k], %[sub4], v[112+4*\\j+\\k]\n\t.endr\n\t"
        ".irp k, 0, 1, 2, 3\n\ts_mov_b64 exec, s[68+8*\\j+2*\\k:69+8*\\j+2*\\k]\n\tds_write_b32 v[112+4*\\j+\\k], v[64+4*\\j+\\k] offset:%c[list]\n\t.endr\n\t"
        "s_mov_b64 exec, -1\n\t.endm\n\t"
        "s_movk_i32 %[o1], 0x1000\n\ts_movk_i32 %[o2], 0x2000\n\ts_movk_i32 %[o3], 0x3000\n\t"
        "v_mov_b32_e32 v92, %[i4]\n\tv_add_u32_e32 v93, 4, %[i4]\n\tv_add_u32_e32 v94, 8, %[i4]\n\tv_add_u32_e32 v95, 12, %[i4]\n\t"
        "s_mov_b32 %[todo], 15\n\ts_mov_b32 %[spin], 0x400000\n\ts_mov_b32 %[over], 0\n\t"
        ".if " STR(FASTIN) "\n\t"
        "s_waitcnt vmcnt(0)\n\t"
        ".irp j, 0, 1, 2, 3\n\tv_xor_b32_e32 v[64+4*\\j], %[key], v[64+4*\\j]\n\t.endr\n\t"
        ".irp j, 0, 1, 2, 3\n\tv_mov_b32_dpp v[80+\\j], v[64+4*\\j] quad_perm:[0,0,0,0] row_mask:0xf bank_mask:0xf\n\t.endr\n\t"
        ".irp j, 0, 1, 2, 3\n\tv_add_u32_e32 v[84+\\j], v[64+4*\\j], v[65+4*\\j]\n\t.endr\n\t"
        ".irp j, 0, 1, 2, 3\n\tv_mov_b32_dpp v[80+\\j], v[80+\\j] row_shr:4 row_mask:0xf bank_mask:0xa\n\t.endr\n\t"
        "s_mov_b64 exec, %[pc0]\n\t"
        ".irp j, 0, 1, 2, 3\n\tv_sub_u32_e32 v[84+\\j], 0, v[65+4*\\j]\n\t.endr\n\t"
        "s_mov_b64 exec, -1\n\t"
        "v_cmp_gt_i32_e64 %[m0], v80, v92\n\tv_cmp_gt_i32_e64 %[m1], v81, v92\n\tv_cmp_gt_i32_e64 %[m2], v82, v92\n\tv_cmp_gt_i32_e64 %[m3], v83, v92\n\t"
        ".irp j, 0, 1, 2, 3\n\tv_add3_u32 v[84+\\j], v[84+\\j], v[66+4*\\j], v[67+4*\\j]\n\t.endr\n\t"
        "v_cndmask_b32_e64 v84, 0, v84, %[m0]\n\tv_cndmask_b32_e64 v85, 0, v85, %[m1]\n\tv_cndmask_b32_e64 v86, 0, v86, %[m2]\n\tv_cndmask_b32_e64 v87, 0, v87, %[m3]\n\t"
        ".irp j, 0, 1, 2, 3\n\tv_add_u32_dpp v[84+\\j], v[84+\\j], v[84+\\j] quad_perm:[1,0,3,2] row_mask:0xf bank_mask:0xf\n\t.endr\n\t"
        ".irp j, 0, 1, 2, 3\n\tv_add_u32_dpp v[84+\\j], v[84+\\j], v[84+\\j] quad_perm:[2,3,0,1] row_mask:0xf bank_mask:0xf\n\t.endr\n\t"
        ".irp j, 0, 1, 2, 3\n\tv_add_u32_dpp v[84+\\j], v[84+\\j], v[84+\\j] row_half_mirror row_mask:0xf bank_mask:0xf\n\t.endr\n\t"
        "v_or3_b32 v96, v80, v81, v82\n\tv_max3_u32 v97, v80, v81, v82\n\tv_or_b32_e32 v96, v96, v83\n\tv_max_u32_e32 v97, v97, v83\n\t"
        "v_lshrrev_b32_e32 v96, 14, v96\n\tv_or3_b32 v96, v96, v84, v85\n\tv_or3_b32 v96, v96, v86, v87\n\t"
        "v_cmp_ne_u32_e32 vcc, 0, v96\n\t"
        "s_cbranch_vccnz 7f\n\t"
        "v_cmp_lt_u32_e32 vcc, %c[cap], v97\n\t"
        "FADD 0\n\tFADD 1\n\tFADD 2\n\tFADD 3\n\t"
        "s_cbranch_vccz 6f\n\ts_mov_b32 %[over], 1\n\t6:\n\t"
        "FPUT 0\n\tFPUT 1\n\tFPUT 2\n\tFPUT 3\n\t"
        "s_mov_b64 exec, %[pc0]\n\t"
        ".irp j, 0, 1, 2, 3\n\tds_write_b32 %[hdra], v[80+\\j] offset:128*\\j\n\t.endr\n\t"
        "s_mov_b64 exec, -1\n\t"
        "s_branch 9f\n\t"
        "7:\n\t.irp j, 0, 1, 2, 3\n\tv_xor_b32_e32 v[64+4*\\j], %[key], v[64+4*\\j]\n\t.endr\n\t"
        ".endif\n\t"
        "0:\n\t"
        "s_mov_b32 %[fresh], 0\n\t"
        "s_waitcnt vmcnt(0)\n\t"
        "IVAL 0, 0\n\tIVAL 1, %[o1]\n\tIVAL 2, %[o2]\n\tIVAL 3, %[o3]\n\t"
        "IRANK 0\n\tIRANK 1\n\tIRANK 2\n\tIRANK 3\n\t"
        "s_andn2_b32 %[todo], %[todo], %[fresh]\n\t"
        "s_cbranch_scc0 9f\n\t"
        "s_sub_u32 %[spin], %[spin], 1\n\t"
        "s_cbranch_scc0 0b\n\t"
        "s_waitcnt vmcnt(0)\n\t"
        "9:\n\t"
        ".purgem IVAL\n\t.purgem IRANK\n\t.purgem FADD\n\t.purgem FPUT"
        : "+{v[64:79]}"(raw), "=&{v[80:95]}"(t0), "=&{v[96:127]}"(t2), [m0] "=&s"(m0), [m1] "=&s"(m1), [m2] "=&s"(m2), [m3] "=&s"(m3),
          [todo] "=&s"(todo), [fresh] "=&s"(fresh), [spin] "=&s"(spin), [o1] "=&s"(o1), [o2] "=&s"(o2), [o3] "=&s"(o3), [over] "=&s"(over)
        : [srd] "s"(srd), [ib] "v"(ib), [key] "s"(key), [i4] "v"(i4), [hdra] "v"(hdra), [pc0] "s"(pc0), [sub4] "s"(SUBB / 4), [four] "v"(four),
          [cnt] "i"(L_CNT), [list] "i"(L_LIST), [sub] "i"(SUBB), [cap] "i"(4 * CAP1)
        : "vcc", "scc", "memory", "s68", "s69", "s70", "s71", "s72", "s73", "s74", "s75", "s76", "s77", "s78", "s79", "s80", "s81", "s82", "s83",
          "s84", "s85", "s86", "s87", "s88", "s89", "s90", "s91", "s92", "s93", "s94", "s95", "s96", "s97", "s98", "s99");
    return over;
}

template <int CTRL, int BANK> __device__ __forceinline__ unsigned dpp(unsigned old, unsigned v) { return __builtin_amdgcn_update_dpp(old, v, CTRL, 0xF, BANK, true); }
__device__ __forceinline__ unsigned bcast8(unsigned v) { v = dpp<0x00, 0xF>(0u, v); return __builtin_amdgcn_update_dpp(v, v, 0x114, 0xF, 0xA, false); }
__device__ __forceinline__ unsigned sum8(unsigned v) { v += dpp<0xB1, 0xF>(0u, v); v += dpp<0x4E, 0xF>(0u, v); return v + dpp<0x141, 0xF>(0u, v); }
__device__ __forceinline__ bf16x4 tr_read(LAS char* p) { return __builtin_amdgcn_ds_read_tr16_b64_v4bf16((LAS bf16x4*)p); }
__device__ __forceinline__ bf16x8 cat(bf16x4 a, bf16x4 b) { return __builtin_shufflevector(a, b, 0, 1, 2, 3, 4, 5, 6, 7); }
__device__ __forceinline__ unsigned pk2(float a, float b) { f32x2 v = {a, b}; bf16x2 r = __builtin_convertvector(v, bf16x2); return __builtin_bit_cast(unsigned, r); }
__device__ __forceinline__ f32x4 mfma(bf16x8 a, bf16x8 b, f32x4 c) { return __builtin_amdgcn_mfma_f32_16x16x32_bf16(a, b, c, 0, 0, 0); }
__device__ __forceinline__ unsigned lds_add(LAS char* p, unsigned v) { return __hip_atomic_fetch_add((LAS unsigned*)p, v, __ATOMIC_RELAXED, __HIP_MEMORY_SCOPE_WORKGROUP); }
__device__ __forceinline__ LAS unsigned* ldsp(unsigned a) { return (LAS unsigned*)(size_t)a; }
__device__ __forceinline__ unsigned byte0_x4(unsigned v) { unsigned r; asm("v_lshlrev_b32_sdwa %0, 2, %1 dst_sel:DWORD dst_unused:UNUSED_PAD src0_sel:DWORD src1_sel:BYTE_0" : "=v"(r) : "v"(v)); return r; }
__device__ __forceinline__ unsigned mad24(unsigned a, unsigned b, unsigned c) { unsigned r; asm("v_mad_u32_u24 %0, %1, %2, %3" : "=v"(r) : "v"(a), "s"(b), "v"(c)); return r; }
__device__ __forceinline__ void lds_arrive(LAS char* p, int lane) {
    asm volatile("s_waitcnt lgkmcnt(0)" ::: "memory");
    if (lane == 0) __hip_atomic_fetch_add((LAS unsigned*)p, 1u, __ATOMIC_RELAXED, __HIP_MEMORY_SCOPE_WORKGROUP);
}
__device__ __forceinline__ void lds_await(LAS char* p, unsigned n) {
    while (__builtin_amdgcn_readfirstlane(*(volatile LAS unsigned*)p) < n) __builtin_amdgcn_s_sleep(1);
    asm volatile("" ::: "memory");
}
__device__ __forceinline__ unsigned rfl(unsigned v) { return __builtin_amdgcn_readfirstlane(v); }

__global__ __launch_bounds__(512, 2) void k_graph(const unsigned short* x_, const int* src_, const int* dst_, const unsigned short* wq_, const unsigned short* wv_,
                                                  unsigned* ws_, unsigned short* out_, int N_, int E_) {
    u32x4 srd_ws, srd_x0, pw, po;
    u32x16 ed, es;
    u64 t_start;
    asm volatile("s_load_dwordx2 s[40:41], %[ka], 0x10\n\ts_load_dwordx2 s[44:45], %[ka], 0x8\n\ts_load_dwordx4 s[60:63], %[ka], 0x30\n\t"
                 "s_getpc_b64 s[68:69]\n\ts_movk_i32 s70, " STR(CODE_BYTES) "\n\ts_mov_b32 s71, 0x20000\n\ts_mov_b32 m0, %c[dump]\n\t"
                 "s_movk_i32 s66, 0x8000\n\tv_lshl_add_u32 v77, %[t], 7, s66\n\t"
                 "buffer_load_dword v77, s[68:71], 0 offen lds\n\t"
                 "s_load_dwordx2 s[48:49], %[ka], 0x28\n\ts_load_dwordx2 s[52:53], %[ka], 0x0\n\ts_load_dwordx4 s[56:59], %[ka], 0x18\n\t"
                 "s_memrealtime s[64:65]\n\t"
                 "v_readfirstlane_b32 s72, %[t]\n\t"
                 "s_and_b32 s66, %[b], 7\n\ts_lshl_b32 s66, s66, 4\n\ts_lshr_b32 s67, %[b], 4\n\ts_or_b32 s66, s66, s67\n\ts_mulk_i32 s66, %c[ch4]\n\t"
                 "s_mov_b32 s43, s71\n\ts_mov_b32 s47, s71\n\t"
                 "v_lshl_add_u32 v77, %[t], 4, s66\n\t"
                 "s_lshr_b32 s67, s72, 5\n\ts_or_b32 s67, s67, %[b]\n\t"
                 "s_movk_i32 s73, 0x1000\n\ts_movk_i32 s74, 0x2000\n\t"
                 "s_bitcmp1_b32 s67, 3\n\ts_waitcnt lgkmcnt(0)\n\ts_cbranch_scc1 1f\n\t"
                 "s_lshl_b32 s42, s63, 2\n\ts_lshl_b32 s46, s63, 2\n\t"
                 "buffer_load_dwordx4 v[64:67], v77, s[40:43], 0 offen\n\tbuffer_load_dwordx4 v[68:71], v77, s[40:43], s73 offen\n\t"
                 "buffer_load_dwordx4 v[72:75], v77, s[40:43], s74 offen\n\t"
                 "s_add_u32 s66, s66, 0x3000\n\ts_cmp_lt_u32 s72, 64\n\ts_cselect_b32 s66, s66, %c[oob]\n\t"
                 "v_lshl_add_u32 v78, %[t], 2, s66\n\t"
                 "buffer_load_dword v76, v78, s[40:43], 0 offen\n\t"
                 "buffer_load_dwordx4 v[80:83], v77, s[44:47], 0 offen\n\tbuffer_load_dwordx4 v[84:87], v77, s[44:47], s73 offen\n\t"
                 "buffer_load_dwordx4 v[88:91], v77, s[44:47], s74 offen\n\tbuffer_load_dword v92, v78, s[44:47], 0 offen\n\t"
                 "s_mov_b32 s51, s71\n\ts_mov_b32 s50, %c[wsb]\n\ts_lshl_b32 s66, %[b], 2\n\t"
                 "v_lshlrev_b32_e32 v77, 2, %[t]\n\tv_mov_b32_e32 v78, s66\n\t"
                 "buffer_load_dword v93, v77, s[48:51], 0 offen sc1\n\tbuffer_load_dword v94, v78, s[48:51], 0 offen sc1\n\t"
                 "1:\n\ts_mov_b32 s51, s71\n\ts_mov_b32 s50, %c[wsb]\n\ts_mov_b32 s55, s71\n\ts_lshl_b32 s54, s62, 8"
                 : "=&{v[64:79]}"(ed), "=&{v[80:95]}"(es)
                 : [ka] "s"(__builtin_amdgcn_kernarg_segment_ptr()), [t] "v"(threadIdx.x), [b] "s"(blockIdx.x),
                   [wsb] "i"(WS_BYTES), [dump] "i"(L_DUMP), [ch4] "i"(CH * 4), [oob] "i"(OOB)
                 : "memory", "scc", "s40", "s41", "s42", "s43", "s44", "s45", "s46", "s47", "s48", "s49", "s50", "s51", "s52", "s53", "s54", "s55",
                   "s56", "s57", "s58", "s59", "s60", "s61", "s62", "s63", "s64", "s65", "s66", "s67", "s68", "s69", "s70", "s71", "s72", "s73", "s74");
    asm volatile("" : "={s[48:51]}"(srd_ws), "={s[52:55]}"(srd_x0), "={s[56:59]}"(pw), "={s[60:63]}"(po), "={s[64:65]}"(t_start));
    FENCE();
    __shared__ __attribute__((aligned(16))) char smem_[L_TOTAL];
    LAS char* smem = (LAS char*)smem_;
    const int t = threadIdx.x, l = t & 63, b = blockIdx.x;
    const int w = rfl(t >> 6);
    const int g = l >> 4, n = l & 15, q = (l >> 2) & 3, p = l & 3, pc = t & 7;
    const unsigned lds0 = (unsigned)(size_t)smem;
    const bool isprod = ((b >> 3) & 1) == 0;
    const int pg = ((b & 7) << 4) | (b >> 4);
    const unsigned e0 = pg * CH + 4 * t, e1 = pg * CH + 3072 + t;

    const void* const x = (const void*)((u64)srd_x0.x | ((u64)srd_x0.y << 32));
    const void* const wq = (const void*)((u64)pw.x | ((u64)pw.y << 32));
    const void* const wv = (const void*)((u64)pw.z | ((u64)pw.w << 32));
    void* const out = (void*)((u64)po.x | ((u64)po.y << 32));
    const int N = po.z, E = po.w;
    const unsigned xbytes = srd_x0.z;
    STAMP_DECL;
    const unsigned ring = lds0 + L_RING + w * 16384;
    const int jn = 8 * n + w;

    unsigned mykey = 0;
    auto node_row = [&](int tile, int m) { return (unsigned)(b + 256 * (8 * (4 * m + g) + tile)) << 8; };
    auto zero_staging = [&]() {
        const u32x4 z = {0u, 0u, 0u, 0u};
#pragma unroll
        for (int k = 0; k < 5; ++k) *(LAS u32x4*)(smem + L_IMG + k * 8192 + t * 16) = z;
    };

    u32x16 addr;
    f32x32 acc, wva, wvb;
    unsigned cv, lista, cnta, qa, keyn, inf, ow, go;
    Srd4 srd_x;
    auto loop_constants = [&]() {
        srd_x = make_srd4(x, xbytes, BIAS * 256);
        lista = lds0 + L_LIST + w * (2 * SUBB) + g * 4; cnta = lds0 + L_CNT + (l & 31) * 4; qa = ring + l * 16; keyn = jn; inf = 0x7F800000u;
        ow = ring + n * 256 + ((g ^ n) << 4) + ((g & 1) << 3); go = (b << 8) + (w << 16) + (g << 19) + ((n ^ g) << 4);
        asm volatile("" : "+s"(srd_x.s0), "+s"(srd_x.s1), "+s"(srd_x.s2), "+s"(srd_x.s3));
        asm volatile("" : "+v"(lista), "+v"(cnta), "+v"(qa), "+v"(keyn), "+v"(inf), "+v"(ow), "+v"(go));
        const unsigned o0 = (n ^ (((g & 1) << 3) | (g >> 1))) << 4;
        const unsigned ra = (4 * (n & 3) + (n >> 2)) * 256 + (((4 * g) ^ (((n & 7) << 1) | (n >> 3))) << 4);
        const int i = 4 * g + q, r8 = i & 7;
        const unsigned ta = (4 * q + g) * 256 + 8 * (p & 1) + (((p >> 1) ^ (((i & 7) << 1) | (i >> 3))) << 4);
        cv = lds0 + L_WV1 + i * 256 + (((p ^ ((r8 & 1) | ((r8 & 6) << 1))) << 4) | ((p & 1) << 3));
#pragma unroll
        for (int s = 0; s < 4; ++s) { addr[s] = ring + (ra ^ (s << 4)); addr[12 + s] = o0 ^ (s << 5); }
#pragma unroll
        for (int c = 0; c < 8; ++c) addr[4 + c] = ring + (ta ^ (c << 5));
        asm volatile("" : "+{v[176:191]}"(addr), "+v"(cv));
    };
    auto clear_sums = [&]() { acc = 0.f; asm volatile("" : "+{v[128:159]}"(acc)); };
    auto wv_resident = [&]() {
#pragma unroll
        for (int a = 0; a < 8; ++a) {
            LAS char* pa = (LAS char*)(size_t)((cv - (L_WV1 - L_WV0)) ^ ((a >> 1) << 6) ^ ((a & 1) << 3));
            const f32x4 f0 = __builtin_bit_cast(f32x4, cat(tr_read(pa), tr_read(pa + 4096)));
            const f32x4 f1 = __builtin_bit_cast(f32x4, cat(tr_read(pa + 16384), tr_read(pa + 16384 + 4096)));
#pragma unroll
            for (int k = 0; k < 4; ++k) { wva[4 * a + k] = f0[k]; wvb[4 * a + k] = f1[k]; }
        }
        asm volatile("" : "+{v[224:255]}"(wva), "+{v[32:63]}"(wvb));
    };

    if (isprod) zero_staging();
    if (t < 8) *(LAS u32x4*)(smem + L_CNT + t * 16) = u32x4{0u, 0u, 0u, 0u};
    WG_BARRIER();

    if (__builtin_expect(w < 4, 1)) {
        if (__builtin_expect(isprod, 1)) {
            if (PRIO) __builtin_amdgcn_s_setprio(2);
            unsigned ob = WS_INBOX + ((t >> 3) * G1 + pg) * 128 + pc * 16, ima = lds0 + L_IMG + (t >> 3) * 144 + pc * 16;
            PIN(ob); PIN(ima);
            unsigned cna = lds0 + L_CNT1 + (t >> 3) * 4;
            PIN(cna);
            const bool v0 = e0 < (unsigned)E, v1 = e0 + 1024 < (unsigned)E, v2 = e0 + 2048 < (unsigned)E, v3 = w == 0 && e1 < (unsigned)E;
            STAMP(1);
            u32x16 rk;
            const u64 over = rank_edges(ed, es, rk, __builtin_amdgcn_ballot_w64(v0), __builtin_amdgcn_ballot_w64(v1), __builtin_amdgcn_ballot_w64(v2),
                                        __builtin_amdgcn_ballot_w64(v3), t * 4);
            mykey = es[14];
            if (__builtin_expect(over != 0, 0)) {
#pragma unroll
                for (int k = 0; k < 13; ++k)
                    if (rk[k] >= 4u * CAP1) {
                        const unsigned o = WS_OVF + (pg * CH + lds_add(smem + L_CNT1 + 2048, 1)) * 4;
                        const unsigned v = ((((es[k] >> 8) & 0xFFFF) - BIAS) << 16) | ed[k];
                        ST1(v, srd_ws, o, " sc1");
                    }
            }
            if (XSYNC) { lds_arrive(smem + L_CNT + 96, l); lds_await(smem + L_CNT + 96, 4); } else WG_BARRIER();
            STAMP(3);
            f32x32 img; u32x16 meta, sums;
            const unsigned no = read_image(img, meta, sums, ima, cna);
            lds_arrive(smem + L_CNT + 84, l);
            if (__builtin_expect(no != 0, 0)) {
                const unsigned o = t == 0 ? WS_NOVF + pg * 4 : OOB;
                ST1(no, srd_ws, o, " sc1");
                VMWAIT(0);
                if (XSYNC) { lds_arrive(smem + L_CNT + 100, l); lds_await(smem + L_CNT + 100, 4); } else WG_BARRIER();
            }
            publish_image(img, meta, sums, srd_ws, ob);
            if (XAFTER) lds_arrive(smem + L_CNT + 104, l);
            STAMP(4);
        } else {
            { const unsigned om = WS_KEY + b * 4; asm volatile("buffer_load_dword %0, %1, %2, 0 offen sc1\n\ts_waitcnt vmcnt(0)" : "=&v"(mykey) : "v"(om), "s"(srd_ws) : "memory"); }
            STAMP(1);
            if (PARTNER) {
                set_m0(lds0 + L_DUMP + w * 256);
                const unsigned o = t < 196 ? ((b - 8) << 8) + ((t >> 1) << 16) + ((t & 1) << 7) : OOB;
                while (__builtin_amdgcn_s_memrealtime() < t_start + X_T) __builtin_amdgcn_s_sleep(1);
                asm volatile("buffer_load_dword %1, %0, 0 offen lds" :: "s"(srd_x0), "v"(o) : "memory");
            }
            if (PRIO) __builtin_amdgcn_s_setprio(2);
            STAMP(2); STAMP(3); STAMP(4);
        }
        STAMP(5);
        mykey = rfl(mykey) + KEY_STEP;
        loop_constants();
        clear_sums();
        unsigned ib = WS_INBOX + b * (G1 * 128) + t * 16;
        PIN(ib);
        u64 t_go = t_start + POLL_T;
        if (POLL_D) { lds_await(smem + L_CNT + 80, 4); t_go = max(t_go, __builtin_amdgcn_s_memrealtime() + POLL_D); }
        while (__builtin_amdgcn_s_memrealtime() < t_go) __builtin_amdgcn_s_sleep(1);
        STAMP(6);
        u32x16 raw;
        inbox_request(raw, srd_ws, ib);
        if (__builtin_expect(inbox_stage(raw, srd_ws, ib, mykey, 16 * pc - 8, lds0 + L_HDR + (t >> 3) * 4) != 0, 0)) {
            LAS unsigned* hp = (LAS unsigned*)(smem + L_HDR) + 32 * (l >> 3 & 3) + 8 * w + (l & 7);
            const unsigned hv = *hp;
            for (u64 m = __builtin_amdgcn_ballot_w64(l < 32 && hv > 4u * CAP1); m; m &= m - 1) {
                const int k = __builtin_ctzll(m), gp = 32 * (k >> 3) + 8 * w + (k & 7);
                unsigned u, no; const unsigned o = WS_OVF + (gp * CH + l) * 4, on = WS_NOVF + gp * 4;
                asm volatile("buffer_load_dword %0, %2, %4, 0 offen sc1\n\tbuffer_load_dword %1, %3, %4, 0 offen sc1\n\ts_waitcnt vmcnt(0)"
                             : "=&v"(u), "=&v"(no) : "v"(o), "v"(on), "s"(srd_ws) : "memory");
                if (rfl(no) <= 64) {
                    if (l < no && (u & 0xFF) == (unsigned)b) {
                        const unsigned e = __builtin_amdgcn_perm((u >> 16) + BIAS, u, 0x01050401u), key = (e >> 21) & 60;
                        *(LAS unsigned*)(smem + L_LIST + key * (SUBB / 4) + min(lds_add(smem + L_CNT + key, 4), SUBB - 4u)) = e;
                    }
                    if (l == k) *hp = hv | 0x80000000u;
                } else *(LAS unsigned*)(smem + L_CNT + 64) = 1;
            }
        }
        if (PRIO) __builtin_amdgcn_s_setprio(0);
        STAMP(7);
    } else {
        const int j = w - 4;
        auto prefetch = [&](int k0, int kn) {
            const unsigned ln = (b >> 3) * (kn / 4) + (t - 256), o = t - 256 < kn / 4 ? k0 * 1024 + ln * 128 : OOB;
            set_m0(lds0 + L_DUMP + w * 256);
            asm volatile("s_nop 0\n\tbuffer_load_dword %1, %0, 0 offen lds" :: "s"(srd_x0), "v"(o) : "memory");
        };
        STAMP(1);
        u32x4 wr[8], xr[8];
        {
            const u32x4 srd_q = make_srd(wq, 32768);
            const unsigned o = j * 8192 + l * 16, k4 = 4096;
            LD4(wr[0], srd_q, o, ""); LD4(wr[1], srd_q, o, " offset:1024"); LD4(wr[2], srd_q, o, " offset:2048"); LD4(wr[3], srd_q, o, " offset:3072");
            LD4S(wr[4], srd_q, o, k4, ""); LD4S(wr[5], srd_q, o, k4, " offset:1024"); LD4S(wr[6], srd_q, o, k4, " offset:2048"); LD4S(wr[7], srd_q, o, k4, " offset:3072");
        }
        unsigned ox[8];
#pragma unroll
        for (int m = 0; m < 8; ++m) { ox[m] = node_row(m < 4 ? j : w, m & 3) + n * 16; PIN(ox[m]); }
        unsigned aqa[8], xfa[4];
        unsigned xa = ring + g * 256 + ((n ^ g) << 4), wa = lds0 + L_WQ + j * 16384 + g * 256 + ((n ^ g) << 4) + ((n & 4) << 1);
        {
            const unsigned aq = lds0 + L_WQ + (8 * g + q) * 256 + ((((4 * p) ^ (q | ((g & 1) << 2))) << 4) | ((p & 1) << 3));
#pragma unroll
            for (int s = 0; s < 4; ++s) { xfa[s] = ring + n * 256 + (((4 * s + g) ^ n) << 4); PIN(xfa[s]); }
#pragma unroll
            for (int c = 0; c < 8; ++c) { aqa[c] = aq ^ ((c >> 1) << 4) ^ ((c & 1) << 3); PIN(aqa[c]); }
            PIN(xa); PIN(wa);
        }
        loop_constants();
        STAMP(2);
        if (isprod && !XSYNC) {
            WG_BARRIER();
            if (__builtin_expect(rfl(*(LAS unsigned*)(smem + L_CNT1 + 2048)) != 0, 0)) WG_BARRIER();
            if (W_SLEEP) __builtin_amdgcn_s_sleep(W_SLEEP);
        }
        STAMP(3);
        auto row_loads = [&]() {
#pragma unroll
            for (int m = 0; m < 8; ++m) LD4(xr[m], srd_x0, ox[m], "");
        };
        asm volatile("s_waitcnt vmcnt(0)" : "+v"(wr[0]), "+v"(wr[1]), "+v"(wr[2]), "+v"(wr[3]), "+v"(wr[4]), "+v"(wr[5]), "+v"(wr[6]), "+v"(wr[7]) :: "memory");
        if (isprod && XAFTER) lds_await(smem + L_CNT + 104, 4);
        else while (__builtin_amdgcn_s_memrealtime() < t_start + X_T) __builtin_amdgcn_s_sleep(1);
        row_loads();
#pragma unroll
        for (int m = 0; m < 8; ++m) {
            const unsigned a = (wa ^ ((m & 2) << 5)) + m * 1024;
            *(LAS u32x2*)(size_t)a = u32x2{wr[m].x, wr[m].y};
            *(LAS u32x2*)(size_t)(a ^ 8) = u32x2{wr[m].z, wr[m].w};
        }
        asm volatile("s_waitcnt vmcnt(0)" : "+v"(xr[0]), "+v"(xr[1]), "+v"(xr[2]), "+v"(xr[3]), "+v"(xr[4]), "+v"(xr[5]), "+v"(xr[6]), "+v"(xr[7]) :: "memory");
#pragma unroll
        for (int m = 0; m < 8; ++m) *(LAS u32x4*)(size_t)((xa ^ ((m & 3) << 6)) + m * 1024) = xr[m];
        STAMP(4);
        lds_arrive(smem + L_CNT + 80, l);
        lds_await(smem + L_CNT + 80, 4);
        STAMP(5);
        bf16x8 xo[2][4], fr[2][8];
        f32x4 qc[2][8];
        auto wq_frags = [&](int u, bf16x8 (&f)[8]) {
#pragma unroll
            for (int a = 0; a < 8; ++a) {
                LAS char* pa = (LAS char*)(size_t)aqa[a] + u * 16384;
                f[a] = cat(tr_read(pa), tr_read(pa + 1024));
            }
        };
#pragma unroll
        for (int u = 0; u < 4; ++u) { xo[0][u] = *(LAS bf16x8*)(size_t)xfa[u]; xo[1][u] = *(LAS bf16x8*)(size_t)(xfa[u] + 4096); }
        wq_frags(0, fr[0]);
        {
            const Srd4 srd_v = make_srd4(wv, 32768);
            const unsigned r0 = (j * 32 + g) << 8, o0 = (n ^ ((g & 1) | ((g & 2) << 1))) << 4;
            const unsigned e[8] = {r0, r0 + 1024, r0 + 2048, r0 + 3072, r0 + 4096, r0 + 5120, r0 + 6144, r0 + 7168};
            const unsigned base = lds0 + (j < 2 ? L_WV0 + j * 16384 : L_WV1 + (j - 2) * 8192);
            const u32x4 off = {o0, o0 ^ 128, o0, o0 ^ 128};
            dma_half(base, srd_v, e, off); dma_half(base + 4096, srd_v, e + 4, off);
        }
        if (PFA_KB) prefetch(0, PFA_KB);
#pragma unroll
        for (int u = 0; u < 4; ++u) {
            if (u < 3) wq_frags(u + 1, fr[(u + 1) & 1]);
            FENCE();
#pragma unroll
            for (int a = 0; a < 8; ++a)
#pragma unroll
                for (int tt = 0; tt < 2; ++tt) qc[tt][a] = mfma(fr[u & 1][a], xo[tt][u], u ? qc[tt][a] : f32x4{0.f, 0.f, 0.f, 0.f});
            FENCE();
            if (!WVLATE && u == 1 && j < 2) { VMWAIT(0); lds_arrive(smem + L_CNT + 88, l); }
        }
        STAMP(6);
        if (PFB_KB) { asm volatile("s_waitcnt vmcnt(%c0)" :: "i"(PFA_KB ? 1 : 0) : "memory"); prefetch(PFA_KB, PFB_KB); }
        if (isprod) lds_await(smem + L_CNT + 84, 4);
#pragma unroll
        for (int tt = 0; tt < 2; ++tt)
#pragma unroll
            for (int s = 0; s < 4; ++s) {
                const float k = -0.08838834764831845f * 1.4426950408889634f;
                const f32x4 c0 = qc[tt][2 * s] * k, c1 = qc[tt][2 * s + 1] * k;
                *(LAS u32x4*)(size_t)(ring - (tt ? 0 : 65536) + s * 1024 + l * 16) = u32x4{pk2(c0[0], c0[1]), pk2(c0[2], c0[3]), pk2(c1[0], c1[1]), pk2(c1[2], c1[3])};
            }
        clear_sums();
        if (!WVLATE) { lds_await(smem + L_CNT + 88, 2); wv_resident(); }
        if (!PFB_KB) asm volatile("s_waitcnt vmcnt(%c0)" :: "i"(PFA_KB ? 1 : 0) : "memory");
        if (PFC_KB) prefetch(PFA_KB + PFB_KB, PFC_KB);
        STAMP(7);
    }
    WG_BARRIER();
    STAMP(8);
    if (LPRIO >= 3) __builtin_amdgcn_s_setprio(3);
    if (LPRIO == 1 && w >= 4) __builtin_amdgcn_s_setprio(1);
    if (LPRIO == 2) { if (w >= 6) __builtin_amdgcn_s_setprio(3); else if (w >= 4) __builtin_amdgcn_s_setprio(2); else if (w >= 2) __builtin_amdgcn_s_setprio(1); }

    u32x16 qf;
    const u32x4 doff = {addr[12], addr[13], addr[14], addr[15]};
    LAS char* mylist = smem + L_LIST + w * (2 * SUBB);
    int gcur = G1, pos = 0;
    unsigned hmask = 0xFFFFFFFFu;
    auto batch = [&](auto first_, const int l) {
        constexpr bool first = decltype(first_)::value;
        auto entries = [&](int grp, unsigned (&e)[8]) {
#pragma unroll
            for (int m = 0; m < 8; ++m) e[m] = *(LAS unsigned*)(size_t)(lista + grp * 128 + m * 16);
        };
        unsigned cw = *(LAS unsigned*)(size_t)(first ? cnta : lds0 + L_CNT + (l & 31) * 4), e0[8], e1[8];
        entries(0, e0);
        if constexpr (first) {
#pragma unroll
            for (int s = 0; s < 4; ++s) {
                const u32x4 v = *(LAS u32x4*)(size_t)(qa + s * 1024);
                qf[4 * s] = v.x; qf[4 * s + 1] = v.y; qf[4 * s + 2] = v.z; qf[4 * s + 3] = v.w;
            }
        }
        entries(1, e1);
        FENCE();
        int lo = __builtin_amdgcn_readlane(cw, 2 * w) >> 2, hi = __builtin_amdgcn_readlane(cw, 2 * w + 1) >> 2;
        if constexpr (first) {
            if (__builtin_amdgcn_readlane(cw, 16)) gcur = 0;
            if (__builtin_amdgcn_ballot_w64(cw > 4u * CAP2)) { gcur = -1; lo = 0; hi = 0; hmask = 0x7FFFFFFFu; }
        }
        const int c = lo + hi, G = (c + 31) >> 5;
        const int nlast = G > 2 ? (((c - 1) & 31) >> 2) + 1 : 8;
        auto join = [&]() {
            for (int k = 0; k < G * 32 - lo; k += 64) {
                const unsigned e = *(LAS unsigned*)(mylist + SUBB + (l + k) * 4);
                if (l + k < G * 32 - lo) *(LAS unsigned*)(mylist + (lo + l + k) * 4) = l + k < hi ? e : PAD;
            }
        };
        const bool early = lo >= 64;
        if (__builtin_expect(!early, 0)) { join(); entries(0, e0); entries(1, e1); }
        if constexpr (first) asm volatile("s_waitcnt lgkmcnt(0)" : "+{v[160:175]}"(qf) :: "memory");
        if (G > 0) dma_group(ring, srd_x, e0, doff);
        if constexpr (first) {
            if (WVLATE == 1) {
                if (G > 1 && w < 6) dma_group(ring + 8192, srd_x, e1, doff);
                wv_resident();
                lds_arrive(smem + L_CNT + 92, l);
                if (w >= 6) { lds_await(smem + L_CNT + 92, 8); if (G > 1) dma_group(ring + 8192, srd_x, e1, doff); }
            } else if (WVLATE == 2) {
                wv_resident();
                lds_arrive(smem + L_CNT + 92, l);
                if (w >= 6) lds_await(smem + L_CNT + 92, 8);
                if (G > 1) dma_group(ring + 8192, srd_x, e1, doff);
            } else {
                if (w < 4) { wv_resident(); lds_arrive(smem + L_CNT + 92, l); }
                else if (w >= 6) lds_await(smem + L_CNT + 92, 4);
                if (G > 1) dma_group(ring + 8192, srd_x, e1, doff);
            }
        } else if (G > 1) dma_group(ring + 8192, srd_x, e1, doff);
        f32x16 cc;
#pragma unroll
        for (int m = 0; m < 8; ++m) {
            cc[m] = (e0[m] & 0xFF) == keyn ? 0.f : __uint_as_float(inf);
            cc[8 + m] = (e1[m] & 0xFF) == keyn ? 0.f : __uint_as_float(inf);
        }
        if (early) join();
        STAMP(9);
        if (G > 0) gather_loop(acc, qf, addr, u32x4{lista, keyn, inf, 0u}, cc, srd_x, ring, G, nlast);
    };
    batch(std::true_type{}, l);
    if (__builtin_expect(gcur < G1, 0)) do {
        int t = threadIdx.x;
        PIN(t);
        const int l = t & 63;
        unsigned lim = G1 * 32;
        if (gcur >= 0) {
            const u64 m0 = __builtin_amdgcn_ballot_w64(l >= gcur && (int)(*(LAS unsigned*)(smem + L_HDR + l * 4) & hmask) > 4 * CAP1);
            const u64 m1 = __builtin_amdgcn_ballot_w64(l + 64 >= gcur && (int)(*(LAS unsigned*)(smem + L_HDR + 256 + l * 4) & hmask) > 4 * CAP1);
            if (!(m0 | m1)) break;
            gcur = m0 ? __builtin_ctzll(m0) : 64 + __builtin_ctzll(m1);
            unsigned v; const unsigned o = WS_NOVF + gcur * 4;
            asm volatile("buffer_load_dword %0, %1, %2, 0 offen sc1\n\ts_waitcnt vmcnt(0)" : "=&v"(v) : "v"(o), "s"(srd_ws) : "memory");
            lim = rfl(v);
        }
        const unsigned i = pos + t;
        const bool cand = t < CAP2 && i < lim;
        unsigned u;
        { const unsigned o = cand ? (gcur < 0 ? WS_INBOX + b * (G1 * 128) : WS_OVF + gcur * CH * 4) + i * 4 : OOB;
          asm volatile("buffer_load_dword %0, %1, %2, 0 offen sc1\n\ts_waitcnt vmcnt(0)" : "=&v"(u) : "v"(o), "s"(srd_ws) : "memory"); }
        WG_BARRIER();
        if (t < 16) *(LAS unsigned*)(smem + L_CNT + t * 4) = 0;
        WG_BARRIER();
        if (cand) {
            unsigned e; bool v;
            if (gcur < 0) { e = (i & 3) ? u : u ^ mykey; v = ((i & 31) - 2) * 4 < *(LAS unsigned*)(smem + L_HDR + (i >> 5) * 4); }
            else { e = (((u >> 16) + BIAS) << 8) | ((u >> 8) & 0xFF); v = (u & 0xFF) == (unsigned)b; }
            if (v) *(LAS unsigned*)(smem + L_LIST + (e & 7) * (2 * SUBB) + lds_add(smem + L_CNT + (e & 7) * 8, 4)) = e;
        }
        pos += CAP2;
        if ((unsigned)pos >= lim) { ++gcur; pos = 0; }
        WG_BARRIER();
        batch(std::false_type{}, l);
    } while (gcur < G1);
    STAMP(10);

    output_tile(acc, wva, wvb, cv, ow, qa, go, make_srd(out, xbytes));
    if (w == 0) { const unsigned o = t == 0 ? WS_KEY + b * 4 : OOB; ST1(mykey, srd_ws, o, " sc1"); }
    STAMP(11);
}

extern "C" void kernel_launch(void* const* d_in, const int* in_sizes, int n_in,
                              void* d_out, int out_size, void* d_ws, size_t ws_size,
                              hipStream_t stream) {
    const int N = in_sizes[0] / 128, E = in_sizes[1];
    k_graph<<<NB, 512, 0, stream>>>((const unsigned short*)d_in[0], (const int*)d_in[1], (const int*)d_in[2],
                                    (const unsigned short*)d_in[3], (const unsigned short*)d_in[4],
                                    (unsigned*)d_ws, (unsigned short*)d_out, N, E);
}
```

```cpp
#include <hip/hip_runtime.h>
#include <hip/hip_bf16.h>
#include <stdint.h>
#include <type_traits>
#define ORD 1
#define SKIP 0
#define STR2(x) #x
#define STR(x) STR2(x)

typedef __attribute__((ext_vector_type(8))) __bf16 bf16x8;
typedef __attribute__((ext_vector_type(4))) __bf16 bf16x4;
typedef __attribute__((ext_vector_type(2))) __bf16 bf16x2;
typedef __attribute__((ext_vector_type(4))) float f32x4;
typedef __attribute__((ext_vector_type(2))) float f32x2;
typedef __attribute__((ext_vector_type(4))) unsigned u32x4;
typedef __attribute__((ext_vector_type(2))) unsigned u32x2;
typedef __attribute__((ext_vector_type(32))) float f32x32;
typedef __attribute__((ext_vector_type(16))) float f32x16;
typedef __attribute__((ext_vector_type(16))) unsigned u32x16;
typedef __attribute__((ext_vector_type(12))) unsigned u32x12;
typedef __attribute__((ext_vector_type(8))) unsigned u32x8;
typedef unsigned long long u64;
#define LAS __attribute__((address_space(3)))

constexpr int NB = 256;
constexpr int G1 = 128;
constexpr int CH = 3136;
constexpr int CAP1 = 30;
constexpr int CAP2 = 192;
constexpr int SUBB = 784;
#define POLL_AT 3
#define POLL_T 290
#define LPRIO 3
#define PKADD 1
#define CODE_BYTES 23552
#define PARTNER 1
#define XSYNC 1
#define WVLATE 1
#define PFA_KB 0
#define PFB_KB 0
#define PFC_KB 1024
#define X_T 0
#define XAFTER 0
#define POLL_D 60
#define FASTIN 1
#define W_SLEEP 3
#define PRIO 1
#define SPLIT 16384
constexpr unsigned KEY_STEP = 0x9E3779B1u;
constexpr unsigned WS_KEY = 0;
constexpr unsigned WS_NOVF = 2048;
constexpr unsigned WS_INBOX = 4096;
constexpr unsigned WS_OVF = WS_INBOX + NB * G1 * 128;
constexpr unsigned WS_BYTES = WS_OVF + G1 * CH * 4;
constexpr unsigned OOB = 0x80000000u;
constexpr unsigned BIAS = 32768 - SPLIT;
constexpr unsigned PAD = 0x00FFFFFFu;

constexpr int L_CNT = 0;
constexpr int L_HDR = 128;
constexpr int L_LIST = 768;
constexpr int L_RING = L_LIST + 16 * SUBB;
constexpr int L_IMG = L_RING;
constexpr int L_CNT1 = L_IMG + NB * 144;
constexpr int L_WQ = L_RING + 2 * 16384 + 8192;
constexpr int L_WV0 = L_RING + 6 * 16384 + 8192;
constexpr int L_WV1 = L_RING + 131072;
constexpr int L_DUMP = L_WV1 + 16384;
constexpr int L_TOTAL = L_DUMP + 2048;
static_assert(WS_KEY == 0 && L_WV1 % 256 == 0 && CH == 3072 + 64 && L_RING % 256 == 0 && L_TOTAL <= 163840, "LDS map");

#define STAMP_DECL
#define STAMP(k)
#define STAMP_FLUSH(base, k0, k1)

__device__ __forceinline__ u32x4 make_srd(const void* p, unsigned bytes, unsigned back = 0) {
    const u64 a = (u64)p - back;
    u32x4 s; s.x = (unsigned)a; s.y = (unsigned)(a >> 32); s.z = bytes + back; s.w = 0x00020000u; return s;
}
#define LD4(dst, srd, off, FL) asm volatile("buffer_load_dwordx4 %0, %1, %2, 0 offen" FL : "=v"(dst) : "v"(off), "s"(srd) : "memory")
#define LD4S(dst, srd, off, so, FL) asm volatile("buffer_load_dwordx4 %0, %1, %2, %3 offen" FL : "=v"(dst) : "v"(off), "s"(srd), "s"(so) : "memory")
#define LD1(dst, srd, off, FL) asm volatile("buffer_load_dword %0, %1, %2, 0 offen" FL : "=v"(dst) : "v"(off), "s"(srd) : "memory")
#define ST1(val, srd, off, FL) asm volatile("buffer_store_dword %0, %1, %2, 0 offen" FL :: "v"(val), "v"(off), "s"(srd) : "memory")
#define PIN(x) asm volatile("" : "+v"(x))
#define VMWAIT(n) asm volatile("s_waitcnt vmcnt(" #n ")" ::: "memory")
#define FENCE() __builtin_amdgcn_sched_barrier(0)
#define WG_BARRIER() do { asm volatile("s_waitcnt lgkmcnt(0)" ::: "memory"); __builtin_amdgcn_s_barrier(); asm volatile("" ::: "memory"); } while (0)

struct Srd4 { u32x4 s0, s1, s2, s3; };
__device__ __forceinline__ Srd4 make_srd4(const void* p, unsigned bytes, unsigned back = 0) {
    return Srd4{make_srd(p, bytes, back), make_srd(p, bytes, back + 1024), make_srd(p, bytes, back + 2048), make_srd(p, bytes, back + 3072)};
}

__device__ __forceinline__ void set_m0(unsigned v) { asm volatile("s_mov_b32 m0, %0" :: "s"(v) : "memory"); }
template <int R> __device__ __forceinline__ void dma_rows(const Srd4& d, unsigned e, unsigned off) {
    unsigned tmp, mask = 0x00FFFF00u;
    asm volatile("v_and_or_b32 %0, %1, %2, %3\n\tbuffer_load_dwordx4 %0, %4, 0 offen offset:%c5 lds"
                 : "=&v"(tmp) : "v"(e), "s"(mask), "v"(off), "s"(R == 0 ? d.s0 : R == 1 ? d.s1 : R == 2 ? d.s2 : d.s3), "i"(R * 1024) : "memory");
}
__device__ __forceinline__ void dma_half(unsigned ldsbase, const Srd4& d, const unsigned* e, const u32x4& off) {
    set_m0(ldsbase);
    dma_rows<0>(d, e[0], off.x); dma_rows<1>(d, e[1], off.y); dma_rows<2>(d, e[2], off.z); dma_rows<3>(d, e[3], off.w);
}
__device__ __forceinline__ void dma_group(unsigned ldsbase, const Srd4& d, const unsigned (&e)[8], const u32x4& off) {
    asm volatile("s_waitcnt lgkmcnt(0)" ::: "memory");
    dma_half(ldsbase, d, e, off); dma_half(ldsbase + 4096, d, e + 4, off);
}

__device__ __forceinline__ void gather_loop(f32x32& acc, const u32x16& qf, const u32x16& addr, u32x4 misc, f32x16 cc,
                                            const Srd4& d, unsigned ring, int G, int nlast) {
    f32x32 t0, t1; u32x12 t2; int tmp, pr; u64 sm; unsigned mask = 0x00FFFF00u;
    asm volatile(
        ".macro GWAIT k\n\ts_cmp_gt_i32 %[t], \\k\n\ts_cbranch_scc1 1f\n\ts_waitcnt vmcnt(\\k)\n\t.endm\n\t"
        ".macro GROWS so\n\t.irp s, 0, 1, 2, 3\n\t"
        "ds_read_b128 v[64+8*\\s:67+8*\\s], v[176+\\s] offset:\\so\n\t"
        "ds_read_b128 v[68+8*\\s:71+8*\\s], v[176+\\s] offset:\\so+4096\n\t.endr\n\t.endm\n\t"
        ".macro GTR c, so\n\t"
        "ds_read_b64_tr_b16 v[96+4*\\c:97+4*\\c], v[180+\\c] offset:\\so\n\t"
        "ds_read_b64_tr_b16 v[98+4*\\c:99+4*\\c], v[180+\\c] offset:\\so+4096\n\t.endm\n\t"
        ".macro GS s, c\n\t"
        "v_mfma_f32_16x16x32_bf16 v[\\c:\\c+3], v[64+8*\\s:67+8*\\s], v[160+4*\\s:163+4*\\s], v[\\c:\\c+3]\n\t"
        "v_mfma_f32_16x16x32_bf16 v[\\c+4:\\c+7], v[68+8*\\s:71+8*\\s], v[160+4*\\s:163+4*\\s], v[\\c+4:\\c+7]\n\t.endm\n\t"
        ".macro GLOAD m, r, srd\n\t"
        "v_and_or_b32 v195, v[212+\\m], %[mask], v[188+\\r]\n\t"
        "buffer_load_dwordx4 v195, \\srd, 0 offen offset:1024*\\r lds\n\t.endm\n\t"
        ".macro GPART m, r, srd\n\ts_cmp_le_i32 %[nl], \\m\n\ts_cbranch_scc1 4f\n\tGLOAD \\m, \\r, \\srd\n\t.endm\n\t"
        ".macro GSUM m\n\tv_mfma_f32_16x16x32_bf16 v[128+4*(\\m):131+4*(\\m)], v[96+4*(\\m):99+4*(\\m)], v[220:223], v[128+4*(\\m):131+4*(\\m)]\n\t.endm\n\t"
        ".macro GCMP m, d\n\tv_cmp_ne_u32_sdwa \\d, v[212+\\m], v193 src0_sel:BYTE_0 src1_sel:DWORD\n\t.endm\n\t"
        ".macro GPAIR m, c\n\t"
        "GSUM \\m\n\tGCMP \\m+1, %[sm]\n\tv_cndmask_b32_e32 v[\\c+\\m], 0, v194, vcc\n\t"
        "GSUM \\m+1\n\t.if \\m < 6\n\tGCMP \\m+2, vcc\n\t.else\n\ts_cmp_gt_i32 %[rem], 0\n\t.endif\n\t"
        "v_cndmask_b32_e64 v[\\c+\\m+1], 0, v194, %[sm]\n\t.endm\n\t"
        ".macro GSTEP so, c, eo\n\t"
        "GTR 0, \\so\n\t"
        "s_waitcnt lgkmcnt(2)\n\tGS 0, \\c\n\t"
        "GTR 1, \\so\n\tGTR 2, \\so\n\tGS 1, \\c\n\t"
        "GTR 3, \\so\n\tGTR 4, \\so\n\tGS 2, \\c\n\t"
        "GTR 5, \\so\n\tGTR 6, \\so\n\tGS 3, \\c\n\t"
        "GTR 7, \\so\n\t"
        "ds_read2_b32 v[212:213], v192 offset0:\\eo offset1:\\eo+4\n\t"
        "ds_read2_b32 v[214:215], v192 offset0:\\eo+8 offset1:\\eo+12\n\t"
        "ds_read2_b32 v[216:217], v192 offset0:\\eo+16 offset1:\\eo+20\n\t"
        "ds_read2_b32 v[218:219], v192 offset0:\\eo+24 offset1:\\eo+28\n\t"
        "s_add_u32 m0, %[ring], \\so\n\t"
        "s_cmp_gt_i32 %[rem], 3\n\t"
        ".irp k, 0, 1, 2, 3, 4, 5, 6, 7\n\tv_exp_f32_e32 v[\\c+\\k], v[\\c+\\k]\n\t.endr\n\t"
        "s_waitcnt lgkmcnt(0)\n\t"
        "s_cbranch_scc0 2f\n\t"
        "GLOAD 0, 0, %[s0]\n\tGLOAD 1, 1, %[s1]\n\tGLOAD 2, 2, %[s2]\n\tGLOAD 3, 3, %[s3]\n\t"
        "s_add_u32 m0, m0, 0x1000\n\t"
        "GLOAD 4, 0, %[s0]\n\tGLOAD 5, 1, %[s1]\n\tGLOAD 6, 2, %[s2]\n\tGLOAD 7, 3, %[s3]\n\t"
        "GSIG \\c\n\t"
        "s_waitcnt vmcnt(8)\n\t"
        "s_branch 1f\n\t"
        "2:\n\t"
        "s_mov_b32 %[t], 0\n\t"
        "s_cmp_eq_u32 %[rem], 3\n\ts_cbranch_scc0 4f\n\t"
        "s_mov_b32 %[t], %[nl]\n\t"
        "GLOAD 0, 0, %[s0]\n\tGPART 1, 1, %[s1]\n\tGPART 2, 2, %[s2]\n\tGPART 3, 3, %[s3]\n\t"
        "s_add_u32 m0, m0, 0x1000\n\t"
        "GPART 4, 0, %[s0]\n\tGPART 5, 1, %[s1]\n\tGPART 6, 2, %[s2]\n\tGPART 7, 3, %[s3]\n\t"
        "4:\n\t"
        "GSIG \\c\n\t"
        "s_waitcnt vmcnt(8)\n\t"
        "GWAIT 7\n\tGWAIT 6\n\tGWAIT 5\n\tGWAIT 4\n\tGWAIT 3\n\tGWAIT 2\n\tGWAIT 1\n\tGWAIT 0\n\t"
        "1:\n\t"
        "GROWS 8192-\\so\n\t"
        "GCMP 0, vcc\n\t"
        "s_add_i32 %[rem], %[rem], -1\n\t"
        "GPAIR 0, \\c\n\tGPAIR 2, \\c\n\tGPAIR 4, \\c\n\tGPAIR 6, \\c\n\t"
        ".endm\n\t"
        ".macro GSIG c\n\t"
        ".if " STR(PKADD) "\n\t.irp k, 0, 2, 4, 6\n\tv_pk_add_f32 v[\\c+\\k:\\c+\\k+1], v[\\c+\\k:\\c+\\k+1], 1.0 op_sel_hi:[1,0]\n\t.endr\n\t.else\n\t"
        ".irp k, 0, 1, 2, 3, 4, 5, 6, 7\n\tv_add_f32_e32 v[\\c+\\k], 1.0, v[\\c+\\k]\n\t.endr\n\t.endif\n\t"
        ".irp k, 0, 1, 2, 3, 4, 5, 6, 7\n\tv_rcp_f32_e32 v[\\c+\\k], v[\\c+\\k]\n\t.endr\n\t"
        ".irp k, 0, 1, 2, 3\n\tv_cvt_pk_bf16_f32 v[220+\\k], v[\\c+2*\\k], v[\\c+2*\\k+1]\n\t.endr\n\t.endm\n\t"
        "s_waitcnt vmcnt(8)\n\t"
        "s_cmp_gt_i32 %[rem], 1\n\ts_cbranch_scc1 1f\n\ts_waitcnt vmcnt(0)\n\t1:\n\t"
        "GROWS 0\n\t"
        ".if " STR(LPRIO) " >= 3\n\ts_mov_b32 %[pr], 0\n\ts_setprio 2\n\t.endif\n\t"
        "0:\n\t"
        ".if " STR(LPRIO) " == 3\n\t"
        "s_cmp_eq_u32 %[pr], 1\n\ts_cbranch_scc0 5f\n\ts_setprio 1\n\t5:\n\t"
        "s_cmp_eq_u32 %[pr], 2\n\ts_cbranch_scc0 5f\n\ts_setprio 0\n\t5:\n\t"
        "s_add_u32 %[pr], %[pr], 1\n\t.endif\n\t"
        "GSTEP 0, 196, 64\n\t"
        "s_cbranch_scc0 9f\n\t"
        "GSTEP 8192, 204, 96\n\t"
        "v_add_u32_e32 v192, 0x100, v192\n\t"
        "s_cbranch_scc1 0b\n\t"
        "9:\n\t"
        "s_waitcnt lgkmcnt(0)\n\ts_nop 6\n\t"
        ".purgem GWAIT\n\t.purgem GROWS\n\t.purgem GTR\n\t.purgem GS\n\t.purgem GLOAD\n\t.purgem GPART\n\t.purgem GSUM\n\t.purgem GCMP\n\t.purgem GPAIR\n\t.purgem GSTEP\n\t.purgem GSIG"
        : "+{v[128:159]}"(acc), "+{v[192:195]}"(misc), "+{v[196:211]}"(cc), "=&{v[64:95]}"(t0), "=&{v[96:127]}"(t1), "=&{v[212:223]}"(t2),
          [rem] "+s"(G), [t] "=&s"(tmp), [pr] "=&s"(pr), [sm] "=&s"(sm)
        : "{v[160:175]}"(qf), "{v[176:191]}"(addr), [s0] "s"(d.s0), [s1] "s"(d.s1), [s2] "s"(d.s2), [s3] "s"(d.s3),
          [mask] "s"(mask), [ring] "s"(ring), [nl] "s"(nlast)
        : "vcc", "scc", "memory");
}

__device__ __forceinline__ void output_tile(f32x32 acc, f32x32 wva, f32x32 wvb, unsigned wa, unsigned ow, unsigned ra, unsigned go, const u32x4& srd) {
    f32x32 t0, t1, t2, t3; unsigned o1, o2, o3;
    asm volatile(
        ".macro VPK k\n\tv_cvt_pk_bf16_f32 v[196+\\k], v[128+2*(\\k)], v[129+2*(\\k)]\n\t.endm\n\t"
        ".macro VRD a, u\n\tds_read_b64_tr_b16 v[64+32*\\u+4*\\a:65+32*\\u+4*\\a], v[212+\\a] offset:8192*\\u\n\t"
        "ds_read_b64_tr_b16 v[66+32*\\u+4*\\a:67+32*\\u+4*\\a], v[212+\\a] offset:8192*\\u+4096\n\t.endm\n\t"
        ".macro VMM a, w, u\n\tv_mfma_f32_16x16x32_bf16 v[160+4*\\a:163+4*\\a], v[\\w+4*\\a:\\w+4*\\a+3], v[196+4*\\u:199+4*\\u], v[160+4*\\a:163+4*\\a]\n\t.endm\n\t"
        ".macro VADR a, r\n\tv_xor_b32_e32 v[212+\\a], ((\\a >> 1) << 6) | ((\\a & 1) << 3), \\r\n\t.endm\n\t"
        ".macro VOUT a\n\tv_cvt_pk_bf16_f32 v[160+4*\\a], v[160+4*\\a], v[161+4*\\a]\n\tv_cvt_pk_bf16_f32 v[161+4*\\a], v[162+4*\\a], v[163+4*\\a]\n\t"
        "ds_write_b64 v[212+\\a], v[160+4*\\a:161+4*\\a]\n\t.endm\n\t"
        "VPK 0\n\tVPK 1\n\tVPK 2\n\tVPK 3\n\t"
        ".irp a, 0, 1, 2, 3, 4, 5, 6, 7\n\tVADR \\a, %[wa]\n\t.endr\n\t"
        ".irp a, 0, 1, 2, 3, 4, 5, 6, 7\n\t"
        "v_mfma_f32_16x16x32_bf16 v[160+4*\\a:163+4*\\a], v[224+4*\\a:227+4*\\a], v[196:199], 0\n\tVRD \\a, 0\n\tVPK 4+\\a\n\t.endr\n\t"
        ".irp a, 0, 1, 2, 3\n\tVMM \\a, 32, 1\n\tVRD \\a, 1\n\tVPK 12+\\a\n\t.endr\n\t"
        "VMM 4, 32, 1\n\tVRD 4, 1\n\ts_mov_b32 %[o1], 0x200000\n\t"
        "VMM 5, 32, 1\n\tVRD 5, 1\n\ts_mov_b32 %[o2], 0x400000\n\t"
        "VMM 6, 32, 1\n\tVRD 6, 1\n\ts_mov_b32 %[o3], 0x600000\n\t"
        "VMM 7, 32, 1\n\tVRD 7, 1\n\ts_waitcnt lgkmcnt(15)\n\t"
        ".irp a, 0, 1, 2, 3, 4, 5, 6, 7\n\tVMM \\a, 64, 2\n\tVADR \\a, %[ow]\n\t.endr\n\t"
        "s_waitcnt lgkmcnt(0)\n\t"
        "VMM 0, 96, 3\n\tv_xor_b32_e32 v220, 0x2040, %[go]\n\tv_xor_b32_e32 v221, 0x4080, %[go]\n\tv_xor_b32_e32 v222, 0x60c0, %[go]\n\t"
        "VMM 1, 96, 3\n\ts_nop 2\n\t"
        "VMM 2, 96, 3\n\tVOUT 0\n\tVMM 3, 96, 3\n\tVOUT 1\n\tVMM 4, 96, 3\n\tVOUT 2\n\tVMM 5, 96, 3\n\tVOUT 3\n\t"
        "VMM 6, 96, 3\n\tVOUT 4\n\tVMM 7, 96, 3\n\tVOUT 5\n\ts_nop 0\n\tVOUT 6\n\ts_nop 0\n\tVOUT 7\n\t"
        ".irp m, 0, 1, 2, 3\n\tds_read_b128 v[64+4*\\m:67+4*\\m], %[ra] offset:1024*\\m\n\t.endr\n\t"
        "s_waitcnt lgkmcnt(3)\n\tbuffer_store_dwordx4 v[64:67], %[go], %[srd], 0 offen sc1\n\t"
        "s_waitcnt lgkmcnt(2)\n\tbuffer_store_dwordx4 v[68:71], v220, %[srd], %[o1] offen sc1\n\t"
        "s_waitcnt lgkmcnt(1)\n\tbuffer_store_dwordx4 v[72:75], v221, %[srd], %[o2] offen sc1\n\t"
        "s_waitcnt lgkmcnt(0)\n\tbuffer_store_dwordx4 v[76:79], v222, %[srd], %[o3] offen sc1\n\ts_nop 1\n\t"
        ".purgem VPK\n\t.purgem VRD\n\t.purgem VMM\n\t.purgem VADR\n\t.purgem VOUT"
        : "=&{v[64:95]}"(t0), "=&{v[96:127]}"(t1), "=&{v[160:191]}"(t2), "=&{v[192:223]}"(t3), [o1] "=&s"(o1), [o2] "=&s"(o2), [o3] "=&s"(o3)
        : "{v[128:159]}"(acc), "{v[224:255]}"(wva), "{v[32:63]}"(wvb), [wa] "v"(wa), [ow] "v"(ow), [ra] "v"(ra), [go] "v"(go), [srd] "s"(srd)
        : "memory");
}

__device__ __forceinline__ u64 rank_edges(u32x16& ed, u32x16& es, u32x16& rk, u64 m0, u64 m1, u64 m2, u64 m3, unsigned keya) {
    u32x16 b4; u64 over; unsigned sel = 0x01050401u, c36 = 36;
    asm volatile(
        ".macro RADD k\n\tv_xor_b32_sdwa v[96+\\k], v[64+\\k], v[64+\\k] dst_sel:DWORD dst_unused:UNUSED_PAD src0_sel:BYTE_0 src1_sel:BYTE_1\n\t"
        "v_lshlrev_b32_e32 v[96+\\k], 2, v[96+\\k]\n\t"
        "ds_add_rtn_u32 v[112+\\k], v[96+\\k], v111 offset:%c[cnt]\n\t.endm\n\t"
        ".macro RENT k\n\tv_add_u32_e32 v[80+\\k], %c[bias], v[80+\\k]\n\tv_perm_b32 v[80+\\k], v[80+\\k], v[64+\\k], %[sel]\n\t.endm\n\t"
        ".macro RPUT k\n\tv_min_u32_e32 v110, %c[cap], v[112+\\k]\n\tv_mad_u32_u24 v110, v[96+\\k], %[c36], v110\n\t"
        "ds_write_b32 v110, v[80+\\k] offset:%c[img]\n\t.endm\n\t"
        "v_mov_b32_e32 v111, 4\n\t"
        ".irp k, 0, 2, 4, 6, 8, 10, 12\n\tv_mov_b64_e32 v[112+\\k:113+\\k], 0\n\t.endr\n\t"
        "s_mov_b64 exec, %[m0]\n\ts_waitcnt vmcnt(9)\n\tRADD 0\n\tRADD 1\n\tRADD 2\n\tRADD 3\n\t"
        "s_mov_b64 exec, %[m1]\n\ts_waitcnt vmcnt(8)\n\tRADD 4\n\tRADD 5\n\tRADD 6\n\tRADD 7\n\t"
        "s_mov_b64 exec, %[m2]\n\ts_waitcnt vmcnt(7)\n\tRADD 8\n\tRADD 9\n\tRADD 10\n\tRADD 11\n\t"
        "s_mov_b64 exec, %[m3]\n\ts_waitcnt vmcnt(6)\n\tRADD 12\n\t"
        "s_mov_b64 exec, %[m0]\n\ts_waitcnt vmcnt(5)\n\tRENT 0\n\tRENT 1\n\tRENT 2\n\tRENT 3\n\t"
        "s_waitcnt lgkmcnt(9)\n\tRPUT 0\n\tRPUT 1\n\tRPUT 2\n\tRPUT 3\n\t"
        "s_mov_b64 exec, %[m1]\n\ts_waitcnt vmcnt(4)\n\tRENT 4\n\tRENT 5\n\tRENT 6\n\tRENT 7\n\t"
        "s_waitcnt lgkmcnt(9)\n\tRPUT 4\n\tRPUT 5\n\tRPUT 6\n\tRPUT 7\n\t"
        "s_mov_b64 exec, %[m2]\n\ts_waitcnt vmcnt(3)\n\tRENT 8\n\tRENT 9\n\tRENT 10\n\tRENT 11\n\t"
        "s_waitcnt lgkmcnt(9)\n\tRPUT 8\n\tRPUT 9\n\tRPUT 10\n\tRPUT 11\n\t"
        "s_mov_b64 exec, %[m3]\n\ts_waitcnt vmcnt(0)\n\tRENT 12\n\t"
        "s_waitcnt lgkmcnt(12)\n\tRPUT 12\n\t"
        "s_mov_b64 exec, -1\n\t"
        "v_add_u32_e32 v93, %c[step], v93\n\t"
        "v_max3_u32 v110, v112, v113, v114\n\tv_max3_u32 v109, v115, v116, v117\n\t"
        "ds_write_b32 %[keya], v93 offset:%c[cnt]+1024\n\t"
        "v_max3_u32 v110, v110, v118, v119\n\tv_max3_u32 v109, v109, v120, v121\n\t"
        "v_max3_u32 v110, v110, v122, v123\n\tv_max3_u32 v110, v110, v109, v124\n\t"
        "v_cmp_le_u32_e32 vcc, %c[cap], v110\n\ts_mov_b64 %[over], vcc\n\t"
        ".purgem RADD\n\t.purgem RENT\n\t.purgem RPUT"
        : "+{v[64:79]}"(ed), "+{v[80:95]}"(es), "=&{v[96:111]}"(b4), "=&{v[112:127]}"(rk), [over] "=&s"(over)
        : [m0] "s"(m0), [m1] "s"(m1), [m2] "s"(m2), [m3] "s"(m3), [keya] "v"(keya), [sel] "s"(sel), [c36] "s"(c36),
          [cnt] "i"(L_CNT1), [img] "i"(L_IMG + 8), [bias] "i"(BIAS), [cap] "i"(4 * CAP1), [step] "i"(KEY_STEP)
        : "vcc", "memory");
    return over;
}
__device__ __forceinline__ unsigned read_image(f32x32& img, u32x16& meta, u32x16& sums, unsigned ima, unsigned cna) {
    unsigned no;
    asm volatile(
        "v_mov_b32_e32 v121, 0\n\tds_read_b32 v120, v121 offset:%c[cnt]+2048\n\t"
        ".irp k, 0, 1, 2, 3, 4, 5, 6, 7\n\tds_read_b128 v[64+4*\\k:67+4*\\k], %[ima] offset:4608*\\k\n\t.endr\n\t"
        ".irp k, 0, 1, 2, 3\n\tds_read2_b32 v[96+2*\\k:97+2*\\k], %[cna] offset0:64*\\k offset1:64*\\k+32\n\t.endr\n\t"
        "s_waitcnt lgkmcnt(11)\n\tv_readfirstlane_b32 %[no], v120\n\t"
        "v_add_u32_e32 v121, 0x400, %[cna]\n\t"
        ".irp k, 0, 1, 2, 3\n\tds_read2_b32 v[104+2*\\k:105+2*\\k], v121 offset0:64*\\k offset1:64*\\k+32\n\t.endr\n\t"
        "s_waitcnt lgkmcnt(12)\n\t"
        ".irp k, 0, 1, 2, 3\n\tv_add3_u32 v[112+\\k], v[64+4*\\k], v[65+4*\\k], v[66+4*\\k]\n\tv_add_u32_e32 v[112+\\k], v[112+\\k], v[67+4*\\k]\n\t.endr\n\t"
        "s_waitcnt lgkmcnt(8)\n\t"
        ".irp k, 4, 5, 6, 7\n\tv_add3_u32 v[112+\\k], v[64+4*\\k], v[65+4*\\k], v[66+4*\\k]\n\tv_add_u32_e32 v[112+\\k], v[112+\\k], v[67+4*\\k]\n\t.endr\n\t"
        ".irp k, 0, 1, 2, 3, 4, 5, 6, 7\n\tv_add_u32_dpp v[112+\\k], v[112+\\k], v[112+\\k] quad_perm:[1,0,3,2] row_mask:0xf bank_mask:0xf\n\t.endr\n\t"
        ".irp k, 0, 1, 2, 3, 4, 5, 6, 7\n\tv_add_u32_dpp v[112+\\k], v[112+\\k], v[112+\\k] quad_perm:[2,3,0,1] row_mask:0xf bank_mask:0xf\n\t.endr\n\t"
        ".irp k, 0, 1, 2, 3, 4, 5, 6, 7\n\tv_add_u32_dpp v[112+\\k], v[112+\\k], v[112+\\k] row_half_mirror row_mask:0xf bank_mask:0xf\n\t.endr\n\t"
        "s_waitcnt lgkmcnt(0)"
        : "=&{v[64:95]}"(img), "=&{v[96:111]}"(meta), "=&{v[112:127]}"(sums), [no] "=&s"(no)
        : [ima] "v"(ima), [cna] "v"(cna), [cnt] "i"(L_CNT1) : "memory");
    return no;
}
__device__ __forceinline__ void publish_image(f32x32 img, u32x16 meta, u32x16 sums, const u32x4& srd, unsigned ob) {
    u64 pc0 = 0x0101010101010101ull; unsigned so;
    asm volatile(
        ".macro PUB k\n\ts_mov_b64 exec, %[pc0]\n\tv_mov_b32_e32 v[64+4*\\k], v[96+\\k]\n\tv_mov_b32_e32 v[65+4*\\k], v[112+\\k]\n\t"
        "s_mov_b64 exec, -1\n\tv_xor_b32_e32 v[64+4*\\k], v[64+4*\\k], v[104+\\k]\n\t"
        "buffer_store_dwordx4 v[64+4*\\k:67+4*\\k], %[ob], %[srd], %[so] offen sc1\n\ts_add_u32 %[so], %[so], %c[step]\n\t.endm\n\t"
        "s_mov_b32 %[so], 0\n\t"
        ".irp k, 0, 1, 2, 3, 4, 5, 6, 7\n\tPUB \\k\n\t.endr\n\t.purgem PUB"
        : "+{v[64:95]}"(img), [so] "=&s"(so)
        : "{v[96:111]}"(meta), "{v[112:127]}"(sums), [srd] "s"(srd), [ob] "v"(ob), [pc0] "s"(pc0), [step] "i"(32 * G1 * 128) : "scc", "memory");
}

__device__ __forceinline__ void inbox_request(u32x16& raw, const u32x4& srd, unsigned ib) {
    unsigned o1 = 0x1000, o2 = 0x2000, o3 = 0x3000;
    asm volatile("buffer_load_dwordx4 v[64:67], %1, %2, 0 offen sc1\n\tbuffer_load_dwordx4 v[68:71], %1, %2, %3 offen sc1\n\t"
                 "buffer_load_dwordx4 v[72:75], %1, %2, %4 offen sc1\n\tbuffer_load_dwordx4 v[76:79], %1, %2, %5 offen sc1"
                 : "=&{v[64:79]}"(raw) : "v"(ib), "s"(srd), "s"(o1), "s"(o2), "s"(o3) : "memory");
}
__device__ __forceinline__ unsigned inbox_stage(u32x16 raw, const u32x4& srd, unsigned ib, unsigned key, int i4, unsigned hdra) {
    u32x16 t0; f32x32 t2; unsigned four = 4; u64 m0, m1, m2, m3; unsigned todo, fresh, spin, o1, o2, o3, over;
    u64 pc0 = 0x0101010101010101ull;
    asm volatile(
        ".macro IVAL j, so\n\t"
        "s_bitcmp1_b32 %[todo], \\j\n\ts_cbranch_scc0 2f\n\t"
        "v_xor_b32_e32 v[64+4*\\j], %[key], v[64+4*\\j]\n\t"
        "v_sub_u32_e32 v97, 0, v[65+4*\\j]\n\t"
        "v_add_u32_e32 v96, v[64+4*\\j], v[65+4*\\j]\n\t"
        "v_mov_b32_dpp v[80+\\j], v[64+4*\\j] quad_perm:[0,0,0,0] row_mask:0xf bank_mask:0xf\n\t"
        "v_cndmask_b32_e64 v96, v96, v97, %[pc0]\n\t"
        "s_nop 0\n\t"
        "v_mov_b32_dpp v[80+\\j], v[80+\\j] row_shr:4 row_mask:0xf bank_mask:0xa\n\t"
        "v_cmp_gt_i32_e32 vcc, v[80+\\j], v92\n\t"
        "v_add3_u32 v96, v96, v[66+4*\\j], v[67+4*\\j]\n\t"
        "v_lshrrev_b32_e32 v97, 14, v[80+\\j]\n\t"
        "v_cndmask_b32_e32 v96, 0, v96, vcc\n\t"
        "s_nop 1\n\tv_add_u32_dpp v96, v96, v96 quad_perm:[1,0,3,2] row_mask:0xf bank_mask:0xf\n\t"
        "s_nop 1\n\tv_add_u32_dpp v96, v96, v96 quad_perm:[2,3,0,1] row_mask:0xf bank_mask:0xf\n\t"
        "s_nop 1\n\tv_add_u32_dpp v96, v96, v96 row_half_mirror row_mask:0xf bank_mask:0xf\n\t"
        "v_or_b32_e32 v96, v96, v97\n\t"
        "v_cmp_ne_u32_e32 vcc, 0, v96\n\t"
        "s_cbranch_vccz 1f\n\t"
        "buffer_load_dwordx4 v[64+4*\\j:67+4*\\j], %[ib], %[srd], \\so offen sc1\n\t"
        "s_branch 2f\n\t"
        "1:\n\ts_bitset1_b32 %[fresh], \\j\n\t"
        "2:\n\t.endm\n\t"
        ".macro IRANK j\n\t"
        "s_bitcmp1_b32 %[fresh], \\j\n\ts_cbranch_scc0 2f\n\t"
        "v_cmp_lt_u32_e64 %[m0], v92, v[80+\\j]\n\tv_cmp_lt_u32_e64 %[m1], v93, v[80+\\j]\n\t"
        "v_cmp_lt_u32_e64 %[m2], v94, v[80+\\j]\n\tv_cmp_lt_u32_e64 %[m3], v95, v[80+\\j]\n\t"
        ".irp k, 0, 1, 2, 3\n\tv_lshrrev_b32_e32 v[84+\\k], 21, v[64+4*\\j+\\k]\n\t.endr\n\t"
        ".irp k, 0, 1, 2, 3\n\tv_and_b32_e32 v[84+\\k], 60, v[84+\\k]\n\t.endr\n\t"
        "v_cmp_lt_u32_e32 vcc, %c[cap], v[80+\\j]\n\t"
        "s_mov_b64 exec, %[m0]\n\tds_add_rtn_u32 v88, v84, %[four] offset:%c[cnt]\n\t"
        "s_mov_b64 exec, %[m1]\n\tds_add_rtn_u32 v89, v85, %[four] offset:%c[cnt]\n\t"
        "s_mov_b64 exec, %[m2]\n\tds_add_rtn_u32 v90, v86, %[four] offset:%c[cnt]\n\t"
        "s_mov_b64 exec, %[m3]\n\tds_add_rtn_u32 v91, v87, %[four] offset:%c[cnt]\n\t"
        "s_mov_b64 exec, %[pc0]\n\tds_write_b32 %[hdra], v[80+\\j] offset:128*\\j\n\t"
        "s_mov_b64 exec, -1\n\t"
        "s_cbranch_vccz 1f\n\t"
        "s_mov_b32 %[over], 1\n\t"
        "1:\n\ts_waitcnt lgkmcnt(1)\n\t"
        ".irp k, 0, 1, 2, 3\n\tv_min_u32_e32 v[88+\\k], %c[sub]-4, v[88+\\k]\n\t.endr\n\t"
        ".irp k, 0, 1, 2, 3\n\tv_mad_u32_u24 v[88+\\k], v[84+\\k], %[sub4], v[88+\\k]\n\t.endr\n\t"
        "s_mov_b64 exec, %[m0]\n\tds_write_b32 v88, v[64+4*\\j] offset:%c[list]\n\t"
        "s_mov_b64 exec, %[m1]\n\tds_write_b32 v89, v[65+4*\\j] offset:%c[list]\n\t"
        "s_mov_b64 exec, %[m2]\n\tds_write_b32 v90, v[66+4*\\j] offset:%c[list]\n\t"
        "s_mov_b64 exec, %[m3]\n\tds_write_b32 v91, v[67+4*\\j] offset:%c[list]\n\t"
        "s_mov_b64 exec, -1\n\t"
        "2:\n\t.endm\n\t"
        ".macro FADD j\n\t"
        ".irp k, 0, 1, 2, 3\n\tv_lshrrev_b32_e32 v[96+4*\\j+\\k], 21, v[64+4*\\j+\\k]\n\t.endr\n\t"
        ".irp k, 0, 1, 2, 3\n\tv_and_b32_e32 v[96+4*\\j+\\k], 60, v[96+4*\\j+\\k]\n\t.endr\n\t"
        "v_cmpx_lt_u32_e64 s[72+8*\\j:73+8*\\j], v94, v[80+\\j]\n\tds_add_rtn_u32 v[114+4*\\j], v[98+4*\\j], %[four] offset:%c[cnt]\n\t"
        "v_cmpx_lt_u32_e64 s[74+8*\\j:75+8*\\j], v95, v[80+\\j]\n\tds_add_rtn_u32 v[115+4*\\j], v[99+4*\\j], %[four] offset:%c[cnt]\n\t"
        "s_mov_b64 exec, -1\n\t"
        "v_cmpx_lt_u32_e64 s[68+8*\\j:69+8*\\j], v92, v[80+\\j]\n\tds_add_rtn_u32 v[112+4*\\j], v[96+4*\\j], %[four] offset:%c[cnt]\n\t"
        "v_cmpx_lt_u32_e64 s[70+8*\\j:71+8*\\j], v93, v[80+\\j]\n\tds_add_rtn_u32 v[113+4*\\j], v[97+4*\\j], %[four] offset:%c[cnt]\n\t"
        "s_mov_b64 exec, -1\n\t.endm\n\t"
        ".macro FPUT j\n\t"
        "s_waitcnt lgkmcnt(12)\n\t"
        ".irp k, 0, 1, 2, 3\n\tv_min_u32_e32 v[112+4*\\j+\\k], %c[sub]-4, v[112+4*\\j+\\k]\n\t.endr\n\t"
        ".irp k, 0, 1, 2, 3\n\tv_mad_u32_u24 v[112+4*\\j+\\k], v[96+4*\\j+\\k], %[sub4], v[112+4*\\j+\\k]\n\t.endr\n\t"
        ".irp k, 0, 1, 2, 3\n\ts_mov_b64 exec, s[68+8*\\j+2*\\k:69+8*\\j+2*\\k]\n\tds_write_b32 v[112+4*\\j+\\k], v[64+4*\\j+\\k] offset:%c[list]\n\t.endr\n\t"
        "s_mov_b64 exec, -1\n\t.endm\n\t"
        "s_movk_i32 %[o1], 0x1000\n\ts_movk_i32 %[o2], 0x2000\n\ts_movk_i32 %[o3], 0x3000\n\t"
        "v_mov_b32_e32 v92, %[i4]\n\tv_add_u32_e32 v93, 4, %[i4]\n\tv_add_u32_e32 v94, 8, %[i4]\n\tv_add_u32_e32 v95, 12, %[i4]\n\t"
        "s_mov_b32 %[todo], 15\n\ts_mov_b32 %[spin], 0x400000\n\ts_mov_b32 %[over], 0\n\t"
        ".if " STR(FASTIN) "\n\t"
        "s_waitcnt vmcnt(0)\n\t"
        ".irp j, 0, 1, 2, 3\n\tv_xor_b32_e32 v[64+4*\\j], %[key], v[64+4*\\j]\n\t.endr\n\t"
        ".irp j, 0, 1, 2, 3\n\tv_mov_b32_dpp v[80+\\j], v[64+4*\\j] quad_perm:[0,0,0,0] row_mask:0xf bank_mask:0xf\n\t.endr\n\t"
        ".irp j, 0, 1, 2, 3\n\tv_add_u32_e32 v[84+\\j], v[64+4*\\j], v[65+4*\\j]\n\t.endr\n\t"
        ".irp j, 0, 1, 2, 3\n\tv_mov_b32_dpp v[80+\\j], v[80+\\j] row_shr:4 row_mask:0xf bank_mask:0xa\n\t.endr\n\t"
        "s_mov_b64 exec, %[pc0]\n\t"
        ".irp j, 0, 1, 2, 3\n\tv_sub_u32_e32 v[84+\\j], 0, v[65+4*\\j]\n\t.endr\n\t"
        "s_mov_b64 exec, -1\n\t"
        "v_cmp_gt_i32_e64 %[m0], v80, v92\n\tv_cmp_gt_i32_e64 %[m1], v81, v92\n\tv_cmp_gt_i32_e64 %[m2], v82, v92\n\tv_cmp_gt_i32_e64 %[m3], v83, v92\n\t"
        ".irp j, 0, 1, 2, 3\n\tv_add3_u32 v[84+\\j], v[84+\\j], v[66+4*\\j], v[67+4*\\j]\n\t.endr\n\t"
        "v_cndmask_b32_e64 v84, 0, v84, %[m0]\n\tv_cndmask_b32_e64 v85, 0, v85, %[m1]\n\tv_cndmask_b32_e64 v86, 0, v86, %[m2]\n\tv_cndmask_b32_e64 v87, 0, v87, %[m3]\n\t"
        ".irp j, 0, 1, 2, 3\n\tv_add_u32_dpp v[84+\\j], v[84+\\j], v[84+\\j] quad_perm:[1,0,3,2] row_mask:0xf bank_mask:0xf\n\t.endr\n\t"
        ".irp j, 0, 1, 2, 3\n\tv_add_u32_dpp v[84+\\j], v[84+\\j], v[84+\\j] quad_perm:[2,3,0,1] row_mask:0xf bank_mask:0xf\n\t.endr\n\t"
        ".irp j, 0, 1, 2, 3\n\tv_add_u32_dpp v[84+\\j], v[84+\\j], v[84+\\j] row_half_mirror row_mask:0xf bank_mask:0xf\n\t.endr\n\t"
        "v_or3_b32 v96, v80, v81, v82\n\tv_max3_u32 v97, v80, v81, v82\n\tv_or_b32_e32 v96, v96, v83\n\tv_max_u32_e32 v97, v97, v83\n\t"
        "v_lshrrev_b32_e32 v96, 14, v96\n\tv_or3_b32 v96, v96, v84, v85\n\tv_or3_b32 v96, v96, v86, v87\n\t"
        "v_cmp_ne_u32_e32 vcc, 0, v96\n\t"
        "s_cbranch_vccnz 7f\n\t"
        "v_cmp_lt_u32_e32 vcc, %c[cap], v97\n\t"
        "FADD 0\n\tFADD 1\n\tFADD 2\n\tFADD 3\n\t"
        "s_cbranch_vccz 6f\n\ts_mov_b32 %[over], 1\n\t6:\n\t"
        "FPUT 0\n\tFPUT 1\n\tFPUT 2\n\tFPUT 3\n\t"
        "s_mov_b64 exec, %[pc0]\n\t"
        ".irp j, 0, 1, 2, 3\n\tds_write_b32 %[hdra], v[80+\\j] offset:128*\\j\n\t.endr\n\t"
        "s_mov_b64 exec, -1\n\t"
        "s_branch 9f\n\t"
        "7:\n\t.irp j, 0, 1, 2, 3\n\tv_xor_b32_e32 v[64+4*\\j], %[key], v[64+4*\\j]\n\t.endr\n\t"
        ".endif\n\t"
        "0:\n\t"
        "s_mov_b32 %[fresh], 0\n\t"
        "s_waitcnt vmcnt(0)\n\t"
        "IVAL 0, 0\n\tIVAL 1, %[o1]\n\tIVAL 2, %[o2]\n\tIVAL 3, %[o3]\n\t"
        "IRANK 0\n\tIRANK 1\n\tIRANK 2\n\tIRANK 3\n\t"
        "s_andn2_b32 %[todo], %[todo], %[fresh]\n\t"
        "s_cbranch_scc0 9f\n\t"
        "s_sub_u32 %[spin], %[spin], 1\n\t"
        "s_cbranch_scc0 0b\n\t"
        "s_waitcnt vmcnt(0)\n\t"
        "9:\n\t"
        ".purgem IVAL\n\t.purgem IRANK\n\t.purgem FADD\n\t.purgem FPUT"
        : "+{v[64:79]}"(raw), "=&{v[80:95]}"(t0), "=&{v[96:127]}"(t2), [m0] "=&s"(m0), [m1] "=&s"(m1), [m2] "=&s"(m2), [m3] "=&s"(m3),
          [todo] "=&s"(todo), [fresh] "=&s"(fresh), [spin] "=&s"(spin), [o1] "=&s"(o1), [o2] "=&s"(o2), [o3] "=&s"(o3), [over] "=&s"(over)
        : [srd] "s"(srd), [ib] "v"(ib), [key] "s"(key), [i4] "v"(i4), [hdra] "v"(hdra), [pc0] "s"(pc0), [sub4] "s"(SUBB / 4), [four] "v"(four),
          [cnt] "i"(L_CNT), [list] "i"(L_LIST), [sub] "i"(SUBB), [cap] "i"(4 * CAP1)
        : "vcc", "scc", "memory", "s68", "s69", "s70", "s71", "s72", "s73", "s74", "s75", "s76", "s77", "s78", "s79", "s80", "s81", "s82", "s83",
          "s84", "s85", "s86", "s87", "s88", "s89", "s90", "s91", "s92", "s93", "s94", "s95", "s96", "s97", "s98", "s99");
    return over;
}

template <int CTRL, int BANK> __device__ __forceinline__ unsigned dpp(unsigned old, unsigned v) { return __builtin_amdgcn_update_dpp(old, v, CTRL, 0xF, BANK, true); }
__device__ __forceinline__ unsigned bcast8(unsigned v) { v = dpp<0x00, 0xF>(0u, v); return __builtin_amdgcn_update_dpp(v, v, 0x114, 0xF, 0xA, false); }
__device__ __forceinline__ unsigned sum8(unsigned v) { v += dpp<0xB1, 0xF>(0u, v); v += dpp<0x4E, 0xF>(0u, v); return v + dpp<0x141, 0xF>(0u, v); }
__device__ __forceinline__ bf16x4 tr_read(LAS char* p) { return __builtin_amdgcn_ds_read_tr16_b64_v4bf16((LAS bf16x4*)p); }
__device__ __forceinline__ bf16x8 cat(bf16x4 a, bf16x4 b) { return __builtin_shufflevector(a, b, 0, 1, 2, 3, 4, 5, 6, 7); }
__device__ __forceinline__ unsigned pk2(float a, float b) { f32x2 v = {a, b}; bf16x2 r = __builtin_convertvector(v, bf16x2); return __builtin_bit_cast(unsigned, r); }
__device__ __forceinline__ f32x4 mfma(bf16x8 a, bf16x8 b, f32x4 c) { return __builtin_amdgcn_mfma_f32_16x16x32_bf16(a, b, c, 0, 0, 0); }
__device__ __forceinline__ unsigned lds_add(LAS char* p, unsigned v) { return __hip_atomic_fetch_add((LAS unsigned*)p, v, __ATOMIC_RELAXED, __HIP_MEMORY_SCOPE_WORKGROUP); }
__device__ __forceinline__ LAS unsigned* ldsp(unsigned a) { return (LAS unsigned*)(size_t)a; }
__device__ __forceinline__ unsigned byte0_x4(unsigned v) { unsigned r; asm("v_lshlrev_b32_sdwa %0, 2, %1 dst_sel:DWORD dst_unused:UNUSED_PAD src0_sel:DWORD src1_sel:BYTE_0" : "=v"(r) : "v"(v)); return r; }
__device__ __forceinline__ unsigned mad24(unsigned a, unsigned b, unsigned c) { unsigned r; asm("v_mad_u32_u24 %0, %1, %2, %3" : "=v"(r) : "v"(a), "s"(b), "v"(c)); return r; }
__device__ __forceinline__ void lds_arrive(LAS char* p, int lane) {
    asm volatile("s_waitcnt lgkmcnt(0)" ::: "memory");
    if (lane == 0) __hip_atomic_fetch_add((LAS unsigned*)p, 1u, __ATOMIC_RELAXED, __HIP_MEMORY_SCOPE_WORKGROUP);
}
__device__ __forceinline__ void lds_await(LAS char* p, unsigned n) {
    while (__builtin_amdgcn_readfirstlane(*(volatile LAS unsigned*)p) < n) __builtin_amdgcn_s_sleep(1);
    asm volatile("" ::: "memory");
}
__device__ __forceinline__ unsigned rfl(unsigned v) { return __builtin_amdgcn_readfirstlane(v); }

__global__ __launch_bounds__(512, 2) void k_graph(const unsigned short* x_, const int* src_, const int* dst_, const unsigned short* wq_, const unsigned short* wv_,
                                                  unsigned* ws_, unsigned short* out_, int N_, int E_) {
    u32x4 srd_ws, srd_x0, pw, po;
    u32x16 ed, es;
    u64 t_start;
    asm volatile("s_load_dwordx2 s[40:41], %[ka], 0x10\n\ts_load_dwordx2 s[44:45], %[ka], 0x8\n\ts_load_dwordx4 s[60:63], %[ka], 0x30\n\t"
                 "s_getpc_b64 s[68:69]\n\ts_movk_i32 s70, " STR(CODE_BYTES) "\n\ts_mov_b32 s71, 0x20000\n\ts_mov_b32 m0, %c[dump]\n\t"
                 "s_movk_i32 s66, 0x8000\n\tv_lshl_add_u32 v77, %[t], 7, s66\n\t"
                 "buffer_load_dword v77, s[68:71], 0 offen lds\n\t"
                 "s_load_dwordx2 s[48:49], %[ka], 0x28\n\ts_load_dwordx2 s[52:53], %[ka], 0x0\n\ts_load_dwordx4 s[56:59], %[ka], 0x18\n\t"
                 "s_memrealtime s[64:65]\n\t"
                 "v_readfirstlane_b32 s72, %[t]\n\t"
                 "s_and_b32 s66, %[b], 7\n\ts_lshl_b32 s66, s66, 4\n\ts_lshr_b32 s67, %[b], 4\n\ts_or_b32 s66, s66, s67\n\ts_mulk_i32 s66, %c[ch4]\n\t"
                 "s_mov_b32 s43, s71\n\ts_mov_b32 s47, s71\n\t"
                 "v_lshl_add_u32 v77, %[t], 4, s66\n\t"
                 "s_lshr_b32 s67, s72, 5\n\ts_or_b32 s67, s67, %[b]\n\t"
                 "s_movk_i32 s73, 0x1000\n\ts_movk_i32 s74, 0x2000\n\t"
                 "s_bitcmp1_b32 s67, 3\n\ts_waitcnt lgkmcnt(0)\n\ts_cbranch_scc1 1f\n\t"
                 "s_lshl_b32 s42, s63, 2\n\ts_lshl_b32 s46, s63, 2\n\t"
                 "buffer_load_dwordx4 v[64:67], v77, s[40:43], 0 offen\n\tbuffer_load_dwordx4 v[68:71], v77, s[40:43], s73 offen\n\t"
                 "buffer_load_dwordx4 v[72:75], v77, s[40:43], s74 offen\n\t"
                 "s_add_u32 s66, s66, 0x3000\n\ts_cmp_lt_u32 s72, 64\n\ts_cselect_b32 s66, s66, %c[oob]\n\t"
                 "v_lshl_add_u32 v78, %[t], 2, s66\n\t"
                 "buffer_load_dword v76, v78, s[40:43], 0 offen\n\t"
                 "buffer_load_dwordx4 v[80:83], v77, s[44:47], 0 offen\n\tbuffer_load_dwordx4 v[84:87], v77, s[44:47], s73 offen\n\t"
                 "buffer_load_dwordx4 v[88:91], v77, s[44:47], s74 offen\n\tbuffer_load_dword v92, v78, s[44:47], 0 offen\n\t"
                 "s_mov_b32 s51, s71\n\ts_mov_b32 s50, %c[wsb]\n\ts_lshl_b32 s66, %[b], 2\n\t"
                 "v_lshlrev_b32_e32 v77, 2, %[t]\n\tv_mov_b32_e32 v78, s66\n\t"
                 "buffer_load_dword v93, v77, s[48:51], 0 offen sc1\n\tbuffer_load_dword v94, v78, s[48:51], 0 offen sc1\n\t"
                 "1:\n\ts_mov_b32 s51, s71\n\ts_mov_b32 s50, %c[wsb]\n\ts_mov_b32 s55, s71\n\ts_lshl_b32 s54, s62, 8"
                 : "=&{v[64:79]}"(ed), "=&{v[80:95]}"(es)
                 : [ka] "s"(__builtin_amdgcn_kernarg_segment_ptr()), [t] "v"(threadIdx.x), [b] "s"(blockIdx.x),
                   [wsb] "i"(WS_BYTES), [dump] "i"(L_DUMP), [ch4] "i"(CH * 4), [oob] "i"(OOB)
                 : "memory", "scc", "s40", "s41", "s42", "s43", "s44", "s45", "s46", "s47", "s48", "s49", "s50", "s51", "s52", "s53", "s54", "s55",
                   "s56", "s57", "s58", "s59", "s60", "s61", "s62", "s63", "s64", "s65", "s66", "s67", "s68", "s69", "s70", "s71", "s72", "s73", "s74");
    asm volatile("" : "={s[48:51]}"(srd_ws), "={s[52:55]}"(srd_x0), "={s[56:59]}"(pw), "={s[60:63]}"(po), "={s[64:65]}"(t_start));
    FENCE();
    __shared__ __attribute__((aligned(16))) char smem_[L_TOTAL];
    LAS char* smem = (LAS char*)smem_;
    const int t = threadIdx.x, l = t & 63, b = blockIdx.x;
    const int w = rfl(t >> 6);
    const int g = l >> 4, n = l & 15, q = (l >> 2) & 3, p = l & 3, pc = t & 7;
    const unsigned lds0 = (unsigned)(size_t)smem;
    const bool isprod = ((b >> 3) & 1) == 0;
    const int pg = ((b & 7) << 4) | (b >> 4);
    const unsigned e0 = pg * CH + 4 * t, e1 = pg * CH + 3072 + t;

    const void* const x = (const void*)((u64)srd_x0.x | ((u64)srd_x0.y << 32));
    const void* const wq = (const void*)((u64)pw.x | ((u64)pw.y << 32));
    const void* const wv = (const void*)((u64)pw.z | ((u64)pw.w << 32));
    void* const out = (void*)((u64)po.x | ((u64)po.y << 32));
    const int N = po.z, E = po.w;
    const unsigned xbytes = srd_x0.z;
    STAMP_DECL;
    const unsigned ring = lds0 + L_RING + w * 16384;
    const int jn = 8 * n + w;

    unsigned mykey = 0;
    auto node_row = [&](int tile, int m) { const unsigned j = 8 * (4 * m + g) + tile; return (j << 16) | ((b ^ j) << 8); };
    auto zero_staging = [&]() {
        const u32x4 z = {0u, 0u, 0u, 0u};
#pragma unroll
        for (int k = 0; k < 5; ++k) *(LAS u32x4*)(smem + L_IMG + k * 8192 + t * 16) = z;
    };

    u32x16 addr;
    f32x32 acc, wva, wvb;
    unsigned cv, lista, cnta, qa, keyn, inf, ow, go;
    Srd4 srd_x;
    auto loop_constants = [&]() {
        srd_x = make_srd4(x, xbytes, BIAS * 256);
        lista = lds0 + L_LIST + w * (2 * SUBB) + g * 4; cnta = lds0 + L_CNT + (l & 31) * 4; qa = ring + l * 16; keyn = jn; inf = 0x7F800000u;
        ow = ring + n * 256 + ((g ^ n) << 4) + ((g & 1) << 3); go = node_row(w, 0) + ((n ^ g) << 4);
        asm volatile("" : "+s"(srd_x.s0), "+s"(srd_x.s1), "+s"(srd_x.s2), "+s"(srd_x.s3));
        asm volatile("" : "+v"(lista), "+v"(cnta), "+v"(qa), "+v"(keyn), "+v"(inf), "+v"(ow), "+v"(go));
        const unsigned o0 = (n ^ (((g & 1) << 3) | (g >> 1))) << 4;
        const unsigned ra = (4 * (n & 3) + (n >> 2)) * 256 + (((4 * g) ^ (((n & 7) << 1) | (n >> 3))) << 4);
        const int i = 4 * g + q, r8 = i & 7;
        const unsigned ta = (4 * q + g) * 256 + 8 * (p & 1) + (((p >> 1) ^ (((i & 7) << 1) | (i >> 3))) << 4);
        cv = lds0 + L_WV1 + i * 256 + (((p ^ ((r8 & 1) | ((r8 & 6) << 1))) << 4) | ((p & 1) << 3));
#pragma unroll
        for (int s = 0; s < 4; ++s) { addr[s] = ring + (ra ^ (s << 4)); addr[12 + s] = o0 ^ (s << 5); }
#pragma unroll
        for (int c = 0; c < 8; ++c) addr[4 + c] = ring + (ta ^ (c << 5));
        asm volatile("" : "+{v[176:191]}"(addr), "+v"(cv));
    };
    auto clear_sums = [&]() { acc = 0.f; asm volatile("" : "+{v[128:159]}"(acc)); };
    auto wv_resident = [&]() {
#pragma unroll
        for (int a = 0; a < 8; ++a) {
            LAS char* pa = (LAS char*)(size_t)((cv - (L_WV1 - L_WV0)) ^ ((a >> 1) << 6) ^ ((a & 1) << 3));
            const f32x4 f0 = __builtin_bit_cast(f32x4, cat(tr_read(pa), tr_read(pa + 4096)));
            const f32x4 f1 = __builtin_bit_cast(f32x4, cat(tr_read(pa + 16384), tr_read(pa + 16384 + 4096)));
#pragma unroll
            for (int k = 0; k < 4; ++k) { wva[4 * a + k] = f0[k]; wvb[4 * a + k] = f1[k]; }
        }
        asm volatile("" : "+{v[224:255]}"(wva), "+{v[32:63]}"(wvb));
    };

    if (isprod) zero_staging();
    if (t < 8) *(LAS u32x4*)(smem + L_CNT + t * 16) = u32x4{0u, 0u, 0u, 0u};
    WG_BARRIER();

    if (__builtin_expect(w < 4, 1)) {
        if (__builtin_expect(isprod, 1)) {
            if (PRIO) __builtin_amdgcn_s_setprio(2);
            unsigned ob = WS_INBOX + ((t >> 3) * G1 + pg) * 128 + pc * 16, ima = lds0 + L_IMG + (t >> 3) * 144 + pc * 16;
            PIN(ob); PIN(ima);
            unsigned cna = lds0 + L_CNT1 + (t >> 3) * 4;
            PIN(cna);
            const bool v0 = e0 < (unsigned)E, v1 = e0 + 1024 < (unsigned)E, v2 = e0 + 2048 < (unsigned)E, v3 = w == 0 && e1 < (unsigned)E;
            STAMP(1);
            u32x16 rk;
            const u64 over = rank_edges(ed, es, rk, __builtin_amdgcn_ballot_w64(v0), __builtin_amdgcn_ballot_w64(v1), __builtin_amdgcn_ballot_w64(v2),
                                        __builtin_amdgcn_ballot_w64(v3), t * 4);
            mykey = es[14];
            if (__builtin_expect(over != 0, 0)) {
#pragma unroll
                for (int k = 0; k < 13; ++k)
                    if (rk[k] >= 4u * CAP1) {
                        const unsigned o = WS_OVF + (pg * CH + lds_add(smem + L_CNT1 + 2048, 1)) * 4;
                        const unsigned v = ((((es[k] >> 8) & 0xFFFF) - BIAS) << 16) | ed[k];
                        ST1(v, srd_ws, o, " sc1");
                    }
            }
            if (XSYNC) { lds_arrive(smem + L_CNT + 96, l); lds_await(smem + L_CNT + 96, 4); } else WG_BARRIER();
            STAMP(3);
            f32x32 img; u32x16 meta, sums;
            const unsigned no = read_image(img, meta, sums, ima, cna);
            lds_arrive(smem + L_CNT + 84, l);
            if (__builtin_expect(no != 0, 0)) {
                const unsigned o = t == 0 ? WS_NOVF + pg * 4 : OOB;
                ST1(no, srd_ws, o, " sc1");
                VMWAIT(0);
                if (XSYNC) { lds_arrive(smem + L_CNT + 100, l); lds_await(smem + L_CNT + 100, 4); } else WG_BARRIER();
            }
            publish_image(img, meta, sums, srd_ws, ob);
            if (XAFTER) lds_arrive(smem + L_CNT + 104, l);
            STAMP(4);
        } else {
            { const unsigned om = WS_KEY + b * 4; asm volatile("buffer_load_dword %0, %1, %2, 0 offen sc1\n\ts_waitcnt vmcnt(0)" : "=&v"(mykey) : "v"(om), "s"(srd_ws) : "memory"); }
            STAMP(1);
            if (PARTNER) {
                set_m0(lds0 + L_DUMP + w * 256);
                const unsigned o = (((b - 8) ^ (t >> 1)) << 8) + ((t >> 1) << 16) + ((t & 1) << 7);
                while (__builtin_amdgcn_s_memrealtime() < t_start + X_T) __builtin_amdgcn_s_sleep(1);
                asm volatile("buffer_load_dword %1, %0, 0 offen lds" :: "s"(srd_x0), "v"(o) : "memory");
            }
            if (PRIO) __builtin_amdgcn_s_setprio(2);
            STAMP(2); STAMP(3); STAMP(4);
        }
        STAMP(5);
        mykey = rfl(mykey) + KEY_STEP;
        loop_constants();
        clear_sums();
        unsigned ib = WS_INBOX + b * (G1 * 128) + t * 16;
        PIN(ib);
        u64 t_go = t_start + POLL_T;
        if (POLL_D) { lds_await(smem + L_CNT + 80, 4); t_go = max(t_go, __builtin_amdgcn_s_memrealtime() + POLL_D); }
        while (__builtin_amdgcn_s_memrealtime() < t_go) __builtin_amdgcn_s_sleep(1);
        STAMP(6);
        u32x16 raw;
        inbox_request(raw, srd_ws, ib);
        if (__builtin_expect(inbox_stage(raw, srd_ws, ib, mykey, 16 * pc - 8, lds0 + L_HDR + (t >> 3) * 4) != 0, 0)) {
            LAS unsigned* hp = (LAS unsigned*)(smem + L_HDR) + 32 * (l >> 3 & 3) + 8 * w + (l & 7);
            const unsigned hv = *hp;
            for (u64 m = __builtin_amdgcn_ballot_w64(l < 32 && hv > 4u * CAP1); m; m &= m - 1) {
                const int k = __builtin_ctzll(m), gp = 32 * (k >> 3) + 8 * w + (k & 7);
                unsigned u, no; const unsigned o = WS_OVF + (gp * CH + l) * 4, on = WS_NOVF + gp * 4;
                asm volatile("buffer_load_dword %0, %2, %4, 0 offen sc1\n\tbuffer_load_dword %1, %3, %4, 0 offen sc1\n\ts_waitcnt vmcnt(0)"
                             : "=&v"(u), "=&v"(no) : "v"(o), "v"(on), "s"(srd_ws) : "memory");
                if (rfl(no) <= 64) {
                    if (l < no && ((u ^ (u >> 8)) & 0xFF) == (unsigned)b) {
                        const unsigned e = __builtin_amdgcn_perm((u >> 16) + BIAS, u, 0x01050401u), key = (e >> 21) & 60;
                        *(LAS unsigned*)(smem + L_LIST + key * (SUBB / 4) + min(lds_add(smem + L_CNT + key, 4), SUBB - 4u)) = e;
                    }
                    if (l == k) *hp = hv | 0x80000000u;
                } else *(LAS unsigned*)(smem + L_CNT + 64) = 1;
            }
        }
        if (PRIO) __builtin_amdgcn_s_setprio(0);
        STAMP(7);
    } else {
        const int j = w - 4;
        auto prefetch = [&](int k0, int kn) {
            const unsigned ln = (b >> 3) * (kn / 4) + (t - 256), o = t - 256 < kn / 4 ? k0 * 1024 + ln * 128 : OOB;
            set_m0(lds0 + L_DUMP + w * 256);
            asm volatile("s_nop 0\n\tbuffer_load_dword %1, %0, 0 offen lds" :: "s"(srd_x0), "v"(o) : "memory");
        };
        STAMP(1);
        u32x4 wr[8], xr[8];
        {
            const u32x4 srd_q = make_srd(wq, 32768);
            const unsigned o = j * 8192 + l * 16, k4 = 4096;
            LD4(wr[0], srd_q, o, ""); LD4(wr[1], srd_q, o, " offset:1024"); LD4(wr[2], srd_q, o, " offset:2048"); LD4(wr[3], srd_q, o, " offset:3072");
            LD4S(wr[4], srd_q, o, k4, ""); LD4S(wr[5], srd_q, o, k4, " offset:1024"); LD4S(wr[6], srd_q, o, k4, " offset:2048"); LD4S(wr[7], srd_q, o, k4, " offset:3072");
        }
        unsigned ox[8];
#pragma unroll
        for (int m = 0; m < 8; ++m) { ox[m] = node_row(m < 4 ? j : w, m & 3) + n * 16; PIN(ox[m]); }
        unsigned aqa[8], xfa[4];
        unsigned xa = ring + g * 256 + ((n ^ g) << 4), wa = lds0 + L_WQ + j * 16384 + g * 256 + ((n ^ g) << 4) + ((n & 4) << 1);
        {
            const unsigned aq = lds0 + L_WQ + (8 * g + q) * 256 + ((((4 * p) ^ (q | ((g & 1) << 2))) << 4) | ((p & 1) << 3));
#pragma unroll
            for (int s = 0; s < 4; ++s) { xfa[s] = ring + n * 256 + (((4 * s + g) ^ n) << 4); PIN(xfa[s]); }
#pragma unroll
            for (int c = 0; c < 8; ++c) { aqa[c] = aq ^ ((c >> 1) << 4) ^ ((c & 1) << 3); PIN(aqa[c]); }
            PIN(xa); PIN(wa);
        }
        loop_constants();
        STAMP(2);
        if (isprod && !XSYNC) {
            WG_BARRIER();
            if (__builtin_expect(rfl(*(LAS unsigned*)(smem + L_CNT1 + 2048)) != 0, 0)) WG_BARRIER();
            if (W_SLEEP) __builtin_amdgcn_s_sleep(W_SLEEP);
        }
        STAMP(3);
        auto row_loads = [&]() {
#pragma unroll
            for (int m = 0; m < 8; ++m) LD4(xr[m], srd_x0, ox[m], "");
        };
        asm volatile("s_waitcnt vmcnt(0)" : "+v"(wr[0]), "+v"(wr[1]), "+v"(wr[2]), "+v"(wr[3]), "+v"(wr[4]), "+v"(wr[5]), "+v"(wr[6]), "+v"(wr[7]) :: "memory");
        if (isprod && XAFTER) lds_await(smem + L_CNT + 104, 4);
        else while (__builtin_amdgcn_s_memrealtime() < t_start + X_T) __builtin_amdgcn_s_sleep(1);
        row_loads();
#pragma unroll
        for (int m = 0; m < 8; ++m) {
            const unsigned a = (wa ^ ((m & 2) << 5)) + m * 1024;
            *(LAS u32x2*)(size_t)a = u32x2{wr[m].x, wr[m].y};
            *(LAS u32x2*)(size_t)(a ^ 8) = u32x2{wr[m].z, wr[m].w};
        }
        asm volatile("s_waitcnt vmcnt(0)" : "+v"(xr[0]), "+v"(xr[1]), "+v"(xr[2]), "+v"(xr[3]), "+v"(xr[4]), "+v"(xr[5]), "+v"(xr[6]), "+v"(xr[7]) :: "memory");
#pragma unroll
        for (int m = 0; m < 8; ++m) *(LAS u32x4*)(size_t)((xa ^ ((m & 3) << 6)) + m * 1024) = xr[m];
        STAMP(4);
        lds_arrive(smem + L_CNT + 80, l);
        lds_await(smem + L_CNT + 80, 4);
        STAMP(5);
        bf16x8 xo[2][4], fr[2][8];
        f32x4 qc[2][8];
        auto wq_frags = [&](int u, bf16x8 (&f)[8]) {
#pragma unroll
            for (int a = 0; a < 8; ++a) {
                LAS char* pa = (LAS char*)(size_t)aqa[a] + u * 16384;
                f[a] = cat(tr_read(pa), tr_read(pa + 1024));
            }
        };
#pragma unroll
        for (int u = 0; u < 4; ++u) { xo[0][u] = *(LAS bf16x8*)(size_t)xfa[u]; xo[1][u] = *(LAS bf16x8*)(size_t)(xfa[u] + 4096); }
        wq_frags(0, fr[0]);
        {
            const Srd4 srd_v = make_srd4(wv, 32768);
            const unsigned r0 = (j * 32 + g) << 8, o0 = (n ^ ((g & 1) | ((g & 2) << 1))) << 4;
            const unsigned e[8] = {r0, r0 + 1024, r0 + 2048, r0 + 3072, r0 + 4096, r0 + 5120, r0 + 6144, r0 + 7168};
            const unsigned base = lds0 + (j < 2 ? L_WV0 + j * 16384 : L_WV1 + (j - 2) * 8192);
            const u32x4 off = {o0, o0 ^ 128, o0, o0 ^ 128};
            dma_half(base, srd_v, e, off); dma_half(base + 4096, srd_v, e + 4, off);
        }
        if (PFA_KB) prefetch(0, PFA_KB);
#pragma unroll
        for (int u = 0; u < 4; ++u) {
            if (u < 3) wq_frags(u + 1, fr[(u + 1) & 1]);
            FENCE();
#pragma unroll
            for (int a = 0; a < 8; ++a)
#pragma unroll
                for (int tt = 0; tt < 2; ++tt) qc[tt][a] = mfma(fr[u & 1][a], xo[tt][u], u ? qc[tt][a] : f32x4{0.f, 0.f, 0.f, 0.f});
            FENCE();
            if (!WVLATE && u == 1 && j < 2) { VMWAIT(0); lds_arrive(smem + L_CNT + 88, l); }
        }
        STAMP(6);
        if (PFB_KB) { asm volatile("s_waitcnt vmcnt(%c0)" :: "i"(PFA_KB ? 1 : 0) : "memory"); prefetch(PFA_KB, PFB_KB); }
        if (isprod) lds_await(smem + L_CNT + 84, 4);
#pragma unroll
        for (int tt = 0; tt < 2; ++tt)
#pragma unroll
            for (int s = 0; s < 4; ++s) {
                const float k = -0.08838834764831845f * 1.4426950408889634f;
                const f32x4 c0 = qc[tt][2 * s] * k, c1 = qc[tt][2 * s + 1] * k;
                *(LAS u32x4*)(size_t)(ring - (tt ? 0 : 65536) + s * 1024 + l * 16) = u32x4{pk2(c0[0], c0[1]), pk2(c0[2], c0[3]), pk2(c1[0], c1[1]), pk2(c1[2], c1[3])};
            }
        clear_sums();
        if (!WVLATE) { lds_await(smem + L_CNT + 88, 2); wv_resident(); }
        if (!PFB_KB) asm volatile("s_waitcnt vmcnt(%c0)" :: "i"(PFA_KB ? 1 : 0) : "memory");
        if (PFC_KB) prefetch(PFA_KB + PFB_KB, PFC_KB);
        STAMP(7);
    }
    WG_BARRIER();
    STAMP(8);
    if (LPRIO >= 3) __builtin_amdgcn_s_setprio(3);
    if (LPRIO == 1 && w >= 4) __builtin_amdgcn_s_setprio(1);
    if (LPRIO == 2) { if (w >= 6) __builtin_amdgcn_s_setprio(3); else if (w >= 4) __builtin_amdgcn_s_setprio(2); else if (w >= 2) __builtin_amdgcn_s_setprio(1); }

    u32x16 qf;
    const u32x4 doff = {addr[12], addr[13], addr[14], addr[15]};
    LAS char* mylist = smem + L_LIST + w * (2 * SUBB);
    int gcur = G1, pos = 0;
    unsigned hmask = 0xFFFFFFFFu;
    auto batch = [&](auto first_, const int l) {
        constexpr bool first = decltype(first_)::value;
        auto entries = [&](int grp, unsigned (&e)[8]) {
#pragma unroll
            for (int m = 0; m < 8; ++m) e[m] = *(LAS unsigned*)(size_t)(lista + grp * 128 + m * 16);
        };
        unsigned cw = *(LAS unsigned*)(size_t)(first ? cnta : lds0 + L_CNT + (l & 31) * 4), e0[8], e1[8];
        entries(0, e0);
        if constexpr (first) {
#pragma unroll
            for (int s = 0; s < 4; ++s) {
                const u32x4 v = *(LAS u32x4*)(size_t)(qa + s * 1024);
                qf[4 * s] = v.x; qf[4 * s + 1] = v.y; qf[4 * s + 2] = v.z; qf[4 * s + 3] = v.w;
            }
        }
        entries(1, e1);
        FENCE();
        int lo = __builtin_amdgcn_readlane(cw, 2 * w) >> 2, hi = __builtin_amdgcn_readlane(cw, 2 * w + 1) >> 2;
        if constexpr (first) {
            if (__builtin_amdgcn_readlane(cw, 16)) gcur = 0;
            if (__builtin_amdgcn_ballot_w64(cw > 4u * CAP2)) { gcur = -1; lo = 0; hi = 0; hmask = 0x7FFFFFFFu; }
        }
        const int c = lo + hi, G = (c + 31) >> 5;
        const int nlast = G > 2 ? (((c - 1) & 31) >> 2) + 1 : 8;
        auto join = [&]() {
            for (int k = 0; k < G * 32 - lo; k += 64) {
                const unsigned e = *(LAS unsigned*)(mylist + SUBB + (l + k) * 4);
                if (l + k < G * 32 - lo) *(LAS unsigned*)(mylist + (lo + l + k) * 4) = l + k < hi ? e : PAD;
            }
        };
        const bool early = lo >= 64;
        if (__builtin_expect(!early, 0)) { join(); entries(0, e0); entries(1, e1); }
        if constexpr (first) asm volatile("s_waitcnt lgkmcnt(0)" : "+{v[160:175]}"(qf) :: "memory");
        if (G > 0) dma_group(ring, srd_x, e0, doff);
        if constexpr (first) {
            if (WVLATE == 1) {
                if (G > 1 && w < 6) dma_group(ring + 8192, srd_x, e1, doff);
                wv_resident();
                lds_arrive(smem + L_CNT + 92, l);
                if (w >= 6) { lds_await(smem + L_CNT + 92, 8); if (G > 1) dma_group(ring + 8192, srd_x, e1, doff); }
            } else if (WVLATE == 2) {
                wv_resident();
                lds_arrive(smem + L_CNT + 92, l);
                if (w >= 6) lds_await(smem + L_CNT + 92, 8);
                if (G > 1) dma_group(ring + 8192, srd_x, e1, doff);
            } else {
                if (w < 4) { wv_resident(); lds_arrive(smem + L_CNT + 92, l); }
                else if (w >= 6) lds_await(smem + L_CNT + 92, 4);
                if (G > 1) dma_group(ring + 8192, srd_x, e1, doff);
            }
        } else if (G > 1) dma_group(ring + 8192, srd_x, e1, doff);
        f32x16 cc;
#pragma unroll
        for (int m = 0; m < 8; ++m) {
            cc[m] = (e0[m] & 0xFF) == keyn ? 0.f : __uint_as_float(inf);
            cc[8 + m] = (e1[m] & 0xFF) == keyn ? 0.f : __uint_as_float(inf);
        }
        if (early) join();
        STAMP(9);
        if (G > 0) gather_loop(acc, qf, addr, u32x4{lista, keyn, inf, 0u}, cc, srd_x, ring, G, nlast);
    };
    batch(std::true_type{}, l);
    if (__builtin_expect(gcur < G1, 0)) do {
        int t = threadIdx.x;
        PIN(t);
        const int l = t & 63;
        unsigned lim = G1 * 32;
        if (gcur >= 0) {
            const u64 m0 = __builtin_amdgcn_ballot_w64(l >= gcur && (int)(*(LAS unsigned*)(smem + L_HDR + l * 4) & hmask) > 4 * CAP1);
            const u64 m1 = __builtin_amdgcn_ballot_w64(l + 64 >= gcur && (int)(*(LAS unsigned*)(smem + L_HDR + 256 + l * 4) & hmask) > 4 * CAP1);
            if (!(m0 | m1)) break;
            gcur = m0 ? __builtin_ctzll(m0) : 64 + __builtin_ctzll(m1);
            unsigned v; const unsigned o = WS_NOVF + gcur * 4;
            asm volatile("buffer_load_dword %0, %1, %2, 0 offen sc1\n\ts_waitcnt vmcnt(0)" : "=&v"(v) : "v"(o), "s"(srd_ws) : "memory");
            lim = rfl(v);
        }
        const unsigned i = pos + t;
        const bool cand = t < CAP2 && i < lim;
        unsigned u;
        { const unsigned o = cand ? (gcur < 0 ? WS_INBOX + b * (G1 * 128) : WS_OVF + gcur * CH * 4) + i * 4 : OOB;
          asm volatile("buffer_load_dword %0, %1, %2, 0 offen sc1\n\ts_waitcnt vmcnt(0)" : "=&v"(u) : "v"(o), "s"(srd_ws) : "memory"); }
        WG_BARRIER();
        if (t < 16) *(LAS unsigned*)(smem + L_CNT + t * 4) = 0;
        WG_BARRIER();
        if (cand) {
            unsigned e; bool v;
            if (gcur < 0) { e = (i & 3) ? u : u ^ mykey; v = ((i & 31) - 2) * 4 < *(LAS unsigned*)(smem + L_HDR + (i >> 5) * 4); }
            else { e = (((u >> 16) + BIAS) << 8) | ((u >> 8) & 0xFF); v = ((u ^ (u >> 8)) & 0xFF) == (unsigned)b; }
            if (v) *(LAS unsigned*)(smem + L_LIST + (e & 7) * (2 * SUBB) + lds_add(smem + L_CNT + (e & 7) * 8, 4)) = e;
        }
        pos += CAP2;
        if ((unsigned)pos >= lim) { ++gcur; pos = 0; }
        WG_BARRIER();
        batch(std::false_type{}, l);
    } while (gcur < G1);
    STAMP(10);

    output_tile(acc, wva, wvb, cv, ow, qa, go, make_srd(out, xbytes));
    if (w == 0) { const unsigned o = t == 0 ? WS_KEY + b * 4 : OOB; ST1(mykey, srd_ws, o, " sc1"); }
    STAMP(11);
}

extern "C" void kernel_launch(void* const* d_in, const int* in_sizes, int n_in,
                              void* d_out, int out_size, void* d_ws, size_t ws_size,
                              hipStream_t stream) {
    const int N = in_sizes[0] / 128, E = in_sizes[1];
    k_graph<<<NB, 512, 0, stream>>>((const unsigned short*)d_in[0], (const int*)d_in[1], (const int*)d_in[2],
                                    (const unsigned short*)d_in[3], (const unsigned short*)d_in[4],
                                    (unsigned*)d_ws, (unsigned short*)d_out, N, E);
}
```

```cpp
#define STAMPS
#define PROBE_A 0
#define PROBE_B 13
#define PROBE_M 0
#include <hip/hip_runtime.h>
#include <hip/hip_bf16.h>
#include <stdint.h>
#include <type_traits>
#define ORD 1
#define SKIP 0
#define STR2(x) #x
#define STR(x) STR2(x)

typedef __attribute__((ext_vector_type(8))) __bf16 bf16x8;
typedef __attribute__((ext_vector_type(4))) __bf16 bf16x4;
typedef __attribute__((ext_vector_type(2))) __bf16 bf16x2;
typedef __attribute__((ext_vector_type(4))) float f32x4;
typedef __attribute__((ext_vector_type(2))) float f32x2;
typedef __attribute__((ext_vector_type(4))) unsigned u32x4;
typedef __attribute__((ext_vector_type(2))) unsigned u32x2;
typedef __attribute__((ext_vector_type(32))) float f32x32;
typedef __attribute__((ext_vector_type(16))) float f32x16;
typedef __attribute__((ext_vector_type(16))) unsigned u32x16;
typedef __attribute__((ext_vector_type(12))) unsigned u32x12;
typedef __attribute__((ext_vector_type(8))) unsigned u32x8;
typedef unsigned long long u64;
#define LAS __attribute__((address_space(3)))

constexpr int NB = 256;
constexpr int G1 = 128;
constexpr int CH = 3136;
constexpr int CAP1 = 30;
constexpr int CAP2 = 192;
constexpr int SUBB = 784;
#define POLL_AT 3
#define POLL_T 290
#define LPRIO 3
#define PKADD 1
#define CODE_BYTES 23552
#define PARTNER 1
#define XSYNC 1
#define WVLATE 1
#define PFA_KB 0
#define PFB_KB 0
#define PFC_KB 1024
#define X_T 0
#define XAFTER 0
#define POLL_D 60
#define FASTIN 1
#define W_SLEEP 3
#define PRIO 1
#define SPLIT 16384
constexpr unsigned KEY_STEP = 0x9E3779B1u;
constexpr unsigned WS_KEY = 0;
constexpr unsigned WS_NOVF = 2048;
constexpr unsigned WS_INBOX = 4096;
constexpr unsigned WS_OVF = WS_INBOX + NB * G1 * 128;
constexpr unsigned WS_BYTES = WS_OVF + G1 * CH * 4;
constexpr unsigned OOB = 0x80000000u;
constexpr unsigned BIAS = 32768 - SPLIT;
constexpr unsigned PAD = 0x00FFFFFFu;

constexpr int L_CNT = 0;
constexpr int L_HDR = 128;
constexpr int L_LIST = 768;
constexpr int L_RING = L_LIST + 16 * SUBB;
constexpr int L_IMG = L_RING;
constexpr int L_CNT1 = L_IMG + NB * 144;
constexpr int L_WQ = L_RING + 2 * 16384 + 8192;
constexpr int L_WV0 = L_RING + 6 * 16384 + 8192;
constexpr int L_WV1 = L_RING + 131072;
constexpr int L_DUMP = L_WV1 + 16384;
constexpr int L_TOTAL = L_DUMP + 2048;
static_assert(WS_KEY == 0 && L_WV1 % 256 == 0 && CH == 3072 + 64 && L_RING % 256 == 0 && L_TOTAL <= 163840, "LDS map");

#define STAMP_N 24
#define STAMP_WGS 256
#define STAMP_OFF (200u << 20)
#define STAMP_ROLE 0
#define STAMP_VALID(w, k) ((k) < 14 && ((STAMP_MASK >> (k)) & 1) && ((k) == 0 || STAMP_ROLE == 2 || ((((w) >> 3) & 1) == STAMP_ROLE)))
#define STAMP_DECL unsigned long long st_[STAMP_N] = {}
#define STAMP_MASK 0xFFFFFF
#define STAMP(k) do { if (!((STAMP_MASK >> (k)) & 1)) break; __builtin_amdgcn_sched_barrier(0); asm volatile("s_memrealtime %0\n\ts_waitcnt lgkmcnt(0)" : "=s"(st_[k]) :: "memory"); __builtin_amdgcn_sched_barrier(0); } while (0)
#define STAMP_THREAD 0
#define STAMP_FLUSH(base, k0, k1) do { if (threadIdx.x == STAMP_THREAD) { _Pragma("unroll") for (int k_ = k0; k_ < k1; ++k_) ((unsigned long long*)((char*)(base) + STAMP_OFF))[(size_t)blockIdx.x * STAMP_N + k_] = st_[k_]; } } while (0)

__device__ __forceinline__ u32x4 make_srd(const void* p, unsigned bytes, unsigned back = 0) {
    const u64 a = (u64)p - back;
    u32x4 s; s.x = (unsigned)a; s.y = (unsigned)(a >> 32); s.z = bytes + back; s.w = 0x00020000u; return s;
}
#define LD4(dst, srd, off, FL) asm volatile("buffer_load_dwordx4 %0, %1, %2, 0 offen" FL : "=v"(dst) : "v"(off), "s"(srd) : "memory")
#define LD4S(dst, srd, off, so, FL) asm volatile("buffer_load_dwordx4 %0, %1, %2, %3 offen" FL : "=v"(dst) : "v"(off), "s"(srd), "s"(so) : "memory")
#define LD1(dst, srd, off, FL) asm volatile("buffer_load_dword %0, %1, %2, 0 offen" FL : "=v"(dst) : "v"(off), "s"(srd) : "memory")
#define ST1(val, srd, off, FL) asm volatile("buffer_store_dword %0, %1, %2, 0 offen" FL :: "v"(val), "v"(off), "s"(srd) : "memory")
#define PIN(x) asm volatile("" : "+v"(x))
#define VMWAIT(n) asm volatile("s_waitcnt vmcnt(" #n ")" ::: "memory")
#define FENCE() __builtin_amdgcn_sched_barrier(0)
#define WG_BARRIER() do { asm volatile("s_waitcnt lgkmcnt(0)" ::: "memory"); __builtin_amdgcn_s_barrier(); asm volatile("" ::: "memory"); } while (0)

struct Srd4 { u32x4 s0, s1, s2, s3; };
__device__ __forceinline__ Srd4 make_srd4(const void* p, unsigned bytes, unsigned back = 0) {
    return Srd4{make_srd(p, bytes, back), make_srd(p, bytes, back + 1024), make_srd(p, bytes, back + 2048), make_srd(p, bytes, back + 3072)};
}

__device__ __forceinline__ void set_m0(unsigned v) { asm volatile("s_mov_b32 m0, %0" :: "s"(v) : "memory"); }
template <int R> __device__ __forceinline__ void dma_rows(const Srd4& d, unsigned e, unsigned off) {
    unsigned tmp, mask = 0x00FFFF00u;
    asm volatile("v_and_or_b32 %0, %1, %2, %3\n\tbuffer_load_dwordx4 %0, %4, 0 offen offset:%c5 lds"
                 : "=&v"(tmp) : "v"(e), "s"(mask), "v"(off), "s"(R == 0 ? d.s0 : R == 1 ? d.s1 : R == 2 ? d.s2 : d.s3), "i"(R * 1024) : "memory");
}
__device__ __forceinline__ void dma_half(unsigned ldsbase, const Srd4& d, const unsigned* e, const u32x4& off) {
    set_m0(ldsbase);
    dma_rows<0>(d, e[0], off.x); dma_rows<1>(d, e[1], off.y); dma_rows<2>(d, e[2], off.z); dma_rows<3>(d, e[3], off.w);
}
__device__ __forceinline__ void dma_group(unsigned ldsbase, const Srd4& d, const unsigned (&e)[8], const u32x4& off) {
    asm volatile("s_waitcnt lgkmcnt(0)" ::: "memory");
    dma_half(ldsbase, d, e, off); dma_half(ldsbase + 4096, d, e + 4, off);
}

__device__ __forceinline__ void gather_loop(f32x32& acc, const u32x16& qf, const u32x16& addr, u32x4 misc, f32x16 cc,
                                            const Srd4& d, unsigned ring, int G, int nlast) {
    f32x32 t0, t1; u32x12 t2; int tmp, pr; u64 sm; unsigned mask = 0x00FFFF00u;
    asm volatile(
        ".macro GWAIT k\n\ts_cmp_gt_i32 %[t], \\k\n\ts_cbranch_scc1 1f\n\ts_waitcnt vmcnt(\\k)\n\t.endm\n\t"
        ".macro GROWS so\n\t.irp s, 0, 1, 2, 3\n\t"
        "ds_read_b128 v[64+8*\\s:67+8*\\s], v[176+\\s] offset:\\so\n\t"
        "ds_read_b128 v[68+8*\\s:71+8*\\s], v[176+\\s] offset:\\so+4096\n\t.endr\n\t.endm\n\t"
        ".macro GTR c, so\n\t"
        "ds_read_b64_tr_b16 v[96+4*\\c:97+4*\\c], v[180+\\c] offset:\\so\n\t"
        "ds_read_b64_tr_b16 v[98+4*\\c:99+4*\\c], v[180+\\c] offset:\\so+4096\n\t.endm\n\t"
        ".macro GS s, c\n\t"
        "v_mfma_f32_16x16x32_bf16 v[\\c:\\c+3], v[64+8*\\s:67+8*\\s], v[160+4*\\s:163+4*\\s], v[\\c:\\c+3]\n\t"
        "v_mfma_f32_16x16x32_bf16 v[\\c+4:\\c+7], v[68+8*\\s:71+8*\\s], v[160+4*\\s:163+4*\\s], v[\\c+4:\\c+7]\n\t.endm\n\t"
        ".macro GLOAD m, r, srd\n\t"
        "v_and_or_b32 v195, v[212+\\m], %[mask], v[188+\\r]\n\t"
        "buffer_load_dwordx4 v195, \\srd, 0 offen offset:1024*\\r lds\n\t.endm\n\t"
        ".macro GPART m, r, srd\n\ts_cmp_le_i32 %[nl], \\m\n\ts_cbranch_scc1 4f\n\tGLOAD \\m, \\r, \\srd\n\t.endm\n\t"
        ".macro GSUM m\n\tv_mfma_f32_16x16x32_bf16 v[128+4*(\\m):131+4*(\\m)], v[96+4*(\\m):99+4*(\\m)], v[220:223], v[128+4*(\\m):131+4*(\\m)]\n\t.endm\n\t"
        ".macro GCMP m, d\n\tv_cmp_ne_u32_sdwa \\d, v[212+\\m], v193 src0_sel:BYTE_0 src1_sel:DWORD\n\t.endm\n\t"
        ".macro GPAIR m, c\n\t"
        "GSUM \\m\n\tGCMP \\m+1, %[sm]\n\tv_cndmask_b32_e32 v[\\c+\\m], 0, v194, vcc\n\t"
        "GSUM \\m+1\n\t.if \\m < 6\n\tGCMP \\m+2, vcc\n\t.else\n\ts_cmp_gt_i32 %[rem], 0\n\t.endif\n\t"
        "v_cndmask_b32_e64 v[\\c+\\m+1], 0, v194, %[sm]\n\t.endm\n\t"
        ".macro GSTEP so, c, eo\n\t"
        "GTR 0, \\so\n\t"
        "s_waitcnt lgkmcnt(2)\n\tGS 0, \\c\n\t"
        "GTR 1, \\so\n\tGTR 2, \\so\n\tGS 1, \\c\n\t"
        "GTR 3, \\so\n\tGTR 4, \\so\n\tGS 2, \\c\n\t"
        "GTR 5, \\so\n\tGTR 6, \\so\n\tGS 3, \\c\n\t"
        "GTR 7, \\so\n\t"
        "ds_read2_b32 v[212:213], v192 offset0:\\eo offset1:\\eo+4\n\t"
        "ds_read2_b32 v[214:215], v192 offset0:\\eo+8 offset1:\\eo+12\n\t"
        "ds_read2_b32 v[216:217], v192 offset0:\\eo+16 offset1:\\eo+20\n\t"
        "ds_read2_b32 v[218:219], v192 offset0:\\eo+24 offset1:\\eo+28\n\t"
        "s_add_u32 m0, %[ring], \\so\n\t"
        "s_cmp_gt_i32 %[rem], 3\n\t"
        ".irp k, 0, 1, 2, 3, 4, 5, 6, 7\n\tv_exp_f32_e32 v[\\c+\\k], v[\\c+\\k]\n\t.endr\n\t"
        "s_waitcnt lgkmcnt(0)\n\t"
        "s_cbranch_scc0 2f\n\t"
        "GLOAD 0, 0, %[s0]\n\tGLOAD 1, 1, %[s1]\n\tGLOAD 2, 2, %[s2]\n\tGLOAD 3, 3, %[s3]\n\t"
        "s_add_u32 m0, m0, 0x1000\n\t"
        "GLOAD 4, 0, %[s0]\n\tGLOAD 5, 1, %[s1]\n\tGLOAD 6, 2, %[s2]\n\tGLOAD 7, 3, %[s3]\n\t"
        "GSIG \\c\n\t"
        "s_waitcnt vmcnt(8)\n\t"
        "s_branch 1f\n\t"
        "2:\n\t"
        "s_mov_b32 %[t], 0\n\t"
        "s_cmp_eq_u32 %[rem], 3\n\ts_cbranch_scc0 4f\n\t"
        "s_mov_b32 %[t], %[nl]\n\t"
        "GLOAD 0, 0, %[s0]\n\tGPART 1, 1, %[s1]\n\tGPART 2, 2, %[s2]\n\tGPART 3, 3, %[s3]\n\t"
        "s_add_u32 m0, m0, 0x1000\n\t"
        "GPART 4, 0, %[s0]\n\tGPART 5, 1, %[s1]\n\tGPART 6, 2, %[s2]\n\tGPART 7, 3, %[s3]\n\t"
        "4:\n\t"
        "GSIG \\c\n\t"
        "s_waitcnt vmcnt(8)\n\t"
        "GWAIT 7\n\tGWAIT 6\n\tGWAIT 5\n\tGWAIT 4\n\tGWAIT 3\n\tGWAIT 2\n\tGWAIT 1\n\tGWAIT 0\n\t"
        "1:\n\t"
        "GROWS 8192-\\so\n\t"
        "GCMP 0, vcc\n\t"
        "s_add_i32 %[rem], %[rem], -1\n\t"
        "GPAIR 0, \\c\n\tGPAIR 2, \\c\n\tGPAIR 4, \\c\n\tGPAIR 6, \\c\n\t"
        ".endm\n\t"
        ".macro GSIG c\n\t"
        ".if " STR(PKADD) "\n\t.irp k, 0, 2, 4, 6\n\tv_pk_add_f32 v[\\c+\\k:\\c+\\k+1], v[\\c+\\k:\\c+\\k+1], 1.0 op_sel_hi:[1,0]\n\t.endr\n\t.else\n\t"
        ".irp k, 0, 1, 2, 3, 4, 5, 6, 7\n\tv_add_f32_e32 v[\\c+\\k], 1.0, v[\\c+\\k]\n\t.endr\n\t.endif\n\t"
        ".irp k, 0, 1, 2, 3, 4, 5, 6, 7\n\tv_rcp_f32_e32 v[\\c+\\k], v[\\c+\\k]\n\t.endr\n\t"
        ".irp k, 0, 1, 2, 3\n\tv_cvt_pk_bf16_f32 v[220+\\k], v[\\c+2*\\k], v[\\c+2*\\k+1]\n\t.endr\n\t.endm\n\t"
        "s_waitcnt vmcnt(8)\n\t"
        "s_cmp_gt_i32 %[rem], 1\n\ts_cbranch_scc1 1f\n\ts_waitcnt vmcnt(0)\n\t1:\n\t"
        "GROWS 0\n\t"
        ".if " STR(LPRIO) " >= 3\n\ts_mov_b32 %[pr], 0\n\ts_setprio 2\n\t.endif\n\t"
        "0:\n\t"
        ".if " STR(LPRIO) " == 3\n\t"
        "s_cmp_eq_u32 %[pr], 1\n\ts_cbranch_scc0 5f\n\ts_setprio 1\n\t5:\n\t"
        "s_cmp_eq_u32 %[pr], 2\n\ts_cbranch_scc0 5f\n\ts_setprio 0\n\t5:\n\t"
        "s_add_u32 %[pr], %[pr], 1\n\t.endif\n\t"
        "GSTEP 0, 196, 64\n\t"
        "s_cbranch_scc0 9f\n\t"
        "GSTEP 8192, 204, 96\n\t"
        "v_add_u32_e32 v192, 0x100, v192\n\t"
        "s_cbranch_scc1 0b\n\t"
        "9:\n\t"
        "s_waitcnt lgkmcnt(0)\n\ts_nop 6\n\t"
        ".purgem GWAIT\n\t.purgem GROWS\n\t.purgem GTR\n\t.purgem GS\n\t.purgem GLOAD\n\t.purgem GPART\n\t.purgem GSUM\n\t.purgem GCMP\n\t.purgem GPAIR\n\t.purgem GSTEP\n\t.purgem GSIG"
        : "+{v[128:159]}"(acc), "+{v[192:195]}"(misc), "+{v[196:211]}"(cc), "=&{v[64:95]}"(t0), "=&{v[96:127]}"(t1), "=&{v[212:223]}"(t2),
          [rem] "+s"(G), [t] "=&s"(tmp), [pr] "=&s"(pr), [sm] "=&s"(sm)
        : "{v[160:175]}"(qf), "{v[176:191]}"(addr), [s0] "s"(d.s0), [s1] "s"(d.s1), [s2] "s"(d.s2), [s3] "s"(d.s3),
          [mask] "s"(mask), [ring] "s"(ring), [nl] "s"(nlast)
        : "vcc", "scc", "memory");
}

__device__ __forceinline__ void output_tile(f32x32 acc, f32x32 wva, f32x32 wvb, unsigned wa, unsigned ow, unsigned ra, unsigned go, const u32x4& srd) {
    f32x32 t0, t1, t2, t3; unsigned o1, o2, o3;
    asm volatile(
        ".macro VPK k\n\tv_cvt_pk_bf16_f32 v[196+\\k], v[128+2*(\\k)], v[129+2*(\\k)]\n\t.endm\n\t"
        ".macro VRD a, u\n\tds_read_b64_tr_b16 v[64+32*\\u+4*\\a:65+32*\\u+4*\\a], v[212+\\a] offset:8192*\\u\n\t"
        "ds_read_b64_tr_b16 v[66+32*\\u+4*\\a:67+32*\\u+4*\\a], v[212+\\a] offset:8192*\\u+4096\n\t.endm\n\t"
        ".macro VMM a, w, u\n\tv_mfma_f32_16x16x32_bf16 v[160+4*\\a:163+4*\\a], v[\\w+4*\\a:\\w+4*\\a+3], v[196+4*\\u:199+4*\\u], v[160+4*\\a:163+4*\\a]\n\t.endm\n\t"
        ".macro VADR a, r\n\tv_xor_b32_e32 v[212+\\a], ((\\a >> 1) << 6) | ((\\a & 1) << 3), \\r\n\t.endm\n\t"
        ".macro VOUT a\n\tv_cvt_pk_bf16_f32 v[160+4*\\a], v[160+4*\\a], v[161+4*\\a]\n\tv_cvt_pk_bf16_f32 v[161+4*\\a], v[162+4*\\a], v[163+4*\\a]\n\t"
        "ds_write_b64 v[212+\\a], v[160+4*\\a:161+4*\\a]\n\t.endm\n\t"
        "VPK 0\n\tVPK 1\n\tVPK 2\n\tVPK 3\n\t"
        ".irp a, 0, 1, 2, 3, 4, 5, 6, 7\n\tVADR \\a, %[wa]\n\t.endr\n\t"
        ".irp a, 0, 1, 2, 3, 4, 5, 6, 7\n\t"
        "v_mfma_f32_16x16x32_bf16 v[160+4*\\a:163+4*\\a], v[224+4*\\a:227+4*\\a], v[196:199], 0\n\tVRD \\a, 0\n\tVPK 4+\\a\n\t.endr\n\t"
        ".irp a, 0, 1, 2, 3\n\tVMM \\a, 32, 1\n\tVRD \\a, 1\n\tVPK 12+\\a\n\t.endr\n\t"
        "VMM 4, 32, 1\n\tVRD 4, 1\n\ts_mov_b32 %[o1], 0x200000\n\t"
        "VMM 5, 32, 1\n\tVRD 5, 1\n\ts_mov_b32 %[o2], 0x400000\n\t"
        "VMM 6, 32, 1\n\tVRD 6, 1\n\ts_mov_b32 %[o3], 0x600000\n\t"
        "VMM 7, 32, 1\n\tVRD 7, 1\n\ts_waitcnt lgkmcnt(15)\n\t"
        ".irp a, 0, 1, 2, 3, 4, 5, 6, 7\n\tVMM \\a, 64, 2\n\tVADR \\a, %[ow]\n\t.endr\n\t"
        "s_waitcnt lgkmcnt(0)\n\t"
        "VMM 0, 96, 3\n\tv_xor_b32_e32 v220, 0x2040, %[go]\n\tv_xor_b32_e32 v221, 0x4080, %[go]\n\tv_xor_b32_e32 v222, 0x60c0, %[go]\n\t"
        "VMM 1, 96, 3\n\ts_nop 2\n\t"
        "VMM 2, 96, 3\n\tVOUT 0\n\tVMM 3, 96, 3\n\tVOUT 1\n\tVMM 4, 96, 3\n\tVOUT 2\n\tVMM 5, 96, 3\n\tVOUT 3\n\t"
        "VMM 6, 96, 3\n\tVOUT 4\n\tVMM 7, 96, 3\n\tVOUT 5\n\ts_nop 0\n\tVOUT 6\n\ts_nop 0\n\tVOUT 7\n\t"
        ".irp m, 0, 1, 2, 3\n\tds_read_b128 v[64+4*\\m:67+4*\\m], %[ra] offset:1024*\\m\n\t.endr\n\t"
        "s_waitcnt lgkmcnt(3)\n\tbuffer_store_dwordx4 v[64:67], %[go], %[srd], 0 offen sc1\n\t"
        "s_waitcnt lgkmcnt(2)\n\tbuffer_store_dwordx4 v[68:71], v220, %[srd], %[o1] offen sc1\n\t"
        "s_waitcnt lgkmcnt(1)\n\tbuffer_store_dwordx4 v[72:75], v221, %[srd], %[o2] offen sc1\n\t"
        "s_waitcnt lgkmcnt(0)\n\tbuffer_store_dwordx4 v[76:79], v222, %[srd], %[o3] offen sc1\n\ts_nop 1\n\t"
        ".purgem VPK\n\t.purgem VRD\n\t.purgem VMM\n\t.purgem VADR\n\t.purgem VOUT"
        : "=&{v[64:95]}"(t0), "=&{v[96:127]}"(t1), "=&{v[160:191]}"(t2), "=&{v[192:223]}"(t3), [o1] "=&s"(o1), [o2] "=&s"(o2), [o3] "=&s"(o3)
        : "{v[128:159]}"(acc), "{v[224:255]}"(wva), "{v[32:63]}"(wvb), [wa] "v"(wa), [ow] "v"(ow), [ra] "v"(ra), [go] "v"(go), [srd] "s"(srd)
        : "memory");
}

__device__ __forceinline__ u64 rank_edges(u32x16& ed, u32x16& es, u32x16& rk, u64 m0, u64 m1, u64 m2, u64 m3, unsigned keya) {
    u32x16 b4; u64 over; unsigned sel = 0x01050401u, c36 = 36;
    asm volatile(
        ".macro RADD k\n\tv_xor_b32_sdwa v[96+\\k], v[64+\\k], v[64+\\k] dst_sel:DWORD dst_unused:UNUSED_PAD src0_sel:BYTE_0 src1_sel:BYTE_1\n\t"
        "v_lshlrev_b32_e32 v[96+\\k], 2, v[96+\\k]\n\t"
        "ds_add_rtn_u32 v[112+\\k], v[96+\\k], v111 offset:%c[cnt]\n\t.endm\n\t"
        ".macro RENT k\n\tv_add_u32_e32 v[80+\\k], %c[bias], v[80+\\k]\n\tv_perm_b32 v[80+\\k], v[80+\\k], v[64+\\k], %[sel]\n\t.endm\n\t"
        ".macro RPUT k\n\tv_min_u32_e32 v110, %c[cap], v[112+\\k]\n\tv_mad_u32_u24 v110, v[96+\\k], %[c36], v110\n\t"
        "ds_write_b32 v110, v[80+\\k] offset:%c[img]\n\t.endm\n\t"
        "v_mov_b32_e32 v111, 4\n\t"
        ".irp k, 0, 2, 4, 6, 8, 10, 12\n\tv_mov_b64_e32 v[112+\\k:113+\\k], 0\n\t.endr\n\t"
        "s_mov_b64 exec, %[m0]\n\ts_waitcnt vmcnt(9)\n\tRADD 0\n\tRADD 1\n\tRADD 2\n\tRADD 3\n\t"
        "s_mov_b64 exec, %[m1]\n\ts_waitcnt vmcnt(8)\n\tRADD 4\n\tRADD 5\n\tRADD 6\n\tRADD 7\n\t"
        "s_mov_b64 exec, %[m2]\n\ts_waitcnt vmcnt(7)\n\tRADD 8\n\tRADD 9\n\tRADD 10\n\tRADD 11\n\t"
        "s_mov_b64 exec, %[m3]\n\ts_waitcnt vmcnt(6)\n\tRADD 12\n\t"
        "s_mov_b64 exec, %[m0]\n\ts_waitcnt vmcnt(5)\n\tRENT 0\n\tRENT 1\n\tRENT 2\n\tRENT 3\n\t"
        "s_waitcnt lgkmcnt(9)\n\tRPUT 0\n\tRPUT 1\n\tRPUT 2\n\tRPUT 3\n\t"
        "s_mov_b64 exec, %[m1]\n\ts_waitcnt vmcnt(4)\n\tRENT 4\n\tRENT 5\n\tRENT 6\n\tRENT 7\n\t"
        "s_waitcnt lgkmcnt(9)\n\tRPUT 4\n\tRPUT 5\n\tRPUT 6\n\tRPUT 7\n\t"
        "s_mov_b64 exec, %[m2]\n\ts_waitcnt vmcnt(3)\n\tRENT 8\n\tRENT 9\n\tRENT 10\n\tRENT 11\n\t"
        "s_waitcnt lgkmcnt(9)\n\tRPUT 8\n\tRPUT 9\n\tRPUT 10\n\tRPUT 11\n\t"
        "s_mov_b64 exec, %[m3]\n\ts_waitcnt vmcnt(0)\n\tRENT 12\n\t"
        "s_waitcnt lgkmcnt(12)\n\tRPUT 12\n\t"
        "s_mov_b64 exec, -1\n\t"
        "v_add_u32_e32 v93, %c[step], v93\n\t"
        "v_max3_u32 v110, v112, v113, v114\n\tv_max3_u32 v109, v115, v116, v117\n\t"
        "ds_write_b32 %[keya], v93 offset:%c[cnt]+1024\n\t"
        "v_max3_u32 v110, v110, v118, v119\n\tv_max3_u32 v109, v109, v120, v121\n\t"
        "v_max3_u32 v110, v110, v122, v123\n\tv_max3_u32 v110, v110, v109, v124\n\t"
        "v_cmp_le_u32_e32 vcc, %c[cap], v110\n\ts_mov_b64 %[over], vcc\n\t"
        ".purgem RADD\n\t.purgem RENT\n\t.purgem RPUT"
        : "+{v[64:79]}"(ed), "+{v[80:95]}"(es), "=&{v[96:111]}"(b4), "=&{v[112:127]}"(rk), [over] "=&s"(over)
        : [m0] "s"(m0), [m1] "s"(m1), [m2] "s"(m2), [m3] "s"(m3), [keya] "v"(keya), [sel] "s"(sel), [c36] "s"(c36),
          [cnt] "i"(L_CNT1), [img] "i"(L_IMG + 8), [bias] "i"(BIAS), [cap] "i"(4 * CAP1), [step] "i"(KEY_STEP)
        : "vcc", "memory");
    return over;
}
__device__ __forceinline__ unsigned read_image(f32x32& img, u32x16& meta, u32x16& sums, unsigned ima, unsigned cna) {
    unsigned no;
    asm volatile(
        "v_mov_b32_e32 v121, 0\n\tds_read_b32 v120, v121 offset:%c[cnt]+2048\n\t"
        ".irp k, 0, 1, 2, 3, 4, 5, 6, 7\n\tds_read_b128 v[64+4*\\k:67+4*\\k], %[ima] offset:4608*\\k\n\t.endr\n\t"
        ".irp k, 0, 1, 2, 3\n\tds_read2_b32 v[96+2*\\k:97+2*\\k], %[cna] offset0:64*\\k offset1:64*\\k+32\n\t.endr\n\t"
        "s_waitcnt lgkmcnt(11)\n\tv_readfirstlane_b32 %[no], v120\n\t"
        "v_add_u32_e32 v121, 0x400, %[cna]\n\t"
        ".irp k, 0, 1, 2, 3\n\tds_read2_b32 v[104+2*\\k:105+2*\\k], v121 offset0:64*\\k offset1:64*\\k+32\n\t.endr\n\t"
        "s_waitcnt lgkmcnt(12)\n\t"
        ".irp k, 0, 1, 2, 3\n\tv_add3_u32 v[112+\\k], v[64+4*\\k], v[65+4*\\k], v[66+4*\\k]\n\tv_add_u32_e32 v[112+\\k], v[112+\\k], v[67+4*\\k]\n\t.endr\n\t"
        "s_waitcnt lgkmcnt(8)\n\t"
        ".irp k, 4, 5, 6, 7\n\tv_add3_u32 v[112+\\k], v[64+4*\\k], v[65+4*\\k], v[66+4*\\k]\n\tv_add_u32_e32 v[112+\\k], v[112+\\k], v[67+4*\\k]\n\t.endr\n\t"
        ".irp k, 0, 1, 2, 3, 4, 5, 6, 7\n\tv_add_u32_dpp v[112+\\k], v[112+\\k], v[112+\\k] quad_perm:[1,0,3,2] row_mask:0xf bank_mask:0xf\n\t.endr\n\t"
        ".irp k, 0, 1, 2, 3, 4, 5, 6, 7\n\tv_add_u32_dpp v[112+\\k], v[112+\\k], v[112+\\k] quad_perm:[2,3,0,1] row_mask:0xf bank_mask:0xf\n\t.endr\n\t"
        ".irp k, 0, 1, 2, 3, 4, 5, 6, 7\n\tv_add_u32_dpp v[112+\\k], v[112+\\k], v[112+\\k] row_half_mirror row_mask:0xf bank_mask:0xf\n\t.endr\n\t"
        "s_waitcnt lgkmcnt(0)"
        : "=&{v[64:95]}"(img), "=&{v[96:111]}"(meta), "=&{v[112:127]}"(sums), [no] "=&s"(no)
        : [ima] "v"(ima), [cna] "v"(cna), [cnt] "i"(L_CNT1) : "memory");
    return no;
}
__device__ __forceinline__ void publish_image(f32x32 img, u32x16 meta, u32x16 sums, const u32x4& srd, unsigned ob) {
    u64 pc0 = 0x0101010101010101ull; unsigned so;
    asm volatile(
        ".macro PUB k\n\ts_mov_b64 exec, %[pc0]\n\tv_mov_b32_e32 v[64+4*\\k], v[96+\\k]\n\tv_mov_b32_e32 v[65+4*\\k], v[112+\\k]\n\t"
        "s_mov_b64 exec, -1\n\tv_xor_b32_e32 v[64+4*\\k], v[64+4*\\k], v[104+\\k]\n\t"
        "buffer_store_dwordx4 v[64+4*\\k:67+4*\\k], %[ob], %[srd], %[so] offen sc1\n\ts_add_u32 %[so], %[so], %c[step]\n\t.endm\n\t"
        "s_mov_b32 %[so], 0\n\t"
        ".irp k, 0, 1, 2, 3, 4, 5, 6, 7\n\tPUB \\k\n\t.endr\n\t.purgem PUB"
        : "+{v[64:95]}"(img), [so] "=&s"(so)
        : "{v[96:111]}"(meta), "{v[112:127]}"(sums), [srd] "s"(srd), [ob] "v"(ob), [pc0] "s"(pc0), [step] "i"(32 * G1 * 128) : "scc", "memory");
}

__device__ __forceinline__ void inbox_request(u32x16& raw, const u32x4& srd, unsigned ib) {
    unsigned o1 = 0x1000, o2 = 0x2000, o3 = 0x3000;
    asm volatile("buffer_load_dwordx4 v[64:67], %1, %2, 0 offen sc1\n\tbuffer_load_dwordx4 v[68:71], %1, %2, %3 offen sc1\n\t"
                 "buffer_load_dwordx4 v[72:75], %1, %2, %4 offen sc1\n\tbuffer_load_dwordx4 v[76:79], %1, %2, %5 offen sc1"
                 : "=&{v[64:79]}"(raw) : "v"(ib), "s"(srd), "s"(o1), "s"(o2), "s"(o3) : "memory");
}
__device__ __forceinline__ unsigned inbox_stage(u32x16 raw, const u32x4& srd, unsigned ib, unsigned key, int i4, unsigned hdra) {
    u32x16 t0; f32x32 t2; unsigned four = 4; u64 m0, m1, m2, m3; unsigned todo, fresh, spin, o1, o2, o3, over;
    u64 pc0 = 0x0101010101010101ull;
    asm volatile(
        ".macro IVAL j, so\n\t"
        "s_bitcmp1_b32 %[todo], \\j\n\ts_cbranch_scc0 2f\n\t"
        "v_xor_b32_e32 v[64+4*\\j], %[key], v[64+4*\\j]\n\t"
        "v_sub_u32_e32 v97, 0, v[65+4*\\j]\n\t"
        "v_add_u32_e32 v96, v[64+4*\\j], v[65+4*\\j]\n\t"
        "v_mov_b32_dpp v[80+\\j], v[64+4*\\j] quad_perm:[0,0,0,0] row_mask:0xf bank_mask:0xf\n\t"
        "v_cndmask_b32_e64 v96, v96, v97, %[pc0]\n\t"
        "s_nop 0\n\t"
        "v_mov_b32_dpp v[80+\\j], v[80+\\j] row_shr:4 row_mask:0xf bank_mask:0xa\n\t"
        "v_cmp_gt_i32_e32 vcc, v[80+\\j], v92\n\t"
        "v_add3_u32 v96, v96, v[66+4*\\j], v[67+4*\\j]\n\t"
        "v_lshrrev_b32_e32 v97, 14, v[80+\\j]\n\t"
        "v_cndmask_b32_e32 v96, 0, v96, vcc\n\t"
        "s_nop 1\n\tv_add_u32_dpp v96, v96, v96 quad_perm:[1,0,3,2] row_mask:0xf bank_mask:0xf\n\t"
        "s_nop 1\n\tv_add_u32_dpp v96, v96, v96 quad_perm:[2,3,0,1] row_mask:0xf bank_mask:0xf\n\t"
        "s_nop 1\n\tv_add_u32_dpp v96, v96, v96 row_half_mirror row_mask:0xf bank_mask:0xf\n\t"
        "v_or_b32_e32 v96, v96, v97\n\t"
        "v_cmp_ne_u32_e32 vcc, 0, v96\n\t"
        "s_cbranch_vccz 1f\n\t"
        "buffer_load_dwordx4 v[64+4*\\j:67+4*\\j], %[ib], %[srd], \\so offen sc1\n\t"
        "s_branch 2f\n\t"
        "1:\n\ts_bitset1_b32 %[fresh], \\j\n\t"
        "2:\n\t.endm\n\t"
        ".macro IRANK j\n\t"
        "s_bitcmp1_b32 %[fresh], \\j\n\ts_cbranch_scc0 2f\n\t"
        "v_cmp_lt_u32_e64 %[m0], v92, v[80+\\j]\n\tv_cmp_lt_u32_e64 %[m1], v93, v[80+\\j]\n\t"
        "v_cmp_lt_u32_e64 %[m2], v94, v[80+\\j]\n\tv_cmp_lt_u32_e64 %[m3], v95, v[80+\\j]\n\t"
        ".irp k, 0, 1, 2, 3\n\tv_lshrrev_b32_e32 v[84+\\k], 21, v[64+4*\\j+\\k]\n\t.endr\n\t"
        ".irp k, 0, 1, 2, 3\n\tv_and_b32_e32 v[84+\\k], 60, v[84+\\k]\n\t.endr\n\t"
        "v_cmp_lt_u32_e32 vcc, %c[cap], v[80+\\j]\n\t"
        "s_mov_b64 exec, %[m0]\n\tds_add_rtn_u32 v88, v84, %[four] offset:%c[cnt]\n\t"
        "s_mov_b64 exec, %[m1]\n\tds_add_rtn_u32 v89, v85, %[four] offset:%c[cnt]\n\t"
        "s_mov_b64 exec, %[m2]\n\tds_add_rtn_u32 v90, v86, %[four] offset:%c[cnt]\n\t"
        "s_mov_b64 exec, %[m3]\n\tds_add_rtn_u32 v91, v87, %[four] offset:%c[cnt]\n\t"
        "s_mov_b64 exec, %[pc0]\n\tds_write_b32 %[hdra], v[80+\\j] offset:128*\\j\n\t"
        "s_mov_b64 exec, -1\n\t"
        "s_cbranch_vccz 1f\n\t"
        "s_mov_b32 %[over], 1\n\t"
        "1:\n\ts_waitcnt lgkmcnt(1)\n\t"
        ".irp k, 0, 1, 2, 3\n\tv_min_u32_e32 v[88+\\k], %c[sub]-4, v[88+\\k]\n\t.endr\n\t"
        ".irp k, 0, 1, 2, 3\n\tv_mad_u32_u24 v[88+\\k], v[84+\\k], %[sub4], v[88+\\k]\n\t.endr\n\t"
        "s_mov_b64 exec, %[m0]\n\tds_write_b32 v88, v[64+4*\\j] offset:%c[list]\n\t"
        "s_mov_b64 exec, %[m1]\n\tds_write_b32 v89, v[65+4*\\j] offset:%c[list]\n\t"
        "s_mov_b64 exec, %[m2]\n\tds_write_b32 v90, v[66+4*\\j] offset:%c[list]\n\t"
        "s_mov_b64 exec, %[m3]\n\tds_write_b32 v91, v[67+4*\\j] offset:%c[list]\n\t"
        "s_mov_b64 exec, -1\n\t"
        "2:\n\t.endm\n\t"
        ".macro FADD j\n\t"
        ".irp k, 0, 1, 2, 3\n\tv_lshrrev_b32_e32 v[96+4*\\j+\\k], 21, v[64+4*\\j+\\k]\n\t.endr\n\t"
        ".irp k, 0, 1, 2, 3\n\tv_and_b32_e32 v[96+4*\\j+\\k], 60, v[96+4*\\j+\\k]\n\t.endr\n\t"
        "v_cmpx_lt_u32_e64 s[72+8*\\j:73+8*\\j], v94, v[80+\\j]\n\tds_add_rtn_u32 v[114+4*\\j], v[98+4*\\j], %[four] offset:%c[cnt]\n\t"
        "v_cmpx_lt_u32_e64 s[74+8*\\j:75+8*\\j], v95, v[80+\\j]\n\tds_add_rtn_u32 v[115+4*\\j], v[99+4*\\j], %[four] offset:%c[cnt]\n\t"
        "s_mov_b64 exec, -1\n\t"
        "v_cmpx_lt_u32_e64 s[68+8*\\j:69+8*\\j], v92, v[80+\\j]\n\tds_add_rtn_u32 v[112+4*\\j], v[96+4*\\j], %[four] offset:%c[cnt]\n\t"
        "v_cmpx_lt_u32_e64 s[70+8*\\j:71+8*\\j], v93, v[80+\\j]\n\tds_add_rtn_u32 v[113+4*\\j], v[97+4*\\j], %[four] offset:%c[cnt]\n\t"
        "s_mov_b64 exec, -1\n\t.endm\n\t"
        ".macro FPUT j\n\t"
        "s_waitcnt lgkmcnt(12)\n\t"
        ".irp k, 0, 1, 2, 3\n\tv_min_u32_e32 v[112+4*\\j+\\k], %c[sub]-4, v[112+4*\\j+\\k]\n\t.endr\n\t"
        ".irp k, 0, 1, 2, 3\n\tv_mad_u32_u24 v[112+4*\\j+\\k], v[96+4*\\j+\\k], %[sub4], v[112+4*\\j+\\k]\n\t.endr\n\t"
        ".irp k, 0, 1, 2, 3\n\ts_mov_b64 exec, s[68+8*\\j+2*\\k:69+8*\\j+2*\\k]\n\tds_write_b32 v[112+4*\\j+\\k], v[64+4*\\j+\\k] offset:%c[list]\n\t.endr\n\t"
        "s_mov_b64 exec, -1\n\t.endm\n\t"
        "s_movk_i32 %[o1], 0x1000\n\ts_movk_i32 %[o2], 0x2000\n\ts_movk_i32 %[o3], 0x3000\n\t"
        "v_mov_b32_e32 v92, %[i4]\n\tv_add_u32_e32 v93, 4, %[i4]\n\tv_add_u32_e32 v94, 8, %[i4]\n\tv_add_u32_e32 v95, 12, %[i4]\n\t"
        "s_mov_b32 %[todo], 15\n\ts_mov_b32 %[spin], 0x400000\n\ts_mov_b32 %[over], 0\n\t"
        ".if " STR(FASTIN) "\n\t"
        "s_waitcnt vmcnt(0)\n\t"
        ".irp j, 0, 1, 2, 3\n\tv_xor_b32_e32 v[64+4*\\j], %[key], v[64+4*\\j]\n\t.endr\n\t"
        ".irp j, 0, 1, 2, 3\n\tv_mov_b32_dpp v[80+\\j], v[64+4*\\j] quad_perm:[0,0,0,0] row_mask:0xf bank_mask:0xf\n\t.endr\n\t"
        ".irp j, 0, 1, 2, 3\n\tv_add_u32_e32 v[84+\\j], v[64+4*\\j], v[65+4*\\j]\n\t.endr\n\t"
        ".irp j, 0, 1, 2, 3\n\tv_mov_b32_dpp v[80+\\j], v[80+\\j] row_shr:4 row_mask:0xf bank_mask:0xa\n\t.endr\n\t"
        "s_mov_b64 exec, %[pc0]\n\t"
        ".irp j, 0, 1, 2, 3\n\tv_sub_u32_e32 v[84+\\j], 0, v[65+4*\\j]\n\t.endr\n\t"
        "s_mov_b64 exec, -1\n\t"
        "v_cmp_gt_i32_e64 %[m0], v80, v92\n\tv_cmp_gt_i32_e64 %[m1], v81, v92\n\tv_cmp_gt_i32_e64 %[m2], v82, v92\n\tv_cmp_gt_i32_e64 %[m3], v83, v92\n\t"
        ".irp j, 0, 1, 2, 3\n\tv_add3_u32 v[84+\\j], v[84+\\j], v[66+4*\\j], v[67+4*\\j]\n\t.endr\n\t"
        "v_cndmask_b32_e64 v84, 0, v84, %[m0]\n\tv_cndmask_b32_e64 v85, 0, v85, %[m1]\n\tv_cndmask_b32_e64 v86, 0, v86, %[m2]\n\tv_cndmask_b32_e64 v87, 0, v87, %[m3]\n\t"
        ".irp j, 0, 1, 2, 3\n\tv_add_u32_dpp v[84+\\j], v[84+\\j], v[84+\\j] quad_perm:[1,0,3,2] row_mask:0xf bank_mask:0xf\n\t.endr\n\t"
        ".irp j, 0, 1, 2, 3\n\tv_add_u32_dpp v[84+\\j], v[84+\\j], v[84+\\j] quad_perm:[2,3,0,1] row_mask:0xf bank_mask:0xf\n\t.endr\n\t"
        ".irp j, 0, 1, 2, 3\n\tv_add_u32_dpp v[84+\\j], v[84+\\j], v[84+\\j] row_half_mirror row_mask:0xf bank_mask:0xf\n\t.endr\n\t"
        "v_or3_b32 v96, v80, v81, v82\n\tv_max3_u32 v97, v80, v81, v82\n\tv_or_b32_e32 v96, v96, v83\n\tv_max_u32_e32 v97, v97, v83\n\t"
        "v_lshrrev_b32_e32 v96, 14, v96\n\tv_or3_b32 v96, v96, v84, v85\n\tv_or3_b32 v96, v96, v86, v87\n\t"
        "v_cmp_ne_u32_e32 vcc, 0, v96\n\t"
        "s_cbranch_vccnz 7f\n\t"
        "v_cmp_lt_u32_e32 vcc, %c[cap], v97\n\t"
        "FADD 0\n\tFADD 1\n\tFADD 2\n\tFADD 3\n\t"
        "s_cbranch_vccz 6f\n\ts_mov_b32 %[over], 1\n\t6:\n\t"
        "FPUT 0\n\tFPUT 1\n\tFPUT 2\n\tFPUT 3\n\t"
        "s_mov_b64 exec, %[pc0]\n\t"
        ".irp j, 0, 1, 2, 3\n\tds_write_b32 %[hdra], v[80+\\j] offset:128*\\j\n\t.endr\n\t"
        "s_mov_b64 exec, -1\n\t"
        "s_branch 9f\n\t"
        "7:\n\t.irp j, 0, 1, 2, 3\n\tv_xor_b32_e32 v[64+4*\\j], %[key], v[64+4*\\j]\n\t.endr\n\t"
        ".endif\n\t"
        "0:\n\t"
        "s_mov_b32 %[fresh], 0\n\t"
        "s_waitcnt vmcnt(0)\n\t"
        "IVAL 0, 0\n\tIVAL 1, %[o1]\n\tIVAL 2, %[o2]\n\tIVAL 3, %[o3]\n\t"
        "IRANK 0\n\tIRANK 1\n\tIRANK 2\n\tIRANK 3\n\t"
        "s_andn2_b32 %[todo], %[todo], %[fresh]\n\t"
        "s_cbranch_scc0 9f\n\t"
        "s_sub_u32 %[spin], %[spin], 1\n\t"
        "s_cbranch_scc0 0b\n\t"
        "s_waitcnt vmcnt(0)\n\t"
        "9:\n\t"
        ".purgem IVAL\n\t.purgem IRANK\n\t.purgem FADD\n\t.purgem FPUT"
        : "+{v[64:79]}"(raw), "=&{v[80:95]}"(t0), "=&{v[96:127]}"(t2), [m0] "=&s"(m0), [m1] "=&s"(m1), [m2] "=&s"(m2), [m3] "=&s"(m3),
          [todo] "=&s"(todo), [fresh] "=&s"(fresh), [spin] "=&s"(spin), [o1] "=&s"(o1), [o2] "=&s"(o2), [o3] "=&s"(o3), [over] "=&s"(over)
        : [srd] "s"(srd), [ib] "v"(ib), [key] "s"(key), [i4] "v"(i4), [hdra] "v"(hdra), [pc0] "s"(pc0), [sub4] "s"(SUBB / 4), [four] "v"(four),
          [cnt] "i"(L_CNT), [list] "i"(L_LIST), [sub] "i"(SUBB), [cap] "i"(4 * CAP1)
        : "vcc", "scc", "memory", "s68", "s69", "s70", "s71", "s72", "s73", "s74", "s75", "s76", "s77", "s78", "s79", "s80", "s81", "s82", "s83",
          "s84", "s85", "s86", "s87", "s88", "s89", "s90", "s91", "s92", "s93", "s94", "s95", "s96", "s97", "s98", "s99");
    return over;
}

template <int CTRL, int BANK> __device__ __forceinline__ unsigned dpp(unsigned old, unsigned v) { return __builtin_amdgcn_update_dpp(old, v, CTRL, 0xF, BANK, true); }
__device__ __forceinline__ unsigned bcast8(unsigned v) { v = dpp<0x00, 0xF>(0u, v); return __builtin_amdgcn_update_dpp(v, v, 0x114, 0xF, 0xA, false); }
__device__ __forceinline__ unsigned sum8(unsigned v) { v += dpp<0xB1, 0xF>(0u, v); v += dpp<0x4E, 0xF>(0u, v); return v + dpp<0x141, 0xF>(0u, v); }
__device__ __forceinline__ bf16x4 tr_read(LAS char* p) { return __builtin_amdgcn_ds_read_tr16_b64_v4bf16((LAS bf16x4*)p); }
__device__ __forceinline__ bf16x8 cat(bf16x4 a, bf16x4 b) { return __builtin_shufflevector(a, b, 0, 1, 2, 3, 4, 5, 6, 7); }
__device__ __forceinline__ unsigned pk2(float a, float b) { f32x2 v = {a, b}; bf16x2 r = __builtin_convertvector(v, bf16x2); return __builtin_bit_cast(unsigned, r); }
__device__ __forceinline__ f32x4 mfma(bf16x8 a, bf16x8 b, f32x4 c) { return __builtin_amdgcn_mfma_f32_16x16x32_bf16(a, b, c, 0, 0, 0); }
__device__ __forceinline__ unsigned lds_add(LAS char* p, unsigned v) { return __hip_atomic_fetch_add((LAS unsigned*)p, v, __ATOMIC_RELAXED, __HIP_MEMORY_SCOPE_WORKGROUP); }
__device__ __forceinline__ LAS unsigned* ldsp(unsigned a) { return (LAS unsigned*)(size_t)a; }
__device__ __forceinline__ unsigned byte0_x4(unsigned v) { unsigned r; asm("v_lshlrev_b32_sdwa %0, 2, %1 dst_sel:DWORD dst_unused:UNUSED_PAD src0_sel:DWORD src1_sel:BYTE_0" : "=v"(r) : "v"(v)); return r; }
__device__ __forceinline__ unsigned mad24(unsigned a, unsigned b, unsigned c) { unsigned r; asm("v_mad_u32_u24 %0, %1, %2, %3" : "=v"(r) : "v"(a), "s"(b), "v"(c)); return r; }
__device__ __forceinline__ void lds_arrive(LAS char* p, int lane) {
    asm volatile("s_waitcnt lgkmcnt(0)" ::: "memory");
    if (lane == 0) __hip_atomic_fetch_add((LAS unsigned*)p, 1u, __ATOMIC_RELAXED, __HIP_MEMORY_SCOPE_WORKGROUP);
}
__device__ __forceinline__ void lds_await(LAS char* p, unsigned n) {
    while (__builtin_amdgcn_readfirstlane(*(volatile LAS unsigned*)p) < n) __builtin_amdgcn_s_sleep(1);
    asm volatile("" ::: "memory");
}
__device__ __forceinline__ unsigned rfl(unsigned v) { return __builtin_amdgcn_readfirstlane(v); }

__global__ __launch_bounds__(512, 2) void k_graph(const unsigned short* x_, const int* src_, const int* dst_, const unsigned short* wq_, const unsigned short* wv_,
                                                  unsigned* ws_, unsigned short* out_, int N_, int E_) {
    u32x4 srd_ws, srd_x0, pw, po;
    u32x16 ed, es;
    u64 t_start;
    asm volatile("s_load_dwordx2 s[40:41], %[ka], 0x10\n\ts_load_dwordx2 s[44:45], %[ka], 0x8\n\ts_load_dwordx4 s[60:63], %[ka], 0x30\n\t"
                 "s_getpc_b64 s[68:69]\n\ts_movk_i32 s70, " STR(CODE_BYTES) "\n\ts_mov_b32 s71, 0x20000\n\ts_mov_b32 m0, %c[dump]\n\t"
                 "s_movk_i32 s66, 0x8000\n\tv_lshl_add_u32 v77, %[t], 7, s66\n\t"
                 "buffer_load_dword v77, s[68:71], 0 offen lds\n\t"
                 "s_load_dwordx2 s[48:49], %[ka], 0x28\n\ts_load_dwordx2 s[52:53], %[ka], 0x0\n\ts_load_dwordx4 s[56:59], %[ka], 0x18\n\t"
                 "s_memrealtime s[64:65]\n\t"
                 "v_readfirstlane_b32 s72, %[t]\n\t"
                 "s_and_b32 s66, %[b], 7\n\ts_lshl_b32 s66, s66, 4\n\ts_lshr_b32 s67, %[b], 4\n\ts_or_b32 s66, s66, s67\n\ts_mulk_i32 s66, %c[ch4]\n\t"
                 "s_mov_b32 s43, s71\n\ts_mov_b32 s47, s71\n\t"
                 "v_lshl_add_u32 v77, %[t], 4, s66\n\t"
                 "s_lshr_b32 s67, s72, 5\n\ts_or_b32 s67, s67, %[b]\n\t"
                 "s_movk_i32 s73, 0x1000\n\ts_movk_i32 s74, 0x2000\n\t"
                 "s_bitcmp1_b32 s67, 3\n\ts_waitcnt lgkmcnt(0)\n\ts_cbranch_scc1 1f\n\t"
                 "s_lshl_b32 s42, s63, 2\n\ts_lshl_b32 s46, s63, 2\n\t"
                 "buffer_load_dwordx4 v[64:67], v77, s[40:43], 0 offen\n\tbuffer_load_dwordx4 v[68:71], v77, s[40:43], s73 offen\n\t"
                 "buffer_load_dwordx4 v[72:75], v77, s[40:43], s74 offen\n\t"
                 "s_add_u32 s66, s66, 0x3000\n\ts_cmp_lt_u32 s72, 64\n\ts_cselect_b32 s66, s66, %c[oob]\n\t"
                 "v_lshl_add_u32 v78, %[t], 2, s66\n\t"
                 "buffer_load_dword v76, v78, s[40:43], 0 offen\n\t"
                 "buffer_load_dwordx4 v[80:83], v77, s[44:47], 0 offen\n\tbuffer_load_dwordx4 v[84:87], v77, s[44:47], s73 offen\n\t"
                 "buffer_load_dwordx4 v[88:91], v77, s[44:47], s74 offen\n\tbuffer_load_dword v92, v78, s[44:47], 0 offen\n\t"
                 "s_mov_b32 s51, s71\n\ts_mov_b32 s50, %c[wsb]\n\ts_lshl_b32 s66, %[b], 2\n\t"
                 "v_lshlrev_b32_e32 v77, 2, %[t]\n\tv_mov_b32_e32 v78, s66\n\t"
                 "buffer_load_dword v93, v77, s[48:51], 0 offen sc1\n\tbuffer_load_dword v94, v78, s[48:51], 0 offen sc1\n\t"
                 "1:\n\ts_mov_b32 s51, s71\n\ts_mov_b32 s50, %c[wsb]\n\ts_mov_b32 s55, s71\n\ts_lshl_b32 s54, s62, 8"
                 : "=&{v[64:79]}"(ed), "=&{v[80:95]}"(es)
                 : [ka] "s"(__builtin_amdgcn_kernarg_segment_ptr()), [t] "v"(threadIdx.x), [b] "s"(blockIdx.x),
                   [wsb] "i"(WS_BYTES), [dump] "i"(L_DUMP), [ch4] "i"(CH * 4), [oob] "i"(OOB)
                 : "memory", "scc", "s40", "s41", "s42", "s43", "s44", "s45", "s46", "s47", "s48", "s49", "s50", "s51", "s52", "s53", "s54", "s55",
                   "s56", "s57", "s58", "s59", "s60", "s61", "s62", "s63", "s64", "s65", "s66", "s67", "s68", "s69", "s70", "s71", "s72", "s73", "s74");
    asm volatile("" : "={s[48:51]}"(srd_ws), "={s[52:55]}"(srd_x0), "={s[56:59]}"(pw), "={s[60:63]}"(po), "={s[64:65]}"(t_start));
    FENCE();
    __shared__ __attribute__((aligned(16))) char smem_[L_TOTAL];
    LAS char* smem = (LAS char*)smem_;
    const int t = threadIdx.x, l = t & 63, b = blockIdx.x;
    const int w = rfl(t >> 6);
    const int g = l >> 4, n = l & 15, q = (l >> 2) & 3, p = l & 3, pc = t & 7;
    const unsigned lds0 = (unsigned)(size_t)smem;
    const bool isprod = ((b >> 3) & 1) == 0;
    const int pg = ((b & 7) << 4) | (b >> 4);
    const unsigned e0 = pg * CH + 4 * t, e1 = pg * CH + 3072 + t;

    const void* const x = (const void*)((u64)srd_x0.x | ((u64)srd_x0.y << 32));
    const void* const wq = (const void*)((u64)pw.x | ((u64)pw.y << 32));
    const void* const wv = (const void*)((u64)pw.z | ((u64)pw.w << 32));
    void* const out = (void*)((u64)po.x | ((u64)po.y << 32));
    const int N = po.z, E = po.w;
    const unsigned xbytes = srd_x0.z;
    unsigned* const ws = (unsigned*)((u64)srd_ws.x | ((u64)srd_ws.y << 32));
    STAMP_DECL;
    st_[0] = t_start;
    const unsigned ring = lds0 + L_RING + w * 16384;
    const int jn = 8 * n + w;

    unsigned mykey = 0;
    auto node_row = [&](int tile, int m) { const unsigned j = 8 * (4 * m + g) + tile; return (j << 16) | ((b ^ j) << 8); };
    auto zero_staging = [&]() {
        const u32x4 z = {0u, 0u, 0u, 0u};
#pragma unroll
        for (int k = 0; k < 5; ++k) *(LAS u32x4*)(smem + L_IMG + k * 8192 + t * 16) = z;
    };

    u32x16 addr;
    f32x32 acc, wva, wvb;
    unsigned cv, lista, cnta, qa, keyn, inf, ow, go;
    Srd4 srd_x;
    auto loop_constants = [&]() {
        srd_x = make_srd4(x, xbytes, BIAS * 256);
        lista = lds0 + L_LIST + w * (2 * SUBB) + g * 4; cnta = lds0 + L_CNT + (l & 31) * 4; qa = ring + l * 16; keyn = jn; inf = 0x7F800000u;
        ow = ring + n * 256 + ((g ^ n) << 4) + ((g & 1) << 3); go = node_row(w, 0) + ((n ^ g) << 4);
        asm volatile("" : "+s"(srd_x.s0), "+s"(srd_x.s1), "+s"(srd_x.s2), "+s"(srd_x.s3));
        asm volatile("" : "+v"(lista), "+v"(cnta), "+v"(qa), "+v"(keyn), "+v"(inf), "+v"(ow), "+v"(go));
        const unsigned o0 = (n ^ (((g & 1) << 3) | (g >> 1))) << 4;
        const unsigned ra = (4 * (n & 3) + (n >> 2)) * 256 + (((4 * g) ^ (((n & 7) << 1) | (n >> 3))) << 4);
        const int i = 4 * g + q, r8 = i & 7;
        const unsigned ta = (4 * q + g) * 256 + 8 * (p & 1) + (((p >> 1) ^ (((i & 7) << 1) | (i >> 3))) << 4);
        cv = lds0 + L_WV1 + i * 256 + (((p ^ ((r8 & 1) | ((r8 & 6) << 1))) << 4) | ((p & 1) << 3));
#pragma unroll
        for (int s = 0; s < 4; ++s) { addr[s] = ring + (ra ^ (s << 4)); addr[12 + s] = o0 ^ (s << 5); }
#pragma unroll
        for (int c = 0; c < 8; ++c) addr[4 + c] = ring + (ta ^ (c << 5));
        asm volatile("" : "+{v[176:191]}"(addr), "+v"(cv));
    };
    auto clear_sums = [&]() { acc = 0.f; asm volatile("" : "+{v[128:159]}"(acc)); };
    auto wv_resident = [&]() {
#pragma unroll
        for (int a = 0; a < 8; ++a) {
            LAS char* pa = (LAS char*)(size_t)((cv - (L_WV1 - L_WV0)) ^ ((a >> 1) << 6) ^ ((a & 1) << 3));
            const f32x4 f0 = __builtin_bit_cast(f32x4, cat(tr_read(pa), tr_read(pa + 4096)));
            const f32x4 f1 = __builtin_bit_cast(f32x4, cat(tr_read(pa + 16384), tr_read(pa + 16384 + 4096)));
#pragma unroll
            for (int k = 0; k < 4; ++k) { wva[4 * a + k] = f0[k]; wvb[4 * a + k] = f1[k]; }
        }
        asm volatile("" : "+{v[224:255]}"(wva), "+{v[32:63]}"(wvb));
    };

    if (isprod) zero_staging();
    if (t < 8) *(LAS u32x4*)(smem + L_CNT + t * 16) = u32x4{0u, 0u, 0u, 0u};
    WG_BARRIER();

    if (__builtin_expect(w < 4, 1)) {
        if (__builtin_expect(isprod, 1)) {
            if (PRIO) __builtin_amdgcn_s_setprio(2);
            unsigned ob = WS_INBOX + ((t >> 3) * G1 + pg) * 128 + pc * 16, ima = lds0 + L_IMG + (t >> 3) * 144 + pc * 16;
            PIN(ob); PIN(ima);
            unsigned cna = lds0 + L_CNT1 + (t >> 3) * 4;
            PIN(cna);
            const bool v0 = e0 < (unsigned)E, v1 = e0 + 1024 < (unsigned)E, v2 = e0 + 2048 < (unsigned)E, v3 = w == 0 && e1 < (unsigned)E;
            STAMP(1);
            u32x16 rk;
            const u64 over = rank_edges(ed, es, rk, __builtin_amdgcn_ballot_w64(v0), __builtin_amdgcn_ballot_w64(v1), __builtin_amdgcn_ballot_w64(v2),
                                        __builtin_amdgcn_ballot_w64(v3), t * 4);
            mykey = es[14];
            if (__builtin_expect(over != 0, 0)) {
#pragma unroll
                for (int k = 0; k < 13; ++k)
                    if (rk[k] >= 4u * CAP1) {
                        const unsigned o = WS_OVF + (pg * CH + lds_add(smem + L_CNT1 + 2048, 1)) * 4;
                        const unsigned v = ((((es[k] >> 8) & 0xFFFF) - BIAS) << 16) | ed[k];
                        ST1(v, srd_ws, o, " sc1");
                    }
            }
            if (XSYNC) { lds_arrive(smem + L_CNT + 96, l); lds_await(smem + L_CNT + 96, 4); } else WG_BARRIER();
            STAMP(3);
            f32x32 img; u32x16 meta, sums;
            const unsigned no = read_image(img, meta, sums, ima, cna);
            lds_arrive(smem + L_CNT + 84, l);
            if (__builtin_expect(no != 0, 0)) {
                const unsigned o = t == 0 ? WS_NOVF + pg * 4 : OOB;
                ST1(no, srd_ws, o, " sc1");
                VMWAIT(0);
                if (XSYNC) { lds_arrive(smem + L_CNT + 100, l); lds_await(smem + L_CNT + 100, 4); } else WG_BARRIER();
            }
            publish_image(img, meta, sums, srd_ws, ob);
            if (XAFTER) lds_arrive(smem + L_CNT + 104, l);
            STAMP(4);
        } else {
            { const unsigned om = WS_KEY + b * 4; asm volatile("buffer_load_dword %0, %1, %2, 0 offen sc1\n\ts_waitcnt vmcnt(0)" : "=&v"(mykey) : "v"(om), "s"(srd_ws) : "memory"); }
            STAMP(1);
            if (PARTNER) {
                set_m0(lds0 + L_DUMP + w * 256);
                const unsigned o = (((b - 8) ^ (t >> 1)) << 8) + ((t >> 1) << 16) + ((t & 1) << 7);
                while (__builtin_amdgcn_s_memrealtime() < t_start + X_T) __builtin_amdgcn_s_sleep(1);
                asm volatile("buffer_load_dword %1, %0, 0 offen lds" :: "s"(srd_x0), "v"(o) : "memory");
            }
            if (PRIO) __builtin_amdgcn_s_setprio(2);
            STAMP(2); STAMP(3); STAMP(4);
        }
        STAMP(5);
        mykey = rfl(mykey) + KEY_STEP;
        loop_constants();
        clear_sums();
        unsigned ib = WS_INBOX + b * (G1 * 128) + t * 16;
        PIN(ib);
        u64 t_go = t_start + POLL_T;
        if (POLL_D) { lds_await(smem + L_CNT + 80, 4); t_go = max(t_go, __builtin_amdgcn_s_memrealtime() + POLL_D); }
        while (__builtin_amdgcn_s_memrealtime() < t_go) __builtin_amdgcn_s_sleep(1);
        STAMP(6);
        u32x16 raw;
        inbox_request(raw, srd_ws, ib);
        if (__builtin_expect(inbox_stage(raw, srd_ws, ib, mykey, 16 * pc - 8, lds0 + L_HDR + (t >> 3) * 4) != 0, 0)) {
            LAS unsigned* hp = (LAS unsigned*)(smem + L_HDR) + 32 * (l >> 3 & 3) + 8 * w + (l & 7);
            const unsigned hv = *hp;
            for (u64 m = __builtin_amdgcn_ballot_w64(l < 32 && hv > 4u * CAP1); m; m &= m - 1) {
                const int k = __builtin_ctzll(m), gp = 32 * (k >> 3) + 8 * w + (k & 7);
                unsigned u, no; const unsigned o = WS_OVF + (gp * CH + l) * 4, on = WS_NOVF + gp * 4;
                asm volatile("buffer_load_dword %0, %2, %4, 0 offen sc1\n\tbuffer_load_dword %1, %3, %4, 0 offen sc1\n\ts_waitcnt vmcnt(0)"
                             : "=&v"(u), "=&v"(no) : "v"(o), "v"(on), "s"(srd_ws) : "memory");
                if (rfl(no) <= 64) {
                    if (l < no && ((u ^ (u >> 8)) & 0xFF) == (unsigned)b) {
                        const unsigned e = __builtin_amdgcn_perm((u >> 16) + BIAS, u, 0x01050401u), key = (e >> 21) & 60;
                        *(LAS unsigned*)(smem + L_LIST + key * (SUBB / 4) + min(lds_add(smem + L_CNT + key, 4), SUBB - 4u)) = e;
                    }
                    if (l == k) *hp = hv | 0x80000000u;
                } else *(LAS unsigned*)(smem + L_CNT + 64) = 1;
            }
        }
        if (PRIO) __builtin_amdgcn_s_setprio(0);
        STAMP(7);
    } else {
        const int j = w - 4;
        auto prefetch = [&](int k0, int kn) {
            const unsigned ln = (b >> 3) * (kn / 4) + (t - 256), o = t - 256 < kn / 4 ? k0 * 1024 + ln * 128 : OOB;
            set_m0(lds0 + L_DUMP + w * 256);
            asm volatile("s_nop 0\n\tbuffer_load_dword %1, %0, 0 offen lds" :: "s"(srd_x0), "v"(o) : "memory");
        };
        STAMP(1);
        u32x4 wr[8], xr[8];
        {
            const u32x4 srd_q = make_srd(wq, 32768);
            const unsigned o = j * 8192 + l * 16, k4 = 4096;
            LD4(wr[0], srd_q, o, ""); LD4(wr[1], srd_q, o, " offset:1024"); LD4(wr[2], srd_q, o, " offset:2048"); LD4(wr[3], srd_q, o, " offset:3072");
            LD4S(wr[4], srd_q, o, k4, ""); LD4S(wr[5], srd_q, o, k4, " offset:1024"); LD4S(wr[6], srd_q, o, k4, " offset:2048"); LD4S(wr[7], srd_q, o, k4, " offset:3072");
        }
        unsigned ox[8];
#pragma unroll
        for (int m = 0; m < 8; ++m) { ox[m] = node_row(m < 4 ? j : w, m & 3) + n * 16; PIN(ox[m]); }
        unsigned aqa[8], xfa[4];
        unsigned xa = ring + g * 256 + ((n ^ g) << 4), wa = lds0 + L_WQ + j * 16384 + g * 256 + ((n ^ g) << 4) + ((n & 4) << 1);
        {
            const unsigned aq = lds0 + L_WQ + (8 * g + q) * 256 + ((((4 * p) ^ (q | ((g & 1) << 2))) << 4) | ((p & 1) << 3));
#pragma unroll
            for (int s = 0; s < 4; ++s) { xfa[s] = ring + n * 256 + (((4 * s + g) ^ n) << 4); PIN(xfa[s]); }
#pragma unroll
            for (int c = 0; c < 8; ++c) { aqa[c] = aq ^ ((c >> 1) << 4) ^ ((c & 1) << 3); PIN(aqa[c]); }
            PIN(xa); PIN(wa);
        }
        loop_constants();
        STAMP(2);
        if (isprod && !XSYNC) {
            WG_BARRIER();
            if (__builtin_expect(rfl(*(LAS unsigned*)(smem + L_CNT1 + 2048)) != 0, 0)) WG_BARRIER();
            if (W_SLEEP) __builtin_amdgcn_s_sleep(W_SLEEP);
        }
        STAMP(3);
        auto row_loads = [&]() {
#pragma unroll
            for (int m = 0; m < 8; ++m) LD4(xr[m], srd_x0, ox[m], "");
        };
        asm volatile("s_waitcnt vmcnt(0)" : "+v"(wr[0]), "+v"(wr[1]), "+v"(wr[2]), "+v"(wr[3]), "+v"(wr[4]), "+v"(wr[5]), "+v"(wr[6]), "+v"(wr[7]) :: "memory");
        if (isprod && XAFTER) lds_await(smem + L_CNT + 104, 4);
        else while (__builtin_amdgcn_s_memrealtime() < t_start + X_T) __builtin_amdgcn_s_sleep(1);
        row_loads();
#pragma unroll
        for (int m = 0; m < 8; ++m) {
            const unsigned a = (wa ^ ((m & 2) << 5)) + m * 1024;
            *(LAS u32x2*)(size_t)a = u32x2{wr[m].x, wr[m].y};
            *(LAS u32x2*)(size_t)(a ^ 8) = u32x2{wr[m].z, wr[m].w};
        }
        asm volatile("s_waitcnt vmcnt(0)" : "+v"(xr[0]), "+v"(xr[1]), "+v"(xr[2]), "+v"(xr[3]), "+v"(xr[4]), "+v"(xr[5]), "+v"(xr[6]), "+v"(xr[7]) :: "memory");
#pragma unroll
        for (int m = 0; m < 8; ++m) *(LAS u32x4*)(size_t)((xa ^ ((m & 3) << 6)) + m * 1024) = xr[m];
        STAMP(4);
        lds_arrive(smem + L_CNT + 80, l);
        lds_await(smem + L_CNT + 80, 4);
        STAMP(5);
        bf16x8 xo[2][4], fr[2][8];
        f32x4 qc[2][8];
        auto wq_frags = [&](int u, bf16x8 (&f)[8]) {
#pragma unroll
            for (int a = 0; a < 8; ++a) {
                LAS char* pa = (LAS char*)(size_t)aqa[a] + u * 16384;
                f[a] = cat(tr_read(pa), tr_read(pa + 1024));
            }
        };
#pragma unroll
        for (int u = 0; u < 4; ++u) { xo[0][u] = *(LAS bf16x8*)(size_t)xfa[u]; xo[1][u] = *(LAS bf16x8*)(size_t)(xfa[u] + 4096); }
        wq_frags(0, fr[0]);
        {
            const Srd4 srd_v = make_srd4(wv, 32768);
            const unsigned r0 = (j * 32 + g) << 8, o0 = (n ^ ((g & 1) | ((g & 2) << 1))) << 4;
            const unsigned e[8] = {r0, r0 + 1024, r0 + 2048, r0 + 3072, r0 + 4096, r0 + 5120, r0 + 6144, r0 + 7168};
            const unsigned base = lds0 + (j < 2 ? L_WV0 + j * 16384 : L_WV1 + (j - 2) * 8192);
            const u32x4 off = {o0, o0 ^ 128, o0, o0 ^ 128};
            dma_half(base, srd_v, e, off); dma_half(base + 4096, srd_v, e + 4, off);
        }
        if (PFA_KB) prefetch(0, PFA_KB);
#pragma unroll
        for (int u = 0; u < 4; ++u) {
            if (u < 3) wq_frags(u + 1, fr[(u + 1) & 1]);
            FENCE();
#pragma unroll
            for (int a = 0; a < 8; ++a)
#pragma unroll
                for (int tt = 0; tt < 2; ++tt) qc[tt][a] = mfma(fr[u & 1][a], xo[tt][u], u ? qc[tt][a] : f32x4{0.f, 0.f, 0.f, 0.f});
            FENCE();
            if (!WVLATE && u == 1 && j < 2) { VMWAIT(0); lds_arrive(smem + L_CNT + 88, l); }
        }
        STAMP(6);
        if (PFB_KB) { asm volatile("s_waitcnt vmcnt(%c0)" :: "i"(PFA_KB ? 1 : 0) : "memory"); prefetch(PFA_KB, PFB_KB); }
        if (isprod) lds_await(smem + L_CNT + 84, 4);
#pragma unroll
        for (int tt = 0; tt < 2; ++tt)
#pragma unroll
            for (int s = 0; s < 4; ++s) {
                const float k = -0.08838834764831845f * 1.4426950408889634f;
                const f32x4 c0 = qc[tt][2 * s] * k, c1 = qc[tt][2 * s + 1] * k;
                *(LAS u32x4*)(size_t)(ring - (tt ? 0 : 65536) + s * 1024 + l * 16) = u32x4{pk2(c0[0], c0[1]), pk2(c0[2], c0[3]), pk2(c1[0], c1[1]), pk2(c1[2], c1[3])};
            }
        clear_sums();
        if (!WVLATE) { lds_await(smem + L_CNT + 88, 2); wv_resident(); }
        if (!PFB_KB) asm volatile("s_waitcnt vmcnt(%c0)" :: "i"(PFA_KB ? 1 : 0) : "memory");
        if (PFC_KB) prefetch(PFA_KB + PFB_KB, PFC_KB);
        STAMP(7);
    }
    WG_BARRIER();
    STAMP(8);
    if (LPRIO >= 3) __builtin_amdgcn_s_setprio(3);
    if (LPRIO == 1 && w >= 4) __builtin_amdgcn_s_setprio(1);
    if (LPRIO == 2) { if (w >= 6) __builtin_amdgcn_s_setprio(3); else if (w >= 4) __builtin_amdgcn_s_setprio(2); else if (w >= 2) __builtin_amdgcn_s_setprio(1); }

    u32x16 qf;
    const u32x4 doff = {addr[12], addr[13], addr[14], addr[15]};
    LAS char* mylist = smem + L_LIST + w * (2 * SUBB);
    int gcur = G1, pos = 0;
    unsigned hmask = 0xFFFFFFFFu;
    auto batch = [&](auto first_, const int l) {
        constexpr bool first = decltype(first_)::value;
        auto entries = [&](int grp, unsigned (&e)[8]) {
#pragma unroll
            for (int m = 0; m < 8; ++m) e[m] = *(LAS unsigned*)(size_t)(lista + grp * 128 + m * 16);
        };
        unsigned cw = *(LAS unsigned*)(size_t)(first ? cnta : lds0 + L_CNT + (l & 31) * 4), e0[8], e1[8];
        entries(0, e0);
        if constexpr (first) {
#pragma unroll
            for (int s = 0; s < 4; ++s) {
                const u32x4 v = *(LAS u32x4*)(size_t)(qa + s * 1024);
                qf[4 * s] = v.x; qf[4 * s + 1] = v.y; qf[4 * s + 2] = v.z; qf[4 * s + 3] = v.w;
            }
        }
        entries(1, e1);
        FENCE();
        int lo = __builtin_amdgcn_readlane(cw, 2 * w) >> 2, hi = __builtin_amdgcn_readlane(cw, 2 * w + 1) >> 2;
        if constexpr (first) {
            if (__builtin_amdgcn_readlane(cw, 16)) gcur = 0;
            if (__builtin_amdgcn_ballot_w64(cw > 4u * CAP2)) { gcur = -1; lo = 0; hi = 0; hmask = 0x7FFFFFFFu; }
        }
        const int c = lo + hi, G = (c + 31) >> 5;
        const int nlast = G > 2 ? (((c - 1) & 31) >> 2) + 1 : 8;
        auto join = [&]() {
            for (int k = 0; k < G * 32 - lo; k += 64) {
                const unsigned e = *(LAS unsigned*)(mylist + SUBB + (l + k) * 4);
                if (l + k < G * 32 - lo) *(LAS unsigned*)(mylist + (lo + l + k) * 4) = l + k < hi ? e : PAD;
            }
        };
        const bool early = lo >= 64;
        if (__builtin_expect(!early, 0)) { join(); entries(0, e0); entries(1, e1); }
        if constexpr (first) asm volatile("s_waitcnt lgkmcnt(0)" : "+{v[160:175]}"(qf) :: "memory");
        if (G > 0) dma_group(ring, srd_x, e0, doff);
        if constexpr (first) {
            if (WVLATE == 1) {
                if (G > 1 && w < 6) dma_group(ring + 8192, srd_x, e1, doff);
                wv_resident();
                lds_arrive(smem + L_CNT + 92, l);
                if (w >= 6) { lds_await(smem + L_CNT + 92, 8); if (G > 1) dma_group(ring + 8192, srd_x, e1, doff); }
            } else if (WVLATE == 2) {
                wv_resident();
                lds_arrive(smem + L_CNT + 92, l);
                if (w >= 6) lds_await(smem + L_CNT + 92, 8);
                if (G > 1) dma_group(ring + 8192, srd_x, e1, doff);
            } else {
                if (w < 4) { wv_resident(); lds_arrive(smem + L_CNT + 92, l); }
                else if (w >= 6) lds_await(smem + L_CNT + 92, 4);
                if (G > 1) dma_group(ring + 8192, srd_x, e1, doff);
            }
        } else if (G > 1) dma_group(ring + 8192, srd_x, e1, doff);
        f32x16 cc;
#pragma unroll
        for (int m = 0; m < 8; ++m) {
            cc[m] = (e0[m] & 0xFF) == keyn ? 0.f : __uint_as_float(inf);
            cc[8 + m] = (e1[m] & 0xFF) == keyn ? 0.f : __uint_as_float(inf);
        }
        if (early) join();
        STAMP(9);
        if (G > 0) gather_loop(acc, qf, addr, u32x4{lista, keyn, inf, 0u}, cc, srd_x, ring, G, nlast);
    };
    batch(std::true_type{}, l);
    if (__builtin_expect(gcur < G1, 0)) do {
        int t = threadIdx.x;
        PIN(t);
        const int l = t & 63;
        unsigned lim = G1 * 32;
        if (gcur >= 0) {
            const u64 m0 = __builtin_amdgcn_ballot_w64(l >= gcur && (int)(*(LAS unsigned*)(smem + L_HDR + l * 4) & hmask) > 4 * CAP1);
            const u64 m1 = __builtin_amdgcn_ballot_w64(l + 64 >= gcur && (int)(*(LAS unsigned*)(smem + L_HDR + 256 + l * 4) & hmask) > 4 * CAP1);
            if (!(m0 | m1)) break;
            gcur = m0 ? __builtin_ctzll(m0) : 64 + __builtin_ctzll(m1);
            unsigned v; const unsigned o = WS_NOVF + gcur * 4;
            asm volatile("buffer_load_dword %0, %1, %2, 0 offen sc1\n\ts_waitcnt vmcnt(0)" : "=&v"(v) : "v"(o), "s"(srd_ws) : "memory");
            lim = rfl(v);
        }
        const unsigned i = pos + t;
        const bool cand = t < CAP2 && i < lim;
        unsigned u;
        { const unsigned o = cand ? (gcur < 0 ? WS_INBOX + b * (G1 * 128) : WS_OVF + gcur * CH * 4) + i * 4 : OOB;
          asm volatile("buffer_load_dword %0, %1, %2, 0 offen sc1\n\ts_waitcnt vmcnt(0)" : "=&v"(u) : "v"(o), "s"(srd_ws) : "memory"); }
        WG_BARRIER();
        if (t < 16) *(LAS unsigned*)(smem + L_CNT + t * 4) = 0;
        WG_BARRIER();
        if (cand) {
            unsigned e; bool v;
            if (gcur < 0) { e = (i & 3) ? u : u ^ mykey; v = ((i & 31) - 2) * 4 < *(LAS unsigned*)(smem + L_HDR + (i >> 5) * 4); }
            else { e = (((u >> 16) + BIAS) << 8) | ((u >> 8) & 0xFF); v = ((u ^ (u >> 8)) & 0xFF) == (unsigned)b; }
            if (v) *(LAS unsigned*)(smem + L_LIST + (e & 7) * (2 * SUBB) + lds_add(smem + L_CNT + (e & 7) * 8, 4)) = e;
        }
        pos += CAP2;
        if ((unsigned)pos >= lim) { ++gcur; pos = 0; }
        WG_BARRIER();
        batch(std::false_type{}, l);
    } while (gcur < G1);
    STAMP(10);

    output_tile(acc, wva, wvb, cv, ow, qa, go, make_srd(out, xbytes));
    if (w == 0) { const unsigned o = t == 0 ? WS_KEY + b * 4 : OOB; ST1(mykey, srd_ws, o, " sc1"); }
    STAMP(11);
    asm volatile("s_waitcnt vmcnt(0)" ::: "memory");
    STAMP(12);
    __syncthreads();
    STAMP(13);
    {
        const unsigned long long tgt = st_[13] + (unsigned long long)PROBE_M * (st_[PROBE_B] - st_[PROBE_A]);
        if (threadIdx.x < 64) { int guard = 0; while (wall_clock64() < tgt && ++guard < 200000) __builtin_amdgcn_s_sleep(2); }
    }
}

extern "C" void kernel_launch(void* const* d_in, const int* in_sizes, int n_in,
                              void* d_out, int out_size, void* d_ws, size_t ws_size,
                              hipStream_t stream) {
    const int N = in_sizes[0] / 128, E = in_sizes[1];
    k_graph<<<NB, 512, 0, stream>>>((const unsigned short*)d_in[0], (const int*)d_in[1], (const int*)d_in[2],
                                    (const unsigned short*)d_in[3], (const unsigned short*)d_in[4],
                                    (unsigned*)d_ws, (unsigned short*)d_out, N, E);
}
```

```cpp
#define POLL_P 90
#define POLL_Q 210
#include <hip/hip_runtime.h>
#include <hip/hip_bf16.h>
#include <stdint.h>
#include <type_traits>
#define ORD 1
#define SKIP 0
#define STR2(x) #x
#define STR(x) STR2(x)

typedef __attribute__((ext_vector_type(8))) __bf16 bf16x8;
typedef __attribute__((ext_vector_type(4))) __bf16 bf16x4;
typedef __attribute__((ext_vector_type(2))) __bf16 bf16x2;
typedef __attribute__((ext_vector_type(4))) float f32x4;
typedef __attribute__((ext_vector_type(2))) float f32x2;
typedef __attribute__((ext_vector_type(4))) unsigned u32x4;
typedef __attribute__((ext_vector_type(2))) unsigned u32x2;
typedef __attribute__((ext_vector_type(32))) float f32x32;
typedef __attribute__((ext_vector_type(16))) float f32x16;
typedef __attribute__((ext_vector_type(16))) unsigned u32x16;
typedef __attribute__((ext_vector_type(12))) unsigned u32x12;
typedef __attribute__((ext_vector_type(8))) unsigned u32x8;
typedef unsigned long long u64;
#define LAS __attribute__((address_space(3)))

constexpr int NB = 256;
constexpr int G1 = 128;
constexpr int CH = 3136;
constexpr int CAP1 = 30;
constexpr int CAP2 = 192;
constexpr int SUBB = 784;
#define POLL_AT 3
#define POLL_T 290
#define LPRIO 3
#define PKADD 1
#define CODE_BYTES 23552
#define PARTNER 1
#define XSYNC 1
#define WVLATE 1
#define PFA_KB 0
#define PFB_KB 0
#define PFC_KB 1024
#define X_T 0
#define XAFTER 0
#define POLL_D 60
#define GATE 1
#define FASTIN 1
#define W_SLEEP 3
#define PRIO 1
#define SPLIT 16384
constexpr unsigned KEY_STEP = 0x9E3779B1u;
constexpr unsigned WS_KEY = 0;
constexpr unsigned WS_NOVF = 2048;
constexpr unsigned WS_INBOX = 4096;
constexpr unsigned WS_OVF = WS_INBOX + NB * G1 * 128;
constexpr unsigned WS_BYTES = WS_OVF + G1 * CH * 4;
constexpr unsigned OOB = 0x80000000u;
constexpr unsigned BIAS = 32768 - SPLIT;
constexpr unsigned PAD = 0x00FFFFFFu;

constexpr int L_CNT = 0;
constexpr int L_HDR = 128;
constexpr int L_LIST = 768;
constexpr int L_RING = L_LIST + 16 * SUBB;
constexpr int L_IMG = L_RING;
constexpr int L_CNT1 = L_IMG + NB * 144;
constexpr int L_WQ = L_RING + 2 * 16384 + 8192;
constexpr int L_WV0 = L_RING + 6 * 16384 + 8192;
constexpr int L_WV1 = L_RING + 131072;
constexpr int L_DUMP = L_WV1 + 16384;
constexpr int L_TOTAL = L_DUMP + 2048;
static_assert(WS_KEY == 0 && L_WV1 % 256 == 0 && CH == 3072 + 64 && L_RING % 256 == 0 && L_TOTAL <= 163840, "LDS map");

#define STAMP_DECL
#define STAMP(k)
#define STAMP_FLUSH(base, k0, k1)

__device__ __forceinline__ u32x4 make_srd(const void* p, unsigned bytes, unsigned back = 0) {
    const u64 a = (u64)p - back;
    u32x4 s; s.x = (unsigned)a; s.y = (unsigned)(a >> 32); s.z = bytes + back; s.w = 0x00020000u; return s;
}
#define LD4(dst, srd, off, FL) asm volatile("buffer_load_dwordx4 %0, %1, %2, 0 offen" FL : "=v"(dst) : "v"(off), "s"(srd) : "memory")
#define LD4S(dst, srd, off, so, FL) asm volatile("buffer_load_dwordx4 %0, %1, %2, %3 offen" FL : "=v"(dst) : "v"(off), "s"(srd), "s"(so) : "memory")
#define LD1(dst, srd, off, FL) asm volatile("buffer_load_dword %0, %1, %2, 0 offen" FL : "=v"(dst) : "v"(off), "s"(srd) : "memory")
#define ST1(val, srd, off, FL) asm volatile("buffer_store_dword %0, %1, %2, 0 offen" FL :: "v"(val), "v"(off), "s"(srd) : "memory")
#define PIN(x) asm volatile("" : "+v"(x))
#define VMWAIT(n) asm volatile("s_waitcnt vmcnt(" #n ")" ::: "memory")
#define FENCE() __builtin_amdgcn_sched_barrier(0)
#define WG_BARRIER() do { asm volatile("s_waitcnt lgkmcnt(0)" ::: "memory"); __builtin_amdgcn_s_barrier(); asm volatile("" ::: "memory"); } while (0)

struct Srd4 { u32x4 s0, s1, s2, s3; };
__device__ __forceinline__ Srd4 make_srd4(const void* p, unsigned bytes, unsigned back = 0) {
    return Srd4{make_srd(p, bytes, back), make_srd(p, bytes, back + 1024), make_srd(p, bytes, back + 2048), make_srd(p, bytes, back + 3072)};
}

__device__ __forceinline__ void set_m0(unsigned v) { asm volatile("s_mov_b32 m0, %0" :: "s"(v) : "memory"); }
template <int R> __device__ __forceinline__ void dma_rows(const Srd4& d, unsigned e, unsigned off) {
    unsigned tmp, mask = 0x00FFFF00u;
    asm volatile("v_and_or_b32 %0, %1, %2, %3\n\tbuffer_load_dwordx4 %0, %4, 0 offen offset:%c5 lds"
                 : "=&v"(tmp) : "v"(e), "s"(mask), "v"(off), "s"(R == 0 ? d.s0 : R == 1 ? d.s1 : R == 2 ? d.s2 : d.s3), "i"(R * 1024) : "memory");
}
__device__ __forceinline__ void dma_half(unsigned ldsbase, const Srd4& d, const unsigned* e, const u32x4& off) {
    set_m0(ldsbase);
    dma_rows<0>(d, e[0], off.x); dma_rows<1>(d, e[1], off.y); dma_rows<2>(d, e[2], off.z); dma_rows<3>(d, e[3], off.w);
}
__device__ __forceinline__ void dma_group(unsigned ldsbase, const Srd4& d, const unsigned (&e)[8], const u32x4& off) {
    asm volatile("s_waitcnt lgkmcnt(0)" ::: "memory");
    dma_half(ldsbase, d, e, off); dma_half(ldsbase + 4096, d, e + 4, off);
}

__device__ __forceinline__ void gather_loop(f32x32& acc, const u32x16& qf, const u32x16& addr, u32x4 misc, f32x16 cc,
                                            const Srd4& d, unsigned ring, int G, int nlast) {
    f32x32 t0, t1; u32x12 t2; int tmp, pr; u64 sm; unsigned mask = 0x00FFFF00u;
    asm volatile(
        ".macro GWAIT k\n\ts_cmp_gt_i32 %[t], \\k\n\ts_cbranch_scc1 1f\n\ts_waitcnt vmcnt(\\k)\n\t.endm\n\t"
        ".macro GROWS so\n\t.irp s, 0, 1, 2, 3\n\t"
        "ds_read_b128 v[64+8*\\s:67+8*\\s], v[176+\\s] offset:\\so\n\t"
        "ds_read_b128 v[68+8*\\s:71+8*\\s], v[176+\\s] offset:\\so+4096\n\t.endr\n\t.endm\n\t"
        ".macro GTR c, so\n\t"
        "ds_read_b64_tr_b16 v[96+4*\\c:97+4*\\c], v[180+\\c] offset:\\so\n\t"
        "ds_read_b64_tr_b16 v[98+4*\\c:99+4*\\c], v[180+\\c] offset:\\so+4096\n\t.endm\n\t"
        ".macro GS s, c\n\t"
        "v_mfma_f32_16x16x32_bf16 v[\\c:\\c+3], v[64+8*\\s:67+8*\\s], v[160+4*\\s:163+4*\\s], v[\\c:\\c+3]\n\t"
        "v_mfma_f32_16x16x32_bf16 v[\\c+4:\\c+7], v[68+8*\\s:71+8*\\s], v[160+4*\\s:163+4*\\s], v[\\c+4:\\c+7]\n\t.endm\n\t"
        ".macro GLOAD m, r, srd\n\t"
        "v_and_or_b32 v195, v[212+\\m], %[mask], v[188+\\r]\n\t"
        "buffer_load_dwordx4 v195, \\srd, 0 offen offset:1024*\\r lds\n\t.endm\n\t"
        ".macro GPART m, r, srd\n\ts_cmp_le_i32 %[nl], \\m\n\ts_cbranch_scc1 4f\n\tGLOAD \\m, \\r, \\srd\n\t.endm\n\t"
        ".macro GSUM m\n\tv_mfma_f32_16x16x32_bf16 v[128+4*(\\m):131+4*(\\m)], v[96+4*(\\m):99+4*(\\m)], v[220:223], v[128+4*(\\m):131+4*(\\m)]\n\t.endm\n\t"
        ".macro GCMP m, d\n\tv_cmp_ne_u32_sdwa \\d, v[212+\\m], v193 src0_sel:BYTE_0 src1_sel:DWORD\n\t.endm\n\t"
        ".macro GPAIR m, c\n\t"
        "GSUM \\m\n\tGCMP \\m+1, %[sm]\n\tv_cndmask_b32_e32 v[\\c+\\m], 0, v194, vcc\n\t"
        "GSUM \\m+1\n\t.if \\m < 6\n\tGCMP \\m+2, vcc\n\t.else\n\ts_cmp_gt_i32 %[rem], 0\n\t.endif\n\t"
        "v_cndmask_b32_e64 v[\\c+\\m+1], 0, v194, %[sm]\n\t.endm\n\t"
        ".macro GSTEP so, c, eo\n\t"
        "GTR 0, \\so\n\t"
        "s_waitcnt lgkmcnt(2)\n\tGS 0, \\c\n\t"
        "GTR 1, \\so\n\tGTR 2, \\so\n\tGS 1, \\c\n\t"
        "GTR 3, \\so\n\tGTR 4, \\so\n\tGS 2, \\c\n\t"
        "GTR 5, \\so\n\tGTR 6, \\so\n\tGS 3, \\c\n\t"
        "GTR 7, \\so\n\t"
        "ds_read2_b32 v[212:213], v192 offset0:\\eo offset1:\\eo+4\n\t"
        "ds_read2_b32 v[214:215], v192 offset0:\\eo+8 offset1:\\eo+12\n\t"
        "ds_read2_b32 v[216:217], v192 offset0:\\eo+16 offset1:\\eo+20\n\t"
        "ds_read2_b32 v[218:219], v192 offset0:\\eo+24 offset1:\\eo+28\n\t"
        "s_add_u32 m0, %[ring], \\so\n\t"
        "s_cmp_gt_i32 %[rem], 3\n\t"
        ".irp k, 0, 1, 2, 3, 4, 5, 6, 7\n\tv_exp_f32_e32 v[\\c+\\k], v[\\c+\\k]\n\t.endr\n\t"
        "s_waitcnt lgkmcnt(0)\n\t"
        "s_cbranch_scc0 2f\n\t"
        "GLOAD 0, 0, %[s0]\n\tGLOAD 1, 1, %[s1]\n\tGLOAD 2, 2, %[s2]\n\tGLOAD 3, 3, %[s3]\n\t"
        "s_add_u32 m0, m0, 0x1000\n\t"
        "GLOAD 4, 0, %[s0]\n\tGLOAD 5, 1, %[s1]\n\tGLOAD 6, 2, %[s2]\n\tGLOAD 7, 3, %[s3]\n\t"
        "GSIG \\c\n\t"
        "s_waitcnt vmcnt(8)\n\t"
        "s_branch 1f\n\t"
        "2:\n\t"
        "s_mov_b32 %[t], 0\n\t"
        "s_cmp_eq_u32 %[rem], 3\n\ts_cbranch_scc0 4f\n\t"
        "s_mov_b32 %[t], %[nl]\n\t"
        "GLOAD 0, 0, %[s0]\n\tGPART 1, 1, %[s1]\n\tGPART 2, 2, %[s2]\n\tGPART 3, 3, %[s3]\n\t"
        "s_add_u32 m0, m0, 0x1000\n\t"
        "GPART 4, 0, %[s0]\n\tGPART 5, 1, %[s1]\n\tGPART 6, 2, %[s2]\n\tGPART 7, 3, %[s3]\n\t"
        "4:\n\t"
        "GSIG \\c\n\t"
        "s_waitcnt vmcnt(8)\n\t"
        "GWAIT 7\n\tGWAIT 6\n\tGWAIT 5\n\tGWAIT 4\n\tGWAIT 3\n\tGWAIT 2\n\tGWAIT 1\n\tGWAIT 0\n\t"
        "1:\n\t"
        "GROWS 8192-\\so\n\t"
        "GCMP 0, vcc\n\t"
        "s_add_i32 %[rem], %[rem], -1\n\t"
        "GPAIR 0, \\c\n\tGPAIR 2, \\c\n\tGPAIR 4, \\c\n\tGPAIR 6, \\c\n\t"
        ".endm\n\t"
        ".macro GSIG c\n\t"
        ".if " STR(PKADD) "\n\t.irp k, 0, 2, 4, 6\n\tv_pk_add_f32 v[\\c+\\k:\\c+\\k+1], v[\\c+\\k:\\c+\\k+1], 1.0 op_sel_hi:[1,0]\n\t.endr\n\t.else\n\t"
        ".irp k, 0, 1, 2, 3, 4, 5, 6, 7\n\tv_add_f32_e32 v[\\c+\\k], 1.0, v[\\c+\\k]\n\t.endr\n\t.endif\n\t"
        ".irp k, 0, 1, 2, 3, 4, 5, 6, 7\n\tv_rcp_f32_e32 v[\\c+\\k], v[\\c+\\k]\n\t.endr\n\t"
        ".irp k, 0, 1, 2, 3\n\tv_cvt_pk_bf16_f32 v[220+\\k], v[\\c+2*\\k], v[\\c+2*\\k+1]\n\t.endr\n\t.endm\n\t"
        "s_waitcnt vmcnt(8)\n\t"
        "s_cmp_gt_i32 %[rem], 1\n\ts_cbranch_scc1 1f\n\ts_waitcnt vmcnt(0)\n\t1:\n\t"
        "GROWS 0\n\t"
        ".if " STR(LPRIO) " >= 3\n\ts_mov_b32 %[pr], 0\n\ts_setprio 2\n\t.endif\n\t"
        "0:\n\t"
        ".if " STR(LPRIO) " == 3\n\t"
        "s_cmp_eq_u32 %[pr], 1\n\ts_cbranch_scc0 5f\n\ts_setprio 1\n\t5:\n\t"
        "s_cmp_eq_u32 %[pr], 2\n\ts_cbranch_scc0 5f\n\ts_setprio 0\n\t5:\n\t"
        "s_add_u32 %[pr], %[pr], 1\n\t.endif\n\t"
        "GSTEP 0, 196, 64\n\t"
        "s_cbranch_scc0 9f\n\t"
        "GSTEP 8192, 204, 96\n\t"
        "v_add_u32_e32 v192, 0x100, v192\n\t"
        "s_cbranch_scc1 0b\n\t"
        "9:\n\t"
        "s_waitcnt lgkmcnt(0)\n\ts_nop 6\n\t"
        ".purgem GWAIT\n\t.purgem GROWS\n\t.purgem GTR\n\t.purgem GS\n\t.purgem GLOAD\n\t.purgem GPART\n\t.purgem GSUM\n\t.purgem GCMP\n\t.purgem GPAIR\n\t.purgem GSTEP\n\t.purgem GSIG"
        : "+{v[128:159]}"(acc), "+{v[192:195]}"(misc), "+{v[196:211]}"(cc), "=&{v[64:95]}"(t0), "=&{v[96:127]}"(t1), "=&{v[212:223]}"(t2),
          [rem] "+s"(G), [t] "=&s"(tmp), [pr] "=&s"(pr), [sm] "=&s"(sm)
        : "{v[160:175]}"(qf), "{v[176:191]}"(addr), [s0] "s"(d.s0), [s1] "s"(d.s1), [s2] "s"(d.s2), [s3] "s"(d.s3),
          [mask] "s"(mask), [ring] "s"(ring), [nl] "s"(nlast)
        : "vcc", "scc", "memory");
}

__device__ __forceinline__ void output_tile(f32x32 acc, f32x32 wva, f32x32 wvb, unsigned wa, unsigned ow, unsigned ra, unsigned go, const u32x4& srd) {
    f32x32 t0, t1, t2, t3; unsigned o1, o2, o3;
    asm volatile(
        ".macro VPK k\n\tv_cvt_pk_bf16_f32 v[196+\\k], v[128+2*(\\k)], v[129+2*(\\k)]\n\t.endm\n\t"
        ".macro VRD a, u\n\tds_read_b64_tr_b16 v[64+32*\\u+4*\\a:65+32*\\u+4*\\a], v[212+\\a] offset:8192*\\u\n\t"
        "ds_read_b64_tr_b16 v[66+32*\\u+4*\\a:67+32*\\u+4*\\a], v[212+\\a] offset:8192*\\u+4096\n\t.endm\n\t"
        ".macro VMM a, w, u\n\tv_mfma_f32_16x16x32_bf16 v[160+4*\\a:163+4*\\a], v[\\w+4*\\a:\\w+4*\\a+3], v[196+4*\\u:199+4*\\u], v[160+4*\\a:163+4*\\a]\n\t.endm\n\t"
        ".macro VADR a, r\n\tv_xor_b32_e32 v[212+\\a], ((\\a >> 1) << 6) | ((\\a & 1) << 3), \\r\n\t.endm\n\t"
        ".macro VOUT a\n\tv_cvt_pk_bf16_f32 v[160+4*\\a], v[160+4*\\a], v[161+4*\\a]\n\tv_cvt_pk_bf16_f32 v[161+4*\\a], v[162+4*\\a], v[163+4*\\a]\n\t"
        "ds_write_b64 v[212+\\a], v[160+4*\\a:161+4*\\a]\n\t.endm\n\t"
        "VPK 0\n\tVPK 1\n\tVPK 2\n\tVPK 3\n\t"
        ".irp a, 0, 1, 2, 3, 4, 5, 6, 7\n\tVADR \\a, %[wa]\n\t.endr\n\t"
        ".irp a, 0, 1, 2, 3, 4, 5, 6, 7\n\t"
        "v_mfma_f32_16x16x32_bf16 v[160+4*\\a:163+4*\\a], v[224+4*\\a:227+4*\\a], v[196:199], 0\n\tVRD \\a, 0\n\tVPK 4+\\a\n\t.endr\n\t"
        ".irp a, 0, 1, 2, 3\n\tVMM \\a, 32, 1\n\tVRD \\a, 1\n\tVPK 12+\\a\n\t.endr\n\t"
        "VMM 4, 32, 1\n\tVRD 4, 1\n\ts_mov_b32 %[o1], 0x200000\n\t"
        "VMM 5, 32, 1\n\tVRD 5, 1\n\ts_mov_b32 %[o2], 0x400000\n\t"
        "VMM 6, 32, 1\n\tVRD 6, 1\n\ts_mov_b32 %[o3], 0x600000\n\t"
        "VMM 7, 32, 1\n\tVRD 7, 1\n\ts_waitcnt lgkmcnt(15)\n\t"
        ".irp a, 0, 1, 2, 3, 4, 5, 6, 7\n\tVMM \\a, 64, 2\n\tVADR \\a, %[ow]\n\t.endr\n\t"
        "s_waitcnt lgkmcnt(0)\n\t"
        "VMM 0, 96, 3\n\tv_xor_b32_e32 v220, 0x2040, %[go]\n\tv_xor_b32_e32 v221, 0x4080, %[go]\n\tv_xor_b32_e32 v222, 0x60c0, %[go]\n\t"
        "VMM 1, 96, 3\n\ts_nop 2\n\t"
        "VMM 2, 96, 3\n\tVOUT 0\n\tVMM 3, 96, 3\n\tVOUT 1\n\tVMM 4, 96, 3\n\tVOUT 2\n\tVMM 5, 96, 3\n\tVOUT 3\n\t"
        "VMM 6, 96, 3\n\tVOUT 4\n\tVMM 7, 96, 3\n\tVOUT 5\n\ts_nop 0\n\tVOUT 6\n\ts_nop 0\n\tVOUT 7\n\t"
        ".irp m, 0, 1, 2, 3\n\tds_read_b128 v[64+4*\\m:67+4*\\m], %[ra] offset:1024*\\m\n\t.endr\n\t"
        "s_waitcnt lgkmcnt(3)\n\tbuffer_store_dwordx4 v[64:67], %[go], %[srd], 0 offen sc1\n\t"
        "s_waitcnt lgkmcnt(2)\n\tbuffer_store_dwordx4 v[68:71], v220, %[srd], %[o1] offen sc1\n\t"
        "s_waitcnt lgkmcnt(1)\n\tbuffer_store_dwordx4 v[72:75], v221, %[srd], %[o2] offen sc1\n\t"
        "s_waitcnt lgkmcnt(0)\n\tbuffer_store_dwordx4 v[76:79], v222, %[srd], %[o3] offen sc1\n\ts_nop 1\n\t"
        ".purgem VPK\n\t.purgem VRD\n\t.purgem VMM\n\t.purgem VADR\n\t.purgem VOUT"
        : "=&{v[64:95]}"(t0), "=&{v[96:127]}"(t1), "=&{v[160:191]}"(t2), "=&{v[192:223]}"(t3), [o1] "=&s"(o1), [o2] "=&s"(o2), [o3] "=&s"(o3)
        : "{v[128:159]}"(acc), "{v[224:255]}"(wva), "{v[32:63]}"(wvb), [wa] "v"(wa), [ow] "v"(ow), [ra] "v"(ra), [go] "v"(go), [srd] "s"(srd)
        : "memory");
}

__device__ __forceinline__ u64 rank_edges(u32x16& ed, u32x16& es, u32x16& rk, u64 m0, u64 m1, u64 m2, u64 m3, unsigned keya) {
    u32x16 b4; u64 over; unsigned sel = 0x01050401u, c36 = 36;
    asm volatile(
        ".macro RADD k\n\tv_xor_b32_sdwa v[96+\\k], v[64+\\k], v[64+\\k] dst_sel:DWORD dst_unused:UNUSED_PAD src0_sel:BYTE_0 src1_sel:BYTE_1\n\t"
        "v_lshlrev_b32_e32 v[96+\\k], 2, v[96+\\k]\n\t"
        "ds_add_rtn_u32 v[112+\\k], v[96+\\k], v111 offset:%c[cnt]\n\t.endm\n\t"
        ".macro RENT k\n\tv_add_u32_e32 v[80+\\k], %c[bias], v[80+\\k]\n\tv_perm_b32 v[80+\\k], v[80+\\k], v[64+\\k], %[sel]\n\t.endm\n\t"
        ".macro RPUT k\n\tv_min_u32_e32 v110, %c[cap], v[112+\\k]\n\tv_mad_u32_u24 v110, v[96+\\k], %[c36], v110\n\t"
        "ds_write_b32 v110, v[80+\\k] offset:%c[img]\n\t.endm\n\t"
        "v_mov_b32_e32 v111, 4\n\t"
        ".irp k, 0, 2, 4, 6, 8, 10, 12\n\tv_mov_b64_e32 v[112+\\k:113+\\k], 0\n\t.endr\n\t"
        "s_mov_b64 exec, %[m0]\n\ts_waitcnt vmcnt(9)\n\tRADD 0\n\tRADD 1\n\tRADD 2\n\tRADD 3\n\t"
        "s_mov_b64 exec, %[m1]\n\ts_waitcnt vmcnt(8)\n\tRADD 4\n\tRADD 5\n\tRADD 6\n\tRADD 7\n\t"
        "s_mov_b64 exec, %[m2]\n\ts_waitcnt vmcnt(7)\n\tRADD 8\n\tRADD 9\n\tRADD 10\n\tRADD 11\n\t"
        "s_mov_b64 exec, %[m3]\n\ts_waitcnt vmcnt(6)\n\tRADD 12\n\t"
        "s_mov_b64 exec, %[m0]\n\ts_waitcnt vmcnt(5)\n\tRENT 0\n\tRENT 1\n\tRENT 2\n\tRENT 3\n\t"
        "s_waitcnt lgkmcnt(9)\n\tRPUT 0\n\tRPUT 1\n\tRPUT 2\n\tRPUT 3\n\t"
        "s_mov_b64 exec, %[m1]\n\ts_waitcnt vmcnt(4)\n\tRENT 4\n\tRENT 5\n\tRENT 6\n\tRENT 7\n\t"
        "s_waitcnt lgkmcnt(9)\n\tRPUT 4\n\tRPUT 5\n\tRPUT 6\n\tRPUT 7\n\t"
        "s_mov_b64 exec, %[m2]\n\ts_waitcnt vmcnt(3)\n\tRENT 8\n\tRENT 9\n\tRENT 10\n\tRENT 11\n\t"
        "s_waitcnt lgkmcnt(9)\n\tRPUT 8\n\tRPUT 9\n\tRPUT 10\n\tRPUT 11\n\t"
        "s_mov_b64 exec, %[m3]\n\ts_waitcnt vmcnt(0)\n\tRENT 12\n\t"
        "s_waitcnt lgkmcnt(12)\n\tRPUT 12\n\t"
        "s_mov_b64 exec, -1\n\t"
        "v_add_u32_e32 v93, %c[step], v93\n\t"
        "v_max3_u32 v110, v112, v113, v114\n\tv_max3_u32 v109, v115, v116, v117\n\t"
        "ds_write_b32 %[keya], v93 offset:%c[cnt]+1024\n\t"
        "v_max3_u32 v110, v110, v118, v119\n\tv_max3_u32 v109, v109, v120, v121\n\t"
        "v_max3_u32 v110, v110, v122, v123\n\tv_max3_u32 v110, v110, v109, v124\n\t"
        "v_cmp_le_u32_e32 vcc, %c[cap], v110\n\ts_mov_b64 %[over], vcc\n\t"
        ".purgem RADD\n\t.purgem RENT\n\t.purgem RPUT"
        : "+{v[64:79]}"(ed), "+{v[80:95]}"(es), "=&{v[96:111]}"(b4), "=&{v[112:127]}"(rk), [over] "=&s"(over)
        : [m0] "s"(m0), [m1] "s"(m1), [m2] "s"(m2), [m3] "s"(m3), [keya] "v"(keya), [sel] "s"(sel), [c36] "s"(c36),
          [cnt] "i"(L_CNT1), [img] "i"(L_IMG + 8), [bias] "i"(BIAS), [cap] "i"(4 * CAP1), [step] "i"(KEY_STEP)
        : "vcc", "memory");
    return over;
}
__device__ __forceinline__ unsigned read_image(f32x32& img, u32x16& meta, u32x16& sums, unsigned ima, unsigned cna) {
    unsigned no;
    asm volatile(
        "v_mov_b32_e32 v121, 0\n\tds_read_b32 v120, v121 offset:%c[cnt]+2048\n\t"
        ".irp k, 0, 1, 2, 3, 4, 5, 6, 7\n\tds_read_b128 v[64+4*\\k:67+4*\\k], %[ima] offset:4608*\\k\n\t.endr\n\t"
        ".irp k, 0, 1, 2, 3\n\tds_read2_b32 v[96+2*\\k:97+2*\\k], %[cna] offset0:64*\\k offset1:64*\\k+32\n\t.endr\n\t"
        "s_waitcnt lgkmcnt(11)\n\tv_readfirstlane_b32 %[no], v120\n\t"
        "v_add_u32_e32 v121, 0x400, %[cna]\n\t"
        ".irp k, 0, 1, 2, 3\n\tds_read2_b32 v[104+2*\\k:105+2*\\k], v121 offset0:64*\\k offset1:64*\\k+32\n\t.endr\n\t"
        "s_waitcnt lgkmcnt(12)\n\t"
        ".irp k, 0, 1, 2, 3\n\tv_add3_u32 v[112+\\k], v[64+4*\\k], v[65+4*\\k], v[66+4*\\k]\n\tv_add_u32_e32 v[112+\\k], v[112+\\k], v[67+4*\\k]\n\t.endr\n\t"
        "s_waitcnt lgkmcnt(8)\n\t"
        ".irp k, 4, 5, 6, 7\n\tv_add3_u32 v[112+\\k], v[64+4*\\k], v[65+4*\\k], v[66+4*\\k]\n\tv_add_u32_e32 v[112+\\k], v[112+\\k], v[67+4*\\k]\n\t.endr\n\t"
        ".irp k, 0, 1, 2, 3, 4, 5, 6, 7\n\tv_add_u32_dpp v[112+\\k], v[112+\\k], v[112+\\k] quad_perm:[1,0,3,2] row_mask:0xf bank_mask:0xf\n\t.endr\n\t"
        ".irp k, 0, 1, 2, 3, 4, 5, 6, 7\n\tv_add_u32_dpp v[112+\\k], v[112+\\k], v[112+\\k] quad_perm:[2,3,0,1] row_mask:0xf bank_mask:0xf\n\t.endr\n\t"
        ".irp k, 0, 1, 2, 3, 4, 5, 6, 7\n\tv_add_u32_dpp v[112+\\k], v[112+\\k], v[112+\\k] row_half_mirror row_mask:0xf bank_mask:0xf\n\t.endr\n\t"
        "s_waitcnt lgkmcnt(0)"
        : "=&{v[64:95]}"(img), "=&{v[96:111]}"(meta), "=&{v[112:127]}"(sums), [no] "=&s"(no)
        : [ima] "v"(ima), [cna] "v"(cna), [cnt] "i"(L_CNT1) : "memory");
    return no;
}
__device__ __forceinline__ void publish_image(f32x32 img, u32x16 meta, u32x16 sums, const u32x4& srd, unsigned ob) {
    u64 pc0 = 0x0101010101010101ull; unsigned so;
    asm volatile(
        ".macro PUB k\n\ts_mov_b64 exec, %[pc0]\n\tv_mov_b32_e32 v[64+4*\\k], v[96+\\k]\n\tv_mov_b32_e32 v[65+4*\\k], v[112+\\k]\n\t"
        "s_mov_b64 exec, -1\n\tv_xor_b32_e32 v[64+4*\\k], v[64+4*\\k], v[104+\\k]\n\t"
        "buffer_store_dwordx4 v[64+4*\\k:67+4*\\k], %[ob], %[srd], %[so] offen sc1\n\ts_add_u32 %[so], %[so], %c[step]\n\t.endm\n\t"
        "s_mov_b32 %[so], 0\n\t"
        ".irp k, 0, 1, 2, 3, 4, 5, 6, 7\n\tPUB \\k\n\t.endr\n\t.purgem PUB"
        : "+{v[64:95]}"(img), [so] "=&s"(so)
        : "{v[96:111]}"(meta), "{v[112:127]}"(sums), [srd] "s"(srd), [ob] "v"(ob), [pc0] "s"(pc0), [step] "i"(32 * G1 * 128) : "scc", "memory");
}

__device__ __forceinline__ void inbox_request(u32x16& raw, const u32x4& srd, unsigned ib) {
    unsigned o1 = 0x1000, o2 = 0x2000, o3 = 0x3000;
    asm volatile("buffer_load_dwordx4 v[64:67], %1, %2, 0 offen sc1\n\tbuffer_load_dwordx4 v[68:71], %1, %2, %3 offen sc1\n\t"
                 "buffer_load_dwordx4 v[72:75], %1, %2, %4 offen sc1\n\tbuffer_load_dwordx4 v[76:79], %1, %2, %5 offen sc1"
                 : "=&{v[64:79]}"(raw) : "v"(ib), "s"(srd), "s"(o1), "s"(o2), "s"(o3) : "memory");
}
__device__ __forceinline__ unsigned inbox_stage(u32x16 raw, const u32x4& srd, unsigned ib, unsigned key, int i4, unsigned hdra) {
    u32x16 t0; f32x32 t2; unsigned four = 4; u64 m0, m1, m2, m3; unsigned todo, fresh, spin, o1, o2, o3, over;
    u64 pc0 = 0x0101010101010101ull;
    asm volatile(
        ".macro IVAL j, so\n\t"
        "s_bitcmp1_b32 %[todo], \\j\n\ts_cbranch_scc0 2f\n\t"
        "v_xor_b32_e32 v[64+4*\\j], %[key], v[64+4*\\j]\n\t"
        "v_sub_u32_e32 v97, 0, v[65+4*\\j]\n\t"
        "v_add_u32_e32 v96, v[64+4*\\j], v[65+4*\\j]\n\t"
        "v_mov_b32_dpp v[80+\\j], v[64+4*\\j] quad_perm:[0,0,0,0] row_mask:0xf bank_mask:0xf\n\t"
        "v_cndmask_b32_e64 v96, v96, v97, %[pc0]\n\t"
        "s_nop 0\n\t"
        "v_mov_b32_dpp v[80+\\j], v[80+\\j] row_shr:4 row_mask:0xf bank_mask:0xa\n\t"
        "v_cmp_gt_i32_e32 vcc, v[80+\\j], v92\n\t"
        "v_add3_u32 v96, v96, v[66+4*\\j], v[67+4*\\j]\n\t"
        "v_lshrrev_b32_e32 v97, 14, v[80+\\j]\n\t"
        "v_cndmask_b32_e32 v96, 0, v96, vcc\n\t"
        "s_nop 1\n\tv_add_u32_dpp v96, v96, v96 quad_perm:[1,0,3,2] row_mask:0xf bank_mask:0xf\n\t"
        "s_nop 1\n\tv_add_u32_dpp v96, v96, v96 quad_perm:[2,3,0,1] row_mask:0xf bank_mask:0xf\n\t"
        "s_nop 1\n\tv_add_u32_dpp v96, v96, v96 row_half_mirror row_mask:0xf bank_mask:0xf\n\t"
        "v_or_b32_e32 v96, v96, v97\n\t"
        "v_cmp_ne_u32_e32 vcc, 0, v96\n\t"
        "s_cbranch_vccz 1f\n\t"
        "buffer_load_dwordx4 v[64+4*\\j:67+4*\\j], %[ib], %[srd], \\so offen sc1\n\t"
        "s_branch 2f\n\t"
        "1:\n\ts_bitset1_b32 %[fresh], \\j\n\t"
        "2:\n\t.endm\n\t"
        ".macro IRANK j\n\t"
        "s_bitcmp1_b32 %[fresh], \\j\n\ts_cbranch_scc0 2f\n\t"
        "v_cmp_lt_u32_e64 %[m0], v92, v[80+\\j]\n\tv_cmp_lt_u32_e64 %[m1], v93, v[80+\\j]\n\t"
        "v_cmp_lt_u32_e64 %[m2], v94, v[80+\\j]\n\tv_cmp_lt_u32_e64 %[m3], v95, v[80+\\j]\n\t"
        ".irp k, 0, 1, 2, 3\n\tv_lshrrev_b32_e32 v[84+\\k], 21, v[64+4*\\j+\\k]\n\t.endr\n\t"
        ".irp k, 0, 1, 2, 3\n\tv_and_b32_e32 v[84+\\k], 60, v[84+\\k]\n\t.endr\n\t"
        "v_cmp_lt_u32_e32 vcc, %c[cap], v[80+\\j]\n\t"
        "s_mov_b64 exec, %[m0]\n\tds_add_rtn_u32 v88, v84, %[four] offset:%c[cnt]\n\t"
        "s_mov_b64 exec, %[m1]\n\tds_add_rtn_u32 v89, v85, %[four] offset:%c[cnt]\n\t"
        "s_mov_b64 exec, %[m2]\n\tds_add_rtn_u32 v90, v86, %[four] offset:%c[cnt]\n\t"
        "s_mov_b64 exec, %[m3]\n\tds_add_rtn_u32 v91, v87, %[four] offset:%c[cnt]\n\t"
        "s_mov_b64 exec, %[pc0]\n\tds_write_b32 %[hdra], v[80+\\j] offset:128*\\j\n\t"
        "s_mov_b64 exec, -1\n\t"
        "s_cbranch_vccz 1f\n\t"
        "s_mov_b32 %[over], 1\n\t"
        "1:\n\ts_waitcnt lgkmcnt(1)\n\t"
        ".irp k, 0, 1, 2, 3\n\tv_min_u32_e32 v[88+\\k], %c[sub]-4, v[88+\\k]\n\t.endr\n\t"
        ".irp k, 0, 1, 2, 3\n\tv_mad_u32_u24 v[88+\\k], v[84+\\k], %[sub4], v[88+\\k]\n\t.endr\n\t"
        "s_mov_b64 exec, %[m0]\n\tds_write_b32 v88, v[64+4*\\j] offset:%c[list]\n\t"
        "s_mov_b64 exec, %[m1]\n\tds_write_b32 v89, v[65+4*\\j] offset:%c[list]\n\t"
        "s_mov_b64 exec, %[m2]\n\tds_write_b32 v90, v[66+4*\\j] offset:%c[list]\n\t"
        "s_mov_b64 exec, %[m3]\n\tds_write_b32 v91, v[67+4*\\j] offset:%c[list]\n\t"
        "s_mov_b64 exec, -1\n\t"
        "2:\n\t.endm\n\t"
        ".macro FADD j\n\t"
        ".irp k, 0, 1, 2, 3\n\tv_lshrrev_b32_e32 v[96+4*\\j+\\k], 21, v[64+4*\\j+\\k]\n\t.endr\n\t"
        ".irp k, 0, 1, 2, 3\n\tv_and_b32_e32 v[96+4*\\j+\\k], 60, v[96+4*\\j+\\k]\n\t.endr\n\t"
        "v_cmpx_lt_u32_e64 s[72+8*\\j:73+8*\\j], v94, v[80+\\j]\n\tds_add_rtn_u32 v[114+4*\\j], v[98+4*\\j], %[four] offset:%c[cnt]\n\t"
        "v_cmpx_lt_u32_e64 s[74+8*\\j:75+8*\\j], v95, v[80+\\j]\n\tds_add_rtn_u32 v[115+4*\\j], v[99+4*\\j], %[four] offset:%c[cnt]\n\t"
        "s_mov_b64 exec, -1\n\t"
        "v_cmpx_lt_u32_e64 s[68+8*\\j:69+8*\\j], v92, v[80+\\j]\n\tds_add_rtn_u32 v[112+4*\\j], v[96+4*\\j], %[four] offset:%c[cnt]\n\t"
        "v_cmpx_lt_u32_e64 s[70+8*\\j:71+8*\\j], v93, v[80+\\j]\n\tds_add_rtn_u32 v[113+4*\\j], v[97+4*\\j], %[four] offset:%c[cnt]\n\t"
        "s_mov_b64 exec, -1\n\t.endm\n\t"
        ".macro FPUT j\n\t"
        "s_waitcnt lgkmcnt(12)\n\t"
        ".irp k, 0, 1, 2, 3\n\tv_min_u32_e32 v[112+4*\\j+\\k], %c[sub]-4, v[112+4*\\j+\\k]\n\t.endr\n\t"
        ".irp k, 0, 1, 2, 3\n\tv_mad_u32_u24 v[112+4*\\j+\\k], v[96+4*\\j+\\k], %[sub4], v[112+4*\\j+\\k]\n\t.endr\n\t"
        ".irp k, 0, 1, 2, 3\n\ts_mov_b64 exec, s[68+8*\\j+2*\\k:69+8*\\j+2*\\k]\n\tds_write_b32 v[112+4*\\j+\\k], v[64+4*\\j+\\k] offset:%c[list]\n\t.endr\n\t"
        "s_mov_b64 exec, -1\n\t.endm\n\t"
        "s_movk_i32 %[o1], 0x1000\n\ts_movk_i32 %[o2], 0x2000\n\ts_movk_i32 %[o3], 0x3000\n\t"
        "v_mov_b32_e32 v92, %[i4]\n\tv_add_u32_e32 v93, 4, %[i4]\n\tv_add_u32_e32 v94, 8, %[i4]\n\tv_add_u32_e32 v95, 12, %[i4]\n\t"
        "s_mov_b32 %[todo], 15\n\ts_mov_b32 %[spin], 0x400000\n\ts_mov_b32 %[over], 0\n\t"
        ".if " STR(FASTIN) "\n\t"
        "s_waitcnt vmcnt(0)\n\t"
        ".irp j, 0, 1, 2, 3\n\tv_xor_b32_e32 v[64+4*\\j], %[key], v[64+4*\\j]\n\t.endr\n\t"
        ".irp j, 0, 1, 2, 3\n\tv_mov_b32_dpp v[80+\\j], v[64+4*\\j] quad_perm:[0,0,0,0] row_mask:0xf bank_mask:0xf\n\t.endr\n\t"
        ".irp j, 0, 1, 2, 3\n\tv_add_u32_e32 v[84+\\j], v[64+4*\\j], v[65+4*\\j]\n\t.endr\n\t"
        ".irp j, 0, 1, 2, 3\n\tv_mov_b32_dpp v[80+\\j], v[80+\\j] row_shr:4 row_mask:0xf bank_mask:0xa\n\t.endr\n\t"
        "s_mov_b64 exec, %[pc0]\n\t"
        ".irp j, 0, 1, 2, 3\n\tv_sub_u32_e32 v[84+\\j], 0, v[65+4*\\j]\n\t.endr\n\t"
        "s_mov_b64 exec, -1\n\t"
        "v_cmp_gt_i32_e64 %[m0], v80, v92\n\tv_cmp_gt_i32_e64 %[m1], v81, v92\n\tv_cmp_gt_i32_e64 %[m2], v82, v92\n\tv_cmp_gt_i32_e64 %[m3], v83, v92\n\t"
        ".irp j, 0, 1, 2, 3\n\tv_add3_u32 v[84+\\j], v[84+\\j], v[66+4*\\j], v[67+4*\\j]\n\t.endr\n\t"
        "v_cndmask_b32_e64 v84, 0, v84, %[m0]\n\tv_cndmask_b32_e64 v85, 0, v85, %[m1]\n\tv_cndmask_b32_e64 v86, 0, v86, %[m2]\n\tv_cndmask_b32_e64 v87, 0, v87, %[m3]\n\t"
        ".irp j, 0, 1, 2, 3\n\tv_add_u32_dpp v[84+\\j], v[84+\\j], v[84+\\j] quad_perm:[1,0,3,2] row_mask:0xf bank_mask:0xf\n\t.endr\n\t"
        ".irp j, 0, 1, 2, 3\n\tv_add_u32_dpp v[84+\\j], v[84+\\j], v[84+\\j] quad_perm:[2,3,0,1] row_mask:0xf bank_mask:0xf\n\t.endr\n\t"
        ".irp j, 0, 1, 2, 3\n\tv_add_u32_dpp v[84+\\j], v[84+\\j], v[84+\\j] row_half_mirror row_mask:0xf bank_mask:0xf\n\t.endr\n\t"
        "v_or3_b32 v96, v80, v81, v82\n\tv_max3_u32 v97, v80, v81, v82\n\tv_or_b32_e32 v96, v96, v83\n\tv_max_u32_e32 v97, v97, v83\n\t"
        "v_lshrrev_b32_e32 v96, 14, v96\n\tv_or3_b32 v96, v96, v84, v85\n\tv_or3_b32 v96, v96, v86, v87\n\t"
        "v_cmp_ne_u32_e32 vcc, 0, v96\n\t"
        "s_cbranch_vccnz 7f\n\t"
        "v_cmp_lt_u32_e32 vcc, %c[cap], v97\n\t"
        "FADD 0\n\tFADD 1\n\tFADD 2\n\tFADD 3\n\t"
        "s_cbranch_vccz 6f\n\ts_mov_b32 %[over], 1\n\t6:\n\t"
        "FPUT 0\n\tFPUT 1\n\tFPUT 2\n\tFPUT 3\n\t"
        "s_mov_b64 exec, %[pc0]\n\t"
        ".irp j, 0, 1, 2, 3\n\tds_write_b32 %[hdra], v[80+\\j] offset:128*\\j\n\t.endr\n\t"
        "s_mov_b64 exec, -1\n\t"
        "s_branch 9f\n\t"
        "7:\n\t.irp j, 0, 1, 2, 3\n\tv_xor_b32_e32 v[64+4*\\j], %[key], v[64+4*\\j]\n\t.endr\n\t"
        ".endif\n\t"
        "0:\n\t"
        "s_mov_b32 %[fresh], 0\n\t"
        "s_waitcnt vmcnt(0)\n\t"
        "IVAL 0, 0\n\tIVAL 1, %[o1]\n\tIVAL 2, %[o2]\n\tIVAL 3, %[o3]\n\t"
        "IRANK 0\n\tIRANK 1\n\tIRANK 2\n\tIRANK 3\n\t"
        "s_andn2_b32 %[todo], %[todo], %[fresh]\n\t"
        "s_cbranch_scc0 9f\n\t"
        "s_sub_u32 %[spin], %[spin], 1\n\t"
        "s_cbranch_scc0 0b\n\t"
        "s_waitcnt vmcnt(0)\n\t"
        "9:\n\t"
        ".purgem IVAL\n\t.purgem IRANK\n\t.purgem FADD\n\t.purgem FPUT"
        : "+{v[64:79]}"(raw), "=&{v[80:95]}"(t0), "=&{v[96:127]}"(t2), [m0] "=&s"(m0), [m1] "=&s"(m1), [m2] "=&s"(m2), [m3] "=&s"(m3),
          [todo] "=&s"(todo), [fresh] "=&s"(fresh), [spin] "=&s"(spin), [o1] "=&s"(o1), [o2] "=&s"(o2), [o3] "=&s"(o3), [over] "=&s"(over)
        : [srd] "s"(srd), [ib] "v"(ib), [key] "s"(key), [i4] "v"(i4), [hdra] "v"(hdra), [pc0] "s"(pc0), [sub4] "s"(SUBB / 4), [four] "v"(four),
          [cnt] "i"(L_CNT), [list] "i"(L_LIST), [sub] "i"(SUBB), [cap] "i"(4 * CAP1)
        : "vcc", "scc", "memory", "s68", "s69", "s70", "s71", "s72", "s73", "s74", "s75", "s76", "s77", "s78", "s79", "s80", "s81", "s82", "s83",
          "s84", "s85", "s86", "s87", "s88", "s89", "s90", "s91", "s92", "s93", "s94", "s95", "s96", "s97", "s98", "s99");
    return over;
}

template <int CTRL, int BANK> __device__ __forceinline__ unsigned dpp(unsigned old, unsigned v) { return __builtin_amdgcn_update_dpp(old, v, CTRL, 0xF, BANK, true); }
__device__ __forceinline__ unsigned bcast8(unsigned v) { v = dpp<0x00, 0xF>(0u, v); return __builtin_amdgcn_update_dpp(v, v, 0x114, 0xF, 0xA, false); }
__device__ __forceinline__ unsigned sum8(unsigned v) { v += dpp<0xB1, 0xF>(0u, v); v += dpp<0x4E, 0xF>(0u, v); return v + dpp<0x141, 0xF>(0u, v); }
__device__ __forceinline__ bf16x4 tr_read(LAS char* p) { return __builtin_amdgcn_ds_read_tr16_b64_v4bf16((LAS bf16x4*)p); }
__device__ __forceinline__ bf16x8 cat(bf16x4 a, bf16x4 b) { return __builtin_shufflevector(a, b, 0, 1, 2, 3, 4, 5, 6, 7); }
__device__ __forceinline__ unsigned pk2(float a, float b) { f32x2 v = {a, b}; bf16x2 r = __builtin_convertvector(v, bf16x2); return __builtin_bit_cast(unsigned, r); }
__device__ __forceinline__ f32x4 mfma(bf16x8 a, bf16x8 b, f32x4 c) { return __builtin_amdgcn_mfma_f32_16x16x32_bf16(a, b, c, 0, 0, 0); }
__device__ __forceinline__ unsigned lds_add(LAS char* p, unsigned v) { return __hip_atomic_fetch_add((LAS unsigned*)p, v, __ATOMIC_RELAXED, __HIP_MEMORY_SCOPE_WORKGROUP); }
__device__ __forceinline__ LAS unsigned* ldsp(unsigned a) { return (LAS unsigned*)(size_t)a; }
__device__ __forceinline__ unsigned byte0_x4(unsigned v) { unsigned r; asm("v_lshlrev_b32_sdwa %0, 2, %1 dst_sel:DWORD dst_unused:UNUSED_PAD src0_sel:DWORD src1_sel:BYTE_0" : "=v"(r) : "v"(v)); return r; }
__device__ __forceinline__ unsigned mad24(unsigned a, unsigned b, unsigned c) { unsigned r; asm("v_mad_u32_u24 %0, %1, %2, %3" : "=v"(r) : "v"(a), "s"(b), "v"(c)); return r; }
__device__ __forceinline__ void lds_arrive(LAS char* p, int lane) {
    asm volatile("s_waitcnt lgkmcnt(0)" ::: "memory");
    if (lane == 0) __hip_atomic_fetch_add((LAS unsigned*)p, 1u, __ATOMIC_RELAXED, __HIP_MEMORY_SCOPE_WORKGROUP);
}
__device__ __forceinline__ void lds_await(LAS char* p, unsigned n) {
    while (__builtin_amdgcn_readfirstlane(*(volatile LAS unsigned*)p) < n) __builtin_amdgcn_s_sleep(1);
    asm volatile("" ::: "memory");
}
__device__ __forceinline__ unsigned rfl(unsigned v) { return __builtin_amdgcn_readfirstlane(v); }

__global__ __launch_bounds__(512, 2) void k_graph(const unsigned short* x_, const int* src_, const int* dst_, const unsigned short* wq_, const unsigned short* wv_,
                                                  unsigned* ws_, unsigned short* out_, int N_, int E_) {
    u32x4 srd_ws, srd_x0, pw, po;
    u32x16 ed, es;
    u64 t_start;
    asm volatile("s_load_dwordx2 s[40:41], %[ka], 0x10\n\ts_load_dwordx2 s[44:45], %[ka], 0x8\n\ts_load_dwordx4 s[60:63], %[ka], 0x30\n\t"
                 "s_getpc_b64 s[68:69]\n\ts_movk_i32 s70, " STR(CODE_BYTES) "\n\ts_mov_b32 s71, 0x20000\n\ts_mov_b32 m0, %c[dump]\n\t"
                 "s_movk_i32 s66, 0x8000\n\tv_lshl_add_u32 v77, %[t], 7, s66\n\t"
                 "buffer_load_dword v77, s[68:71], 0 offen lds\n\t"
                 "s_load_dwordx2 s[48:49], %[ka], 0x28\n\ts_load_dwordx2 s[52:53], %[ka], 0x0\n\ts_load_dwordx4 s[56:59], %[ka], 0x18\n\t"
                 "s_memrealtime s[64:65]\n\t"
                 "v_readfirstlane_b32 s72, %[t]\n\t"
                 "s_and_b32 s66, %[b], 7\n\ts_lshl_b32 s66, s66, 4\n\ts_lshr_b32 s67, %[b], 4\n\ts_or_b32 s66, s66, s67\n\ts_mulk_i32 s66, %c[ch4]\n\t"
                 "s_mov_b32 s43, s71\n\ts_mov_b32 s47, s71\n\t"
                 "v_lshl_add_u32 v77, %[t], 4, s66\n\t"
                 "s_lshr_b32 s67, s72, 5\n\ts_or_b32 s67, s67, %[b]\n\t"
                 "s_movk_i32 s73, 0x1000\n\ts_movk_i32 s74, 0x2000\n\t"
                 "s_bitcmp1_b32 s67, 3\n\ts_waitcnt lgkmcnt(0)\n\ts_cbranch_scc1 1f\n\t"
                 "s_lshl_b32 s42, s63, 2\n\ts_lshl_b32 s46, s63, 2\n\t"
                 "buffer_load_dwordx4 v[64:67], v77, s[40:43], 0 offen\n\tbuffer_load_dwordx4 v[68:71], v77, s[40:43], s73 offen\n\t"
                 "buffer_load_dwordx4 v[72:75], v77, s[40:43], s74 offen\n\t"
                 "s_add_u32 s66, s66, 0x3000\n\ts_cmp_lt_u32 s72, 64\n\ts_cselect_b32 s66, s66, %c[oob]\n\t"
                 "v_lshl_add_u32 v78, %[t], 2, s66\n\t"
                 "buffer_load_dword v76, v78, s[40:43], 0 offen\n\t"
                 "buffer_load_dwordx4 v[80:83], v77, s[44:47], 0 offen\n\tbuffer_load_dwordx4 v[84:87], v77, s[44:47], s73 offen\n\t"
                 "buffer_load_dwordx4 v[88:91], v77, s[44:47], s74 offen\n\tbuffer_load_dword v92, v78, s[44:47], 0 offen\n\t"
                 "s_mov_b32 s51, s71\n\ts_mov_b32 s50, %c[wsb]\n\ts_lshl_b32 s66, %[b], 2\n\t"
                 "v_lshlrev_b32_e32 v77, 2, %[t]\n\tv_mov_b32_e32 v78, s66\n\t"
                 "buffer_load_dword v93, v77, s[48:51], 0 offen sc1\n\tbuffer_load_dword v94, v78, s[48:51], 0 offen sc1\n\t"
                 "1:\n\ts_mov_b32 s51, s71\n\ts_mov_b32 s50, %c[wsb]\n\ts_mov_b32 s55, s71\n\ts_lshl_b32 s54, s62, 8"
                 : "=&{v[64:79]}"(ed), "=&{v[80:95]}"(es)
                 : [ka] "s"(__builtin_amdgcn_kernarg_segment_ptr()), [t] "v"(threadIdx.x), [b] "s"(blockIdx.x),
                   [wsb] "i"(WS_BYTES), [dump] "i"(L_DUMP), [ch4] "i"(CH * 4), [oob] "i"(OOB)
                 : "memory", "scc", "s40", "s41", "s42", "s43", "s44", "s45", "s46", "s47", "s48", "s49", "s50", "s51", "s52", "s53", "s54", "s55",
                   "s56", "s57", "s58", "s59", "s60", "s61", "s62", "s63", "s64", "s65", "s66", "s67", "s68", "s69", "s70", "s71", "s72", "s73", "s74");
    asm volatile("" : "={s[48:51]}"(srd_ws), "={s[52:55]}"(srd_x0), "={s[56:59]}"(pw), "={s[60:63]}"(po), "={s[64:65]}"(t_start));
    FENCE();
    __shared__ __attribute__((aligned(16))) char smem_[L_TOTAL];
    LAS char* smem = (LAS char*)smem_;
    const int t = threadIdx.x, l = t & 63, b = blockIdx.x;
    const int w = rfl(t >> 6);
    const int g = l >> 4, n = l & 15, q = (l >> 2) & 3, p = l & 3, pc = t & 7;
    const unsigned lds0 = (unsigned)(size_t)smem;
    const bool isprod = ((b >> 3) & 1) == 0;
    const int pg = ((b & 7) << 4) | (b >> 4);
    const unsigned e0 = pg * CH + 4 * t, e1 = pg * CH + 3072 + t;

    const void* const x = (const void*)((u64)srd_x0.x | ((u64)srd_x0.y << 32));
    const void* const wq = (const void*)((u64)pw.x | ((u64)pw.y << 32));
    const void* const wv = (const void*)((u64)pw.z | ((u64)pw.w << 32));
    void* const out = (void*)((u64)po.x | ((u64)po.y << 32));
    const int N = po.z, E = po.w;
    const unsigned xbytes = srd_x0.z;
    STAMP_DECL;
    const unsigned ring = lds0 + L_RING + w * 16384;
    const int jn = 8 * n + w;

    unsigned mykey = 0;
    u64 t_go = t_start + POLL_T;
    auto node_row = [&](int tile, int m) { const unsigned j = 8 * (4 * m + g) + tile; return (j << 16) | ((b ^ j) << 8); };
    auto zero_staging = [&]() {
        const u32x4 z = {0u, 0u, 0u, 0u};
#pragma unroll
        for (int k = 0; k < 5; ++k) *(LAS u32x4*)(smem + L_IMG + k * 8192 + t * 16) = z;
    };

    u32x16 addr;
    f32x32 acc, wva, wvb;
    unsigned cv, lista, cnta, qa, keyn, inf, ow, go;
    Srd4 srd_x;
    auto loop_constants = [&]() {
        srd_x = make_srd4(x, xbytes, BIAS * 256);
        lista = lds0 + L_LIST + w * (2 * SUBB) + g * 4; cnta = lds0 + L_CNT + (l & 31) * 4; qa = ring + l * 16; keyn = jn; inf = 0x7F800000u;
        ow = ring + n * 256 + ((g ^ n) << 4) + ((g & 1) << 3); go = node_row(w, 0) + ((n ^ g) << 4);
        asm volatile("" : "+s"(srd_x.s0), "+s"(srd_x.s1), "+s"(srd_x.s2), "+s"(srd_x.s3));
        asm volatile("" : "+v"(lista), "+v"(cnta), "+v"(qa), "+v"(keyn), "+v"(inf), "+v"(ow), "+v"(go));
        const unsigned o0 = (n ^ (((g & 1) << 3) | (g >> 1))) << 4;
        const unsigned ra = (4 * (n & 3) + (n >> 2)) * 256 + (((4 * g) ^ (((n & 7) << 1) | (n >> 3))) << 4);
        const int i = 4 * g + q, r8 = i & 7;
        const unsigned ta = (4 * q + g) * 256 + 8 * (p & 1) + (((p >> 1) ^ (((i & 7) << 1) | (i >> 3))) << 4);
        cv = lds0 + L_WV1 + i * 256 + (((p ^ ((r8 & 1) | ((r8 & 6) << 1))) << 4) | ((p & 1) << 3));
#pragma unroll
        for (int s = 0; s < 4; ++s) { addr[s] = ring + (ra ^ (s << 4)); addr[12 + s] = o0 ^ (s << 5); }
#pragma unroll
        for (int c = 0; c < 8; ++c) addr[4 + c] = ring + (ta ^ (c << 5));
        asm volatile("" : "+{v[176:191]}"(addr), "+v"(cv));
    };
    auto clear_sums = [&]() { acc = 0.f; asm volatile("" : "+{v[128:159]}"(acc)); };
    auto wv_resident = [&]() {
#pragma unroll
        for (int a = 0; a < 8; ++a) {
            LAS char* pa = (LAS char*)(size_t)((cv - (L_WV1 - L_WV0)) ^ ((a >> 1) << 6) ^ ((a & 1) << 3));
            const f32x4 f0 = __builtin_bit_cast(f32x4, cat(tr_read(pa), tr_read(pa + 4096)));
            const f32x4 f1 = __builtin_bit_cast(f32x4, cat(tr_read(pa + 16384), tr_read(pa + 16384 + 4096)));
#pragma unroll
            for (int k = 0; k < 4; ++k) { wva[4 * a + k] = f0[k]; wvb[4 * a + k] = f1[k]; }
        }
        asm volatile("" : "+{v[224:255]}"(wva), "+{v[32:63]}"(wvb));
    };

    if (isprod) zero_staging();
    if (t < 8) *(LAS u32x4*)(smem + L_CNT + t * 16) = u32x4{0u, 0u, 0u, 0u};
    WG_BARRIER();

    if (__builtin_expect(w < 4, 1)) {
        if (__builtin_expect(isprod, 1)) {
            if (PRIO) __builtin_amdgcn_s_setprio(2);
            unsigned ob = WS_INBOX + ((t >> 3) * G1 + pg) * 128 + pc * 16, ima = lds0 + L_IMG + (t >> 3) * 144 + pc * 16;
            PIN(ob); PIN(ima);
            unsigned cna = lds0 + L_CNT1 + (t >> 3) * 4;
            PIN(cna);
            const bool v0 = e0 < (unsigned)E, v1 = e0 + 1024 < (unsigned)E, v2 = e0 + 2048 < (unsigned)E, v3 = w == 0 && e1 < (unsigned)E;
            STAMP(1);
            u32x16 rk;
            const u64 over = rank_edges(ed, es, rk, __builtin_amdgcn_ballot_w64(v0), __builtin_amdgcn_ballot_w64(v1), __builtin_amdgcn_ballot_w64(v2),
                                        __builtin_amdgcn_ballot_w64(v3), t * 4);
            mykey = es[14];
            if (__builtin_expect(over != 0, 0)) {
#pragma unroll
                for (int k = 0; k < 13; ++k)
                    if (rk[k] >= 4u * CAP1) {
                        const unsigned o = WS_OVF + (pg * CH + lds_add(smem + L_CNT1 + 2048, 1)) * 4;
                        const unsigned v = ((((es[k] >> 8) & 0xFFFF) - BIAS) << 16) | ed[k];
                        ST1(v, srd_ws, o, " sc1");
                    }
            }
            if (XSYNC) { lds_arrive(smem + L_CNT + 96, l); lds_await(smem + L_CNT + 96, 4); } else WG_BARRIER();
            STAMP(3);
            f32x32 img; u32x16 meta, sums;
            const unsigned no = read_image(img, meta, sums, ima, cna);
            lds_arrive(smem + L_CNT + 84, l);
            if (__builtin_expect(no != 0, 0)) {
                const unsigned o = t == 0 ? WS_NOVF + pg * 4 : OOB;
                ST1(no, srd_ws, o, " sc1");
                VMWAIT(0);
                if (XSYNC) { lds_arrive(smem + L_CNT + 100, l); lds_await(smem + L_CNT + 100, 4); } else WG_BARRIER();
            }
            publish_image(img, meta, sums, srd_ws, ob);
            if (GATE) t_go = max(t_go, __builtin_amdgcn_s_memrealtime() + POLL_P);
            STAMP(4);
        } else {
            { const unsigned om = WS_KEY + b * 4; asm volatile("buffer_load_dword %0, %1, %2, 0 offen sc1\n\ts_waitcnt vmcnt(0)" : "=&v"(mykey) : "v"(om), "s"(srd_ws) : "memory"); }
            STAMP(1);
            if (PARTNER) {
                set_m0(lds0 + L_DUMP + w * 256);
                const unsigned o = (((b - 8) ^ (t >> 1)) << 8) + ((t >> 1) << 16) + ((t & 1) << 7);
                while (__builtin_amdgcn_s_memrealtime() < t_start + X_T) __builtin_amdgcn_s_sleep(1);
                asm volatile("buffer_load_dword %1, %0, 0 offen lds" :: "s"(srd_x0), "v"(o) : "memory");
            }
            if (GATE) { lds_await(smem + L_CNT + 104, 4); t_go = max(t_go, __builtin_amdgcn_s_memrealtime() + POLL_Q); }
            if (PRIO) __builtin_amdgcn_s_setprio(2);
            STAMP(2); STAMP(3); STAMP(4);
        }
        STAMP(5);
        mykey = rfl(mykey) + KEY_STEP;
        loop_constants();
        clear_sums();
        unsigned ib = WS_INBOX + b * (G1 * 128) + t * 16;
        PIN(ib);
        if (POLL_D && !GATE) { lds_await(smem + L_CNT + 80, 4); t_go = max(t_go, __builtin_amdgcn_s_memrealtime() + POLL_D); }
        while (__builtin_amdgcn_s_memrealtime() < t_go) __builtin_amdgcn_s_sleep(1);
        STAMP(6);
        u32x16 raw;
        inbox_request(raw, srd_ws, ib);
        if (__builtin_expect(inbox_stage(raw, srd_ws, ib, mykey, 16 * pc - 8, lds0 + L_HDR + (t >> 3) * 4) != 0, 0)) {
            LAS unsigned* hp = (LAS unsigned*)(smem + L_HDR) + 32 * (l >> 3 & 3) + 8 * w + (l & 7);
            const unsigned hv = *hp;
            for (u64 m = __builtin_amdgcn_ballot_w64(l < 32 && hv > 4u * CAP1); m; m &= m - 1) {
                const int k = __builtin_ctzll(m), gp = 32 * (k >> 3) + 8 * w + (k & 7);
                unsigned u, no; const unsigned o = WS_OVF + (gp * CH + l) * 4, on = WS_NOVF + gp * 4;
                asm volatile("buffer_load_dword %0, %2, %4, 0 offen sc1\n\tbuffer_load_dword %1, %3, %4, 0 offen sc1\n\ts_waitcnt vmcnt(0)"
                             : "=&v"(u), "=&v"(no) : "v"(o), "v"(on), "s"(srd_ws) : "memory");
                if (rfl(no) <= 64) {
                    if (l < no && ((u ^ (u >> 8)) & 0xFF) == (unsigned)b) {
                        const unsigned e = __builtin_amdgcn_perm((u >> 16) + BIAS, u, 0x01050401u), key = (e >> 21) & 60;
                        *(LAS unsigned*)(smem + L_LIST + key * (SUBB / 4) + min(lds_add(smem + L_CNT + key, 4), SUBB - 4u)) = e;
                    }
                    if (l == k) *hp = hv | 0x80000000u;
                } else *(LAS unsigned*)(smem + L_CNT + 64) = 1;
            }
        }
        if (PRIO) __builtin_amdgcn_s_setprio(0);
        STAMP(7);
    } else {
        const int j = w - 4;
        auto prefetch = [&](int k0, int kn) {
            const unsigned ln = (b >> 3) * (kn / 4) + (t - 256), o = t - 256 < kn / 4 ? k0 * 1024 + ln * 128 : OOB;
            set_m0(lds0 + L_DUMP + w * 256);
            asm volatile("s_nop 0\n\tbuffer_load_dword %1, %0, 0 offen lds" :: "s"(srd_x0), "v"(o) : "memory");
        };
        STAMP(1);
        u32x4 wr[8], xr[8];
        {
            const u32x4 srd_q = make_srd(wq, 32768);
            const unsigned o = j * 8192 + l * 16, k4 = 4096;
            LD4(wr[0], srd_q, o, ""); LD4(wr[1], srd_q, o, " offset:1024"); LD4(wr[2], srd_q, o, " offset:2048"); LD4(wr[3], srd_q, o, " offset:3072");
            LD4S(wr[4], srd_q, o, k4, ""); LD4S(wr[5], srd_q, o, k4, " offset:1024"); LD4S(wr[6], srd_q, o, k4, " offset:2048"); LD4S(wr[7], srd_q, o, k4, " offset:3072");
        }
        unsigned ox[8];
#pragma unroll
        for (int m = 0; m < 8; ++m) { ox[m] = node_row(m < 4 ? j : w, m & 3) + n * 16; PIN(ox[m]); }
        unsigned aqa[8], xfa[4];
        unsigned xa = ring + g * 256 + ((n ^ g) << 4), wa = lds0 + L_WQ + j * 16384 + g * 256 + ((n ^ g) << 4) + ((n & 4) << 1);
        {
            const unsigned aq = lds0 + L_WQ + (8 * g + q) * 256 + ((((4 * p) ^ (q | ((g & 1) << 2))) << 4) | ((p & 1) << 3));
#pragma unroll
            for (int s = 0; s < 4; ++s) { xfa[s] = ring + n * 256 + (((4 * s + g) ^ n) << 4); PIN(xfa[s]); }
#pragma unroll
            for (int c = 0; c < 8; ++c) { aqa[c] = aq ^ ((c >> 1) << 4) ^ ((c & 1) << 3); PIN(aqa[c]); }
            PIN(xa); PIN(wa);
        }
        loop_constants();
        STAMP(2);
        if (isprod && !XSYNC) {
            WG_BARRIER();
            if (__builtin_expect(rfl(*(LAS unsigned*)(smem + L_CNT1 + 2048)) != 0, 0)) WG_BARRIER();
            if (W_SLEEP) __builtin_amdgcn_s_sleep(W_SLEEP);
        }
        STAMP(3);
        auto row_loads = [&]() {
#pragma unroll
            for (int m = 0; m < 8; ++m) LD4(xr[m], srd_x0, ox[m], "");
        };
        asm volatile("s_waitcnt vmcnt(0)" : "+v"(wr[0]), "+v"(wr[1]), "+v"(wr[2]), "+v"(wr[3]), "+v"(wr[4]), "+v"(wr[5]), "+v"(wr[6]), "+v"(wr[7]) :: "memory");
        if (GATE && !isprod) lds_arrive(smem + L_CNT + 104, l);
        row_loads();
#pragma unroll
        for (int m = 0; m < 8; ++m) {
            const unsigned a = (wa ^ ((m & 2) << 5)) + m * 1024;
            *(LAS u32x2*)(size_t)a = u32x2{wr[m].x, wr[m].y};
            *(LAS u32x2*)(size_t)(a ^ 8) = u32x2{wr[m].z, wr[m].w};
        }
        asm volatile("s_waitcnt vmcnt(0)" : "+v"(xr[0]), "+v"(xr[1]), "+v"(xr[2]), "+v"(xr[3]), "+v"(xr[4]), "+v"(xr[5]), "+v"(xr[6]), "+v"(xr[7]) :: "memory");
#pragma unroll
        for (int m = 0; m < 8; ++m) *(LAS u32x4*)(size_t)((xa ^ ((m & 3) << 6)) + m * 1024) = xr[m];
        STAMP(4);
        lds_arrive(smem + L_CNT + 80, l);
        lds_await(smem + L_CNT + 80, 4);
        STAMP(5);
        bf16x8 xo[2][4], fr[2][8];
        f32x4 qc[2][8];
        auto wq_frags = [&](int u, bf16x8 (&f)[8]) {
#pragma unroll
            for (int a = 0; a < 8; ++a) {
                LAS char* pa = (LAS char*)(size_t)aqa[a] + u * 16384;
                f[a] = cat(tr_read(pa), tr_read(pa + 1024));
            }
        };
#pragma unroll
        for (int u = 0; u < 4; ++u) { xo[0][u] = *(LAS bf16x8*)(size_t)xfa[u]; xo[1][u] = *(LAS bf16x8*)(size_t)(xfa[u] + 4096); }
        wq_frags(0, fr[0]);
        {
            const Srd4 srd_v = make_srd4(wv, 32768);
            const unsigned r0 = (j * 32 + g) << 8, o0 = (n ^ ((g & 1) | ((g & 2) << 1))) << 4;
            const unsigned e[8] = {r0, r0 + 1024, r0 + 2048, r0 + 3072, r0 + 4096, r0 + 5120, r0 + 6144, r0 + 7168};
            const unsigned base = lds0 + (j < 2 ? L_WV0 + j * 16384 : L_WV1 + (j - 2) * 8192);
            const u32x4 off = {o0, o0 ^ 128, o0, o0 ^ 128};
            dma_half(base, srd_v, e, off); dma_half(base + 4096, srd_v, e + 4, off);
        }
        if (PFA_KB) prefetch(0, PFA_KB);
#pragma unroll
        for (int u = 0; u < 4; ++u) {
            if (u < 3) wq_frags(u + 1, fr[(u + 1) & 1]);
            FENCE();
#pragma unroll
            for (int a = 0; a < 8; ++a)
#pragma unroll
                for (int tt = 0; tt < 2; ++tt) qc[tt][a] = mfma(fr[u & 1][a], xo[tt][u], u ? qc[tt][a] : f32x4{0.f, 0.f, 0.f, 0.f});
            FENCE();
            if (!WVLATE && u == 1 && j < 2) { VMWAIT(0); lds_arrive(smem + L_CNT + 88, l); }
        }
        STAMP(6);
        if (PFB_KB) { asm volatile("s_waitcnt vmcnt(%c0)" :: "i"(PFA_KB ? 1 : 0) : "memory"); prefetch(PFA_KB, PFB_KB); }
        if (isprod) lds_await(smem + L_CNT + 84, 4);
#pragma unroll
        for (int tt = 0; tt < 2; ++tt)
#pragma unroll
            for (int s = 0; s < 4; ++s) {
                const float k = -0.08838834764831845f * 1.4426950408889634f;
                const f32x4 c0 = qc[tt][2 * s] * k, c1 = qc[tt][2 * s + 1] * k;
                *(LAS u32x4*)(size_t)(ring - (tt ? 0 : 65536) + s * 1024 + l * 16) = u32x4{pk2(c0[0], c0[1]), pk2(c0[2], c0[3]), pk2(c1[0], c1[1]), pk2(c1[2], c1[3])};
            }
        clear_sums();
        if (!WVLATE) { lds_await(smem + L_CNT + 88, 2); wv_resident(); }
        if (!PFB_KB) asm volatile("s_waitcnt vmcnt(%c0)" :: "i"(PFA_KB ? 1 : 0) : "memory");
        if (PFC_KB) prefetch(PFA_KB + PFB_KB, PFC_KB);
        STAMP(7);
    }
    WG_BARRIER();
    STAMP(8);
    if (LPRIO >= 3) __builtin_amdgcn_s_setprio(3);
    if (LPRIO == 1 && w >= 4) __builtin_amdgcn_s_setprio(1);
    if (LPRIO == 2) { if (w >= 6) __builtin_amdgcn_s_setprio(3); else if (w >= 4) __builtin_amdgcn_s_setprio(2); else if (w >= 2) __builtin_amdgcn_s_setprio(1); }

    u32x16 qf;
    const u32x4 doff = {addr[12], addr[13], addr[14], addr[15]};
    LAS char* mylist = smem + L_LIST + w * (2 * SUBB);
    int gcur = G1, pos = 0;
    unsigned hmask = 0xFFFFFFFFu;
    auto batch = [&](auto first_, const int l) {
        constexpr bool first = decltype(first_)::value;
        auto entries = [&](int grp, unsigned (&e)[8]) {
#pragma unroll
            for (int m = 0; m < 8; ++m) e[m] = *(LAS unsigned*)(size_t)(lista + grp * 128 + m * 16);
        };
        unsigned cw = *(LAS unsigned*)(size_t)(first ? cnta : lds0 + L_CNT + (l & 31) * 4), e0[8], e1[8];
        entries(0, e0);
        if constexpr (first) {
#pragma unroll
            for (int s = 0; s < 4; ++s) {
                const u32x4 v = *(LAS u32x4*)(size_t)(qa + s * 1024);
                qf[4 * s] = v.x; qf[4 * s + 1] = v.y; qf[4 * s + 2] = v.z; qf[4 * s + 3] = v.w;
            }
        }
        entries(1, e1);
        FENCE();
        int lo = __builtin_amdgcn_readlane(cw, 2 * w) >> 2, hi = __builtin_amdgcn_readlane(cw, 2 * w + 1) >> 2;
        if constexpr (first) {
            if (__builtin_amdgcn_readlane(cw, 16)) gcur = 0;
            if (__builtin_amdgcn_ballot_w64(cw > 4u * CAP2)) { gcur = -1; lo = 0; hi = 0; hmask = 0x7FFFFFFFu; }
        }
        const int c = lo + hi, G = (c + 31) >> 5;
        const int nlast = G > 2 ? (((c - 1) & 31) >> 2) + 1 : 8;
        auto join = [&]() {
            for (int k = 0; k < G * 32 - lo; k += 64) {
                const unsigned e = *(LAS unsigned*)(mylist + SUBB + (l + k) * 4);
                if (l + k < G * 32 - lo) *(LAS unsigned*)(mylist + (lo + l + k) * 4) = l + k < hi ? e : PAD;
            }
        };
        const bool early = lo >= 64;
        if (__builtin_expect(!early, 0)) { join(); entries(0, e0); entries(1, e1); }
        if constexpr (first) asm volatile("s_waitcnt lgkmcnt(0)" : "+{v[160:175]}"(qf) :: "memory");
        if (G > 0) dma_group(ring, srd_x, e0, doff);
        if constexpr (first) {
            if (WVLATE == 1) {
                if (G > 1 && w < 6) dma_group(ring + 8192, srd_x, e1, doff);
                wv_resident();
                lds_arrive(smem + L_CNT + 92, l);
                if (w >= 6) { lds_await(smem + L_CNT + 92, 8); if (G > 1) dma_group(ring + 8192, srd_x, e1, doff); }
            } else if (WVLATE == 2) {
                wv_resident();
                lds_arrive(smem + L_CNT + 92, l);
                if (w >= 6) lds_await(smem + L_CNT + 92, 8);
                if (G > 1) dma_group(ring + 8192, srd_x, e1, doff);
            } else {
                if (w < 4) { wv_resident(); lds_arrive(smem + L_CNT + 92, l); }
                else if (w >= 6) lds_await(smem + L_CNT + 92, 4);
                if (G > 1) dma_group(ring + 8192, srd_x, e1, doff);
            }
        } else if (G > 1) dma_group(ring + 8192, srd_x, e1, doff);
        f32x16 cc;
#pragma unroll
        for (int m = 0; m < 8; ++m) {
            cc[m] = (e0[m] & 0xFF) == keyn ? 0.f : __uint_as_float(inf);
            cc[8 + m] = (e1[m] & 0xFF) == keyn ? 0.f : __uint_as_float(inf);
        }
        if (early) join();
        STAMP(9);
        if (G > 0) gather_loop(acc, qf, addr, u32x4{lista, keyn, inf, 0u}, cc, srd_x, ring, G, nlast);
    };
    batch(std::true_type{}, l);
    if (__builtin_expect(gcur < G1, 0)) do {
        int t = threadIdx.x;
        PIN(t);
        const int l = t & 63;
        unsigned lim = G1 * 32;
        if (gcur >= 0) {
            const u64 m0 = __builtin_amdgcn_ballot_w64(l >= gcur && (int)(*(LAS unsigned*)(smem + L_HDR + l * 4) & hmask) > 4 * CAP1);
            const u64 m1 = __builtin_amdgcn_ballot_w64(l + 64 >= gcur && (int)(*(LAS unsigned*)(smem + L_HDR + 256 + l * 4) & hmask) > 4 * CAP1);
            if (!(m0 | m1)) break;
            gcur = m0 ? __builtin_ctzll(m0) : 64 + __builtin_ctzll(m1);
            unsigned v; const unsigned o = WS_NOVF + gcur * 4;
            asm volatile("buffer_load_dword %0, %1, %2, 0 offen sc1\n\ts_waitcnt vmcnt(0)" : "=&v"(v) : "v"(o), "s"(srd_ws) : "memory");
            lim = rfl(v);
        }
        const unsigned i = pos + t;
        const bool cand = t < CAP2 && i < lim;
        unsigned u;
        { const unsigned o = cand ? (gcur < 0 ? WS_INBOX + b * (G1 * 128) : WS_OVF + gcur * CH * 4) + i * 4 : OOB;
          asm volatile("buffer_load_dword %0, %1, %2, 0 offen sc1\n\ts_waitcnt vmcnt(0)" : "=&v"(u) : "v"(o), "s"(srd_ws) : "memory"); }
        WG_BARRIER();
        if (t < 16) *(LAS unsigned*)(smem + L_CNT + t * 4) = 0;
        WG_BARRIER();
        if (cand) {
            unsigned e; bool v;
            if (gcur < 0) { e = (i & 3) ? u : u ^ mykey; v = ((i & 31) - 2) * 4 < *(LAS unsigned*)(smem + L_HDR + (i >> 5) * 4); }
            else { e = (((u >> 16) + BIAS) << 8) | ((u >> 8) & 0xFF); v = ((u ^ (u >> 8)) & 0xFF) == (unsigned)b; }
            if (v) *(LAS unsigned*)(smem + L_LIST + (e & 7) * (2 * SUBB) + lds_add(smem + L_CNT + (e & 7) * 8, 4)) = e;
        }
        pos += CAP2;
        if ((unsigned)pos >= lim) { ++gcur; pos = 0; }
        WG_BARRIER();
        batch(std::false_type{}, l);
    } while (gcur < G1);
    STAMP(10);

    output_tile(acc, wva, wvb, cv, ow, qa, go, make_srd(out, xbytes));
    if (w == 0) { const unsigned o = t == 0 ? WS_KEY + b * 4 : OOB; ST1(mykey, srd_ws, o, " sc1"); }
    STAMP(11);
}

extern "C" void kernel_launch(void* const* d_in, const int* in_sizes, int n_in,
                              void* d_out, int out_size, void* d_ws, size_t ws_size,
                              hipStream_t stream) {
    const int N = in_sizes[0] / 128, E = in_sizes[1];
    k_graph<<<NB, 512, 0, stream>>>((const unsigned short*)d_in[0], (const int*)d_in[1], (const int*)d_in[2],
                                    (const unsigned short*)d_in[3], (const unsigned short*)d_in[4],
                                    (unsigned*)d_ws, (unsigned short*)d_out, N, E);
}
```

```cpp
#include <hip/hip_runtime.h>
#include <stdint.h>
#include <type_traits>
#define STR2(x) #x
#define STR(x) STR2(x)

typedef __attribute__((ext_vector_type(8))) __bf16 bf16x8;
typedef __attribute__((ext_vector_type(4))) __bf16 bf16x4;
typedef __attribute__((ext_vector_type(2))) __bf16 bf16x2;
typedef __attribute__((ext_vector_type(4))) float f32x4;
typedef __attribute__((ext_vector_type(2))) float f32x2;
typedef __attribute__((ext_vector_type(4))) unsigned u32x4;
typedef __attribute__((ext_vector_type(2))) unsigned u32x2;
typedef __attribute__((ext_vector_type(32))) float f32x32;
typedef __attribute__((ext_vector_type(16))) float f32x16;
typedef __attribute__((ext_vector_type(16))) unsigned u32x16;
typedef __attribute__((ext_vector_type(12))) unsigned u32x12;
typedef unsigned long long u64;
#define LAS __attribute__((address_space(3)))

constexpr int NB = 256;
constexpr int G1 = 128;
constexpr int CH = 3136;
constexpr int CAP1 = 30;
constexpr int CAPX = 16;
constexpr int CAP2 = 192;
constexpr int SUBB = 784;
constexpr int SPLIT = 16384;
constexpr int PF_KB = 1024;
constexpr int POLL_T = 300;
constexpr int POLL_P = 70;
constexpr int POLL_Q = 180;
#define CODE_BYTES 23936

constexpr unsigned KEY_STEP = 0x9E3779B1u;
constexpr unsigned WS_KEY = 0;
constexpr unsigned WS_NOVF = 2048;
constexpr unsigned WS_INBOX = 4096;
constexpr unsigned WS_OVF = WS_INBOX + NB * G1 * 128;
constexpr unsigned WS_EXT = WS_OVF + G1 * CH * 4;
constexpr unsigned WS_BYTES = WS_EXT + G1 * CAPX * 8;
constexpr unsigned OOB = 0x80000000u;
constexpr unsigned BIAS = 32768 - SPLIT;
constexpr unsigned PAD = 0x00FFFFFFu;

constexpr int L_CNT = 0;
constexpr int L_STAGED = 80, L_IMGREAD = 84, L_WVREAD = 92, L_RANKED = 96, L_SPILLED = 100, L_WQHERE = 104;
constexpr int L_HDR = 128;
constexpr int L_LIST = 768;
constexpr int L_RING = L_LIST + 16 * SUBB;
constexpr int L_IMG = L_RING;
constexpr int L_CNT1 = L_IMG + NB * 144;
constexpr int L_WQ = L_RING + 2 * 16384 + 8192;
constexpr int L_WV0 = L_RING + 6 * 16384 + 8192;
constexpr int L_WV1 = L_RING + 131072;
constexpr int L_DUMP = L_WV1 + 16384;
constexpr int L_TOTAL = L_DUMP + 2048;
static_assert(PF_KB == 1024 && CAPX == 16 && WS_EXT % 128 == 0 && WS_KEY == 0 && L_WV1 % 256 == 0 && CH == 3072 + 64 && L_RING % 256 == 0 && L_TOTAL <= 163840, "LDS map");

__device__ __forceinline__ u32x4 make_srd(const void* p, unsigned bytes, unsigned back = 0) {
    const u64 a = (u64)p - back;
    u32x4 s; s.x = (unsigned)a; s.y = (unsigned)(a >> 32); s.z = bytes + back; s.w = 0x00020000u; return s;
}
#define LD4(dst, srd, off, FL) asm volatile("buffer_load_dwordx4 %0, %1, %2, 0 offen" FL : "=v"(dst) : "v"(off), "s"(srd) : "memory")
#define LD4S(dst, srd, off, so, FL) asm volatile("buffer_load_dwordx4 %0, %1, %2, %3 offen" FL : "=v"(dst) : "v"(off), "s"(srd), "s"(so) : "memory")
#define ST1(val, srd, off, FL) asm volatile("buffer_store_dword %0, %1, %2, 0 offen" FL :: "v"(val), "v"(off), "s"(srd) : "memory")
#define PIN(x) asm volatile("" : "+v"(x))
#define VMWAIT(n) asm volatile("s_waitcnt vmcnt(" #n ")" ::: "memory")
#define FENCE() __builtin_amdgcn_sched_barrier(0)
#define WG_BARRIER() do { asm volatile("s_waitcnt lgkmcnt(0)" ::: "memory"); __builtin_amdgcn_s_barrier(); asm volatile("" ::: "memory"); } while (0)

struct Srd4 { u32x4 s0, s1, s2, s3; };
__device__ __forceinline__ Srd4 make_srd4(const void* p, unsigned bytes, unsigned back = 0) {
    return Srd4{make_srd(p, bytes, back), make_srd(p, bytes, back + 1024), make_srd(p, bytes, back + 2048), make_srd(p, bytes, back + 3072)};
}

__device__ __forceinline__ void set_m0(unsigned v) { asm volatile("s_mov_b32 m0, %0" :: "s"(v) : "memory"); }
template <int R> __device__ __forceinline__ void dma_rows(const Srd4& d, unsigned e, unsigned off) {
    unsigned tmp, mask = 0x00FFFF00u;
    asm volatile("v_and_or_b32 %0, %1, %2, %3\n\tbuffer_load_dwordx4 %0, %4, 0 offen offset:%c5 lds"
                 : "=&v"(tmp) : "v"(e), "s"(mask), "v"(off), "s"(R == 0 ? d.s0 : R == 1 ? d.s1 : R == 2 ? d.s2 : d.s3), "i"(R * 1024) : "memory");
}
__device__ __forceinline__ void dma_half(unsigned ldsbase, const Srd4& d, const unsigned* e, const u32x4& off) {
    set_m0(ldsbase);
    dma_rows<0>(d, e[0], off.x); dma_rows<1>(d, e[1], off.y); dma_rows<2>(d, e[2], off.z); dma_rows<3>(d, e[3], off.w);
}
__device__ __forceinline__ void dma_group(unsigned ldsbase, const Srd4& d, const unsigned (&e)[8], const u32x4& off) {
    asm volatile("s_waitcnt lgkmcnt(0)" ::: "memory");
    dma_half(ldsbase, d, e, off); dma_half(ldsbase + 4096, d, e + 4, off);
}

__device__ __forceinline__ void gather_loop(f32x32& acc, const u32x16& qf, const u32x16& addr, u32x4 misc, f32x16 cc,
                                            const Srd4& d, unsigned ring, int G, int nlast) {
    f32x32 t0, t1; u32x12 t2; int tmp; u64 sm; unsigned mask = 0x00FFFF00u;
    asm volatile(
        ".macro GWAIT k\n\ts_cmp_gt_i32 %[t], \\k\n\ts_cbranch_scc1 1f\n\ts_waitcnt vmcnt(\\k)\n\t.endm\n\t"
        ".macro GROWS so\n\t.irp s, 0, 1, 2, 3\n\t"
        "ds_read_b128 v[64+8*\\s:67+8*\\s], v[176+\\s] offset:\\so\n\t"
        "ds_read_b128 v[68+8*\\s:71+8*\\s], v[176+\\s] offset:\\so+4096\n\t.endr\n\t.endm\n\t"
        ".macro GTR c, so\n\t"
        "ds_read_b64_tr_b16 v[96+4*\\c:97+4*\\c], v[180+\\c] offset:\\so\n\t"
        "ds_read_b64_tr_b16 v[98+4*\\c:99+4*\\c], v[180+\\c] offset:\\so+4096\n\t.endm\n\t"
        ".macro GS s, c\n\t"
        "v_mfma_f32_16x16x32_bf16 v[\\c:\\c+3], v[64+8*\\s:67+8*\\s], v[160+4*\\s:163+4*\\s], v[\\c:\\c+3]\n\t"
        "v_mfma_f32_16x16x32_bf16 v[\\c+4:\\c+7], v[68+8*\\s:71+8*\\s], v[160+4*\\s:163+4*\\s], v[\\c+4:\\c+7]\n\t.endm\n\t"
        ".macro GLOAD m, r, srd\n\t"
        "v_and_or_b32 v195, v[212+\\m], %[mask], v[188+\\r]\n\t"
        "buffer_load_dwordx4 v195, \\srd, 0 offen offset:1024*\\r lds\n\t.endm\n\t"
        ".macro GPART m, r, srd\n\ts_cmp_le_i32 %[nl], \\m\n\ts_cbranch_scc1 4f\n\tGLOAD \\m, \\r, \\srd\n\t.endm\n\t"
        ".macro GSUM m\n\tv_mfma_f32_16x16x32_bf16 v[128+4*(\\m):131+4*(\\m)], v[96+4*(\\m):99+4*(\\m)], v[220:223], v[128+4*(\\m):131+4*(\\m)]\n\t.endm\n\t"
        ".macro GCMP m, d\n\tv_cmp_ne_u32_sdwa \\d, v[212+\\m], v193 src0_sel:BYTE_0 src1_sel:DWORD\n\t.endm\n\t"
        ".macro GPAIR m, c\n\t"
        "GSUM \\m\n\tGCMP \\m+1, %[sm]\n\tv_cndmask_b32_e32 v[\\c+\\m], 0, v194, vcc\n\t"
        "GSUM \\m+1\n\t.if \\m < 6\n\tGCMP \\m+2, vcc\n\t.else\n\ts_cmp_gt_i32 %[rem], 0\n\t.endif\n\t"
        "v_cndmask_b32_e64 v[\\c+\\m+1], 0, v194, %[sm]\n\t.endm\n\t"
        ".macro GSTEP so, c, eo\n\t"
        "s_cmp_gt_i32 %[rem], 5\n\ts_cbranch_scc1 5f\n\ts_setprio 1\n\t"
        "s_cmp_gt_i32 %[rem], 3\n\ts_cbranch_scc1 5f\n\ts_setprio 0\n\t5:\n\t"
        "GTR 0, \\so\n\t"
        "s_waitcnt lgkmcnt(2)\n\tGS 0, \\c\n\t"
        "GTR 1, \\so\n\tGTR 2, \\so\n\tGS 1, \\c\n\t"
        "GTR 3, \\so\n\tGTR 4, \\so\n\tGS 2, \\c\n\t"
        "GTR 5, \\so\n\tGTR 6, \\so\n\tGS 3, \\c\n\t"
        "GTR 7, \\so\n\t"
        "ds_read2_b32 v[212:213], v192 offset0:\\eo offset1:\\eo+4\n\t"
        "ds_read2_b32 v[214:215], v192 offset0:\\eo+8 offset1:\\eo+12\n\t"
        "ds_read2_b32 v[216:217], v192 offset0:\\eo+16 offset1:\\eo+20\n\t"
        "ds_read2_b32 v[218:219], v192 offset0:\\eo+24 offset1:\\eo+28\n\t"
        "s_add_u32 m0, %[ring], \\so\n\t"
        "s_cmp_gt_i32 %[rem], 3\n\t"
        ".irp k, 0, 1, 2, 3, 4, 5, 6, 7\n\tv_exp_f32_e32 v[\\c+\\k], v[\\c+\\k]\n\t.endr\n\t"
        "s_waitcnt lgkmcnt(0)\n\t"
        "s_cbranch_scc0 2f\n\t"
        "GLOAD 0, 0, %[s0]\n\tGLOAD 1, 1, %[s1]\n\tGLOAD 2, 2, %[s2]\n\tGLOAD 3, 3, %[s3]\n\t"
        "s_add_u32 m0, m0, 0x1000\n\t"
        "GLOAD 4, 0, %[s0]\n\tGLOAD 5, 1, %[s1]\n\tGLOAD 6, 2, %[s2]\n\tGLOAD 7, 3, %[s3]\n\t"
        "GSIG \\c\n\t"
        "s_waitcnt vmcnt(8)\n\t"
        "s_branch 1f\n\t"
        "2:\n\t"
        "s_mov_b32 %[t], 0\n\t"
        "s_cmp_eq_u32 %[rem], 3\n\ts_cbranch_scc0 4f\n\t"
        "s_mov_b32 %[t], %[nl]\n\t"
        "GLOAD 0, 0, %[s0]\n\tGPART 1, 1, %[s1]\n\tGPART 2, 2, %[s2]\n\tGPART 3, 3, %[s3]\n\t"
        "s_add_u32 m0, m0, 0x1000\n\t"
        "GPART 4, 0, %[s0]\n\tGPART 5, 1, %[s1]\n\tGPART 6, 2, %[s2]\n\tGPART 7, 3, %[s3]\n\t"
        "4:\n\t"
        "GSIG \\c\n\t"
        "s_waitcnt vmcnt(8)\n\t"
        "GWAIT 7\n\tGWAIT 6\n\tGWAIT 5\n\tGWAIT 4\n\tGWAIT 3\n\tGWAIT 2\n\tGWAIT 1\n\tGWAIT 0\n\t"
        "1:\n\t"
        "GROWS 8192-\\so\n\t"
        "GCMP 0, vcc\n\t"
        "s_add_i32 %[rem], %[rem], -1\n\t"
        "GPAIR 0, \\c\n\tGPAIR 2, \\c\n\tGPAIR 4, \\c\n\tGPAIR 6, \\c\n\t"
        ".endm\n\t"
        ".macro GSIG c\n\t"
        ".irp k, 0, 2, 4, 6\n\tv_pk_add_f32 v[\\c+\\k:\\c+\\k+1], v[\\c+\\k:\\c+\\k+1], 1.0 op_sel_hi:[1,0]\n\t.endr\n\t"
        ".irp k, 0, 1, 2, 3, 4, 5, 6, 7\n\tv_rcp_f32_e32 v[\\c+\\k], v[\\c+\\k]\n\t.endr\n\t"
        ".irp k, 0, 1, 2, 3\n\tv_cvt_pk_bf16_f32 v[220+\\k], v[\\c+2*\\k], v[\\c+2*\\k+1]\n\t.endr\n\t.endm\n\t"
        "s_waitcnt vmcnt(8)\n\t"
        "s_cmp_gt_i32 %[rem], 1\n\ts_cbranch_scc1 1f\n\ts_waitcnt vmcnt(0)\n\t1:\n\t"
        "GROWS 0\n\t"
        "s_setprio 2\n\t"
        "0:\n\t"
        "GSTEP 0, 196, 64\n\t"
        "s_cbranch_scc0 9f\n\t"
        "GSTEP 8192, 204, 96\n\t"
        "v_add_u32_e32 v192, 0x100, v192\n\t"
        "s_cbranch_scc1 0b\n\t"
        "9:\n\t"
        "s_waitcnt lgkmcnt(0)\n\ts_nop 6\n\t"
        ".purgem GWAIT\n\t.purgem GROWS\n\t.purgem GTR\n\t.purgem GS\n\t.purgem GLOAD\n\t.purgem GPART\n\t.purgem GSUM\n\t.purgem GCMP\n\t.purgem GPAIR\n\t.purgem GSTEP\n\t.purgem GSIG"
        : "+{v[128:159]}"(acc), "+{v[192:195]}"(misc), "+{v[196:211]}"(cc), "=&{v[64:95]}"(t0), "=&{v[96:127]}"(t1), "=&{v[212:223]}"(t2),
          [rem] "+s"(G), [t] "=&s"(tmp), [sm] "=&s"(sm)
        : "{v[160:175]}"(qf), "{v[176:191]}"(addr), [s0] "s"(d.s0), [s1] "s"(d.s1), [s2] "s"(d.s2), [s3] "s"(d.s3),
          [mask] "s"(mask), [ring] "s"(ring), [nl] "s"(nlast)
        : "vcc", "scc", "memory");
}

__device__ __forceinline__ void output_tile(f32x32 acc, f32x32 wva, f32x32 wvb, unsigned wa, unsigned ow, unsigned ra, unsigned go, const u32x4& srd) {
    f32x32 t0, t1, t2, t3; unsigned o1, o2, o3;
    asm volatile(
        ".macro VPK k\n\tv_cvt_pk_bf16_f32 v[196+\\k], v[128+2*(\\k)], v[129+2*(\\k)]\n\t.endm\n\t"
        ".macro VRD a, u\n\tds_read_b64_tr_b16 v[64+32*\\u+4*\\a:65+32*\\u+4*\\a], v[212+\\a] offset:8192*\\u\n\t"
        "ds_read_b64_tr_b16 v[66+32*\\u+4*\\a:67+32*\\u+4*\\a], v[212+\\a] offset:8192*\\u+4096\n\t.endm\n\t"
        ".macro VMM a, w, u\n\tv_mfma_f32_16x16x32_bf16 v[160+4*\\a:163+4*\\a], v[\\w+4*\\a:\\w+4*\\a+3], v[196+4*\\u:199+4*\\u], v[160+4*\\a:163+4*\\a]\n\t.endm\n\t"
        ".macro VADR a, r\n\tv_xor_b32_e32 v[212+\\a], ((\\a >> 1) << 6) | ((\\a & 1) << 3), \\r\n\t.endm\n\t"
        ".macro VOUT a\n\tv_cvt_pk_bf16_f32 v[160+4*\\a], v[160+4*\\a], v[161+4*\\a]\n\tv_cvt_pk_bf16_f32 v[161+4*\\a], v[162+4*\\a], v[163+4*\\a]\n\t"
        "ds_write_b64 v[212+\\a], v[160+4*\\a:161+4*\\a]\n\t.endm\n\t"
        "VPK 0\n\tVPK 1\n\tVPK 2\n\tVPK 3\n\t"
        ".irp a, 0, 1, 2, 3, 4, 5, 6, 7\n\tVADR \\a, %[wa]\n\t.endr\n\t"
        ".irp a, 0, 1, 2, 3, 4, 5, 6, 7\n\t"
        "v_mfma_f32_16x16x32_bf16 v[160+4*\\a:163+4*\\a], v[224+4*\\a:227+4*\\a], v[196:199], 0\n\tVRD \\a, 0\n\tVPK 4+\\a\n\t.endr\n\t"
        ".irp a, 0, 1, 2, 3\n\tVMM \\a, 32, 1\n\tVRD \\a, 1\n\tVPK 12+\\a\n\t.endr\n\t"
        "VMM 4, 32, 1\n\tVRD 4, 1\n\ts_mov_b32 %[o1], 0x200000\n\t"
        "VMM 5, 32, 1\n\tVRD 5, 1\n\ts_mov_b32 %[o2], 0x400000\n\t"
        "VMM 6, 32, 1\n\tVRD 6, 1\n\ts_mov_b32 %[o3], 0x600000\n\t"
        "VMM 7, 32, 1\n\tVRD 7, 1\n\ts_waitcnt lgkmcnt(15)\n\t"
        ".irp a, 0, 1, 2, 3, 4, 5, 6, 7\n\tVMM \\a, 64, 2\n\tVADR \\a, %[ow]\n\t.endr\n\t"
        "s_waitcnt lgkmcnt(0)\n\t"
        "VMM 0, 96, 3\n\tv_xor_b32_e32 v220, 0x2040, %[go]\n\tv_xor_b32_e32 v221, 0x4080, %[go]\n\tv_xor_b32_e32 v222, 0x60c0, %[go]\n\t"
        "VMM 1, 96, 3\n\ts_nop 2\n\t"
        "VMM 2, 96, 3\n\tVOUT 0\n\tVMM 3, 96, 3\n\tVOUT 1\n\tVMM 4, 96, 3\n\tVOUT 2\n\tVMM 5, 96, 3\n\tVOUT 3\n\t"
        "VMM 6, 96, 3\n\tVOUT 4\n\tVMM 7, 96, 3\n\tVOUT 5\n\ts_nop 0\n\tVOUT 6\n\ts_nop 0\n\tVOUT 7\n\t"
        ".irp m, 0, 1, 2, 3\n\tds_read_b128 v[64+4*\\m:67+4*\\m], %[ra] offset:1024*\\m\n\t.endr\n\t"
        "s_waitcnt lgkmcnt(3)\n\tbuffer_store_dwordx4 v[64:67], %[go], %[srd], 0 offen sc1\n\t"
        "s_waitcnt lgkmcnt(2)\n\tbuffer_store_dwordx4 v[68:71], v220, %[srd], %[o1] offen sc1\n\t"
        "s_waitcnt lgkmcnt(1)\n\tbuffer_store_dwordx4 v[72:75], v221, %[srd], %[o2] offen sc1\n\t"
        "s_waitcnt lgkmcnt(0)\n\tbuffer_store_dwordx4 v[76:79], v222, %[srd], %[o3] offen sc1\n\ts_nop 1\n\t"
        ".purgem VPK\n\t.purgem VRD\n\t.purgem VMM\n\t.purgem VADR\n\t.purgem VOUT"
        : "=&{v[64:95]}"(t0), "=&{v[96:127]}"(t1), "=&{v[160:191]}"(t2), "=&{v[192:223]}"(t3), [o1] "=&s"(o1), [o2] "=&s"(o2), [o3] "=&s"(o3)
        : "{v[128:159]}"(acc), "{v[224:255]}"(wva), "{v[32:63]}"(wvb), [wa] "v"(wa), [ow] "v"(ow), [ra] "v"(ra), [go] "v"(go), [srd] "s"(srd)
        : "memory");
}

__device__ __forceinline__ u64 rank_edges(u32x16 ed, u32x16 es, unsigned& mykey, u32x16& pd, u32x16& pe, u32x16& pr, u64 m0, u64 m1, u64 m2, u64 m3, unsigned keya) {
    u32x16 b4, rk; u64 over; unsigned sel = 0x01050401u, c36 = 36;
    asm volatile(
        ".macro RADD k\n\tv_xor_b32_sdwa v[96+\\k], v[64+\\k], v[64+\\k] dst_sel:DWORD dst_unused:UNUSED_PAD src0_sel:BYTE_0 src1_sel:BYTE_1\n\t"
        "v_lshlrev_b32_e32 v[96+\\k], 2, v[96+\\k]\n\t"
        "ds_add_rtn_u32 v[112+\\k], v[96+\\k], v111 offset:%c[cnt]\n\t.endm\n\t"
        ".macro RENT k\n\tv_add_u32_e32 v[80+\\k], %c[bias], v[80+\\k]\n\tv_perm_b32 v[80+\\k], v[80+\\k], v[64+\\k], %[sel]\n\t.endm\n\t"
        ".macro RPUT k\n\tv_min_u32_e32 v110, %c[cap], v[112+\\k]\n\tv_mad_u32_u24 v110, v[96+\\k], %[c36], v110\n\t"
        "ds_write_b32 v110, v[80+\\k] offset:%c[img]\n\t.endm\n\t"
        "v_mov_b32_e32 v111, 4\n\t"
        ".irp k, 0, 2, 4, 6, 8, 10, 12\n\tv_mov_b64_e32 v[112+\\k:113+\\k], 0\n\t.endr\n\t"
        "s_mov_b64 exec, %[m0]\n\ts_waitcnt vmcnt(9)\n\tRADD 0\n\tRADD 1\n\tRADD 2\n\tRADD 3\n\t"
        "s_mov_b64 exec, %[m1]\n\ts_waitcnt vmcnt(8)\n\tRADD 4\n\tRADD 5\n\tRADD 6\n\tRADD 7\n\t"
        "s_mov_b64 exec, %[m2]\n\ts_waitcnt vmcnt(7)\n\tRADD 8\n\tRADD 9\n\tRADD 10\n\tRADD 11\n\t"
        "s_mov_b64 exec, %[m3]\n\ts_waitcnt vmcnt(6)\n\tRADD 12\n\t"
        "s_mov_b64 exec, %[m0]\n\ts_waitcnt vmcnt(5)\n\tRENT 0\n\tRENT 1\n\tRENT 2\n\tRENT 3\n\t"
        "s_waitcnt lgkmcnt(9)\n\tRPUT 0\n\tRPUT 1\n\tRPUT 2\n\tRPUT 3\n\t"
        "s_mov_b64 exec, %[m1]\n\ts_waitcnt vmcnt(4)\n\tRENT 4\n\tRENT 5\n\tRENT 6\n\tRENT 7\n\t"
        "s_waitcnt lgkmcnt(9)\n\tRPUT 4\n\tRPUT 5\n\tRPUT 6\n\tRPUT 7\n\t"
        "s_mov_b64 exec, %[m2]\n\ts_waitcnt vmcnt(3)\n\tRENT 8\n\tRENT 9\n\tRENT 10\n\tRENT 11\n\t"
        "s_waitcnt lgkmcnt(9)\n\tRPUT 8\n\tRPUT 9\n\tRPUT 10\n\tRPUT 11\n\t"
        "s_mov_b64 exec, %[m3]\n\ts_waitcnt vmcnt(0)\n\tRENT 12\n\t"
        "s_waitcnt lgkmcnt(12)\n\tRPUT 12\n\t"
        "s_mov_b64 exec, -1\n\t"
        "v_add_u32_e32 v93, %c[step], v93\n\tv_mov_b32_e32 %[mk], v94\n\t"
        "v_max3_u32 v110, v112, v113, v114\n\tv_max3_u32 v109, v115, v116, v117\n\t"
        "ds_write_b32 %[keya], v93 offset:%c[cnt]+1024\n\t"
        "v_max3_u32 v110, v110, v118, v119\n\tv_max3_u32 v109, v109, v120, v121\n\t"
        "v_max3_u32 v110, v110, v122, v123\n\tv_max3_u32 v110, v110, v109, v124\n\t"
        "v_cmp_le_u32_e32 vcc, %c[cap], v110\n\ts_mov_b64 %[over], vcc\n\t"
        "s_cbranch_vccz 1f\n\t"
        "v_mov_b32_e32 v109, 0\n\tv_mov_b32_e32 v110, 0\n\t"
        ".irp k, 0, 1, 2, 3, 4, 5, 6, 7, 8, 9, 10, 11, 12\n\tv_cmp_le_u32_e32 vcc, %c[cap], v[112+\\k]\n\tv_mov_b32_e32 v[128+\\k], v[64+\\k]\n\t"
        "v_mov_b32_e32 v[144+\\k], v[80+\\k]\n\tv_addc_co_u32_e32 v109, vcc, 0, v109, vcc\n\tv_mov_b32_e32 v[160+\\k], v[112+\\k]\n\t.endr\n\t"
        "s_mov_b64 exec, %[over]\n\tds_add_u32 v110, v109 offset:%c[cnt]+2048\n\ts_mov_b64 exec, -1\n\t"
        "1:\n\t"
        ".purgem RADD\n\t.purgem RENT\n\t.purgem RPUT"
        : "+{v[64:79]}"(ed), "+{v[80:95]}"(es), "=&{v[96:111]}"(b4), "=&{v[112:127]}"(rk),
          "=&{v[128:143]}"(pd), "=&{v[144:159]}"(pe), "=&{v[160:175]}"(pr), [over] "=&s"(over), [mk] "=&v"(mykey)
        : [m0] "s"(m0), [m1] "s"(m1), [m2] "s"(m2), [m3] "s"(m3), [keya] "v"(keya), [sel] "s"(sel), [c36] "s"(c36),
          [cnt] "i"(L_CNT1), [img] "i"(L_IMG + 8), [bias] "i"(BIAS), [cap] "i"(4 * CAP1), [step] "i"(KEY_STEP)
        : "vcc", "memory");
    return over;
}
__device__ __forceinline__ unsigned read_image(f32x32& img, u32x16& meta, u32x16& sums, unsigned ima, unsigned cna) {
    unsigned no;
    asm volatile(
        "v_mov_b32_e32 v121, 0\n\tds_read_b32 v120, v121 offset:%c[cnt]+2048\n\t"
        ".irp k, 0, 1, 2, 3, 4, 5, 6, 7\n\tds_read_b128 v[64+4*\\k:67+4*\\k], %[ima] offset:4608*\\k\n\t.endr\n\t"
        ".irp k, 0, 1, 2, 3\n\tds_read2_b32 v[96+2*\\k:97+2*\\k], %[cna] offset0:64*\\k offset1:64*\\k+32\n\t.endr\n\t"
        "s_waitcnt lgkmcnt(11)\n\tv_readfirstlane_b32 %[no], v120\n\t"
        "v_add_u32_e32 v121, 0x400, %[cna]\n\t"
        ".irp k, 0, 1, 2, 3\n\tds_read2_b32 v[104+2*\\k:105+2*\\k], v121 offset0:64*\\k offset1:64*\\k+32\n\t.endr\n\t"
        "s_waitcnt lgkmcnt(12)\n\t"
        ".irp k, 0, 1, 2, 3\n\tv_add3_u32 v[112+\\k], v[64+4*\\k], v[65+4*\\k], v[66+4*\\k]\n\tv_add_u32_e32 v[112+\\k], v[112+\\k], v[67+4*\\k]\n\t.endr\n\t"
        "s_waitcnt lgkmcnt(8)\n\t"
        ".irp k, 4, 5, 6, 7\n\tv_add3_u32 v[112+\\k], v[64+4*\\k], v[65+4*\\k], v[66+4*\\k]\n\tv_add_u32_e32 v[112+\\k], v[112+\\k], v[67+4*\\k]\n\t.endr\n\t"
        ".irp k, 0, 1, 2, 3, 4, 5, 6, 7\n\tv_add_u32_dpp v[112+\\k], v[112+\\k], v[112+\\k] quad_perm:[1,0,3,2] row_mask:0xf bank_mask:0xf\n\t.endr\n\t"
        ".irp k, 0, 1, 2, 3, 4, 5, 6, 7\n\tv_add_u32_dpp v[112+\\k], v[112+\\k], v[112+\\k] quad_perm:[2,3,0,1] row_mask:0xf bank_mask:0xf\n\t.endr\n\t"
        ".irp k, 0, 1, 2, 3, 4, 5, 6, 7\n\tv_add_u32_dpp v[112+\\k], v[112+\\k], v[112+\\k] row_half_mirror row_mask:0xf bank_mask:0xf\n\t.endr\n\t"
        "s_waitcnt lgkmcnt(0)"
        : "=&{v[64:95]}"(img), "=&{v[96:111]}"(meta), "=&{v[112:127]}"(sums), [no] "=&s"(no)
        : [ima] "v"(ima), [cna] "v"(cna), [cnt] "i"(L_CNT1) : "memory");
    return no;
}
__device__ __forceinline__ void publish_image(f32x32 img, u32x16 meta, u32x16 sums, const u32x4& srd, unsigned ob) {
    u64 pc0 = 0x0101010101010101ull; unsigned so;
    asm volatile(
        ".macro PUB k\n\ts_mov_b64 exec, %[pc0]\n\tv_mov_b32_e32 v[64+4*\\k], v[96+\\k]\n\tv_mov_b32_e32 v[65+4*\\k], v[112+\\k]\n\t"
        "s_mov_b64 exec, -1\n\tv_xor_b32_e32 v[64+4*\\k], v[64+4*\\k], v[104+\\k]\n\t"
        "buffer_store_dwordx4 v[64+4*\\k:67+4*\\k], %[ob], %[srd], %[so] offen sc1\n\ts_add_u32 %[so], %[so], %c[step]\n\t.endm\n\t"
        "s_mov_b32 %[so], 0\n\t"
        ".irp k, 0, 1, 2, 3, 4, 5, 6, 7\n\tPUB \\k\n\t.endr\n\t.purgem PUB"
        : "+{v[64:95]}"(img), [so] "=&s"(so)
        : "{v[96:111]}"(meta), "{v[112:127]}"(sums), [srd] "s"(srd), [ob] "v"(ob), [pc0] "s"(pc0), [step] "i"(32 * G1 * 128) : "scc", "memory");
}

__device__ __forceinline__ void inbox_request(u32x16& raw, const u32x4& srd, unsigned ib) {
    unsigned o1 = 0x1000, o2 = 0x2000, o3 = 0x3000;
    asm volatile("buffer_load_dwordx4 v[64:67], %1, %2, 0 offen sc1\n\tbuffer_load_dwordx4 v[68:71], %1, %2, %3 offen sc1\n\t"
                 "buffer_load_dwordx4 v[72:75], %1, %2, %4 offen sc1\n\tbuffer_load_dwordx4 v[76:79], %1, %2, %5 offen sc1"
                 : "=&{v[64:79]}"(raw) : "v"(ib), "s"(srd), "s"(o1), "s"(o2), "s"(o3) : "memory");
}
__device__ __forceinline__ unsigned inbox_stage(u32x16 raw, const u32x4& srd, unsigned ib, unsigned key, int i4, unsigned hdra) {
    u32x16 t0; f32x32 t2; unsigned four = 4; u64 m0, m1, m2, m3; unsigned todo, fresh, spin, o1, o2, o3, over;
    u64 pc0 = 0x0101010101010101ull;
    asm volatile(
        ".macro IVAL j, so\n\t"
        "s_bitcmp1_b32 %[todo], \\j\n\ts_cbranch_scc0 2f\n\t"
        "v_xor_b32_e32 v[64+4*\\j], %[key], v[64+4*\\j]\n\t"
        "v_sub_u32_e32 v97, 0, v[65+4*\\j]\n\t"
        "v_add_u32_e32 v96, v[64+4*\\j], v[65+4*\\j]\n\t"
        "v_mov_b32_dpp v[80+\\j], v[64+4*\\j] quad_perm:[0,0,0,0] row_mask:0xf bank_mask:0xf\n\t"
        "v_cndmask_b32_e64 v96, v96, v97, %[pc0]\n\t"
        "s_nop 0\n\t"
        "v_mov_b32_dpp v[80+\\j], v[80+\\j] row_shr:4 row_mask:0xf bank_mask:0xa\n\t"
        "v_cmp_gt_i32_e32 vcc, v[80+\\j], v92\n\t"
        "v_add3_u32 v96, v96, v[66+4*\\j], v[67+4*\\j]\n\t"
        "v_lshrrev_b32_e32 v97, 14, v[80+\\j]\n\t"
        "v_cndmask_b32_e32 v96, 0, v96, vcc\n\t"
        "s_nop 1\n\tv_add_u32_dpp v96, v96, v96 quad_perm:[1,0,3,2] row_mask:0xf bank_mask:0xf\n\t"
        "s_nop 1\n\tv_add_u32_dpp v96, v96, v96 quad_perm:[2,3,0,1] row_mask:0xf bank_mask:0xf\n\t"
        "s_nop 1\n\tv_add_u32_dpp v96, v96, v96 row_half_mirror row_mask:0xf bank_mask:0xf\n\t"
        "v_or_b32_e32 v96, v96, v97\n\t"
        "v_cmp_ne_u32_e32 vcc, 0, v96\n\t"
        "s_cbranch_vccz 1f\n\t"
        "buffer_load_dwordx4 v[64+4*\\j:67+4*\\j], %[ib], %[srd], \\so offen sc1\n\t"
        "s_branch 2f\n\t"
        "1:\n\ts_bitset1_b32 %[fresh], \\j\n\t"
        "2:\n\t.endm\n\t"
        ".macro IRANK j\n\t"
        "s_bitcmp1_b32 %[fresh], \\j\n\ts_cbranch_scc0 2f\n\t"
        "v_cmp_lt_u32_e64 %[m0], v92, v[80+\\j]\n\tv_cmp_lt_u32_e64 %[m1], v93, v[80+\\j]\n\t"
        "v_cmp_lt_u32_e64 %[m2], v94, v[80+\\j]\n\tv_cmp_lt_u32_e64 %[m3], v95, v[80+\\j]\n\t"
        ".irp k, 0, 1, 2, 3\n\tv_lshrrev_b32_e32 v[84+\\k], 21, v[64+4*\\j+\\k]\n\t.endr\n\t"
        ".irp k, 0, 1, 2, 3\n\tv_and_b32_e32 v[84+\\k], 60, v[84+\\k]\n\t.endr\n\t"
        "v_cmp_lt_u32_e32 vcc, %c[cap], v[80+\\j]\n\t"
        "s_mov_b64 exec, %[m0]\n\tds_add_rtn_u32 v88, v84, %[four] offset:%c[cnt]\n\t"
        "s_mov_b64 exec, %[m1]\n\tds_add_rtn_u32 v89, v85, %[four] offset:%c[cnt]\n\t"
        "s_mov_b64 exec, %[m2]\n\tds_add_rtn_u32 v90, v86, %[four] offset:%c[cnt]\n\t"
        "s_mov_b64 exec, %[m3]\n\tds_add_rtn_u32 v91, v87, %[four] offset:%c[cnt]\n\t"
        "s_mov_b64 exec, %[pc0]\n\tds_write_b32 %[hdra], v[80+\\j] offset:128*\\j\n\t"
        "s_mov_b64 exec, -1\n\t"
        "s_cbranch_vccz 1f\n\t"
        "s_mov_b32 %[over], 1\n\t"
        "1:\n\ts_waitcnt lgkmcnt(1)\n\t"
        ".irp k, 0, 1, 2, 3\n\tv_min_u32_e32 v[88+\\k], %c[sub]-4, v[88+\\k]\n\t.endr\n\t"
        ".irp k, 0, 1, 2, 3\n\tv_mad_u32_u24 v[88+\\k], v[84+\\k], %[sub4], v[88+\\k]\n\t.endr\n\t"
        "s_mov_b64 exec, %[m0]\n\tds_write_b32 v88, v[64+4*\\j] offset:%c[list]\n\t"
        "s_mov_b64 exec, %[m1]\n\tds_write_b32 v89, v[65+4*\\j] offset:%c[list]\n\t"
        "s_mov_b64 exec, %[m2]\n\tds_write_b32 v90, v[66+4*\\j] offset:%c[list]\n\t"
        "s_mov_b64 exec, %[m3]\n\tds_write_b32 v91, v[67+4*\\j] offset:%c[list]\n\t"
        "s_mov_b64 exec, -1\n\t"
        "2:\n\t.endm\n\t"
        ".macro FADD j\n\t"
        ".irp k, 0, 1, 2, 3\n\tv_lshrrev_b32_e32 v[96+4*\\j+\\k], 21, v[64+4*\\j+\\k]\n\t.endr\n\t"
        ".irp k, 0, 1, 2, 3\n\tv_and_b32_e32 v[96+4*\\j+\\k], 60, v[96+4*\\j+\\k]\n\t.endr\n\t"
        "v_cmpx_lt_u32_e64 s[72+8*\\j:73+8*\\j], v94, v[80+\\j]\n\tds_add_rtn_u32 v[114+4*\\j], v[98+4*\\j], %[four] offset:%c[cnt]\n\t"
        "v_cmpx_lt_u32_e64 s[74+8*\\j:75+8*\\j], v95, v[80+\\j]\n\tds_add_rtn_u32 v[115+4*\\j], v[99+4*\\j], %[four] offset:%c[cnt]\n\t"
        "s_mov_b64 exec, -1\n\t"
        "v_cmpx_lt_u32_e64 s[68+8*\\j:69+8*\\j], v92, v[80+\\j]\n\tds_add_rtn_u32 v[112+4*\\j], v[96+4*\\j], %[four] offset:%c[cnt]\n\t"
        "v_cmpx_lt_u32_e64 s[70+8*\\j:71+8*\\j], v93, v[80+\\j]\n\tds_add_rtn_u32 v[113+4*\\j], v[97+4*\\j], %[four] offset:%c[cnt]\n\t"
        "s_mov_b64 exec, -1\n\t.endm\n\t"
        ".macro FPUT j\n\t"
        "s_waitcnt lgkmcnt(12)\n\t"
        ".irp k, 0, 1, 2, 3\n\tv_min_u32_e32 v[112+4*\\j+\\k], %c[sub]-4, v[112+4*\\j+\\k]\n\t.endr\n\t"
        ".irp k, 0, 1, 2, 3\n\tv_mad_u32_u24 v[112+4*\\j+\\k], v[96+4*\\j+\\k], %[sub4], v[112+4*\\j+\\k]\n\t.endr\n\t"
        ".irp k, 0, 1, 2, 3\n\ts_mov_b64 exec, s[68+8*\\j+2*\\k:69+8*\\j+2*\\k]\n\tds_write_b32 v[112+4*\\j+\\k], v[64+4*\\j+\\k] offset:%c[list]\n\t.endr\n\t"
        "s_mov_b64 exec, -1\n\t.endm\n\t"
        "s_movk_i32 %[o1], 0x1000\n\ts_movk_i32 %[o2], 0x2000\n\ts_movk_i32 %[o3], 0x3000\n\t"
        "v_mov_b32_e32 v92, %[i4]\n\tv_add_u32_e32 v93, 4, %[i4]\n\tv_add_u32_e32 v94, 8, %[i4]\n\tv_add_u32_e32 v95, 12, %[i4]\n\t"
        "s_mov_b32 %[todo], 15\n\ts_mov_b32 %[spin], 0x400000\n\ts_mov_b32 %[over], 0\n\t"
        "s_waitcnt vmcnt(0)\n\t"
        ".irp j, 0, 1, 2, 3\n\tv_xor_b32_e32 v[64+4*\\j], %[key], v[64+4*\\j]\n\t.endr\n\t"
        ".irp j, 0, 1, 2, 3\n\tv_mov_b32_dpp v[80+\\j], v[64+4*\\j] quad_perm:[0,0,0,0] row_mask:0xf bank_mask:0xf\n\t.endr\n\t"
        ".irp j, 0, 1, 2, 3\n\tv_add_u32_e32 v[84+\\j], v[64+4*\\j], v[65+4*\\j]\n\t.endr\n\t"
        ".irp j, 0, 1, 2, 3\n\tv_mov_b32_dpp v[80+\\j], v[80+\\j] row_shr:4 row_mask:0xf bank_mask:0xa\n\t.endr\n\t"
        "s_mov_b64 exec, %[pc0]\n\t"
        ".irp j, 0, 1, 2, 3\n\tv_sub_u32_e32 v[84+\\j], 0, v[65+4*\\j]\n\t.endr\n\t"
        "s_mov_b64 exec, -1\n\t"
        "v_cmp_gt_i32_e64 %[m0], v80, v92\n\tv_cmp_gt_i32_e64 %[m1], v81, v92\n\tv_cmp_gt_i32_e64 %[m2], v82, v92\n\tv_cmp_gt_i32_e64 %[m3], v83, v92\n\t"
        ".irp j, 0, 1, 2, 3\n\tv_add3_u32 v[84+\\j], v[84+\\j], v[66+4*\\j], v[67+4*\\j]\n\t.endr\n\t"
        "v_cndmask_b32_e64 v84, 0, v84, %[m0]\n\tv_cndmask_b32_e64 v85, 0, v85, %[m1]\n\tv_cndmask_b32_e64 v86, 0, v86, %[m2]\n\tv_cndmask_b32_e64 v87, 0, v87, %[m3]\n\t"
        ".irp j, 0, 1, 2, 3\n\tv_add_u32_dpp v[84+\\j], v[84+\\j], v[84+\\j] quad_perm:[1,0,3,2] row_mask:0xf bank_mask:0xf\n\t.endr\n\t"
        ".irp j, 0, 1, 2, 3\n\tv_add_u32_dpp v[84+\\j], v[84+\\j], v[84+\\j] quad_perm:[2,3,0,1] row_mask:0xf bank_mask:0xf\n\t.endr\n\t"
        ".irp j, 0, 1, 2, 3\n\tv_add_u32_dpp v[84+\\j], v[84+\\j], v[84+\\j] row_half_mirror row_mask:0xf bank_mask:0xf\n\t.endr\n\t"
        "v_or3_b32 v96, v80, v81, v82\n\tv_max3_u32 v97, v80, v81, v82\n\tv_or_b32_e32 v96, v96, v83\n\tv_max_u32_e32 v97, v97, v83\n\t"
        "v_lshrrev_b32_e32 v96, 14, v96\n\tv_or3_b32 v96, v96, v84, v85\n\tv_or3_b32 v96, v96, v86, v87\n\t"
        "v_cmp_ne_u32_e32 vcc, 0, v96\n\t"
        "s_cbranch_vccnz 7f\n\t"
        "v_cmp_lt_u32_e32 vcc, %c[cap], v97\n\t"
        "FADD 0\n\tFADD 1\n\tFADD 2\n\tFADD 3\n\t"
        "s_cbranch_vccz 6f\n\ts_mov_b32 %[over], 1\n\t6:\n\t"
        "FPUT 0\n\tFPUT 1\n\tFPUT 2\n\tFPUT 3\n\t"
        "s_mov_b64 exec, %[pc0]\n\t"
        ".irp j, 0, 1, 2, 3\n\tds_write_b32 %[hdra], v[80+\\j] offset:128*\\j\n\t.endr\n\t"
        "s_mov_b64 exec, -1\n\t"
        "s_branch 9f\n\t"
        "7:\n\t.irp j, 0, 1, 2, 3\n\tv_xor_b32_e32 v[64+4*\\j], %[key], v[64+4*\\j]\n\t.endr\n\t"
        "0:\n\t"
        "s_mov_b32 %[fresh], 0\n\t"
        "s_waitcnt vmcnt(0)\n\t"
        "IVAL 0, 0\n\tIVAL 1, %[o1]\n\tIVAL 2, %[o2]\n\tIVAL 3, %[o3]\n\t"
        "IRANK 0\n\tIRANK 1\n\tIRANK 2\n\tIRANK 3\n\t"
        "s_andn2_b32 %[todo], %[todo], %[fresh]\n\t"
        "s_cbranch_scc0 9f\n\t"
        "s_sub_u32 %[spin], %[spin], 1\n\t"
        "s_cbranch_scc0 0b\n\t"
        "s_waitcnt vmcnt(0)\n\t"
        "9:\n\t"
        ".purgem IVAL\n\t.purgem IRANK\n\t.purgem FADD\n\t.purgem FPUT"
        : "+{v[64:79]}"(raw), "=&{v[80:95]}"(t0), "=&{v[96:127]}"(t2), [m0] "=&s"(m0), [m1] "=&s"(m1), [m2] "=&s"(m2), [m3] "=&s"(m3),
          [todo] "=&s"(todo), [fresh] "=&s"(fresh), [spin] "=&s"(spin), [o1] "=&s"(o1), [o2] "=&s"(o2), [o3] "=&s"(o3), [over] "=&s"(over)
        : [srd] "s"(srd), [ib] "v"(ib), [key] "s"(key), [i4] "v"(i4), [hdra] "v"(hdra), [pc0] "s"(pc0), [sub4] "s"(SUBB / 4), [four] "v"(four),
          [cnt] "i"(L_CNT), [list] "i"(L_LIST), [sub] "i"(SUBB), [cap] "i"(4 * CAP1)
        : "vcc", "scc", "memory", "s68", "s69", "s70", "s71", "s72", "s73", "s74", "s75", "s76", "s77", "s78", "s79", "s80", "s81", "s82", "s83",
          "s84", "s85", "s86", "s87", "s88", "s89", "s90", "s91", "s92", "s93", "s94", "s95", "s96", "s97", "s98", "s99");
    return over;
}

__device__ __forceinline__ bf16x4 tr_read(LAS char* p) { return __builtin_amdgcn_ds_read_tr16_b64_v4bf16((LAS bf16x4*)p); }
__device__ __forceinline__ bf16x8 cat(bf16x4 a, bf16x4 b) { return __builtin_shufflevector(a, b, 0, 1, 2, 3, 4, 5, 6, 7); }
__device__ __forceinline__ unsigned pk2(float a, float b) { f32x2 v = {a, b}; bf16x2 r = __builtin_convertvector(v, bf16x2); return __builtin_bit_cast(unsigned, r); }
__device__ __forceinline__ f32x4 mfma(bf16x8 a, bf16x8 b, f32x4 c) { return __builtin_amdgcn_mfma_f32_16x16x32_bf16(a, b, c, 0, 0, 0); }
__device__ __forceinline__ unsigned lds_add(LAS char* p, unsigned v) { return __hip_atomic_fetch_add((LAS unsigned*)p, v, __ATOMIC_RELAXED, __HIP_MEMORY_SCOPE_WORKGROUP); }
template <int OFF> __device__ __forceinline__ void lds_arrive(int lane) {
    unsigned one = 1;
    asm volatile("s_waitcnt lgkmcnt(0)\n\ts_mov_b64 exec, 1\n\tds_add_u32 %0, %1 offset:%c2\n\ts_mov_b64 exec, -1" :: "v"(lane), "v"(one), "i"(OFF) : "memory");
}
template <int OFF, int N, int NAP = 1> __device__ __forceinline__ void lds_await(int lane) {
    unsigned v;
    asm volatile("s_mov_b64 exec, 1\n\t0:\n\tds_read_b32 %0, %1 offset:%c2\n\ts_waitcnt lgkmcnt(0)\n\tv_cmp_gt_u32_e32 vcc, %c3, %0\n\t"
                 "s_cbranch_vccz 1f\n\t.if %c4\n\ts_sleep %c4\n\t.endif\n\ts_branch 0b\n\t1:\n\ts_mov_b64 exec, -1" : "=&v"(v) : "v"(lane), "i"(OFF), "i"(N), "i"(NAP) : "vcc", "memory");
}
__device__ __forceinline__ unsigned record_check(unsigned v, unsigned key) { return ~v ^ __builtin_rotateleft32(key, 13); }
__device__ __forceinline__ unsigned rfl(unsigned v) { return __builtin_amdgcn_readfirstlane(v); }

__global__ __launch_bounds__(512, 2) void k_graph(const unsigned short* x_, const int* src_, const int* dst_, const unsigned short* wq_, const unsigned short* wv_,
                                                  unsigned* ws_, unsigned short* out_, int N_, int E_) {
    u32x4 srd_ws, srd_x0, pw, po;
    u32x16 ed, es;
    u64 t_start;
    asm volatile("s_load_dwordx2 s[40:41], %[ka], 0x10\n\ts_load_dwordx2 s[44:45], %[ka], 0x8\n\ts_load_dwordx4 s[60:63], %[ka], 0x30\n\t"
                 "s_getpc_b64 s[68:69]\n\ts_movk_i32 s70, " STR(CODE_BYTES) "\n\ts_mov_b32 s71, 0x20000\n\ts_mov_b32 m0, %c[dump]\n\t"
                 "s_movk_i32 s66, 0x8000\n\tv_lshl_add_u32 v77, %[t], 7, s66\n\t"
                 "buffer_load_dword v77, s[68:71], 0 offen lds\n\t"
                 "s_load_dwordx2 s[48:49], %[ka], 0x28\n\ts_load_dwordx2 s[52:53], %[ka], 0x0\n\ts_load_dwordx4 s[56:59], %[ka], 0x18\n\t"
                 "s_memrealtime s[64:65]\n\t"
                 "v_readfirstlane_b32 s72, %[t]\n\t"
                 "s_and_b32 s66, %[b], 7\n\ts_lshl_b32 s66, s66, 4\n\ts_lshr_b32 s67, %[b], 4\n\ts_or_b32 s66, s66, s67\n\ts_mulk_i32 s66, %c[ch4]\n\t"
                 "s_mov_b32 s43, s71\n\ts_mov_b32 s47, s71\n\t"
                 "v_lshl_add_u32 v77, %[t], 4, s66\n\t"
                 "s_lshr_b32 s67, s72, 5\n\ts_or_b32 s67, s67, %[b]\n\t"
                 "s_movk_i32 s73, 0x1000\n\ts_movk_i32 s74, 0x2000\n\t"
                 "s_bitcmp1_b32 s67, 3\n\ts_waitcnt lgkmcnt(0)\n\ts_cbranch_scc1 1f\n\t"
                 "s_lshl_b32 s42, s63, 2\n\ts_lshl_b32 s46, s63, 2\n\t"
                 "buffer_load_dwordx4 v[64:67], v77, s[40:43], 0 offen nt\n\tbuffer_load_dwordx4 v[68:71], v77, s[40:43], s73 offen nt\n\t"
                 "buffer_load_dwordx4 v[72:75], v77, s[40:43], s74 offen nt\n\t"
                 "s_add_u32 s66, s66, 0x3000\n\ts_cmp_lt_u32 s72, 64\n\ts_cselect_b32 s66, s66, %c[oob]\n\t"
                 "v_lshl_add_u32 v78, %[t], 2, s66\n\t"
                 "buffer_load_dword v76, v78, s[40:43], 0 offen nt\n\t"
                 "buffer_load_dwordx4 v[80:83], v77, s[44:47], 0 offen nt\n\tbuffer_load_dwordx4 v[84:87], v77, s[44:47], s73 offen nt\n\t"
                 "buffer_load_dwordx4 v[88:91], v77, s[44:47], s74 offen nt\n\tbuffer_load_dword v92, v78, s[44:47], 0 offen nt\n\t"
                 "s_mov_b32 s51, s71\n\ts_mov_b32 s50, %c[wsb]\n\ts_lshl_b32 s66, %[b], 2\n\t"
                 "v_lshlrev_b32_e32 v77, 2, %[t]\n\tv_mov_b32_e32 v78, s66\n\t"
                 "buffer_load_dword v93, v77, s[48:51], 0 offen sc1\n\tbuffer_load_dword v94, v78, s[48:51], 0 offen sc1\n\t"
                 "1:\n\ts_mov_b32 s51, s71\n\ts_mov_b32 s50, %c[wsb]\n\ts_mov_b32 s55, s71\n\ts_lshl_b32 s54, s62, 8"
                 : "=&{v[64:79]}"(ed), "=&{v[80:95]}"(es)
                 : [ka] "s"(__builtin_amdgcn_kernarg_segment_ptr()), [t] "v"(threadIdx.x), [b] "s"(blockIdx.x),
                   [wsb] "i"(WS_BYTES), [dump] "i"(L_DUMP), [ch4] "i"(CH * 4), [oob] "i"(OOB)
                 : "memory", "scc", "s40", "s41", "s42", "s43", "s44", "s45", "s46", "s47", "s48", "s49", "s50", "s51", "s52", "s53", "s54", "s55",
                   "s56", "s57", "s58", "s59", "s60", "s61", "s62", "s63", "s64", "s65", "s66", "s67", "s68", "s69", "s70", "s71", "s72", "s73", "s74");
    asm volatile("" : "={s[48:51]}"(srd_ws), "={s[52:55]}"(srd_x0), "={s[56:59]}"(pw), "={s[60:63]}"(po), "={s[64:65]}"(t_start));
    FENCE();
    __shared__ __attribute__((aligned(16))) char smem_[L_TOTAL];
    LAS char* smem = (LAS char*)smem_;
    const int t = threadIdx.x, l = t & 63, b = blockIdx.x;
    const int w = rfl(t >> 6);
    const int g = l >> 4, n = l & 15, q = (l >> 2) & 3, p = l & 3, pc = t & 7;
    const unsigned lds0 = (unsigned)(size_t)smem;
    const bool isprod = ((b >> 3) & 1) == 0;
    const int pg = ((b & 7) << 4) | (b >> 4);
    const unsigned e0 = pg * CH + 4 * t, e1 = pg * CH + 3072 + t;

    const void* const x = (const void*)((u64)srd_x0.x | ((u64)srd_x0.y << 32));
    const void* const wq = (const void*)((u64)pw.x | ((u64)pw.y << 32));
    const void* const wv = (const void*)((u64)pw.z | ((u64)pw.w << 32));
    void* const out = (void*)((u64)po.x | ((u64)po.y << 32));
    const unsigned E = po.w;
    const unsigned xbytes = srd_x0.z;
    const unsigned ring = lds0 + L_RING + w * 16384;

    unsigned mykey = 0;
    u64 t_go = t_start + POLL_T;
    auto node_row = [&](int tile, int m) { const unsigned j = 8 * (4 * m + g) + tile; return (j << 16) | ((b ^ j) << 8); };

    u32x16 addr;
    f32x32 acc, wva, wvb;
    unsigned cv, lista, cnta, qa, keyn, inf, ow, go;
    Srd4 srd_x;
    auto loop_constants = [&]() {
        srd_x = make_srd4(x, xbytes, BIAS * 256);
        lista = lds0 + L_LIST + w * (2 * SUBB) + g * 4; cnta = lds0 + L_CNT + (l & 31) * 4; qa = ring + l * 16; keyn = 8 * n + w; inf = 0x7F800000u;
        ow = ring + n * 256 + ((g ^ n) << 4) + ((g & 1) << 3); go = node_row(w, 0) + ((n ^ g) << 4);
        asm volatile("" : "+s"(srd_x.s0), "+s"(srd_x.s1), "+s"(srd_x.s2), "+s"(srd_x.s3));
        asm volatile("" : "+v"(lista), "+v"(cnta), "+v"(qa), "+v"(keyn), "+v"(inf), "+v"(ow), "+v"(go));
        const unsigned o0 = (n ^ (((g & 1) << 3) | (g >> 1))) << 4;
        const unsigned ra = (4 * (n & 3) + (n >> 2)) * 256 + (((4 * g) ^ (((n & 7) << 1) | (n >> 3))) << 4);
        const int i = 4 * g + q, r8 = i & 7;
        const unsigned ta = (4 * q + g) * 256 + 8 * (p & 1) + (((p >> 1) ^ (((i & 7) << 1) | (i >> 3))) << 4);
        cv = lds0 + L_WV1 + i * 256 + (((p ^ ((r8 & 1) | ((r8 & 6) << 1))) << 4) | ((p & 1) << 3));
#pragma unroll
        for (int s = 0; s < 4; ++s) { addr[s] = ring + (ra ^ (s << 4)); addr[12 + s] = o0 ^ (s << 5); }
#pragma unroll
        for (int c = 0; c < 8; ++c) addr[4 + c] = ring + (ta ^ (c << 5));
        asm volatile("" : "+{v[176:191]}"(addr), "+v"(cv));
    };
    auto clear_sums = [&]() { acc = 0.f; asm volatile("" : "+{v[128:159]}"(acc)); };
    auto wv_resident = [&]() {
#pragma unroll
        for (int a = 0; a < 8; ++a) {
            LAS char* pa = (LAS char*)(size_t)((cv - (L_WV1 - L_WV0)) ^ ((a >> 1) << 6) ^ ((a & 1) << 3));
            const f32x4 f0 = __builtin_bit_cast(f32x4, cat(tr_read(pa), tr_read(pa + 4096)));
            const f32x4 f1 = __builtin_bit_cast(f32x4, cat(tr_read(pa + 16384), tr_read(pa + 16384 + 4096)));
#pragma unroll
            for (int k = 0; k < 4; ++k) { wva[4 * a + k] = f0[k]; wvb[4 * a + k] = f1[k]; }
        }
        asm volatile("" : "+{v[224:255]}"(wva), "+{v[32:63]}"(wvb));
    };

    if (isprod) {
#pragma unroll
        for (int k = 0; k < 5; ++k) *(LAS u32x4*)(smem + L_IMG + k * 8192 + t * 16) = u32x4{0u, 0u, 0u, 0u};
    }
    if (t < 8) *(LAS u32x4*)(smem + L_CNT + t * 16) = u32x4{0u, 0u, 0u, 0u};
    WG_BARRIER();

    if (__builtin_expect(w < 4, 1)) {
        if (__builtin_expect(isprod, 1)) {
            __builtin_amdgcn_s_setprio(2);
            unsigned ob = WS_INBOX + ((t >> 3) * G1 + pg) * 128 + pc * 16, ima = lds0 + L_IMG + (t >> 3) * 144 + pc * 16;
            PIN(ob); PIN(ima);
            unsigned cna = lds0 + L_CNT1 + (t >> 3) * 4;
            PIN(cna);
            const bool v0 = e0 < E, v1 = e0 + 1024 < E, v2 = e0 + 2048 < E, v3 = w == 0 && e1 < E;
            u32x16 pd, pe, pr;
            const u64 over = rank_edges(ed, es, mykey, pd, pe, pr, __builtin_amdgcn_ballot_w64(v0), __builtin_amdgcn_ballot_w64(v1), __builtin_amdgcn_ballot_w64(v2),
                                        __builtin_amdgcn_ballot_w64(v3), t * 4);
            lds_arrive<L_RANKED>(l); lds_await<L_RANKED, 4, 0>(l);
            f32x32 img; u32x16 meta, sums;
            const unsigned no = read_image(img, meta, sums, ima, cna);
            lds_arrive<L_IMGREAD>(l);
            auto spill = [&](bool few) {
                unsigned pend = 0;
#pragma unroll
                for (int k = 0; k < 13; ++k) pend |= pr[k] >= 4u * CAP1 ? 1u << k : 0u;
#pragma clang loop unroll(disable)
                do {
                    const unsigned low = pend & (0u - pend);
                    unsigned d = 0, e = 0;
#pragma unroll
                    for (int k = 0; k < 13; ++k) { d = low == 1u << k ? pd[k] : d; e = low == 1u << k ? pe[k] : e; }
                    const unsigned v = ((((e >> 8) & 0xFFFF) - BIAS) << 16) | d;
                    const unsigned key = *(LAS unsigned*)(smem + L_CNT1 + 1024 + ((v ^ (v >> 8)) & 0xFF) * 4);
                    const unsigned ps = lds_add(smem + (pend ? L_CNT1 + 2052 : L_DUMP + 4 * l), 1);
                    if (few) *(LAS u32x2*)(smem + (pend ? L_CNT1 + 2064 + ps * 8 : L_DUMP + 512 + 8 * l)) = u32x2{v ^ key, record_check(v, key)};
                    else { const unsigned o = pend ? WS_OVF + (pg * CH + ps) * 4 : OOB; ST1(v, srd_ws, o, " sc1"); }
                    pend ^= low;
                } while (__builtin_amdgcn_ballot_w64(pend != 0));
            };
            if (__builtin_expect(no > CAPX, 0)) {
                if (over) spill(false);
                const unsigned o = t == 0 ? WS_NOVF + pg * 4 : OOB;
                ST1(no, srd_ws, o, " sc1");
                VMWAIT(0);
                lds_arrive<L_SPILLED>(l); lds_await<L_SPILLED, 4>(l);
#pragma unroll
                for (int k = 0; k < 8; ++k) meta[k] |= meta[k] > 4u * CAP1;
            }
            publish_image(img, meta, sums, srd_ws, ob);
            if (__builtin_expect(no - 1 < CAPX, 0)) {
                if (over) spill(true);
                lds_arrive<L_SPILLED>(l);
                if (w == 0) {
                    lds_await<L_SPILLED, 4>(l);
                    const u32x4 r = *(LAS u32x4*)(smem + L_CNT1 + 2064 + pc * 16);
                    const unsigned o = l < 8 ? WS_EXT + pg * CAPX * 8 + l * 16 : OOB;
                    asm volatile("buffer_store_dwordx4 %0, %1, %2, 0 offen sc1\n\ts_nop 1" :: "v"(r), "v"(o), "s"(srd_ws) : "memory");
                }
            }
            t_go = max(t_go, __builtin_amdgcn_s_memrealtime() + POLL_P);
        } else {
            { const unsigned om = WS_KEY + b * 4; asm volatile("buffer_load_dword %0, %1, %2, 0 offen sc1\n\ts_waitcnt vmcnt(0)" : "=&v"(mykey) : "v"(om), "s"(srd_ws) : "memory"); }
            {
                set_m0(lds0 + L_DUMP + w * 256);
                const unsigned o = (((b - 8) ^ (t >> 1)) << 8) + ((t >> 1) << 16) + ((t & 1) << 7);
                asm volatile("s_nop 0\n\tbuffer_load_dword %1, %0, 0 offen lds" :: "s"(srd_x0), "v"(o) : "memory");
            }
            lds_await<L_WQHERE, 4>(l);
            t_go = max(t_go, __builtin_amdgcn_s_memrealtime() + POLL_Q);
            __builtin_amdgcn_s_setprio(2);
        }
        mykey = rfl(mykey) + KEY_STEP;
        loop_constants();
        clear_sums();
        unsigned ib = WS_INBOX + b * (G1 * 128) + t * 16;
        PIN(ib);
        while (__builtin_amdgcn_s_memrealtime() < t_go) __builtin_amdgcn_s_sleep(1);
        u32x16 raw;
        inbox_request(raw, srd_ws, ib);
        if (__builtin_expect(inbox_stage(raw, srd_ws, ib, mykey, 16 * pc - 8, lds0 + L_HDR + (t >> 3) * 4) != 0, 0)) {
            const unsigned hv = *((LAS unsigned*)(smem + L_HDR) + 32 * (l >> 3 & 3) + 8 * w + (l & 7));
            if (l < 32 && (hv & 1)) *(LAS unsigned*)(smem + L_CNT + 64) = 1;
            for (u64 m = __builtin_amdgcn_ballot_w64(l < 32 && hv > 4u * CAP1 && !(hv & 1)); m; m &= m - 1) {
                const int k = __builtin_ctzll(m), gp = 32 * (k >> 3) + 8 * w + (k & 7);
                const unsigned want = (__builtin_amdgcn_readlane(hv, k) >> 2) - CAP1, o = l < CAPX ? WS_EXT + (gp * CAPX + l) * 8 : OOB;
                unsigned u; bool mine;
                for (unsigned spin = 0; spin < (1u << 22); ++spin) {
                    u32x2 r;
                    asm volatile("buffer_load_dwordx2 %0, %1, %2, 0 offen sc1\n\ts_waitcnt vmcnt(0)" : "=&v"(r) : "v"(o), "s"(srd_ws) : "memory");
                    u = r.x ^ mykey;
                    mine = l < CAPX && r.y == record_check(u, mykey) && ((u ^ (u >> 8)) & 0xFF) == (unsigned)b;
                    if (__builtin_popcountll(__builtin_amdgcn_ballot_w64(mine)) == want) break;
                }
                if (mine) {
                    const unsigned e = __builtin_amdgcn_perm((u >> 16) + BIAS, u, 0x01050401u), key = (e >> 21) & 60;
                    *(LAS unsigned*)(smem + L_LIST + key * (SUBB / 4) + min(lds_add(smem + L_CNT + key, 4), SUBB - 4u)) = e;
                }
            }
        }
        __builtin_amdgcn_s_setprio(0);
    } else {
        const int j = w - 4;
        u32x4 wr[8], xr[8];
        {
            const u32x4 srd_q = make_srd(wq, 32768);
            const unsigned o = j * 8192 + l * 16, k4 = 4096;
            LD4(wr[0], srd_q, o, ""); LD4(wr[1], srd_q, o, " offset:1024"); LD4(wr[2], srd_q, o, " offset:2048"); LD4(wr[3], srd_q, o, " offset:3072");
            LD4S(wr[4], srd_q, o, k4, ""); LD4S(wr[5], srd_q, o, k4, " offset:1024"); LD4S(wr[6], srd_q, o, k4, " offset:2048"); LD4S(wr[7], srd_q, o, k4, " offset:3072");
        }
        unsigned ox[8];
#pragma unroll
        for (int m = 0; m < 8; ++m) { ox[m] = node_row(m < 4 ? j : w, m & 3) + n * 16; PIN(ox[m]); }
        unsigned aqa[8], xfa[4];
        unsigned xa = ring + g * 256 + ((n ^ g) << 4), wa = lds0 + L_WQ + j * 16384 + g * 256 + ((n ^ g) << 4) + ((n & 4) << 1);
        {
            const unsigned aq = lds0 + L_WQ + (8 * g + q) * 256 + ((((4 * p) ^ (q | ((g & 1) << 2))) << 4) | ((p & 1) << 3));
#pragma unroll
            for (int s = 0; s < 4; ++s) { xfa[s] = ring + n * 256 + (((4 * s + g) ^ n) << 4); PIN(xfa[s]); }
#pragma unroll
            for (int c = 0; c < 8; ++c) { aqa[c] = aq ^ ((c >> 1) << 4) ^ ((c & 1) << 3); PIN(aqa[c]); }
            PIN(xa); PIN(wa);
        }
        loop_constants();
        asm volatile("s_waitcnt vmcnt(0)" : "+v"(wr[0]), "+v"(wr[1]), "+v"(wr[2]), "+v"(wr[3]), "+v"(wr[4]), "+v"(wr[5]), "+v"(wr[6]), "+v"(wr[7]) :: "memory");
        if (!isprod) lds_arrive<L_WQHERE>(l);
#pragma unroll
        for (int m = 0; m < 8; ++m) LD4(xr[m], srd_x0, ox[m], "");
#pragma unroll
        for (int m = 0; m < 8; ++m) {
            const unsigned a = (wa ^ ((m & 2) << 5)) + m * 1024;
            *(LAS u32x2*)(size_t)a = u32x2{wr[m].x, wr[m].y};
            *(LAS u32x2*)(size_t)(a ^ 8) = u32x2{wr[m].z, wr[m].w};
        }
        asm volatile("s_waitcnt vmcnt(0)" : "+v"(xr[0]), "+v"(xr[1]), "+v"(xr[2]), "+v"(xr[3]), "+v"(xr[4]), "+v"(xr[5]), "+v"(xr[6]), "+v"(xr[7]) :: "memory");
#pragma unroll
        for (int m = 0; m < 8; ++m) *(LAS u32x4*)(size_t)((xa ^ ((m & 3) << 6)) + m * 1024) = xr[m];
        lds_arrive<L_STAGED>(l);
        lds_await<L_STAGED, 4>(l);
        bf16x8 xo[2][4], fr[2][8];
        f32x4 qc[2][8];
        auto wq_frags = [&](int u, bf16x8 (&f)[8]) {
#pragma unroll
            for (int a = 0; a < 8; ++a) {
                LAS char* pa = (LAS char*)(size_t)aqa[a] + u * 16384;
                f[a] = cat(tr_read(pa), tr_read(pa + 1024));
            }
        };
#pragma unroll
        for (int u = 0; u < 4; ++u) { xo[0][u] = *(LAS bf16x8*)(size_t)xfa[u]; xo[1][u] = *(LAS bf16x8*)(size_t)(xfa[u] + 4096); }
        wq_frags(0, fr[0]);
        {
            const Srd4 srd_v = make_srd4(wv, 32768);
            const unsigned r0 = (j * 32 + g) << 8, o0 = (n ^ ((g & 1) | ((g & 2) << 1))) << 4;
            const unsigned e[8] = {r0, r0 + 1024, r0 + 2048, r0 + 3072, r0 + 4096, r0 + 5120, r0 + 6144, r0 + 7168};
            const unsigned base = lds0 + (j < 2 ? L_WV0 + j * 16384 : L_WV1 + (j - 2) * 8192);
            const u32x4 off = {o0, o0 ^ 128, o0, o0 ^ 128};
            dma_half(base, srd_v, e, off); dma_half(base + 4096, srd_v, e + 4, off);
        }
#pragma unroll
        for (int u = 0; u < 4; ++u) {
            if (u < 3) wq_frags(u + 1, fr[(u + 1) & 1]);
            FENCE();
#pragma unroll
            for (int a = 0; a < 8; ++a)
#pragma unroll
                for (int tt = 0; tt < 2; ++tt) qc[tt][a] = mfma(fr[u & 1][a], xo[tt][u], u ? qc[tt][a] : f32x4{0.f, 0.f, 0.f, 0.f});
            FENCE();
        }
        if (isprod) lds_await<L_IMGREAD, 4>(l);
#pragma unroll
        for (int tt = 0; tt < 2; ++tt)
#pragma unroll
            for (int s = 0; s < 4; ++s) {
                const float k = -0.08838834764831845f * 1.4426950408889634f;
                const f32x4 c0 = qc[tt][2 * s] * k, c1 = qc[tt][2 * s + 1] * k;
                *(LAS u32x4*)(size_t)(ring - (tt ? 0 : 65536) + s * 1024 + l * 16) = u32x4{pk2(c0[0], c0[1]), pk2(c0[2], c0[3]), pk2(c1[0], c1[1]), pk2(c1[2], c1[3])};
            }
        clear_sums();
        VMWAIT(0);
        {
            const unsigned o = ((b >> 3) * (PF_KB / 4) + t - 256) * 128;
            set_m0(lds0 + L_DUMP + w * 256);
            asm volatile("s_nop 0\n\tbuffer_load_dword %1, %0, 0 offen lds" :: "s"(srd_x0), "v"(o) : "memory");
        }
    }
    WG_BARRIER();
    __builtin_amdgcn_s_setprio(3);

    u32x16 qf;
    const u32x4 doff = {addr[12], addr[13], addr[14], addr[15]};
    LAS char* mylist = smem + L_LIST + w * (2 * SUBB);
    int gcur = G1, pos = 0;
    unsigned redo = 0;
    auto batch = [&](auto first_, const int l) {
        constexpr bool first = decltype(first_)::value;
        auto entries = [&](int grp, unsigned (&e)[8]) {
#pragma unroll
            for (int m = 0; m < 8; ++m) e[m] = *(LAS unsigned*)(size_t)(lista + grp * 128 + m * 16);
        };
        unsigned cw = *(LAS unsigned*)(size_t)(first ? cnta : lds0 + L_CNT + (l & 31) * 4), e0[8], e1[8];
        entries(0, e0);
        if constexpr (first) {
#pragma unroll
            for (int s = 0; s < 4; ++s) {
                const u32x4 v = *(LAS u32x4*)(size_t)(qa + s * 1024);
                qf[4 * s] = v.x; qf[4 * s + 1] = v.y; qf[4 * s + 2] = v.z; qf[4 * s + 3] = v.w;
            }
        }
        entries(1, e1);
        FENCE();
        int lo = __builtin_amdgcn_readlane(cw, 2 * w) >> 2, hi = __builtin_amdgcn_readlane(cw, 2 * w + 1) >> 2;
        if constexpr (first) {
            if (__builtin_amdgcn_readlane(cw, 16)) gcur = 0;
            if (__builtin_amdgcn_ballot_w64(cw > 4u * CAP2)) { gcur = -1; lo = 0; hi = 0; redo = 1; }
        }
        const int c = lo + hi, G = (c + 31) >> 5;
        const int nlast = G > 2 ? (((c - 1) & 31) >> 2) + 1 : 8;
        auto join = [&]() {
            for (int k = 0; k < G * 32 - lo; k += 64) {
                const unsigned e = *(LAS unsigned*)(mylist + SUBB + (l + k) * 4);
                if (l + k < G * 32 - lo) *(LAS unsigned*)(mylist + (lo + l + k) * 4) = l + k < hi ? e : PAD;
            }
        };
        const bool early = lo >= 64;
        if (__builtin_expect(!early, 0)) { join(); entries(0, e0); entries(1, e1); }
        if constexpr (first) asm volatile("s_waitcnt lgkmcnt(0)" : "+{v[160:175]}"(qf) :: "memory");
        if (G > 0) dma_group(ring, srd_x, e0, doff);
        if constexpr (first) {
            if (G > 1 && w < 6) dma_group(ring + 8192, srd_x, e1, doff);
            wv_resident();
            lds_arrive<L_WVREAD>(l);
            if (w >= 6) { lds_await<L_WVREAD, 8>(l); if (G > 1) dma_group(ring + 8192, srd_x, e1, doff); }
        } else if (G > 1) dma_group(ring + 8192, srd_x, e1, doff);
        f32x16 cc;
#pragma unroll
        for (int m = 0; m < 8; ++m) {
            cc[m] = (e0[m] & 0xFF) == keyn ? 0.f : __uint_as_float(inf);
            cc[8 + m] = (e1[m] & 0xFF) == keyn ? 0.f : __uint_as_float(inf);
        }
        if (early) join();
        if (G > 0) gather_loop(acc, qf, addr, u32x4{lista, keyn, inf, 0u}, cc, srd_x, ring, G, nlast);
    };
    batch(std::true_type{}, l);
    if (__builtin_expect(gcur < G1, 0)) do {
        int t = threadIdx.x;
        PIN(t);
        const int l = t & 63;
        unsigned lim = G1 * 32;
        bool records = false;
        if (gcur >= 0) {
            const unsigned h0 = *(LAS unsigned*)(smem + L_HDR + l * 4), h1 = *(LAS unsigned*)(smem + L_HDR + 256 + l * 4);
            const u64 m0 = __builtin_amdgcn_ballot_w64(l >= gcur && h0 > 4u * CAP1 && ((h0 | redo) & 1));
            const u64 m1 = __builtin_amdgcn_ballot_w64(l + 64 >= gcur && h1 > 4u * CAP1 && ((h1 | redo) & 1));
            if (!(m0 | m1)) break;
            gcur = m0 ? __builtin_ctzll(m0) : 64 + __builtin_ctzll(m1);
            records = !(rfl(*(LAS unsigned*)(smem + L_HDR + gcur * 4)) & 1);
            unsigned v; const unsigned o = WS_NOVF + gcur * 4;
            asm volatile("buffer_load_dword %0, %1, %2, 0 offen sc1\n\ts_waitcnt vmcnt(0)" : "=&v"(v) : "v"(o), "s"(srd_ws) : "memory");
            lim = records ? CAPX : min(rfl(v), (unsigned)CH);
        }
        const unsigned i = pos + t;
        bool cand = t < CAP2 && i < lim;
        unsigned u, u2;
        { const unsigned o = cand ? (gcur < 0 ? WS_INBOX + b * (G1 * 128) + i * 4 : records ? WS_EXT + (gcur * CAPX + i) * 8 : WS_OVF + (gcur * CH + i) * 4) : OOB;
          asm volatile("buffer_load_dword %0, %2, %3, 0 offen sc1\n\tbuffer_load_dword %1, %2, %3, 0 offen offset:4 sc1\n\ts_waitcnt vmcnt(0)"
                       : "=&v"(u), "=&v"(u2) : "v"(o), "s"(srd_ws) : "memory"); }
        if (records) { u ^= mykey; cand = cand && u2 == record_check(u, mykey); }
        WG_BARRIER();
        if (t < 16) *(LAS unsigned*)(smem + L_CNT + t * 4) = 0;
        WG_BARRIER();
        if (cand) {
            unsigned e; bool v;
            if (gcur < 0) { e = (i & 3) ? u : u ^ mykey; v = ((i & 31) - 2) * 4 < *(LAS unsigned*)(smem + L_HDR + (i >> 5) * 4); }
            else { e = (((u >> 16) + BIAS) << 8) | ((u >> 8) & 0xFF); v = ((u ^ (u >> 8)) & 0xFF) == (unsigned)b; }
            if (v) *(LAS unsigned*)(smem + L_LIST + (e & 7) * (2 * SUBB) + lds_add(smem + L_CNT + (e & 7) * 8, 4)) = e;
        }
        pos += CAP2;
        if ((unsigned)pos >= lim) { ++gcur; pos = 0; }
        WG_BARRIER();
        batch(std::false_type{}, l);
    } while (gcur < G1);
    output_tile(acc, wva, wvb, cv, ow, qa, go, make_srd(out, xbytes));
    if (w == 0) { const unsigned o = t == 0 ? WS_KEY + b * 4 : OOB; ST1(mykey, srd_ws, o, " sc1"); }
}

extern "C" void kernel_launch(void* const* d_in, const int* in_sizes, int n_in,
                              void* d_out, int out_size, void* d_ws, size_t ws_size,
                              hipStream_t stream) {
    const int N = in_sizes[0] / 128, E = in_sizes[1];
    k_graph<<<NB, 512, 0, stream>>>((const unsigned short*)d_in[0], (const int*)d_in[1], (const int*)d_in[2],
                                    (const unsigned short*)d_in[3], (const unsigned short*)d_in[4],
                                    (unsigned*)d_ws, (unsigned short*)d_out, N, E);
}
```
